```python
import torch

def _softmax(x, axis=-1):
    return torch.softmax(x, dim=axis)
def _gelu(x):
    return torch.nn.functional.gelu(x, approximate="tanh")
def _silu(x):
    return torch.nn.functional.silu(x)
def _sigmoid(x):
    return torch.sigmoid(x)
def _layernorm(x, axis=-1, eps=1e-5):
    m = x.mean(dim=axis, keepdim=True)
    v = x.var(dim=axis, keepdim=True, unbiased=False)
    return (x - m) / torch.sqrt(v + eps)
def _scatter_add(tgt, idx, src, axis=0):
    return tgt.clone().index_add_(axis, idx, src)
def _segment_sum(src, seg, n):
    out = torch.zeros((n,) + src.shape[1:], dtype=src.dtype)
    return out.index_add_(0, seg, src)
def _pad(x, pw):
    flat = tuple(v for pair in reversed(pw) for v in pair)
    return torch.nn.functional.pad(x, flat)

kernel_name = 'concat_heads__ssm_conv_h3__ssm_conv_s5__prompt'

def setup_inputs():

    _w = (lambda a: (lambda t: t.to(torch.bfloat16) if t.is_floating_point() else t)(torch.as_tensor(a)))
    def _A_setup():
        B, S, D = 1, 2048, 1024
        ks = 4
        return {
            "q": _w((lambda *s: torch.randn(*s, dtype=torch.bfloat16) * 0.05)(B, S, D)),
            "k": _w((lambda *s: torch.randn(*s, dtype=torch.bfloat16) * 0.2)(B, S, D)),
            "v": _w((lambda *s: torch.randn(*s, dtype=torch.bfloat16) * 0.2)(B, S, D)),
            "shift_k": _w((lambda *s: torch.randn(*s, dtype=torch.bfloat16) * 0.2)(D, ks) * 0.5),
            "ssm_k": _w((lambda *s: torch.randn(*s, dtype=torch.bfloat16) * 0.8)(D, S) * (1.0 / S ** 0.5)),
            "ssm_D": _w((lambda *s: torch.randn(*s, dtype=torch.bfloat16) * 0.2)(D)),
        }
    _A_in = _A_setup()

    def _B_setup():
        B, S, D = 1, 2048, 1024
        P = D
        Lambda_re = -0.5 * (lambda _x: torch.exp(torch.as_tensor(_x)))((lambda *s: torch.randn(*s, dtype=torch.bfloat16) * 0.2)(P))
        Lambda_im = (lambda *s: torch.randn(*s, dtype=torch.bfloat16) * 0.4)(P) * 1.0
        B_re = (lambda *s: torch.randn(*s, dtype=torch.bfloat16) * 0.2)(P, D) * (D ** -0.5)
        B_im = (lambda *s: torch.randn(*s, dtype=torch.bfloat16) * 0.2)(P, D) * (D ** -0.5)
        C_re = (lambda *s: torch.randn(*s, dtype=torch.bfloat16) * 0.2)(D, P) * (P ** -0.5)
        C_im = (lambda *s: torch.randn(*s, dtype=torch.bfloat16) * 0.2)(D, P) * (P ** -0.5)
        log_dt = (lambda *s: torch.randn(*s, dtype=torch.bfloat16) * 0.2)(P) * 0.3 - 2.0
        Dskip = (lambda *s: torch.randn(*s, dtype=torch.bfloat16) * 0.05)(D)
        return {
            "x": _w((lambda *s: torch.randn(*s, dtype=torch.bfloat16) * 0.05)(B, S, D)),
            "Lambda_re": _w(Lambda_re),
            "Lambda_im": _w(Lambda_im),
            "B_re": _w(B_re),
            "B_im": _w(B_im),
            "C_re": _w(C_re),
            "C_im": _w(C_im),
            "log_dt": _w(log_dt),
            "Dskip": _w(Dskip),
        }
    _B_in = _B_setup()

    return {
        "A_q": _A_in["q"],
        "A_k": _A_in["k"],
        "A_v": _A_in["v"],
        "A_shift_k": _A_in["shift_k"],
        "A_ssm_k": _A_in["ssm_k"],
        "A_ssm_D": _A_in["ssm_D"],
        "B_x": _B_in["x"],
        "B_Lambda_re": _B_in["Lambda_re"],
        "B_Lambda_im": _B_in["Lambda_im"],
        "B_B_re": _B_in["B_re"],
        "B_B_im": _B_in["B_im"],
        "B_C_re": _B_in["C_re"],
        "B_C_im": _B_in["C_im"],
        "B_log_dt": _B_in["log_dt"],
        "B_Dskip": _B_in["Dskip"],
    }

def reference(A_q, A_k, A_v, A_shift_k, A_ssm_k, A_ssm_D, B_x, B_Lambda_re, B_Lambda_im, B_B_re, B_B_im, B_C_re, B_C_im, B_log_dt, B_Dskip):
    def _A_ref(q, k, v, shift_k, ssm_k, ssm_D):
        B, S, D = q.shape
        ks = shift_k.shape[-1]
        k_pad = _pad(k, ((0, 0), (ks - 1, 0), (0, 0)))
        k_shift = torch.zeros_like(k)
        for t in range(ks):
            k_shift = k_shift + k_pad[:, t:t + S, :] * shift_k[:, ks - 1 - t][None, None, :]
        kv = k_shift * v
        L = 2 * S
        kv_pad = _pad(kv, ((0, 0), (0, S), (0, 0)))
        kf_pad = _pad(ssm_k, ((0, 0), (0, S)))
        kv_f = (lambda x, axes=(-2,-1): torch.fft.rfft2(x.float(), dim=axes))(kv_pad[:, None, :, :], axes=(1, 2))
        kf_f = (lambda x, axes=(-2,-1): torch.fft.rfft2(x.float(), dim=axes))(kf_pad[:, None, :], axes=(1, 2))
        kf_f = (lambda x, *d: x.permute(*d))(kf_f, 1, 2, 0)
        y_f = kv_f * kf_f[None, :, :, :]
        y = (lambda x, s, axes=(-2,-1): torch.fft.irfft2(x, s=s, dim=axes).to(torch.bfloat16))(y_f, (1, L), axes=(1, 2))[:, 0, :S, :]
        y = y + kv * ssm_D[None, None, :]
        return q * y

    def _B_ref(x, Lambda_re, Lambda_im, B_re, B_im, C_re, C_im, log_dt, Dskip):
        Bsz, L, H = x.shape
        Lam = Lambda_re + 1j * Lambda_im
        Bmat = B_re + 1j * B_im
        Cmat = C_re + 1j * C_im
        dt = torch.exp(log_dt)
        Lam_bar = torch.exp(dt * Lam)
        B_bar = ((Lam_bar - 1.0) / Lam)[:, None] * Bmat
        Bu = torch.einsum("bld,pd->blp", x + 0j, B_bar)
        h = torch.zeros_like(Bu[:, 0, :])
        hs = []
        for t in range(L):
            h = Lam_bar[None, :] * h + Bu[:, t, :]
            hs.append(h)
        hseq = torch.stack(hs, 1)
        y = 2.0 * torch.einsum("blp,dp->bld", hseq, Cmat).real + x * Dskip[None, None, :]
        return y

    a = _A_ref(A_q, A_k, A_v, A_shift_k, A_ssm_k, A_ssm_D)
    b = _B_ref(B_x, B_Lambda_re, B_Lambda_im, B_B_re, B_B_im, B_C_re, B_C_im, B_log_dt, B_Dskip)
    return torch.concatenate([a, b], axis=-1)
```

```cpp
#include <hip/hip_runtime.h>
#include <stdint.h>
#include <type_traits>

namespace fk {

constexpr int S = 2048, D = 1024, P = 1024;

typedef __attribute__((ext_vector_type(8))) short bf16x8;
typedef __attribute__((ext_vector_type(4))) short s16x4;
typedef __attribute__((ext_vector_type(16))) float f32x16;
typedef __attribute__((ext_vector_type(4))) float f32x4;
typedef __attribute__((ext_vector_type(2))) float f32x2;
typedef __attribute__((ext_vector_type(4))) unsigned u32x4;
typedef __attribute__((ext_vector_type(2))) unsigned u32x2;
#define LDSP(T, a) ((__attribute__((address_space(3))) T*)(uintptr_t)(unsigned)(a))

__device__ __forceinline__ float bf2f(unsigned v) { return __uint_as_float(v << 16); }
__device__ __forceinline__ float bflo(unsigned w) { return __uint_as_float(w << 16); }
__device__ __forceinline__ float bfhi(unsigned w) { return __uint_as_float(w & 0xffff0000u); }
__device__ __forceinline__ uint16_t f2bf(float f) { __bf16 b = (__bf16)f; return __builtin_bit_cast(uint16_t, b); }
__device__ __forceinline__ float rbf(float f) { return bf2f(f2bf(f)); }
__device__ __forceinline__ unsigned pack2(float lo, float hi) { return (unsigned)f2bf(lo) | ((unsigned)f2bf(hi) << 16); }
__device__ __forceinline__ void cmul(float ar, float ai, float br, float bi, float& cr, float& ci) { cr = ar * br - ai * bi; ci = ar * bi + ai * br; }

template <int I, int N, class F>
__device__ __forceinline__ void static_for(F&& f) { if constexpr (I < N) { f(std::integral_constant<int, I>{}); static_for<I + 1, N>(f); } }

#define TS_DECL
#define TS(i)
#define TS_FLUSH()

#define WAIT_V(n) asm volatile("s_waitcnt vmcnt(%0)" ::"n"((n) > 63 ? 63 : (n)) : "memory")
#define WAIT_L(n) asm volatile("s_waitcnt lgkmcnt(%0)" ::"n"((n) > 15 ? 15 : (n)) : "memory")
#define BAR() asm volatile("s_barrier" ::: "memory")
#define SB() __builtin_amdgcn_sched_barrier(0)
template <class T> __device__ __forceinline__ void tie_(T& x) { asm volatile("" : "+v"(x)); }
#define TIE(x) tie_(x)
#define DEF_LD(name, ins) template <int OFF, class T> __device__ __forceinline__ void name(T& dst, unsigned voff, const char* sb) { \
    asm volatile(ins : "=v"(dst) : "v"(voff), "s"(sb), "n"(OFF) : "memory"); }
DEF_LD(gld_u16_, "global_load_ushort %0, %1, %2 offset:%3")
DEF_LD(gld_b32_, "global_load_dword %0, %1, %2 offset:%3")
DEF_LD(gld_b64_, "global_load_dwordx2 %0, %1, %2 offset:%3")
DEF_LD(gld_b128_, "global_load_dwordx4 %0, %1, %2 offset:%3")
DEF_LD(gld_b64_sc1_, "global_load_dwordx2 %0, %1, %2 offset:%3 sc1")
DEF_LD(gld_b128_sc1_, "global_load_dwordx4 %0, %1, %2 offset:%3 sc1")
#define DEF_ST(name, ins) template <int OFF, class T> __device__ __forceinline__ void name(unsigned voff, const T& v, const char* sb) { \
    asm volatile(ins :: "v"(voff), "v"(v), "s"(sb), "n"(OFF) : "memory"); }
DEF_ST(gst_b128_, "global_store_dwordx4 %0, %1, %2 offset:%3\n\ts_nop 1")
DEF_ST(gst_b128_sc1_, "global_store_dwordx4 %0, %1, %2 offset:%3 sc1\n\ts_nop 1")
DEF_ST(gst_b64_sc1_, "global_store_dwordx2 %0, %1, %2 offset:%3 sc1")
#define GLD_U16(dst, voff, sb) gld_u16_<0>(dst, voff, sb)
#define GLD_B32(dst, voff, sb, off) gld_b32_<(off)>(dst, voff, sb)
#define GLD_B64(dst, voff, sb, off) gld_b64_<(off)>(dst, voff, sb)
#define GLD_B128(dst, voff, sb, off) gld_b128_<(off)>(dst, voff, sb)
#define GLD_B64_SC1(dst, voff, sb, off) gld_b64_sc1_<(off)>(dst, voff, sb)
#define GLD_B128_SC1(dst, voff, sb, off) gld_b128_sc1_<(off)>(dst, voff, sb)
#define GST_B128(voff, v, sb, off) gst_b128_<(off)>(voff, v, sb)
#define GST_B128_SC1(voff, v, sb, off) gst_b128_sc1_<(off)>(voff, v, sb)
#define GST_B64_SC1(voff, v, sb) gst_b64_sc1_<0>(voff, v, sb)
__device__ __forceinline__ void dma_(unsigned ldsaddr, unsigned voff, const char* sb) {
    asm volatile("s_mov_b32 m0, %0\n\ts_nop 0\n\tglobal_load_lds_dwordx4 %1, %2" :: "s"(ldsaddr), "v"(voff), "s"(sb) : "memory"); }
#define DMA(ldsaddr, voff, sb) dma_(ldsaddr, voff, sb)
template <int OFF, class T> __device__ __forceinline__ void dsr128_(T& dst, unsigned addr) { asm volatile("ds_read_b128 %0, %1 offset:%2" : "=v"(dst) : "v"(addr), "n"(OFF)); }
template <int OFF, class T> __device__ __forceinline__ void dsr64_(T& dst, unsigned addr) { asm volatile("ds_read_b64 %0, %1 offset:%2" : "=v"(dst) : "v"(addr), "n"(OFF)); }
#define DSR128(dst, addr, off) dsr128_<(off)>(dst, addr)
#define DSR64(dst, addr, off) dsr64_<(off)>(dst, addr)

constexpr size_t WS_H = 0, WS_KVT = (size_t)8 << 20, WS_ZT = (size_t)12 << 20, WS_E = (size_t)16 << 20, WS_FLAG = WS_E + (256 << 10);
constexpr int FL_KV = 0, FL_Z = 4096, FL_H = 8192, FL_EPOCH = 16384;
constexpr unsigned SPIN_CAP = 1u << 17;

constexpr int G1STAGE = 32768;
constexpr int L_KVT = 131072;
constexpr int L_AGG = 131072, L_ES = 135168;
constexpr int L_HT = 98304;
constexpr int CST = 4288;
constexpr int UOFF = 4 * CST, UPL = 768;
constexpr int CHSZ = UOFF + 8 * UPL;
constexpr int L_C = 98304, G2STAGE = 16384;
constexpr int L_ZI = 0;
constexpr int L_Y = 24576;
static_assert(4 * CHSZ <= L_HT, "lds");
static_assert(L_Y + 2 * 128 * 68 * 4 <= L_C, "lds");

__device__ __forceinline__ int img_off(int r, int c) { return (r >> 1) * 256 + ((((r & 1) * 8 + c) ^ ((r >> 1) & 15)) << 4); }

constexpr int PF = 2;
constexpr int g1_dma(int g) { return ((g + PF) / 4 >= 1 && (g + PF) / 4 + 3 < 16) ? 1 : 0; }
constexpr int g1_extra(int g) { return g == 50 ? 2 : (g == 60 ? 1 : 0); }
constexpr int g1_cnt(int s) {
    int g = 4 * s - PF, c = 0;
    if (s <= 3) { c = (3 - s) * 4 + 4 + 2; for (int t = 0; t < g; ++t) c += g1_dma(t) + g1_extra(t); }
    else { int t1 = 4 * (s - 3) - PF + 3; c = g1_extra(t1); for (int t = t1 + 1; t < g; ++t) c += g1_dma(t) + g1_extra(t); }
    return c;
}
constexpr int AW = 8;
constexpr int g2_dma(int g) { return ((g + PF) / 4 >= 1 && (g + PF) / 4 + 3 < 16 && (g + PF) % 4 < 2) ? 1 : 0; }
constexpr int g2_a(int g) { return g + AW < 64 ? 1 : 0; }
constexpr int G2_Q = 12, G2_ZF = 28, G2_ZL = 38, G2_ZC = 50;
constexpr int g2_extra(int g) { return (g >= G2_Q && g < G2_Q + 9) ? 1 : (g == G2_ZF ? 1 : ((g == G2_ZL || g == G2_ZL + 1) ? 1 : 0)); }
constexpr int g2_ops(int g) { return g2_dma(g) + g2_a(g) + g2_extra(g); }
constexpr int g2_cnt_stage(int s) {
    int g = 4 * s - PF, t1 = 4 * (s - 3) - PF + 1, c = g2_a(t1) + g2_extra(t1);
    for (int t = t1 + 1; t < g; ++t) c += g2_ops(t);
    return c;
}
constexpr int g2_cnt_a(int g) {
    int c = 0;
    if (g < AW) { c = AW - 1 - g + 2; for (int t = 0; t < g; ++t) c += g2_ops(t); }
    else { c = g2_extra(g - AW); for (int t = g - AW + 1; t < g; ++t) c += g2_ops(t); }
    return c;
}

struct Args {
    const char *q, *k, *v, *sk, *ssmk, *ssmD, *x, *lre, *lim, *bre, *bim, *cre, *cim, *logdt, *dskip;
    char *out, *ws;
};

__global__ __launch_bounds__(512, 2) void fused(Args a) {
    __shared__ __attribute__((aligned(1024))) char shm[163840];
    char* const ws = a.ws;
    TS_DECL TS(0);
    const int tid = threadIdx.x, lane = tid & 63, wid = __builtin_amdgcn_readfirstlane(tid >> 6);
    const int b = blockIdx.x, xcd = b & 7, idx = b >> 3;
    const int tm = (xcd >> 1) * 4 + (idx >> 3), tn = (xcd & 1) * 8 + (idx & 7);
    const int wm = wid & 3, wn = wid >> 2, l31 = lane & 31, lh = lane >> 5;
    const unsigned lb = (unsigned)(uintptr_t)shm;
    const char* const flg = ws + WS_FLAG;

    unsigned c_dt, c_re, c_im; u32x2 ep;
    {
        const unsigned po = (tn * 64 + wn * 32 + l31) * 2, z = 0;
        GLD_U16(c_dt, po, a.logdt); GLD_U16(c_re, po, a.lre); GLD_U16(c_im, po, a.lim);
        GLD_B64_SC1(ep, z, flg + FL_EPOCH, 0);
    }
    unsigned kk[11], vv[8]; u32x4 kw;
    {
        const int s0 = idx * 64 + wid * 8;
        const unsigned vo = xcd * 256 + lane * 4;
#pragma unroll
        for (int i = 0; i < 11; ++i) { int s = s0 - 3 + i; s = s < 0 ? 0 : s; GLD_B32(kk[i], vo, a.k + (size_t)s * 2048, 0); }
#pragma unroll
        for (int i = 0; i < 8; ++i) GLD_B32(vv[i], vo, a.v + (size_t)(s0 + i) * 2048, 0);
        GLD_B128(kw, vo * 4, a.sk, 0);
    }
    unsigned dv[4];
#pragma unroll
    for (int i = 0; i < 4; ++i) {
        const int pair = i * 4 + (lane >> 4), v = (lane & 15) ^ pair;
        dv[i] = (pair * 2 + (v >> 3)) * 2048 + (v & 7) * 16;
    }
    const char* g1src = wid < 4 ? a.x + (size_t)(tm * 128 + wid * 32) * 2048
                                : ((wid & 1) ? a.bim : a.bre) + (size_t)(tn * 64 + ((wid >> 1) & 1) * 32) * 2048;
    const int rot = (b * 5) & 15;
    const unsigned g1dst = lb + wid * 4096;
#define G1_ISSUE(st, i) DMA(g1dst + ((st) & 3) * G1STAGE + (i) * 1024, dv[i], g1src + ((((st) + rot) & 15) << 7))
    static_for<0, 16>([&](auto ic) { constexpr int n = decltype(ic)::value; G1_ISSUE(n >> 2, n & 3); });
    const int chn = xcd * 128 + idx * 4 + wm;
    u32x4 sw[2]; u32x2 sp[2];
    {
        const char* krow = a.ssmk + (size_t)chn * 4096;
        const unsigned o = (wn * 128 + lane) * 16;
        GLD_B128(sw[0], o, krow, 0); GLD_B128(sw[1], o, krow, 1024);
        const unsigned o0 = o ? o - 8 : 0;
        GLD_B64(sp[0], o0, krow, 0); GLD_B64(sp[1], o, krow, 1024 - 8);
    }
    float Lr, Li, cr, ci, L16r, L16i, L128r, L128i, Prr, Pri;
    unsigned tagA, tagB;
    {
        WAIT_V(20 + 16 + 4);
        TIE(c_dt); TIE(c_re); TIE(c_im); TIE(ep);
        const float dt = rbf(expf(bf2f(c_dt)));
        const float lr = bf2f(c_re), li = bf2f(c_im);
        const float e = expf(dt * lr);
        float sn, cs;
        sincosf(dt * li, &sn, &cs);
        Lr = e * cs; Li = e * sn;
        const float nr = Lr - 1.f, ni = Li, den = lr * lr + li * li;
        cr = (nr * lr + ni * li) / den; ci = (ni * lr - nr * li) / den;
        float pr = Lr, pi = Li;
#pragma unroll
        for (int it = 0; it < 4; ++it) { float tr, ti; cmul(pr, pi, pr, pi, tr, ti); pr = tr; pi = ti; }
        L16r = pr; L16i = pi;
        float qr = lh ? pr : 1.f, qi = lh ? pi : 0.f;
#pragma unroll
        for (int it = 0; it < 3; ++it) {
            float tr, ti; cmul(pr, pi, pr, pi, tr, ti); pr = tr; pi = ti;
            if (it < 2 && ((wm >> it) & 1)) { cmul(qr, qi, pr, pi, tr, ti); qr = tr; qi = ti; }
        }
        L128r = pr; L128i = pi; Prr = qr; Pri = qi;
        TIE(Lr); TIE(Li); TIE(cr); TIE(ci); TIE(L16r); TIE(L16i); TIE(L128r); TIE(L128i); TIE(Prr); TIE(Pri);
        const unsigned e0 = __builtin_amdgcn_readfirstlane(ep[0]), e1 = __builtin_amdgcn_readfirstlane(ep[1]);
        tagA = e0 + 1; tagB = (e1 + (tagA == 0 ? 1u : 0u));
    }
    const unsigned tagG = tagA ^ tagB ^ 0x9E3779B9u;
    {
        WAIT_V(16 + 4);
#pragma unroll
        for (int i = 0; i < 11; ++i) TIE(kk[i]);
#pragma unroll
        for (int i = 0; i < 8; ++i) TIE(vv[i]);
        TIE(kw);
        if (idx == 0 && wid == 0) { kk[0] = 0; kk[1] = 0; kk[2] = 0; }
#pragma unroll
        for (int c = 0; c < 2; ++c) {
            const float w0 = bflo(kw[2 * c]), w1 = bfhi(kw[2 * c]), w2 = bflo(kw[2 * c + 1]), w3 = bfhi(kw[2 * c + 1]);
            float o[8];
#pragma unroll
            for (int i = 0; i < 8; ++i) {
                float t = rbf((c ? bfhi(kk[i]) : bflo(kk[i])) * w3);
                t = rbf(t + rbf((c ? bfhi(kk[i + 1]) : bflo(kk[i + 1])) * w2));
                t = rbf(t + rbf((c ? bfhi(kk[i + 2]) : bflo(kk[i + 2])) * w1));
                t = rbf(t + rbf((c ? bfhi(kk[i + 3]) : bflo(kk[i + 3])) * w0));
                o[i] = t * (c ? bfhi(vv[i]) : bflo(vv[i]));
            }
            *LDSP(u32x4, lb + L_KVT + (lane * 2 + c) * 128 + ((wid ^ (lane & 7)) << 4)) =
                u32x4{pack2(o[7], o[6]), pack2(o[5], o[4]), pack2(o[3], o[2]), pack2(o[1], o[0])};
        }
    }
    TS(1);
    WAIT_V(12 + 4); WAIT_L(0); BAR();
    TS(2);
    {
        const char* kvd = ws + WS_KVT + (size_t)(xcd * 128) * 4096 + idx * 128;
#pragma unroll
        for (int it = 0; it < 2; ++it) {
            const int ch = (tid >> 3) + 64 * it;
            const u32x4 r = *LDSP(u32x4, lb + L_KVT + tid * 16 + it * 8192);
            GST_B128_SC1(ch * 4096 + (((tid & 7) ^ ((ch >> 1) & 7)) << 4), r, kvd, 0);
        }
    }

    f32x16 acc[2];
    u32x2 fl;
    unsigned touch0, touch1;
    {
#pragma unroll
        for (int j = 0; j < 2; ++j)
#pragma unroll
            for (int r = 0; r < 16; ++r) acc[j][r] = 0.f;
        const int prow = 16 * ((l31 >> 2) & 1) + 4 * (l31 >> 3) + (l31 & 3);
        const int aoff = img_off(wm * 32 + prow, lh), boff = img_off(128 + wn * 64 + l31, lh);
        unsigned aad[4], bad[4];
#pragma unroll
        for (int ks = 0; ks < 4; ++ks) { aad[ks] = lb + (aoff ^ (ks * 32)); bad[ks] = lb + (boff ^ (ks * 32)); }
        bf16x8 fa[PF + 1], fb[PF + 1][2];
#define G1_FRAGS(g) do { constexpr int bo_ = (((g) >> 2) & 3) * G1STAGE, hi_ = bo_ >= 65536 ? 65536 : 0; \
        DSR128(fa[(g) % (PF + 1)], aad[(g) & 3] + hi_, bo_ - hi_); \
        DSR128(fb[(g) % (PF + 1)][0], bad[(g) & 3] + hi_, bo_ - hi_); DSR128(fb[(g) % (PF + 1)][1], bad[(g) & 3] + hi_, bo_ - hi_ + 4096); } while (0)
        static_for<0, PF>([&](auto ic) { constexpr int g = decltype(ic)::value; G1_FRAGS(g); });
        static_for<0, 64>([&](auto ic) {
            constexpr int g = decltype(ic)::value, gn = g + PF;
            if constexpr ((gn & 3) == 0 && gn < 64) {
                WAIT_V(g1_cnt(gn >> 2)); WAIT_L(0); BAR();
                if constexpr ((gn >> 2) == 4) {
                    if (tid == 0) { const u32x2 t = {tagA, tagB}; GST_B64_SC1(xcd * 256 + idx * 8, t, flg + FL_KV); }
                }
            }
            if constexpr (gn < 64) G1_FRAGS(gn);
            SB();
            WAIT_L((63 - g < PF ? 63 - g : PF) * 3);
            TIE(fa[g % (PF + 1)]); TIE(fb[g % (PF + 1)][0]); TIE(fb[g % (PF + 1)][1]);
            acc[0] = __builtin_amdgcn_mfma_f32_32x32x16_bf16(fa[g % (PF + 1)], fb[g % (PF + 1)][0], acc[0], 0, 0, 0);
            SB();
            if constexpr (g1_dma(g)) G1_ISSUE(gn / 4 + 3, gn & 3);
            if constexpr (g == 50) {
                const unsigned line = idx * 512 + tid;
                GLD_B32(touch0, ((xcd & 1) * 512 + ((line >> 4) & 511)) * 2048 + (line & 15) * 128, (idx & 16) ? a.cim : a.cre, 0);
                GLD_B32(touch1, ((tid >> 1) & 63) * 2048 + (tid & 1) * 128, a.q + (size_t)idx * 64 * 2048 + xcd * 256, 0);
            }
            if constexpr (g == 60) GLD_B64_SC1(fl, xcd * 256 + l31 * 8, flg + FL_KV, 0);
            SB();
            acc[1] = __builtin_amdgcn_mfma_f32_32x32x16_bf16(fa[g % (PF + 1)], fb[g % (PF + 1)][1], acc[1], 0, 0, 0);
            SB();
        });
#undef G1_FRAGS
    }
    TS(3);
    float cinr, cini;
    {
        float gr = 0.f, gi = 0.f;
#pragma unroll
        for (int r = 0; r < 16; ++r) {
            const float nr = Lr * gr - Li * gi + acc[0][r], ni = Lr * gi + Li * gr + acc[1][r];
            gr = nr; gi = ni;
        }
        const int run = wm * 2 + lh;
        *LDSP(f32x2, lb + L_AGG + (run * 64 + wn * 32 + l31) * 8) = f32x2{gr, gi};
        WAIT_L(0); BAR();
        float fr = 0.f, fi = 0.f;
        cinr = 0.f; cini = 0.f;
#pragma unroll
        for (int j = 0; j < 8; ++j) {
            const f32x2 e = *LDSP(f32x2, lb + L_AGG + (j * 64 + wn * 32 + l31) * 8);
            const float nr = L16r * fr - L16i * fi + e[0], ni = L16r * fi + L16i * fr + e[1];
            fr = nr; fi = ni;
            if (j + 1 == run) { cinr = fr; cini = fi; }
        }
        if (wm == 0 && lh == 0) {
            const u32x4 gq = {__float_as_uint(fr), tagA, __float_as_uint(fi), tagG};
            GST_B128_SC1((tm * 1024 + tn * 64 + wn * 32 + l31) * 16, gq, ws + WS_E, 0);
        }
    }
    TS(4);
    const unsigned cb = lb + wm * CHSZ;
    u32x4 uu[2], eg[2];
    const int ej0 = 2 * wid - (15 - tm);
    {
        WAIT_V(0);
        TIE(fl); TIE(touch0); TIE(touch1);
        for (unsigned spin = 0; !__all(fl[0] == tagA && fl[1] == tagB) && spin < SPIN_CAP; ++spin) {
            __builtin_amdgcn_s_sleep(1);
            GLD_B64_SC1(fl, xcd * 256 + l31 * 8, flg + FL_KV, 0);
            WAIT_V(0); TIE(fl);
        }
        const char* urow = ws + WS_KVT + (size_t)chn * 4096;
        GLD_B128_SC1(uu[0], (wn * 128 + lane) * 16, urow, 0); GLD_B128_SC1(uu[1], (wn * 128 + lane) * 16, urow, 1024);
#pragma unroll
        for (int i = 0; i < 2; ++i) { const int j = ej0 + i < 0 ? 0 : (ej0 + i > 15 ? 15 : ej0 + i); GLD_B128_SC1(eg[i], (tn * 64 + lane) * 16, ws + WS_E + j * 16384, 0); }
    }
    {
        TIE(sw[0]); TIE(sw[1]); TIE(sp[0]); TIE(sp[1]);
        if (wn == 0 && lane == 0) sp[0] = u32x2{0u, 0u};
        const u32x4 z4 = {0, 0, 0, 0};
        if (wn == 0) { if (lane < 32) *LDSP(u32x4, cb + (lane >> 3) * CST + (lane & 7) * 16) = z4; }
        *LDSP(u32x4, cb + UOFF + (wn * 4 + (lane >> 4)) * UPL + (lane & 15) * 16) = z4;
#pragma unroll
        for (int it = 0; it < 2; ++it) {
            const unsigned q = cb + 128 + (wn * 128 + it * 64 + lane) * 16;
            const unsigned d0 = sw[it][0], d1 = sw[it][1], d2 = sw[it][2], d3 = sw[it][3], e0 = sp[it][0], e1 = sp[it][1];
            const unsigned a0 = __builtin_amdgcn_alignbyte(d0, e1, 2), a1 = __builtin_amdgcn_alignbyte(d1, d0, 2), a2 = __builtin_amdgcn_alignbyte(d2, d1, 2),
                           a3 = __builtin_amdgcn_alignbyte(d3, d2, 2);
            *LDSP(u32x4, q) = sw[it];
            *LDSP(u32x4, q + CST) = u32x4{a0, a1, a2, a3};
            *LDSP(u32x4, q + 2 * CST) = u32x4{e1, d0, d1, d2};
            *LDSP(u32x4, q + 3 * CST) = u32x4{__builtin_amdgcn_alignbyte(e1, e0, 2), a0, a1, a2};
            if (it == 1 && wn == 1 && lane == 63) {
                *LDSP(u32x2, q + 16 + CST) = u32x2{d3 >> 16, 0u};
                *LDSP(u32x2, q + 16 + 2 * CST) = u32x2{d3, 0u};
                *LDSP(u32x2, q + 16 + 3 * CST) = u32x2{a3, d3 >> 16};
            }
        }
#pragma unroll
        for (int r = 0; r < 16; ++r) { const float re = acc[0][r], im = acc[1][r]; acc[0][r] = cr * re - ci * im; acc[1][r] = cr * im + ci * re; }
        TIE(acc[0]); TIE(acc[1]);
        WAIT_V(2);
        TIE(uu[0]); TIE(uu[1]);
#pragma unroll
        for (int it = 0; it < 2; ++it) { const int m = wn * 128 + it * 64 + lane; *LDSP(u32x4, cb + UOFF + (m & 7) * UPL + (16 + (m >> 3)) * 16) = uu[it]; }
    }
    TS(5);
    {
        WAIT_V(0);
        TIE(eg[0]); TIE(eg[1]);
        const bool n0 = ej0 >= 0, n1 = ej0 + 1 >= 0 && wid < 7;
        for (unsigned spin = 0; spin < SPIN_CAP; ++spin) {
            const bool ok = (!n0 || (eg[0][1] == tagA && eg[0][3] == tagG)) && (!n1 || (eg[1][1] == tagA && eg[1][3] == tagG));
            if (__all(ok)) break;
            __builtin_amdgcn_s_sleep(1);
#pragma unroll
            for (int i = 0; i < 2; ++i) { const int j = ej0 + i < 0 ? 0 : (ej0 + i > 15 ? 15 : ej0 + i); GLD_B128_SC1(eg[i], (tn * 64 + lane) * 16, ws + WS_E + j * 16384, 0); }
            WAIT_V(0); TIE(eg[0]); TIE(eg[1]);
        }
        *LDSP(u32x2, lb + L_ES + (2 * wid * 64 + lane) * 8) = n0 ? u32x2{eg[0][0], eg[0][2]} : u32x2{0u, 0u};
        if (wid < 7) *LDSP(u32x2, lb + L_ES + ((2 * wid + 1) * 64 + lane) * 8) = n1 ? u32x2{eg[1][0], eg[1][2]} : u32x2{0u, 0u};
        WAIT_L(0); BAR();
    }
    TS(6);
    {
        float gr = 0.f, gi = 0.f;
#pragma unroll
        for (int j = 0; j < 15; ++j) {
            const f32x2 e = *LDSP(f32x2, lb + L_ES + (j * 64 + wn * 32 + l31) * 8);
            const float nr = L128r * gr - L128i * gi + e[0], ni = L128r * gi + L128i * gr + e[1];
            gr = nr; gi = ni;
        }
        const float tr = cinr + Prr * gr - Pri * gi, ti = cini + Prr * gi + Pri * gr;
        float hr = cr * tr - ci * ti, hi = cr * ti + ci * tr;
        float o[2][16];
#pragma unroll
        for (int r = 0; r < 16; ++r) {
            const float nr = Lr * hr - Li * hi + acc[0][r], ni = Lr * hi + Li * hr + acc[1][r];
            hr = nr; hi = ni;
            o[0][r] = hr; o[1][r] = -hi;
        }
        const unsigned ht = lb + L_HT + wid * 4096;
#pragma unroll
        for (int c = 0; c < 2; ++c)
#pragma unroll
            for (int e = 0; e < 2; ++e)
                *LDSP(u32x4, ht + (c * 32 + l31) * 64 + (((lh * 2 + e) ^ ((l31 >> 1) & 3)) << 4)) =
                    u32x4{pack2(o[c][8 * e], o[c][8 * e + 1]), pack2(o[c][8 * e + 2], o[c][8 * e + 3]), pack2(o[c][8 * e + 4], o[c][8 * e + 5]), pack2(o[c][8 * e + 6], o[c][8 * e + 7])};
        const char* hd = ws + WS_H + (size_t)((tm * 4 + wm) * 2) * 65536 + (tn * 4 + wn * 2) * 1024;
        typedef __attribute__((ext_vector_type(4))) short v4i16;
#pragma unroll
        for (int c = 0; c < 2; ++c)
#pragma unroll
            for (int hf = 0; hf < 2; ++hf) {
                v4i16 t[2];
#pragma unroll
                for (int j = 0; j < 2; ++j) {
                    const int row = c * 32 + hf * 16 + 8 * lh + 4 * j + ((lane & 15) >> 2);
                    const int chunk = 2 * ((lane >> 4) & 1) + ((lane & 3) >> 1);
                    t[j] = __builtin_amdgcn_ds_read_tr16_b64_v4i16(LDSP(v4i16, ht + row * 64 + ((chunk ^ ((row >> 1) & 3)) << 4) + 8 * (lane & 1)));
                }
                const bf16x8 f = __builtin_shufflevector(t[0], t[1], 0, 1, 2, 3, 4, 5, 6, 7);
                GST_B128_SC1(lane * 16 + c * 65536 + hf * 1024, f, hd, 0);
            }
    }
    TS(7);
    unsigned cv[2];
#pragma unroll
    for (int i = 0; i < 2; ++i) {
        const int pair = (wid * 2 + i) * 4 + (lane >> 4), v = (lane & 15) ^ (pair & 15);
        cv[i] = (((pair * 2 + (v >> 3)) & 63)) * 2048 + (v & 7) * 16;
    }
    const char* g2src = (wid < 4 ? a.cre : a.cim) + (size_t)(tn * 64) * 2048;
    const unsigned g2dst = lb + L_C + wid * 2048;
#define G2_ISSUE(st, i) DMA(g2dst + ((st) & 3) * G2STAGE + (i) * 1024, cv[i], g2src + (st) * 128)
    f32x4 ca[4][2];
    {
        const int r = lane & 15, g4 = lane >> 4;
        const unsigned kb = cb + ((3 - r) & 3) * CST + 2 * (64 + r - 8 * g4 - 7 + ((3 - r) & 3)) - 64;
        const unsigned ub = cb + UOFF + g4 * UPL + r * 16;
#pragma unroll
        for (int i = 0; i < 4; ++i) { ca[i][0] = f32x4{0, 0, 0, 0}; ca[i][1] = f32x4{0, 0, 0, 0}; }
        s16x4 FA[8][2]; bf16x8 FB[2][2];
#define CV_A(q) do { DSR64(FA[((q) + 2) & 7][0], kb, 32 * ((q) + 2)); DSR64(FA[((q) + 2) & 7][1], kb, 32 * ((q) + 2) + 8); } while (0)
#define CV_B(s, ct) DSR128(FB[(s) & 1][ct], ub, (1 - ((s) & 1)) * 4 * UPL + (16 + 16 * (ct) - ((s) >> 1)) * 16)
        auto conv_range = [&](auto s0c, auto s1c) {
            constexpr int s0 = decltype(s0c)::value, s1 = decltype(s1c)::value;
            CV_A(2 * s0 - 2); CV_A(2 * s0 - 1); CV_A(2 * s0); CV_A(2 * s0 + 1); CV_B(s0, 0); CV_B(s0, 1);
            static_for<s0, s1>([&](auto ic) {
                constexpr int s = decltype(ic)::value;
                constexpr int nnext = s + 1 < s1 ? (4 + ((s + 1) < 32 ? 2 : 1)) : 0;
                if constexpr (s == s0 + 6) {
                    WAIT_V(0); BAR();
                    if (tid == 0) { const u32x2 t = {tagA, tagB}; GST_B64_SC1(tm * 256 + tn * 8, t, flg + FL_H); }
                    static_for<0, 8>([&](auto jc) { constexpr int n = decltype(jc)::value; G2_ISSUE(n >> 1, n & 1); });
                }
                if constexpr (s == s1 - 4) GLD_B64_SC1(fl, tm * 256 + (lane & 15) * 8, flg + FL_H, 0);
                if constexpr (s + 1 < s1) {
                    CV_A(2 * s + 2); CV_A(2 * s + 3);
                    if constexpr (s + 1 < 32) CV_B(s + 1, 0);
                    CV_B(s + 1, 1);
                }
                SB();
                WAIT_L(nnext);
#pragma unroll
                for (int bi = 0; bi < 4; ++bi) { TIE(FA[(2 * s + bi) & 7][0]); TIE(FA[(2 * s + bi) & 7][1]); }
                if constexpr (s < 32) TIE(FB[s & 1][0]);
                TIE(FB[s & 1][1]);
#pragma unroll
                for (int bi = 0; bi < 4; ++bi) {
                    if (2 * s - 2 + bi >= 0) {
                        const bf16x8 fa = __builtin_shufflevector(FA[(2 * s + bi) & 7][0], FA[(2 * s + bi) & 7][1], 0, 1, 2, 3, 4, 5, 6, 7);
                        if constexpr (s < 32) ca[bi][0] = __builtin_amdgcn_mfma_f32_16x16x32_bf16(fa, FB[s & 1][0], ca[bi][0], 0, 0, 0);
                        ca[bi][1] = __builtin_amdgcn_mfma_f32_16x16x32_bf16(fa, FB[s & 1][1], ca[bi][1], 0, 0, 0);
                    }
                }
                SB();
            });
        };
        if (wn == 0) conv_range(std::integral_constant<int, 0>{}, std::integral_constant<int, 24>{});
        else conv_range(std::integral_constant<int, 24>{}, std::integral_constant<int, 64>{});
#undef CV_A
#undef CV_B
    }
    TS(8);
    bf16x8 fa2[AW];
    const char* Aw = ws + WS_H + (size_t)((tm * 4 + wm) * 2 + wn) * 65536;
    const unsigned l16 = lane * 16;
#define G2_A(g) GLD_B128_SC1(fa2[(g) % AW], l16, Aw + ((g) >> 2) * 4096, ((g) & 3) * 1024)
    unsigned dskl;
    {
        GLD_U16(dskl, 0u * lane, a.ssmD + chn * 2);
        WAIT_V(0);
        TIE(fl); TIE(dskl);
        for (unsigned spin = 0; !__all(fl[0] == tagA && fl[1] == tagB) && spin < SPIN_CAP; ++spin) {
            __builtin_amdgcn_s_sleep(1);
            GLD_B64_SC1(fl, tm * 256 + (lane & 15) * 8, flg + FL_H, 0);
            WAIT_V(0); TIE(fl);
        }
        static_for<0, AW>([&](auto ic) { constexpr int g = decltype(ic)::value; G2_A(g); });
        if (b == 255 && tid == 0) { const u32x2 t = {tagA, tagB}; GST_B64_SC1(0u * lane, t, flg + FL_EPOCH); }
    }
    TS(9);
    {
        const int r = lane & 15, g4 = lane >> 4;
#pragma unroll
        for (int bi = 0; bi < 4; ++bi) *LDSP(f32x4, cb + bi * CST + wn * 2048 + l16) = wn ? ca[bi][0] : ca[bi][1];
        WAIT_L(0); BAR();
        const float Dd = bf2f(dskl);
        const unsigned zs = cb + wn * 2 * CST + 3072;
#pragma unroll
        for (int bi = 0; bi < 4; ++bi) {
            const f32x4 o = *LDSP(f32x4, cb + bi * CST + (1 - wn) * 2048 + l16);
            const f32x4 y = (wn ? ca[bi][1] : ca[bi][0]) + o;
            const u32x2 kvr = *LDSP(u32x2, cb + UOFF + (2 * bi + (g4 >> 1)) * UPL + (16 + 16 * wn + r) * 16 + (1 - (g4 & 1)) * 8);
            const float kv3 = bflo(kvr[0]), kv2 = bfhi(kvr[0]), kv1 = bflo(kvr[1]), kv0 = bfhi(kvr[1]);
            const float z0 = rbf(y[0]) + rbf(kv0 * Dd), z1 = rbf(y[1]) + rbf(kv1 * Dd), z2 = rbf(y[2]) + rbf(kv2 * Dd), z3 = rbf(y[3]) + rbf(kv3 * Dd);
            *LDSP(u32x2, zs + (r >> 3) * CST + (r & 7) * 128 + bi * 32 + g4 * 8) = u32x2{pack2(z0, z1), pack2(z2, z3)};
        }
        const char* zd = ws + WS_ZT + (size_t)chn * 4096 + wn * 2048;
#pragma unroll
        for (int it = 0; it < 2; ++it) { const u32x4 t = *LDSP(u32x4, zs + it * CST + l16); GST_B128_SC1(l16 + it * 1024, t, zd, 0); }
    }
    TS(10);
    u32x2 qv[4], xv[4], dsv; u32x4 zv[2];
    bool zok = false;
    const char* zsrc = ws + WS_ZT + (size_t)(xcd * 128 + wid * 16) * 4096 + idx * 128;
    const unsigned zoff = (lane >> 3) * 4096 + (lane & 7) * 16;
    char* const oa = a.out + (size_t)(idx * 64) * 8192 + (xcd * 128 + wid * 16) * 4;
    auto part_a_stage = [&]() {
#pragma unroll
        for (int it = 0; it < 2; ++it) *LDSP(u32x4, lb + L_ZI + wid * 2560 + ((lane >> 3) + 8 * it) * 160 + (lane & 7) * 16) = zv[it];
    };
    auto part_a_out = [&](int m) {
        typedef __attribute__((ext_vector_type(4))) short v4i16;
        const v4i16 t = __builtin_amdgcn_ds_read_tr16_b64_v4i16(LDSP(v4i16, lb + L_ZI + wid * 2560 + (lane >> 2) * 160 + m * 32 + (lane & 3) * 8));
        const u32x2 z = __builtin_bit_cast(u32x2, t);
        const f32x4 o = {rbf(bflo(qv[m][0]) * bflo(z[0])), rbf(bfhi(qv[m][0]) * bfhi(z[0])), rbf(bflo(qv[m][1]) * bflo(z[1])), rbf(bfhi(qv[m][1]) * bfhi(z[1]))};
        GST_B128((m * 16 + (lane & 15)) * 8192 + (lane >> 4) * 16, o, oa, 0);
    };
    {
#pragma unroll
        for (int j = 0; j < 2; ++j)
#pragma unroll
            for (int r = 0; r < 16; ++r) acc[j][r] = 0.f;
        const int boff = img_off(wn * 64 + l31, lh);
        unsigned bad[4];
#pragma unroll
        for (int ks = 0; ks < 4; ++ks) bad[ks] = lb + L_C + (boff ^ (ks * 32));
        bf16x8 fb[PF + 1][2];
#define G2_FRAGS(g) do { constexpr int bo_ = (((g) >> 2) & 3) * G2STAGE; \
        DSR128(fb[(g) % (PF + 1)][0], bad[(g) & 3], bo_); DSR128(fb[(g) % (PF + 1)][1], bad[(g) & 3], bo_ + 4096); } while (0)
        static_for<0, PF>([&](auto ic) { constexpr int g = decltype(ic)::value; G2_FRAGS(g); });
        static_for<0, 64>([&](auto ic) {
            constexpr int g = decltype(ic)::value, gn = g + PF;
            if constexpr ((gn & 3) == 0 && gn < 64) {
                if constexpr ((gn >> 2) >= 4) WAIT_V(g2_cnt_stage(gn >> 2));
                WAIT_L(0); BAR();
                if constexpr (g == 10) {
                    if (tid == 0) { const u32x2 t = {tagA, tagB}; GST_B64_SC1(xcd * 256 + idx * 8, t, flg + FL_Z); }
                }
            }
            if constexpr (gn < 64) G2_FRAGS(gn);
            SB();
            asm volatile("s_waitcnt vmcnt(%0) lgkmcnt(%1)" ::"n"(g2_cnt_a(g)), "n"((63 - g < PF ? 63 - g : PF) * 2) : "memory");
            TIE(fa2[g % AW]); TIE(fb[g % (PF + 1)][0]); TIE(fb[g % (PF + 1)][1]);
            acc[0] = __builtin_amdgcn_mfma_f32_32x32x16_bf16(fb[g % (PF + 1)][0], fa2[g % AW], acc[0], 0, 0, 0);
            SB();
            if constexpr (g2_dma(g)) G2_ISSUE(gn / 4 + 3, gn & 3);
            SB();
            acc[1] = __builtin_amdgcn_mfma_f32_32x32x16_bf16(fb[g % (PF + 1)][1], fa2[g % AW], acc[1], 0, 0, 0);
            SB();
            if constexpr (g2_a(g)) G2_A(g + AW);
            if constexpr (g >= G2_Q && g < G2_Q + 4)
                GLD_B64(qv[g - G2_Q], ((g - G2_Q) * 16 + (lane & 15)) * 2048 + (lane >> 4) * 8, a.q + (size_t)(idx * 64) * 2048 + (xcd * 128 + wid * 16) * 2, 0);
            if constexpr (g >= G2_Q + 4 && g < G2_Q + 8)
                GLD_B64(xv[g - G2_Q - 4], ((g - G2_Q - 4) * 32 + (tid >> 4)) * 2048 + (tid & 15) * 8, a.x + (size_t)(tm * 128) * 2048 + tn * 128, 0);
            if constexpr (g == G2_Q + 8) GLD_B64(dsv, (tid & 15) * 8, a.dskip + tn * 128, 0);
            if constexpr (g == G2_ZF) GLD_B64_SC1(fl, xcd * 256 + l31 * 8, flg + FL_Z, 0);
            if constexpr (g == G2_ZL) { TIE(fl); zok = __all(fl[0] == tagA && fl[1] == tagB); GLD_B128_SC1(zv[0], zoff, zsrc, 0); }
            if constexpr (g == G2_ZL + 1) GLD_B128_SC1(zv[1], zoff, zsrc + 8 * 4096, 0);
            if constexpr (g == G2_ZC) {
                TIE(zv[0]); TIE(zv[1]);
#pragma unroll
                for (int m = 0; m < 4; ++m) TIE(qv[m]);
                if (zok) part_a_stage();
            }
            if constexpr (g > G2_ZC + 1 && g <= G2_ZC + 9 && ((g - G2_ZC) & 1) == 0) { if (zok) part_a_out((g - G2_ZC - 2) >> 1); }
            SB();
        });
#undef G2_FRAGS
    }
    TS(11);
    {
        const unsigned yb = lb + L_Y + (wn * 128 + wm * 32 + l31) * 272 + lh * 16;
#pragma unroll
        for (int j = 0; j < 2; ++j)
#pragma unroll
            for (int rq = 0; rq < 4; ++rq)
                *LDSP(f32x4, yb + j * 128 + rq * 32) = f32x4{acc[j][4 * rq], acc[j][4 * rq + 1], acc[j][4 * rq + 2], acc[j][4 * rq + 3]};
        WAIT_L(0); BAR();
        TIE(dsv);
#pragma unroll
        for (int it = 0; it < 4; ++it) TIE(xv[it]);
        const float ds0 = bflo(dsv[0]), ds1 = bfhi(dsv[0]), ds2 = bflo(dsv[1]), ds3 = bfhi(dsv[1]);
        char* const ob = a.out + (size_t)(tm * 128) * 8192 + 4096 + tn * 256;
#pragma unroll
        for (int it = 0; it < 4; ++it) {
            const int row = it * 32 + (tid >> 4);
            const f32x4 y = *LDSP(f32x4, lb + L_Y + row * 272 + (tid & 15) * 16) + *LDSP(f32x4, lb + L_Y + (128 + row) * 272 + (tid & 15) * 16);
            const f32x4 o = {2.f * y[0] + rbf(bflo(xv[it][0]) * ds0), 2.f * y[1] + rbf(bfhi(xv[it][0]) * ds1),
                             2.f * y[2] + rbf(bflo(xv[it][1]) * ds2), 2.f * y[3] + rbf(bfhi(xv[it][1]) * ds3)};
            GST_B128(row * 8192 + (tid & 15) * 16, o, ob, 0);
        }
    }
    if (!zok) {
        for (unsigned spin = 0; spin < SPIN_CAP; ++spin) {
            GLD_B64_SC1(fl, xcd * 256 + l31 * 8, flg + FL_Z, 0);
            WAIT_V(0); TIE(fl);
            if (__all(fl[0] == tagA && fl[1] == tagB)) break;
            __builtin_amdgcn_s_sleep(1);
        }
        GLD_B128_SC1(zv[0], zoff, zsrc, 0); GLD_B128_SC1(zv[1], zoff, zsrc + 8 * 4096, 0);
        WAIT_V(0); TIE(zv[0]); TIE(zv[1]);
        part_a_stage();
#pragma unroll
        for (int m = 0; m < 4; ++m) part_a_out(m);
    }
    TS(12);
    TS_FLUSH();
}

}

extern "C" void kernel_launch(void* const* d_in, const int* in_sizes, int n_in, void* d_out, int out_size, void* d_ws, size_t ws_size,
                              hipStream_t stream) {
    (void)in_sizes; (void)n_in; (void)out_size; (void)ws_size;
    fk::Args a;
    a.q = (const char*)d_in[0]; a.k = (const char*)d_in[1]; a.v = (const char*)d_in[2]; a.sk = (const char*)d_in[3];
    a.ssmk = (const char*)d_in[4]; a.ssmD = (const char*)d_in[5]; a.x = (const char*)d_in[6]; a.lre = (const char*)d_in[7];
    a.lim = (const char*)d_in[8]; a.bre = (const char*)d_in[9]; a.bim = (const char*)d_in[10]; a.cre = (const char*)d_in[11];
    a.cim = (const char*)d_in[12]; a.logdt = (const char*)d_in[13]; a.dskip = (const char*)d_in[14];
    a.out = (char*)d_out; a.ws = (char*)d_ws;
    hipLaunchKernelGGL(fk::fused, dim3(256), dim3(512), 0, stream, a);
}
```

```cpp
#include <hip/hip_runtime.h>
#include <stdint.h>
#include <type_traits>

namespace fk {

constexpr int S = 2048, D = 1024, P = 1024;

typedef __attribute__((ext_vector_type(8))) short bf16x8;
typedef __attribute__((ext_vector_type(4))) short s16x4;
typedef __attribute__((ext_vector_type(16))) float f32x16;
typedef __attribute__((ext_vector_type(4))) float f32x4;
typedef __attribute__((ext_vector_type(2))) float f32x2;
typedef __attribute__((ext_vector_type(4))) unsigned u32x4;
typedef __attribute__((ext_vector_type(2))) unsigned u32x2;
#define LDSP(T, a) ((__attribute__((address_space(3))) T*)(uintptr_t)(unsigned)(a))

__device__ __forceinline__ float bf2f(unsigned v) { return __uint_as_float(v << 16); }
__device__ __forceinline__ float bflo(unsigned w) { return __uint_as_float(w << 16); }
__device__ __forceinline__ float bfhi(unsigned w) { return __uint_as_float(w & 0xffff0000u); }
__device__ __forceinline__ uint16_t f2bf(float f) { __bf16 b = (__bf16)f; return __builtin_bit_cast(uint16_t, b); }
__device__ __forceinline__ float rbf(float f) { return bf2f(f2bf(f)); }
typedef __attribute__((ext_vector_type(2))) __bf16 bf16x2;
__device__ __forceinline__ unsigned pack2(float lo, float hi) { return __builtin_bit_cast(unsigned, __builtin_convertvector(f32x2{lo, hi}, bf16x2)); }
__device__ __forceinline__ float rb1(float x) { return __uint_as_float(pack2(0.f, x)); }
__device__ __forceinline__ void cmac(float& xr, float& xi, float ar, float ai, float nai, float hr, float hi) {
    asm("v_fmac_f32 %0, %2, %5\n\tv_fmac_f32 %1, %2, %6\n\tv_fmac_f32 %0, %4, %6\n\tv_fmac_f32 %1, %3, %5" : "+v"(xr), "+v"(xi) : "v"(ar), "v"(ai), "v"(nai), "v"(hr), "v"(hi));
}
__device__ __forceinline__ void cmad(float& o_r, float& o_i, float ar, float ai, float nai, float hr, float hi, float er, float ei) {
    asm("v_fma_f32 %0, %2, %5, %7\n\tv_fma_f32 %1, %2, %6, %8\n\tv_fmac_f32 %0, %4, %6\n\tv_fmac_f32 %1, %3, %5" : "=&v"(o_r), "=&v"(o_i) : "v"(ar), "v"(ai), "v"(nai), "v"(hr), "v"(hi), "v"(er), "v"(ei));
}
__device__ __forceinline__ void cmul4(float& o_r, float& o_i, float ar, float ai, float nai, float hr, float hi) {
    asm("v_mul_f32 %0, %2, %5\n\tv_mul_f32 %1, %2, %6\n\tv_fmac_f32 %0, %4, %6\n\tv_fmac_f32 %1, %3, %5" : "=&v"(o_r), "=&v"(o_i) : "v"(ar), "v"(ai), "v"(nai), "v"(hr), "v"(hi));
}
__device__ __forceinline__ void cmul(float ar, float ai, float br, float bi, float& cr, float& ci) { cr = ar * br - ai * bi; ci = ar * bi + ai * br; }

template <int I, int N, class F>
__device__ __forceinline__ void static_for(F&& f) { if constexpr (I < N) { f(std::integral_constant<int, I>{}); static_for<I + 1, N>(f); } }

#define TS_DECL
#define TS(i)
#define TS_FLUSH()

#define WAIT_V(n) asm volatile("s_waitcnt vmcnt(%0)" ::"n"((n) > 63 ? 63 : (n)) : "memory")
#define WAIT_L(n) asm volatile("s_waitcnt lgkmcnt(%0)" ::"n"((n) > 15 ? 15 : (n)) : "memory")
#define BAR() asm volatile("s_barrier" ::: "memory")
#define SB() __builtin_amdgcn_sched_barrier(0)
template <class T> __device__ __forceinline__ void tie_(T& x) { asm volatile("" : "+v"(x)); }
#define TIE(x) tie_(x)
#define DEF_LD(name, ins) template <int OFF, class T> __device__ __forceinline__ void name(T& dst, unsigned voff, const char* sb) { \
    asm volatile(ins : "=v"(dst) : "v"(voff), "s"(sb), "n"(OFF) : "memory"); }
DEF_LD(gld_u16_, "global_load_ushort %0, %1, %2 offset:%3")
DEF_LD(gld_b32_, "global_load_dword %0, %1, %2 offset:%3")
DEF_LD(gld_b64_, "global_load_dwordx2 %0, %1, %2 offset:%3")
DEF_LD(gld_b128_, "global_load_dwordx4 %0, %1, %2 offset:%3")
DEF_LD(gld_b64_nt_, "global_load_dwordx2 %0, %1, %2 offset:%3 nt")
DEF_LD(gld_b128_nt_, "global_load_dwordx4 %0, %1, %2 offset:%3 nt")
DEF_LD(gld_b64_sc1_, "global_load_dwordx2 %0, %1, %2 offset:%3 sc1")
DEF_LD(gld_b128_sc1_, "global_load_dwordx4 %0, %1, %2 offset:%3 sc1")
#define DEF_ST(name, ins) template <int OFF, class T> __device__ __forceinline__ void name(unsigned voff, const T& v, const char* sb) { \
    asm volatile(ins :: "v"(voff), "v"(v), "s"(sb), "n"(OFF) : "memory"); }
DEF_ST(gst_b128_, "global_store_dwordx4 %0, %1, %2 offset:%3\n\ts_nop 1")
DEF_ST(gst_b128_nt_, "global_store_dwordx4 %0, %1, %2 offset:%3 nt\n\ts_nop 1")
DEF_ST(gst_b128_sc1_, "global_store_dwordx4 %0, %1, %2 offset:%3 sc1\n\ts_nop 1")
DEF_ST(gst_b64_sc1_, "global_store_dwordx2 %0, %1, %2 offset:%3 sc1")
#define GLD_U16(dst, voff, sb) gld_u16_<0>(dst, voff, sb)
#define GLD_B32(dst, voff, sb, off) gld_b32_<(off)>(dst, voff, sb)
#define GLD_B64(dst, voff, sb, off) gld_b64_<(off)>(dst, voff, sb)
#define GLD_B128(dst, voff, sb, off) gld_b128_<(off)>(dst, voff, sb)
#define GLD_B64_NT(dst, voff, sb, off) gld_b64_nt_<(off)>(dst, voff, sb)
#define GLD_B128_NT(dst, voff, sb, off) gld_b128_nt_<(off)>(dst, voff, sb)
#define GLD_B64_SC1(dst, voff, sb, off) gld_b64_sc1_<(off)>(dst, voff, sb)
#define GLD_B128_SC1(dst, voff, sb, off) gld_b128_sc1_<(off)>(dst, voff, sb)
#define GST_B128(voff, v, sb, off) gst_b128_<(off)>(voff, v, sb)
#define GST_B128_NT(voff, v, sb, off) gst_b128_nt_<(off)>(voff, v, sb)
#define GST_B128_SC1(voff, v, sb, off) gst_b128_sc1_<(off)>(voff, v, sb)
#define GST_B64_SC1(voff, v, sb) gst_b64_sc1_<0>(voff, v, sb)
__device__ __forceinline__ void dma_(unsigned ldsaddr, unsigned voff, const char* sb) {
    asm volatile("s_mov_b32 m0, %0\n\ts_nop 0\n\tglobal_load_lds_dwordx4 %1, %2" :: "s"(ldsaddr), "v"(voff), "s"(sb) : "memory"); }
__device__ __forceinline__ void dma_nt_(unsigned ldsaddr, unsigned voff, const char* sb) {
    asm volatile("s_mov_b32 m0, %0\n\ts_nop 0\n\tglobal_load_lds_dwordx4 %1, %2 nt" :: "s"(ldsaddr), "v"(voff), "s"(sb) : "memory"); }
#define DMA(ldsaddr, voff, sb) dma_(ldsaddr, voff, sb)
template <int OFF, class T> __device__ __forceinline__ void dsr128_(T& dst, unsigned addr) { asm volatile("ds_read_b128 %0, %1 offset:%2" : "=v"(dst) : "v"(addr), "n"(OFF)); }
template <int OFF, class T> __device__ __forceinline__ void dsr64_(T& dst, unsigned addr) { asm volatile("ds_read_b64 %0, %1 offset:%2" : "=v"(dst) : "v"(addr), "n"(OFF)); }
#define DSR128(dst, addr, off) dsr128_<(off)>(dst, addr)
#define DSR64(dst, addr, off) dsr64_<(off)>(dst, addr)

__device__ __forceinline__ void kv4(float* t, const float* e, float w0, float w1, float w2, float w3, const float* v) {
    float p0, p1, p2, p3;
    asm(
        "v_mul_f32 %0, %8, %18\n\t"
        "v_mul_f32 %1, %9, %18\n\t"
        "v_mul_f32 %2, %10, %18\n\t"
        "v_mul_f32 %3, %11, %18\n\t"
        "v_cvt_pk_bf16_f32 %0, 0, %0\n\t"
        "v_cvt_pk_bf16_f32 %1, 0, %1\n\t"
        "v_cvt_pk_bf16_f32 %2, 0, %2\n\t"
        "v_cvt_pk_bf16_f32 %3, 0, %3\n\t"
        "v_mul_f32 %4, %9, %17\n\t"
        "v_mul_f32 %5, %10, %17\n\t"
        "v_mul_f32 %6, %11, %17\n\t"
        "v_mul_f32 %7, %12, %17\n\t"
        "v_cvt_pk_bf16_f32 %4, 0, %4\n\t"
        "v_cvt_pk_bf16_f32 %5, 0, %5\n\t"
        "v_cvt_pk_bf16_f32 %6, 0, %6\n\t"
        "v_cvt_pk_bf16_f32 %7, 0, %7\n\t"
        "v_add_f32 %0, %0, %4\n\t"
        "v_add_f32 %1, %1, %5\n\t"
        "v_add_f32 %2, %2, %6\n\t"
        "v_add_f32 %3, %3, %7\n\t"
        "v_cvt_pk_bf16_f32 %0, 0, %0\n\t"
        "v_cvt_pk_bf16_f32 %1, 0, %1\n\t"
        "v_cvt_pk_bf16_f32 %2, 0, %2\n\t"
        "v_cvt_pk_bf16_f32 %3, 0, %3\n\t"
        "v_mul_f32 %4, %10, %16\n\t"
        "v_mul_f32 %5, %11, %16\n\t"
        "v_mul_f32 %6, %12, %16\n\t"
        "v_mul_f32 %7, %13, %16\n\t"
        "v_cvt_pk_bf16_f32 %4, 0, %4\n\t"
        "v_cvt_pk_bf16_f32 %5, 0, %5\n\t"
        "v_cvt_pk_bf16_f32 %6, 0, %6\n\t"
        "v_cvt_pk_bf16_f32 %7, 0, %7\n\t"
        "v_add_f32 %0, %0, %4\n\t"
        "v_add_f32 %1, %1, %5\n\t"
        "v_add_f32 %2, %2, %6\n\t"
        "v_add_f32 %3, %3, %7\n\t"
        "v_cvt_pk_bf16_f32 %0, 0, %0\n\t"
        "v_cvt_pk_bf16_f32 %1, 0, %1\n\t"
        "v_cvt_pk_bf16_f32 %2, 0, %2\n\t"
        "v_cvt_pk_bf16_f32 %3, 0, %3\n\t"
        "v_mul_f32 %4, %11, %15\n\t"
        "v_mul_f32 %5, %12, %15\n\t"
        "v_mul_f32 %6, %13, %15\n\t"
        "v_mul_f32 %7, %14, %15\n\t"
        "v_cvt_pk_bf16_f32 %4, 0, %4\n\t"
        "v_cvt_pk_bf16_f32 %5, 0, %5\n\t"
        "v_cvt_pk_bf16_f32 %6, 0, %6\n\t"
        "v_cvt_pk_bf16_f32 %7, 0, %7\n\t"
        "v_add_f32 %0, %0, %4\n\t"
        "v_add_f32 %1, %1, %5\n\t"
        "v_add_f32 %2, %2, %6\n\t"
        "v_add_f32 %3, %3, %7\n\t"
        "v_cvt_pk_bf16_f32 %0, 0, %0\n\t"
        "v_cvt_pk_bf16_f32 %1, 0, %1\n\t"
        "v_cvt_pk_bf16_f32 %2, 0, %2\n\t"
        "v_cvt_pk_bf16_f32 %3, 0, %3\n\t"
        "v_mul_f32 %0, %0, %19\n\t"
        "v_mul_f32 %1, %1, %20\n\t"
        "v_mul_f32 %2, %2, %21\n\t"
        "v_mul_f32 %3, %3, %22\n\t"
        : "=&v"(t[0]), "=&v"(t[1]), "=&v"(t[2]), "=&v"(t[3]), "=&v"(p0), "=&v"(p1), "=&v"(p2), "=&v"(p3)
        : "v"(e[0]), "v"(e[1]), "v"(e[2]), "v"(e[3]), "v"(e[4]), "v"(e[5]), "v"(e[6]), "v"(w0), "v"(w1), "v"(w2), "v"(w3), "v"(v[0]), "v"(v[1]), "v"(v[2]), "v"(v[3]));
}
__device__ __forceinline__ void lb_post(unsigned myaddr, unsigned seq) { asm volatile("ds_write_b32 %0, %1" :: "v"(myaddr), "v"(seq) : "memory"); }
__device__ __forceinline__ void lb_wait(unsigned addr, unsigned seq) {
    unsigned v;
    asm volatile("1:\n\tds_read_b32 %0, %1\n\ts_waitcnt lgkmcnt(0)\n\tv_cmp_gt_u32 vcc, %2, %0\n\ts_cbranch_vccnz 1b" : "=&v"(v) : "v"(addr), "v"(seq) : "vcc", "memory");
}

constexpr size_t WS_H = 0, WS_KVT = (size_t)8 << 20, WS_ZT = (size_t)12 << 20, WS_E = (size_t)16 << 20, WS_FLAG = WS_E + (256 << 10);
constexpr int FL_KV = 0, FL_Z = 4096, FL_H = 8192, FL_EPOCH = 16384;
constexpr unsigned SPIN_CAP = 1u << 17;
#define EXP 0
#define KVB 1
#define PAUSE 0
#define FILLLATE 0
#define CVPRIO 0
#define PRO1 4
#define MPRIO 2
#define THR 0
#define ZB 3
#define QSTEP 20
#define CVBAR 52

constexpr int G1STAGE = 32768;
constexpr int L_KS = 131072, L_VS = 147456;
constexpr int CST = 4288;
constexpr int UOFF = 4 * CST, UPL = 768;
constexpr int CHSZ = UOFF + 8 * UPL;
constexpr int L_AGG = 4 * CHSZ, L_SY = L_AGG + 4096;
constexpr int L_HT = 98304, L_ES = 114688;
constexpr int L_C = 98304, G2STAGE = 16384;
constexpr int L_ZI = 0;
constexpr int L_Z2 = 20480;
constexpr int L_Y = 40960;
static_assert(L_SY + 64 <= L_HT, "lds");
static_assert(L_Y + 2 * 128 * 36 * 4 <= L_AGG && L_Z2 + 64 * 264 <= L_Y, "lds");

__device__ __forceinline__ int img_off(int r, int c) { return (r >> 1) * 256 + ((((r & 1) * 8 + c) ^ ((r >> 1) & 15)) << 4); }

constexpr int PF = 2;
constexpr int g1_upto(int g) {
    int n = 16;
    for (int t = 0; t <= g; ++t) {
        int sb = (t + PF) / 4, sub = (t + PF) % 4, allowed = sb >= 1 ? 4 * (sb + 3) + sub + 1 : 16;
        if (allowed > 64) allowed = 64;
        const bool pause = t >= 4 * KVB - PF && t < 4 * KVB - PF + PAUSE;
        if (!pause) n = n + 2 < allowed ? n + 2 : allowed;
    }
    return n;
}
constexpr int G1_X = 54;
constexpr int g1_extra(int g) { return (g >= G1_X && g < G1_X + 4) ? 1 : 0; }
constexpr int g1_cnt(int s) { int g = 4 * s - PF, c = g1_upto(g - 1) - 4 * (s + 1); for (int t = 0; t < g; ++t) c += g1_extra(t); return c; }
static_assert(g1_upto(G1_X - 1) == 64, "schedule");
constexpr int AW = 8;
constexpr int g2_dma(int g) { return ((g + PF) / 4 >= 1 && (g + PF) / 4 + 3 < 16 && (g + PF) % 4 < 2) ? 1 : 0; }
constexpr int g2_a(int g) { return g + AW < 64 ? 1 : 0; }
constexpr int g2_ops(int g) { return g2_dma(g) + g2_a(g); }
constexpr int g2_cnt_stage(int s) {
    int g = 4 * s - PF, t1 = 4 * (s - 3) - PF + 1, c = g2_a(t1);
    for (int t = t1 + 1; t < g; ++t) c += g2_ops(t);
    return c;
}
constexpr int g2_cnt_a(int g) {
    int c = 0;
    if (g < AW) { c = AW - 1 - g; for (int t = 0; t < g; ++t) c += g2_ops(t); }
    else { for (int t = g - AW + 1; t < g; ++t) c += g2_ops(t); }
    return c;
}
constexpr int cv_bars(int s) { return (s >= CVBAR && ((s - CVBAR) % 4) == 0) ? 1 : 0; }
constexpr int cv_nbar() { int c = 0; for (int s = 0; s < 64; ++s) c += cv_bars(s); return c; }

struct Args {
    const char *q, *k, *v, *sk, *ssmk, *ssmD, *x, *lre, *lim, *bre, *bim, *cre, *cim, *logdt, *dskip;
    char *out, *ws;
};

__global__ __launch_bounds__(768, 3) void fused(Args a) {
    __shared__ __attribute__((aligned(1024))) char shm[163840];
    char* const ws = a.ws;
    TS_DECL TS(0);
    const int tid = threadIdx.x, lane = tid & 63, wid = __builtin_amdgcn_readfirstlane(tid >> 6);
    const int b = blockIdx.x, xcd = b & 7, idx = b >> 3;
    const int tm = (xcd >> 1) * 4 + (idx >> 3), tn = (xcd & 1) * 8 + (idx & 7);
    const int wm = wid & 3, wn = (wid >> 2) & 1, l31 = lane & 31, lh = lane >> 5;
    const unsigned lb = (unsigned)(uintptr_t)shm;
    const char* const flg = ws + WS_FLAG;
    const unsigned l16 = lane * 16;
    typedef __attribute__((ext_vector_type(4))) short v4i16;

    if (wid >= 8) {
        const int chn = xcd * 128 + idx * 4 + wm;
        const unsigned cb = lb + wm * CHSZ;
        const int r = lane & 15, g4 = lane >> 4;
        __builtin_amdgcn_s_setprio(3);
        u32x2 ep, fl, kw[2]; unsigned hal[2], dskl; u32x4 sw[4], uu[4];
        {
            const unsigned src = g4 * 2048 + ((r ^ (2 * g4)) << 4);
            const char* ks = a.k + (size_t)(idx * 64 + wm * 16) * 2048 + xcd * 256;
            const char* vs = a.v + (size_t)(idx * 64 + wm * 16) * 2048 + xcd * 256;
#pragma unroll
            for (int i = 0; i < 4; ++i) {
                dma_nt_(lb + L_KS + wm * 4096 + i * 1024, src ^ ((i >> 1) * 128), ks + i * 8192);
                dma_nt_(lb + L_VS + wm * 4096 + i * 1024, src ^ ((i >> 1) * 128), vs + i * 8192);
            }
            const unsigned ho = (idx ? idx * 64 - 4 + g4 : 0) * 2048 + xcd * 256 + wm * 64 + r * 2;
            gld_u16_<0>(hal[0], ho, a.k); gld_u16_<32>(hal[1], ho, a.k);
            GLD_B64_SC1(ep, 0u * lane, flg + FL_EPOCH, 0);
            const unsigned c0 = xcd * 128 + wm * 32 + r;
            GLD_B64(kw[0], c0 * 8, a.sk, 0); GLD_B64(kw[1], c0 * 8, a.sk, 128);
            GLD_U16(dskl, 0u * lane, a.ssmD + chn * 2);
        }
        TS(1);
        WAIT_V(0);
        BAR();
        TS(2);
        TIE(ep); TIE(kw[0]); TIE(kw[1]); TIE(hal[0]); TIE(hal[1]); TIE(dskl);
        const unsigned e0 = __builtin_amdgcn_readfirstlane(ep[0]), e1 = __builtin_amdgcn_readfirstlane(ep[1]);
        const unsigned tagA = e0 + 1, tagB = e1 + (tagA == 0 ? 1u : 0u);
        const u32x2 tag = {tagA, tagB};
        u32x4 kvo[4];
        {
            const int q = r >> 2, pp = r & 3;
            u32x2 kt[4][3], vt[4][2];
            unsigned hm[2][3];
#pragma unroll
            for (int u = 0; u < 4; ++u) {
                const int p = u >> 1, oh = u & 1;
                auto tr = [&](int base, int row) -> u32x2 {
                    const int rr = row + q, ch = (wm * 4 + p * 2 + (pp >> 1)) ^ (2 * (q | (((rr >> 3) & 1) << 2)));
                    return __builtin_bit_cast(u32x2, __builtin_amdgcn_ds_read_tr16_b64_v4i16(LDSP(v4i16, lb + base + rr * 256 + ch * 16 + (pp & 1) * 8)));
                };
                const int row0 = (g4 + 4 * oh) * 8;
                kt[u][0] = tr(L_KS, (oh == 0 && g4 == 0) ? 0 : row0 - 4);
                kt[u][1] = tr(L_KS, row0); kt[u][2] = tr(L_KS, row0 + 4); vt[u][0] = tr(L_VS, row0); vt[u][1] = tr(L_VS, row0 + 4);
                if (oh == 0) { hm[p][0] = __shfl(hal[p], r + 16); hm[p][1] = __shfl(hal[p], r + 32); hm[p][2] = __shfl(hal[p], r + 48); }
            }
            SB();
            const int sl = ((lane & 3) * 16 + (lane >> 2)) * 4;
#pragma unroll
            for (int u = 0; u < 4; ++u) {
                const int p = u >> 1, oh = u & 1;
                u32x2 k0 = kt[u][0];
                const u32x2 k1 = kt[u][1], k2 = kt[u][2], v0 = vt[u][0], v1 = vt[u][1];
                if (oh == 0 && g4 == 0) k0 = idx ? u32x2{hm[p][0] << 16, hm[p][1] | (hm[p][2] << 16)} : u32x2{0u, 0u};
                const float w0 = bflo(kw[p][0]), w1 = bfhi(kw[p][0]), w2 = bflo(kw[p][1]), w3 = bfhi(kw[p][1]);
                const float e[11] = {bfhi(k0[0]), bflo(k0[1]), bfhi(k0[1]), bflo(k1[0]), bfhi(k1[0]), bflo(k1[1]), bfhi(k1[1]), bflo(k2[0]), bfhi(k2[0]), bflo(k2[1]), bfhi(k2[1])};
                const float vf[8] = {bflo(v0[0]), bfhi(v0[0]), bflo(v0[1]), bfhi(v0[1]), bflo(v1[0]), bfhi(v1[0]), bflo(v1[1]), bfhi(v1[1])};
                float t[8];
                kv4(t, e, w0, w1, w2, w3, vf);
                kv4(t + 4, e + 4, w0, w1, w2, w3, vf + 4);
                const u32x4 o = {pack2(t[7], t[6]), pack2(t[5], t[4]), pack2(t[3], t[2]), pack2(t[1], t[0])};
#pragma unroll
                for (int j = 0; j < 4; ++j) kvo[u][j] = __builtin_amdgcn_ds_bpermute(sl, o[j]);
                SB();
                if (u == 2) BAR();
            }
#pragma unroll
            for (int u = 0; u < 4; ++u) TIE(kvo[u]);
        }
        TS(3);
        {
            const char* kvd = ws + WS_KVT + (size_t)(xcd * 64 + wm * 16) * 8192 + idx * 256;
            static_for<0, 4>([&](auto uc) {
                constexpr int u = decltype(uc)::value;
                BAR();
                GST_B128_SC1((lane >> 3) * 8192 + (lane & 7) * 16, kvo[u], kvd + (u >> 1) * 65536, (u & 1) * 128);
            });
        }
        BAR(); BAR();
        WAIT_V(0);
        BAR();
        if (tid == 512) GST_B64_SC1(xcd * 256 + idx * 8, tag, flg + FL_KV);
        BAR(); BAR(); BAR(); BAR(); BAR(); BAR();
        GLD_B64_SC1(fl, xcd * 256 + l31 * 8, flg + FL_KV, 0);
        {
            const char* krow = a.ssmk + (size_t)chn * 4096;
            GLD_B128_NT(sw[0], l16, krow, 0); GLD_B128_NT(sw[1], l16, krow, 1024); GLD_B128_NT(sw[2], l16, krow, 2048); GLD_B128_NT(sw[3], l16, krow, 3072);
        }
        BAR();
        WAIT_V(4); TIE(fl);
        const char* usrc = ws + WS_KVT + (size_t)(xcd * 64 + idx * 2) * 8192 + wm * 4096;
        GLD_B128_SC1(uu[0], l16, usrc, 0); GLD_B128_SC1(uu[1], l16, usrc, 1024); GLD_B128_SC1(uu[2], l16, usrc, 2048); GLD_B128_SC1(uu[3], l16, usrc, 3072);
        BAR();
        TS(4);
        const unsigned ud = lb + ((wm & 2) + ((lane >> 2) & 1)) * CHSZ + UOFF + (((lane >> 3) & 1) * 4 + (lane & 3)) * UPL + (16 + (wm & 1) * 16 + g4) * 16;
        {
            const u32x4 z4 = {0, 0, 0, 0};
            if (lane < 16) *LDSP(unsigned, lb + L_SY + lane * 4) = 0;
            if (lane < 32) *LDSP(u32x4, cb + (lane >> 3) * CST + (lane & 7) * 16) = z4;
            *LDSP(u32x4, cb + UOFF + g4 * UPL + r * 16) = z4;
            *LDSP(u32x4, cb + UOFF + (4 + g4) * UPL + r * 16) = z4;
        }
        WAIT_L(0);
        TS(5);
        BAR();
        __builtin_amdgcn_s_setprio(CVPRIO);
        WAIT_V(4);
        {
#pragma unroll
            for (int it = 0; it < 4; ++it) TIE(sw[it]);
#pragma unroll
            for (int it = 0; it < 4; ++it) {
                const unsigned q = cb + 128 + it * 1024 + l16;
                const unsigned d0 = sw[it][0], d1 = sw[it][1], d2 = sw[it][2], d3 = sw[it][3];
                const unsigned o0 = it ? __builtin_amdgcn_readlane(sw[it ? it - 1 : 0][2], 63) : 0u, o1 = it ? __builtin_amdgcn_readlane(sw[it ? it - 1 : 0][3], 63) : 0u;
                const unsigned x0 = __builtin_amdgcn_update_dpp(o0, d2, 0x138, 0xf, 0xf, false), x1 = __builtin_amdgcn_update_dpp(o1, d3, 0x138, 0xf, 0xf, false);
                const unsigned a0 = __builtin_amdgcn_alignbyte(d0, x1, 2), a1 = __builtin_amdgcn_alignbyte(d1, d0, 2), a2 = __builtin_amdgcn_alignbyte(d2, d1, 2),
                               a3 = __builtin_amdgcn_alignbyte(d3, d2, 2);
                *LDSP(u32x4, q) = sw[it];
                *LDSP(u32x4, q + CST) = u32x4{a0, a1, a2, a3};
                *LDSP(u32x4, q + 2 * CST) = u32x4{x1, d0, d1, d2};
                *LDSP(u32x4, q + 3 * CST) = u32x4{__builtin_amdgcn_alignbyte(x1, x0, 2), a0, a1, a2};
                if (it == 3 && lane == 63) {
                    *LDSP(u32x2, q + 16 + CST) = u32x2{d3 >> 16, 0u};
                    *LDSP(u32x2, q + 16 + 2 * CST) = u32x2{d3, 0u};
                    *LDSP(u32x2, q + 16 + 3 * CST) = u32x2{a3, d3 >> 16};
                }
            }
        }
        WAIT_V(0);
#pragma unroll
        for (int it = 0; it < 4; ++it) TIE(uu[it]);
        if (!__all(fl[0] == tagA && fl[1] == tagB)) {
            for (unsigned spin = 0; spin < SPIN_CAP; ++spin) {
                GLD_B64_SC1(fl, xcd * 256 + l31 * 8, flg + FL_KV, 0);
                WAIT_V(0); TIE(fl);
                if (__all(fl[0] == tagA && fl[1] == tagB)) break;
                __builtin_amdgcn_s_sleep(8);
            }
            GLD_B128_SC1(uu[0], l16, usrc, 0); GLD_B128_SC1(uu[1], l16, usrc, 1024); GLD_B128_SC1(uu[2], l16, usrc, 2048); GLD_B128_SC1(uu[3], l16, usrc, 3072);
            WAIT_V(0);
#pragma unroll
            for (int it = 0; it < 4; ++it) TIE(uu[it]);
        }
#pragma unroll
        for (int it = 0; it < 4; ++it) *LDSP(u32x4, ud + it * 64) = uu[it];
        lb_post(lb + L_SY + 32 + wm * 4, 1); lb_wait(lb + L_SY + 32 + (wm ^ 1) * 4, 1);
        TS(6);
        f32x4 ca[4][2];
        u32x2 qv[8];
        {
            const unsigned kb = cb + ((3 - r) & 3) * CST + 2 * (64 + r - 8 * g4 - 7 + ((3 - r) & 3)) - 64;
            const unsigned ub = cb + UOFF + g4 * UPL + r * 16;
            const char* qs = a.q + (size_t)(idx * 64 + wm * 16) * 2048 + xcd * 256;
#pragma unroll
            for (int i = 0; i < 4; ++i) { ca[i][0] = f32x4{0, 0, 0, 0}; ca[i][1] = f32x4{0, 0, 0, 0}; }
            s16x4 FA[8][2]; bf16x8 FB[3][2];
#define CV_A(q) do { DSR64(FA[((q) + 2) & 7][0], kb, 32 * ((q) + 2)); DSR64(FA[((q) + 2) & 7][1], kb, 32 * ((q) + 2) + 8); } while (0)
#define CV_B(s, ct) DSR128(FB[(s) % 3][ct], ub, (1 - ((s) & 1)) * 4 * UPL + (16 + 16 * (ct) - ((s) >> 1)) * 16)
#define CV_STEP(s) do { CV_A(2 * (s)); CV_A(2 * (s) + 1); if constexpr ((s) < 32) CV_B(s, 0); CV_B(s, 1); } while (0)
            CV_STEP(0); CV_STEP(1);
            static_for<0, 64>([&](auto ic) {
                constexpr int s = decltype(ic)::value;
                if constexpr (s >= QSTEP && s < QSTEP + 8) GLD_B64_NT(qv[s - QSTEP], lh * 2048 + l31 * 8, qs + (s - QSTEP) * 4096, 0);
                WAIT_L(s + 1 < 64 ? (s + 1 < 32 ? 6 : 5) : 0);
#pragma unroll
                for (int bi = (s ? 0 : 2); bi < 4; ++bi) { TIE(FA[(2 * s + bi) & 7][0]); TIE(FA[(2 * s + bi) & 7][1]); }
                if constexpr (s < 32) TIE(FB[s % 3][0]);
                TIE(FB[s % 3][1]);
                static_for<0, 4>([&](auto bc) {
                    constexpr int bi = decltype(bc)::value;
                    if constexpr (s > 0 || bi >= 2) {
                        const bf16x8 fa = __builtin_shufflevector(FA[(2 * s + bi) & 7][0], FA[(2 * s + bi) & 7][1], 0, 1, 2, 3, 4, 5, 6, 7);
                        if constexpr (s < 32) ca[bi][0] = __builtin_amdgcn_mfma_f32_16x16x32_bf16(fa, FB[s % 3][0], ca[bi][0], 0, 0, 0);
                        ca[bi][1] = __builtin_amdgcn_mfma_f32_16x16x32_bf16(fa, FB[s % 3][1], ca[bi][1], 0, 0, 0);
                    }
                    SB();
                    if constexpr (s + 2 < 64) {
                        if constexpr (bi < 2) CV_A(2 * (s + 2) + bi);
                        if constexpr (bi == 2 && s + 2 < 32) CV_B(s + 2, 0);
                        if constexpr (bi == 3 - (s + 2 >= 32)) CV_B(s + 2, 1);
                    }
                    SB();
                });
                if constexpr (cv_bars(s)) BAR();
            });
#undef CV_A
#undef CV_B
#undef CV_STEP
        }
        TS(7);
        __builtin_amdgcn_s_setprio(3);
        int nb = cv_nbar();
#define HB() do { if (nb < 15) BAR(); ++nb; } while (0)
        {
            const float Dd = bf2f(dskl);
            const char* zd = ws + WS_ZT + (size_t)chn * 4096;
#pragma unroll
            for (int ct = 0; ct < 2; ++ct) {
#pragma unroll
                for (int bi = 0; bi < 4; ++bi) {
                    const u32x2 kvr = *LDSP(u32x2, cb + UOFF + (2 * bi + (g4 >> 1)) * UPL + (16 + 16 * ct + r) * 16 + (1 - (g4 & 1)) * 8);
                    const f32x4 y = ca[bi][ct];
                    *LDSP(u32x2, cb + ct * 2304 + r * 144 + bi * 32 + g4 * 8) =
                        u32x2{pack2(rb1(y[0]) + rb1(bfhi(kvr[1]) * Dd), rb1(y[1]) + rb1(bflo(kvr[1]) * Dd)), pack2(rb1(y[2]) + rb1(bfhi(kvr[0]) * Dd), rb1(y[3]) + rb1(bflo(kvr[0]) * Dd))};
                }
                const u32x4 t0 = *LDSP(u32x4, cb + ct * 2304 + (lane >> 3) * 144 + (lane & 7) * 16), t1 = *LDSP(u32x4, cb + ct * 2304 + (8 + (lane >> 3)) * 144 + (lane & 7) * 16);
                GST_B128_SC1(l16 + ct * 2048, t0, zd, 0); GST_B128_SC1(l16 + ct * 2048, t1, zd, 1024);
                if (ZB >= 3 - ct) HB();
            }
        }
        if (ZB >= 1) HB();
        WAIT_V(0);
        lb_post(lb + L_SY + 32 + wm * 4, 3);
        if (wm == 0) {
            lb_wait(lb + L_SY + 32 + (lane & 3) * 4, 3);
            if (lane == 0) GST_B64_SC1(xcd * 256 + idx * 8, tag, flg + FL_Z);
        }
        TS(8);
        HB(); HB();
        if (wm == 0) GLD_B64_SC1(fl, xcd * 256 + l31 * 8, flg + FL_Z, 0);
        HB(); HB();
        if (wm == 0) { WAIT_V(0); TIE(fl); lb_post(lb + L_SY + 48, __all(fl[0] == tagA && fl[1] == tagB) ? 2 : 1); }
        lb_wait(lb + L_SY + 48, 1);
        if (__builtin_amdgcn_readfirstlane(*LDSP(unsigned, lb + L_SY + 48)) != 2) {
            while (nb < 15) { BAR(); ++nb; }
            for (unsigned spin = 0; spin < SPIN_CAP; ++spin) {
                GLD_B64_SC1(fl, xcd * 256 + l31 * 8, flg + FL_Z, 0);
                WAIT_V(0); TIE(fl);
                if (__all(fl[0] == tagA && fl[1] == tagB)) break;
                __builtin_amdgcn_s_sleep(8);
            }
        }
        u32x4 zv[4];
        {
            const char* zsrc = ws + WS_ZT + (size_t)(xcd * 128 + wm * 32) * 4096 + idx * 128;
#pragma unroll
            for (int it = 0; it < 4; ++it) { GLD_B128_SC1(zv[it], (lane >> 3) * 4096 + (lane & 7) * 16, zsrc + it * 32768, 0); if (it == 1) HB(); }
        }
        HB(); HB();
        WAIT_V(0);
        TS(9);
        {
            const unsigned zi = lb + L_ZI + wm * 5120;
#pragma unroll
            for (int it = 0; it < 4; ++it) { TIE(zv[it]); *LDSP(u32x4, zi + ((lane >> 3) + 8 * it) * 160 + (lane & 7) * 16) = zv[it]; }
#pragma unroll
            for (int n = 0; n < 8; ++n) {
                const int m = n >> 1, hf = n & 1;
                const v4i16 z = __builtin_amdgcn_ds_read_tr16_b64_v4i16(LDSP(v4i16, zi + (hf * 16 + (lane >> 2)) * 160 + m * 32 + (lane & 3) * 8));
                *LDSP(v4i16, lb + L_Z2 + (m * 16 + r) * 264 + wm * 64 + hf * 32 + g4 * 8) = z;
            }
            lb_post(lb + L_SY + 32 + wm * 4, 4);
        }
        HB();
        lb_wait(lb + L_SY + 32 + (lane & 3) * 4, 4);
        char* const oa = a.out + (size_t)(idx * 64 + wm * 16) * 8192 + xcd * 512;
#pragma unroll
        for (int n = 0; n < 8; ++n) {
            TIE(qv[n]);
            const u32x2 z = *LDSP(u32x2, lb + L_Z2 + (wm * 16 + n * 2 + lh) * 264 + l31 * 8);
            const f32x4 o = {rb1(bflo(qv[n][0]) * bflo(z[0])), rb1(bfhi(qv[n][0]) * bfhi(z[0])), rb1(bflo(qv[n][1]) * bflo(z[1])), rb1(bfhi(qv[n][1]) * bfhi(z[1]))};
            GST_B128_NT(lh * 8192 + l31 * 16, o, oa + n * 16384, 0);
            if (n < 7) HB();
        }
#undef HB
        TS(10);
        TS_FLUSH();
        return;
    }

    unsigned c_dt, c_re, c_im;
    u32x2 ep, fl;
    {
        GLD_B64_SC1(ep, 0u * lane, flg + FL_EPOCH, 0);
        const unsigned po = (tn * 64 + wn * 32 + l31) * 2;
        GLD_U16(c_dt, po, a.logdt); GLD_U16(c_re, po, a.lre); GLD_U16(c_im, po, a.lim);
    }
    unsigned dv[4];
#pragma unroll
    for (int i = 0; i < 4; ++i) {
        const int pair = i * 4 + (lane >> 4), v = (lane & 15) ^ pair;
        dv[i] = (pair * 2 + (v >> 3)) * 2048 + (v & 7) * 16;
    }
    const char* g1src = wid < 4 ? a.x + (size_t)(tm * 128 + wid * 32) * 2048
                                : ((wid & 1) ? a.bim : a.bre) + (size_t)(tn * 64 + ((wid >> 1) & 1) * 32) * 2048;
    const int rot = (b * 5) & 15;
    const unsigned g1dst = lb + wid * 4096;
#define G1_ISSUE(st, i) DMA(g1dst + ((st) & 3) * G1STAGE + (i) * 1024, dv[i], g1src + ((((st) + rot) & 15) << 7))
    static_for<0, PRO1>([&](auto ic) { constexpr int n = decltype(ic)::value; G1_ISSUE(n >> 2, n & 3); });
    TS(14);
    float Lr, Li, nLi, cr, ci, nci, L16r, L16i, L128r, L128i, Prr, Pri;
    unsigned tagA, tagB;
    {
        WAIT_V(PRO1);
        TIE(c_dt); TIE(c_re); TIE(c_im); TIE(ep);
        const float dt = rbf(expf(bf2f(c_dt)));
        const float lr = bf2f(c_re), li = bf2f(c_im);
        const float e = expf(dt * lr);
        float sn, cs;
        sincosf(dt * li, &sn, &cs);
        Lr = e * cs; Li = e * sn;
        const float nr = Lr - 1.f, ni = Li, den = lr * lr + li * li;
        cr = (nr * lr + ni * li) / den; ci = (ni * lr - nr * li) / den;
        float pr = Lr, pi = Li;
#pragma unroll
        for (int it = 0; it < 4; ++it) { float tr, ti; cmul(pr, pi, pr, pi, tr, ti); pr = tr; pi = ti; }
        L16r = pr; L16i = pi;
        float qr = lh ? pr : 1.f, qi = lh ? pi : 0.f;
#pragma unroll
        for (int it = 0; it < 3; ++it) {
            float tr, ti; cmul(pr, pi, pr, pi, tr, ti); pr = tr; pi = ti;
            if (it < 2 && ((wm >> it) & 1)) { cmul(qr, qi, pr, pi, tr, ti); qr = tr; qi = ti; }
        }
        L128r = pr; L128i = pi; Prr = qr; Pri = qi; nLi = -Li; nci = -ci;
        TIE(Lr); TIE(Li); TIE(cr); TIE(ci); TIE(L16r); TIE(L16i); TIE(L128r); TIE(L128i); TIE(Prr); TIE(Pri);
        const unsigned e0 = __builtin_amdgcn_readfirstlane(ep[0]), e1 = __builtin_amdgcn_readfirstlane(ep[1]);
        tagA = e0 + 1; tagB = (e1 + (tagA == 0 ? 1u : 0u));
    }
    const unsigned tagG = tagA ^ tagB ^ 0x9E3779B9u;
    const u32x2 tag = {tagA, tagB};
    TS(1);
    BAR();
    static_for<PRO1, 16>([&](auto ic) { constexpr int n = decltype(ic)::value; if (THR) WAIT_V(THR - 1); G1_ISSUE(n >> 2, n & 3); });
    TS(15);
    const int rot2 = (2 * tm) & 15;
    const char* g2src = (wid < 4 ? a.cre : a.cim) + (size_t)(tn * 64 + wm * 16) * 2048;
    const unsigned g2dst = lb + L_C + wid * 2048;
    const unsigned cvx = (wm & 1) * 128;
#define G2_ISSUE(st, i) DMA(g2dst + (((st) + 2) & 3) * G2STAGE + (i) * 1024, g2v[i], g2src + ((((st) + rot2) & 15) << 7))
    unsigned g2v[2];
#pragma unroll
    for (int i = 0; i < 2; ++i) {
        const int pair = i * 4 + (lane >> 4), v = (lane & 15) ^ pair ^ ((wm & 1) * 8);
        g2v[i] = (pair * 2 + (v >> 3)) * 2048 + (v & 7) * 16;
    }
    (void)cvx;

    f32x16 acc[2];
    u32x2 xv[4], dsv;
    {
#pragma unroll
        for (int j = 0; j < 2; ++j)
#pragma unroll
            for (int r = 0; r < 16; ++r) acc[j][r] = 0.f;
        const int prow = 16 * ((l31 >> 2) & 1) + 4 * (l31 >> 3) + (l31 & 3);
        const int aoff = img_off(wm * 32 + prow, lh), boff = img_off(128 + wn * 64 + l31, lh);
        unsigned aad[4], bad[4];
#pragma unroll
        for (int ks = 0; ks < 4; ++ks) { aad[ks] = lb + (aoff ^ (ks * 32)); bad[ks] = lb + (boff ^ (ks * 32)); }
        bf16x8 fa[PF + 1], fb[PF + 1][2];
        WAIT_V(12); BAR();
        TS(2);
#define G1_FRAGS(g) do { constexpr int bo_ = (((g) >> 2) & 3) * G1STAGE, hi_ = bo_ >= 65536 ? 65536 : 0; \
        DSR128(fa[(g) % (PF + 1)], aad[(g) & 3] + hi_, bo_ - hi_); \
        DSR128(fb[(g) % (PF + 1)][0], bad[(g) & 3] + hi_, bo_ - hi_); DSR128(fb[(g) % (PF + 1)][1], bad[(g) & 3] + hi_, bo_ - hi_ + 4096); } while (0)
        static_for<0, PF>([&](auto ic) { constexpr int g = decltype(ic)::value; G1_FRAGS(g); });
        static_for<0, 64>([&](auto ic) {
            constexpr int g = decltype(ic)::value, gn = g + PF;
            constexpr int n0 = g1_upto(g - 1), n1 = g1_upto(g);
            if constexpr ((gn & 3) == 0 && gn < 64) { static_assert(g1_cnt(gn >> 2) >= 0, "schedule"); WAIT_V(g1_cnt(gn >> 2)); WAIT_L(0); BAR(); if constexpr (((gn >> 2) & 1) == 0 || gn >= 52) TS(gn >= 52 ? 23 + (gn >> 2) - 13 : 16 + (gn >> 3)); }
            if constexpr (gn < 64) G1_FRAGS(gn);
            SB();
            WAIT_L((63 - g < PF ? 63 - g : PF) * 3);
            TIE(fa[g % (PF + 1)]); TIE(fb[g % (PF + 1)][0]); TIE(fb[g % (PF + 1)][1]);
            acc[0] = __builtin_amdgcn_mfma_f32_32x32x16_bf16(fa[g % (PF + 1)], fb[g % (PF + 1)][0], acc[0], 0, 0, 0);
            SB();
            if constexpr (n1 > n0) G1_ISSUE(n0 >> 2, n0 & 3);
            if constexpr (g >= G1_X && g < G1_X + 4) G2_ISSUE((g - G1_X) >> 1, (g - G1_X) & 1);
            SB();
            acc[1] = __builtin_amdgcn_mfma_f32_32x32x16_bf16(fa[g % (PF + 1)], fb[g % (PF + 1)][1], acc[1], 0, 0, 0);
            SB();
            if constexpr (n1 > n0 + 1) { G1_ISSUE((n0 + 1) >> 2, (n0 + 1) & 3); SB(); }
        });
#undef G1_FRAGS
    }
        TS(3);
    __builtin_amdgcn_s_setprio(MPRIO);
    float ur[16], ui[16];
    float cinr, cini;
    unsigned pl8 = lb + L_AGG + (wn * 32 + l31) * 8;
    const unsigned pmine = lb + L_SY + wid * 4, pall = lb + L_SY + (lane & 7) * 4;
    TIE(pl8);
    {
        asm volatile("s_nop 7\n\ts_nop 3" : "+v"(acc[0]), "+v"(acc[1]));
#pragma unroll
        for (int r = 0; r < 16; ++r) { ur[r] = acc[0][r]; ui[r] = acc[1][r]; }
        float gr = ur[0], gi = ui[0];
#pragma unroll
        for (int r = 1; r < 16; ++r) { float nr, ni; cmad(nr, ni, Lr, Li, nLi, gr, gi, ur[r], ui[r]); gr = nr; gi = ni; }
        const int run = wm * 2 + lh;
        *LDSP(f32x2, pl8 + run * 512) = f32x2{gr, gi};
        WAIT_L(0); BAR();
        f32x2 e[8];
#pragma unroll
        for (int j = 0; j < 8; ++j) e[j] = *LDSP(f32x2, pl8 + j * 512);
        const float nL16i = -L16i;
        cinr = 0.f; cini = 0.f;
#pragma unroll
        for (int j = 1; j < 8; ++j) { float er = e[j][0], ei = e[j][1]; cmac(er, ei, L16r, L16i, nL16i, e[j - 1][0], e[j - 1][1]); e[j] = f32x2{er, ei}; }
        if (wm == 0 && lh == 0) {
            const u32x4 gq = {__float_as_uint(e[7][0]), tagA, __float_as_uint(e[7][1]), tagG};
            GST_B128_SC1((tm * 1024 + tn * 64 + wn * 32 + l31) * 16, gq, ws + WS_E, 0);
        }
#pragma unroll
        for (int j = 0; j < 7; ++j) { if (j + 1 == run) { cinr = e[j][0]; cini = e[j][1]; } }
    }
    TS(4);
    {
        u32x4 eg[2];
        const int ej0 = 2 * wid - (15 - tm);
        const char* es0 = ws + WS_E + (ej0 < 0 ? 0 : ej0) * 16384;
        const char* es1 = ws + WS_E + (ej0 + 1 < 0 ? 0 : (ej0 + 1 > 15 ? 15 : ej0 + 1)) * 16384;
        const unsigned eo = (tn * 64 + lane) * 16;
#pragma unroll
        for (int r = 0; r < 16; ++r) { float nr, ni; cmul4(nr, ni, cr, ci, nci, ur[r], ui[r]); ur[r] = nr; ui[r] = ni; TIE(ur[r]); TIE(ui[r]); }
        TIE(cinr); TIE(cini);
        const bool n0 = ej0 >= 0, n1 = ej0 + 1 >= 0 && wid < 7;
        for (unsigned spin = 0; spin < SPIN_CAP; ++spin) {
            GLD_B128_SC1(eg[0], eo, es0, 0); GLD_B128_SC1(eg[1], eo, es1, 0);
            WAIT_V(0); TIE(eg[0]); TIE(eg[1]);
            const bool ok = (!n0 || (eg[0][1] == tagA && eg[0][3] == tagG)) && (!n1 || (eg[1][1] == tagA && eg[1][3] == tagG));
            if (__all(ok)) break;
            __builtin_amdgcn_s_sleep(2);
        }
        TS(5);
        *LDSP(u32x2, lb + L_ES + (2 * wid * 64 + lane) * 8) = n0 ? u32x2{eg[0][0], eg[0][2]} : u32x2{0u, 0u};
        if (wid < 7) *LDSP(u32x2, lb + L_ES + ((2 * wid + 1) * 64 + lane) * 8) = n1 ? u32x2{eg[1][0], eg[1][2]} : u32x2{0u, 0u};
        lb_post(pmine, 1); lb_wait(pall, 1);
    }
    TS(6);
    {
        f32x2 e[15];
#pragma unroll
        for (int j = 0; j < 15; ++j) e[j] = *LDSP(f32x2, pl8 + (L_ES - L_AGG) + j * 512);
        const float nL128i = -L128i, nPri = -Pri;
        float gr = e[0][0], gi = e[0][1];
#pragma unroll
        for (int j = 1; j < 15; ++j) { float er = e[j][0], ei = e[j][1]; cmac(er, ei, L128r, L128i, nL128i, gr, gi); gr = er; gi = ei; }
        cmac(cinr, cini, Prr, Pri, nPri, gr, gi);
        float hr, hi;
        cmul4(hr, hi, cr, ci, nci, cinr, cini);
#pragma unroll
        for (int r = 0; r < 16; ++r) { cmac(ur[r], ui[r], Lr, Li, nLi, hr, hi); hr = ur[r]; hi = ui[r]; }
        const unsigned ht = lb + L_HT + wid * 2048;
        const char* hd = ws + WS_H + (size_t)((tm * 4 + wm) * 2) * 65536 + (tn * 4 + wn * 2) * 1024;
        unsigned ta[2];
#pragma unroll
        for (int j = 0; j < 2; ++j) {
            const int row = 8 * lh + 4 * j + ((lane & 15) >> 2), chunk = 2 * ((lane >> 4) & 1) + ((lane & 3) >> 1);
            ta[j] = ht + row * 64 + ((chunk ^ ((row >> 1) & 3)) << 4) + 8 * (lane & 1);
        }
        v4i16 t[2][2][2];
#pragma unroll
        for (int c = 0; c < 2; ++c) {
#pragma unroll
            for (int e2 = 0; e2 < 2; ++e2) {
                const unsigned o = ht + l31 * 64 + (((lh * 2 + e2) ^ ((l31 >> 1) & 3)) << 4);
                const int r = 8 * e2;
                *LDSP(u32x4, o) = c ? u32x4{pack2(-ui[r], -ui[r + 1]), pack2(-ui[r + 2], -ui[r + 3]), pack2(-ui[r + 4], -ui[r + 5]), pack2(-ui[r + 6], -ui[r + 7])}
                                    : u32x4{pack2(ur[r], ur[r + 1]), pack2(ur[r + 2], ur[r + 3]), pack2(ur[r + 4], ur[r + 5]), pack2(ur[r + 6], ur[r + 7])};
            }
#pragma unroll
            for (int hf = 0; hf < 2; ++hf)
#pragma unroll
                for (int j = 0; j < 2; ++j) t[c][hf][j] = __builtin_amdgcn_ds_read_tr16_b64_v4i16(LDSP(v4i16, ta[j] + hf * 1024));
        }
        static_for<0, 4>([&](auto ic) {
            constexpr int n = decltype(ic)::value;
            const bf16x8 f = __builtin_shufflevector(t[n >> 1][n & 1][0], t[n >> 1][n & 1][1], 0, 1, 2, 3, 4, 5, 6, 7);
            GST_B128_SC1(l16, f, hd + (n >> 1) * 65536, (n & 1) * 1024);
        });
    }
    TS(7);
    bf16x8 fa2[AW];
    const char* Aw = ws + WS_H + (size_t)((tm * 4 + wm) * 2 + wn) * 65536;
#define G2_A(g) GLD_B128_SC1(fa2[(g) % AW], l16, Aw + (((((g) >> 2) + rot2) & 15) << 12), ((g) & 3) * 1024)
    {
#pragma unroll
        for (int it = 0; it < 4; ++it)
            GLD_B64(xv[it], (lane >> 3) * 2048 + (lane & 7) * 8, a.x + (size_t)(tm * 128 + wm * 32 + it * 8) * 2048 + tn * 128 + wn * 64, 0);
        GLD_B64(dsv, (lane & 7) * 8, a.dskip + tn * 128 + wn * 64, 0);
        WAIT_V(5);
        lb_post(pmine, 2); lb_wait(pall, 2);
        if (tid == 0) GST_B64_SC1(tm * 256 + tn * 8, tag, flg + FL_H);
        TS(8);
        static_for<4, 8>([&](auto ic) { constexpr int n = decltype(ic)::value; G2_ISSUE(n >> 1, n & 1); });
        if (wid == 0) {
            for (unsigned spin = 0; spin < SPIN_CAP; ++spin) {
                GLD_B64_SC1(fl, tm * 256 + (lane & 15) * 8, flg + FL_H, 0);
                WAIT_V(0); TIE(fl);
                if (__all(fl[0] == tagA && fl[1] == tagB)) break;
                __builtin_amdgcn_s_sleep(2);
            }
            lb_post(pmine, 3);
            if (b == 255 && tid == 0) GST_B64_SC1(0u * lane, tag, flg + FL_EPOCH);
        } else lb_wait(lb + L_SY, 3);
        static_for<0, AW>([&](auto ic) { constexpr int g = decltype(ic)::value; G2_A(g); });
    }
    TS(9);
    __builtin_amdgcn_s_setprio(0);
    {
#pragma unroll
        for (int j = 0; j < 2; ++j)
#pragma unroll
            for (int r = 0; r < 16; ++r) acc[j][r] = 0.f;
        const int boff = img_off(wn * 64 + l31, lh);
        unsigned bad[4];
#pragma unroll
        for (int ks = 0; ks < 4; ++ks) bad[ks] = lb + L_C + (boff ^ (ks * 32));
        bf16x8 fb[PF + 1][2];
#define G2_FRAGS(g) do { constexpr int bo_ = ((((g) >> 2) + 2) & 3) * G2STAGE; \
        DSR128(fb[(g) % (PF + 1)][0], bad[(g) & 3], bo_); DSR128(fb[(g) % (PF + 1)][1], bad[(g) & 3], bo_ + 4096); } while (0)
        static_for<0, PF>([&](auto ic) { constexpr int g = decltype(ic)::value; G2_FRAGS(g); });
        static_for<0, 64>([&](auto ic) {
            constexpr int g = decltype(ic)::value, gn = g + PF;
            if constexpr ((gn & 3) == 0 && gn < 64) {
                if constexpr ((gn >> 2) >= 4) WAIT_V(g2_cnt_stage(gn >> 2));
                WAIT_L(0); BAR();
                if constexpr (g == 2) TS(10);
            }
            if constexpr (gn < 64) G2_FRAGS(gn);
            SB();
            asm volatile("s_waitcnt vmcnt(%0) lgkmcnt(%1)" ::"n"(g2_cnt_a(g)), "n"((63 - g < PF ? 63 - g : PF) * 2) : "memory");
            TIE(fa2[g % AW]); TIE(fb[g % (PF + 1)][0]); TIE(fb[g % (PF + 1)][1]);
            acc[0] = __builtin_amdgcn_mfma_f32_32x32x16_bf16(fb[g % (PF + 1)][0], fa2[g % AW], acc[0], 0, 0, 0);
            SB();
            if constexpr (g2_dma(g)) G2_ISSUE(gn / 4 + 3, gn & 3);
            SB();
            acc[1] = __builtin_amdgcn_mfma_f32_32x32x16_bf16(fb[g % (PF + 1)][1], fa2[g % AW], acc[1], 0, 0, 0);
            SB();
            if constexpr (g2_a(g)) G2_A(g + AW);
            SB();
        });
#undef G2_FRAGS
    }
    TS(11);
    {
        lb_wait(lb + L_SY + 32 + (lane & 3) * 4, 3);
        const unsigned ys = lb + L_Y + (wn * 128 + wm * 32) * 144;
        const unsigned yp = lb + L_Y + ((1 - wn) * 128 + wm * 32) * 144;
        const f32x16 snd = wn ? acc[0] : acc[1], own = wn ? acc[1] : acc[0];
#pragma unroll
        for (int rq = 0; rq < 4; ++rq) *LDSP(f32x4, ys + l31 * 144 + rq * 32 + lh * 16) = f32x4{snd[4 * rq], snd[4 * rq + 1], snd[4 * rq + 2], snd[4 * rq + 3]};
        lb_post(pmine, 4);
        TIE(dsv);
#pragma unroll
        for (int it = 0; it < 4; ++it) TIE(xv[it]);
        const float ds0 = bflo(dsv[0]), ds1 = bfhi(dsv[0]), ds2 = bflo(dsv[1]), ds3 = bfhi(dsv[1]);
        f32x4 xd[4];
#pragma unroll
        for (int it = 0; it < 4; ++it) { xd[it] = f32x4{rb1(bflo(xv[it][0]) * ds0), rb1(bfhi(xv[it][0]) * ds1), rb1(bflo(xv[it][1]) * ds2), rb1(bfhi(xv[it][1]) * ds3)}; TIE(xd[it]); }
        lb_wait(lb + L_SY + (wid ^ 4) * 4, 4);
#pragma unroll
        for (int rq = 0; rq < 4; ++rq) {
            const unsigned o = yp + l31 * 144 + rq * 32 + lh * 16;
            *LDSP(f32x4, o) = *LDSP(f32x4, o) + f32x4{own[4 * rq], own[4 * rq + 1], own[4 * rq + 2], own[4 * rq + 3]};
        }
        char* const ob = a.out + (size_t)(tm * 128 + wm * 32) * 8192 + 4096 + tn * 256 + wn * 128;
#pragma unroll
        for (int it = 0; it < 4; ++it) {
            const f32x4 y = *LDSP(f32x4, yp + (it * 8 + (lane >> 3)) * 144 + (lane & 7) * 16);
            const f32x4 o = {2.f * y[0] + xd[it][0], 2.f * y[1] + xd[it][1], 2.f * y[2] + xd[it][2], 2.f * y[3] + xd[it][3]};
            GST_B128_NT((lane >> 3) * 8192 + (lane & 7) * 16, o, ob + it * 65536, 0);
        }
    }
    TS(12);
    TS_FLUSH();
}

}

extern "C" void kernel_launch(void* const* d_in, const int* in_sizes, int n_in, void* d_out, int out_size, void* d_ws, size_t ws_size,
                              hipStream_t stream) {
    (void)in_sizes; (void)n_in; (void)out_size; (void)ws_size;
    fk::Args a;
    a.q = (const char*)d_in[0]; a.k = (const char*)d_in[1]; a.v = (const char*)d_in[2]; a.sk = (const char*)d_in[3];
    a.ssmk = (const char*)d_in[4]; a.ssmD = (const char*)d_in[5]; a.x = (const char*)d_in[6]; a.lre = (const char*)d_in[7];
    a.lim = (const char*)d_in[8]; a.bre = (const char*)d_in[9]; a.bim = (const char*)d_in[10]; a.cre = (const char*)d_in[11];
    a.cim = (const char*)d_in[12]; a.logdt = (const char*)d_in[13]; a.dskip = (const char*)d_in[14];
    a.out = (char*)d_out; a.ws = (char*)d_ws;
    hipLaunchKernelGGL(fk::fused, dim3(256), dim3(768), 0, stream, a);
}
```

```cpp
#include <hip/hip_runtime.h>
#include <stdint.h>
#include <type_traits>

namespace fk {

constexpr int S = 2048, D = 1024, P = 1024;

typedef __attribute__((ext_vector_type(8))) short bf16x8;
typedef __attribute__((ext_vector_type(4))) short s16x4;
typedef __attribute__((ext_vector_type(16))) float f32x16;
typedef __attribute__((ext_vector_type(4))) float f32x4;
typedef __attribute__((ext_vector_type(2))) float f32x2;
typedef __attribute__((ext_vector_type(4))) unsigned u32x4;
typedef __attribute__((ext_vector_type(2))) unsigned u32x2;
#define LDSP(T, a) ((__attribute__((address_space(3))) T*)(uintptr_t)(unsigned)(a))

__device__ __forceinline__ float bf2f(unsigned v) { return __uint_as_float(v << 16); }
__device__ __forceinline__ float bflo(unsigned w) { return __uint_as_float(w << 16); }
__device__ __forceinline__ float bfhi(unsigned w) { return __uint_as_float(w & 0xffff0000u); }
__device__ __forceinline__ uint16_t f2bf(float f) { __bf16 b = (__bf16)f; return __builtin_bit_cast(uint16_t, b); }
__device__ __forceinline__ float rbf(float f) { return bf2f(f2bf(f)); }
typedef __attribute__((ext_vector_type(2))) __bf16 bf16x2;
__device__ __forceinline__ unsigned pack2(float lo, float hi) { return __builtin_bit_cast(unsigned, __builtin_convertvector(f32x2{lo, hi}, bf16x2)); }
__device__ __forceinline__ float rb1(float x) { return __uint_as_float(pack2(0.f, x)); }
__device__ __forceinline__ void cmac(float& xr, float& xi, float ar, float ai, float nai, float hr, float hi) {
    asm("v_fmac_f32 %0, %2, %5\n\tv_fmac_f32 %1, %2, %6\n\tv_fmac_f32 %0, %4, %6\n\tv_fmac_f32 %1, %3, %5" : "+v"(xr), "+v"(xi) : "v"(ar), "v"(ai), "v"(nai), "v"(hr), "v"(hi));
}
__device__ __forceinline__ void cmad(float& o_r, float& o_i, float ar, float ai, float nai, float hr, float hi, float er, float ei) {
    asm("v_fma_f32 %0, %2, %5, %7\n\tv_fma_f32 %1, %2, %6, %8\n\tv_fmac_f32 %0, %4, %6\n\tv_fmac_f32 %1, %3, %5" : "=&v"(o_r), "=&v"(o_i) : "v"(ar), "v"(ai), "v"(nai), "v"(hr), "v"(hi), "v"(er), "v"(ei));
}
__device__ __forceinline__ void mac4(float& x, float& y, float a0, float a1, float b0, float b1, float h0, float h1) {
    asm("v_fmac_f32 %0, %2, %6\n\tv_fmac_f32 %1, %4, %7\n\tv_fmac_f32 %0, %3, %7\n\tv_fmac_f32 %1, %5, %6" : "+v"(x), "+v"(y) : "v"(a0), "v"(a1), "v"(b0), "v"(b1), "v"(h0), "v"(h1));
}
__device__ __forceinline__ void mul4(float& x, float& y, float a0, float a1, float b0, float b1, float h0, float h1) {
    asm("v_mul_f32 %0, %2, %6\n\tv_mul_f32 %1, %4, %7\n\tv_fmac_f32 %0, %3, %7\n\tv_fmac_f32 %1, %5, %6" : "=&v"(x), "=&v"(y) : "v"(a0), "v"(a1), "v"(b0), "v"(b1), "v"(h0), "v"(h1));
}
__device__ __forceinline__ void cmul4(float& o_r, float& o_i, float ar, float ai, float nai, float hr, float hi) {
    asm("v_mul_f32 %0, %2, %5\n\tv_mul_f32 %1, %2, %6\n\tv_fmac_f32 %0, %4, %6\n\tv_fmac_f32 %1, %3, %5" : "=&v"(o_r), "=&v"(o_i) : "v"(ar), "v"(ai), "v"(nai), "v"(hr), "v"(hi));
}
__device__ __forceinline__ void cmul(float ar, float ai, float br, float bi, float& cr, float& ci) { cr = ar * br - ai * bi; ci = ar * bi + ai * br; }

template <int I, int N, class F>
__device__ __forceinline__ void static_for(F&& f) { if constexpr (I < N) { f(std::integral_constant<int, I>{}); static_for<I + 1, N>(f); } }

#define TS_DECL
#define TS(i)
#define TS_FLUSH()

#define WAIT_V(n) asm volatile("s_waitcnt vmcnt(%0)" ::"n"((n) > 63 ? 63 : (n)) : "memory")
#define WAIT_L(n) asm volatile("s_waitcnt lgkmcnt(%0)" ::"n"((n) > 15 ? 15 : (n)) : "memory")
#define BAR() asm volatile("s_barrier" ::: "memory")
#define SB() __builtin_amdgcn_sched_barrier(0)
template <class T> __device__ __forceinline__ void tie_(T& x) { asm volatile("" : "+v"(x)); }
#define TIE(x) tie_(x)
#define DEF_LD(name, ins) template <int OFF, class T> __device__ __forceinline__ void name(T& dst, unsigned voff, const char* sb) { \
    asm volatile(ins : "=v"(dst) : "v"(voff), "s"(sb), "n"(OFF) : "memory"); }
DEF_LD(gld_u16_, "global_load_ushort %0, %1, %2 offset:%3")
DEF_LD(gld_b32_, "global_load_dword %0, %1, %2 offset:%3")
DEF_LD(gld_b64_, "global_load_dwordx2 %0, %1, %2 offset:%3")
DEF_LD(gld_b128_, "global_load_dwordx4 %0, %1, %2 offset:%3")
DEF_LD(gld_b64_nt_, "global_load_dwordx2 %0, %1, %2 offset:%3 nt")
DEF_LD(gld_b128_nt_, "global_load_dwordx4 %0, %1, %2 offset:%3 nt")
DEF_LD(gld_b64_sc1_, "global_load_dwordx2 %0, %1, %2 offset:%3 sc1")
DEF_LD(gld_b128_sc1_, "global_load_dwordx4 %0, %1, %2 offset:%3 sc1")
#define DEF_ST(name, ins) template <int OFF, class T> __device__ __forceinline__ void name(unsigned voff, const T& v, const char* sb) { \
    asm volatile(ins :: "v"(voff), "v"(v), "s"(sb), "n"(OFF) : "memory"); }
DEF_ST(gst_b128_, "global_store_dwordx4 %0, %1, %2 offset:%3\n\ts_nop 1")
DEF_ST(gst_b128_nt_, "global_store_dwordx4 %0, %1, %2 offset:%3 nt\n\ts_nop 1")
DEF_ST(gst_b128_sc1_, "global_store_dwordx4 %0, %1, %2 offset:%3 sc1\n\ts_nop 1")
DEF_ST(gst_b64_sc1_, "global_store_dwordx2 %0, %1, %2 offset:%3 sc1")
#define GLD_U16(dst, voff, sb) gld_u16_<0>(dst, voff, sb)
#define GLD_B32(dst, voff, sb, off) gld_b32_<(off)>(dst, voff, sb)
#define GLD_B64(dst, voff, sb, off) gld_b64_<(off)>(dst, voff, sb)
#define GLD_B128(dst, voff, sb, off) gld_b128_<(off)>(dst, voff, sb)
#define GLD_B64_NT(dst, voff, sb, off) gld_b64_nt_<(off)>(dst, voff, sb)
#define GLD_B128_NT(dst, voff, sb, off) gld_b128_nt_<(off)>(dst, voff, sb)
#define GLD_B64_SC1(dst, voff, sb, off) gld_b64_sc1_<(off)>(dst, voff, sb)
#define GLD_B128_SC1(dst, voff, sb, off) gld_b128_sc1_<(off)>(dst, voff, sb)
#define GST_B128(voff, v, sb, off) gst_b128_<(off)>(voff, v, sb)
#define GST_B128_NT(voff, v, sb, off) gst_b128_nt_<(off)>(voff, v, sb)
#define GST_B128_SC1(voff, v, sb, off) gst_b128_sc1_<(off)>(voff, v, sb)
#define GST_B64_SC1(voff, v, sb) gst_b64_sc1_<0>(voff, v, sb)
__device__ __forceinline__ void dma_(unsigned ldsaddr, unsigned voff, const char* sb) {
    asm volatile("s_mov_b32 m0, %0\n\ts_nop 0\n\tglobal_load_lds_dwordx4 %1, %2" :: "s"(ldsaddr), "v"(voff), "s"(sb) : "memory"); }
__device__ __forceinline__ void dma_nt_(unsigned ldsaddr, unsigned voff, const char* sb) {
    asm volatile("s_mov_b32 m0, %0\n\ts_nop 0\n\tglobal_load_lds_dwordx4 %1, %2 nt" :: "s"(ldsaddr), "v"(voff), "s"(sb) : "memory"); }
#define DMA(ldsaddr, voff, sb) dma_(ldsaddr, voff, sb)
template <int OFF, class T> __device__ __forceinline__ void dsr128_(T& dst, unsigned addr) { asm volatile("ds_read_b128 %0, %1 offset:%2" : "=v"(dst) : "v"(addr), "n"(OFF)); }
template <int OFF, class T> __device__ __forceinline__ void dsr64_(T& dst, unsigned addr) { asm volatile("ds_read_b64 %0, %1 offset:%2" : "=v"(dst) : "v"(addr), "n"(OFF)); }
#define DSR128(dst, addr, off) dsr128_<(off)>(dst, addr)
#define DSR64(dst, addr, off) dsr64_<(off)>(dst, addr)

__device__ __forceinline__ void kv4(float* t, const float* e, float w0, float w1, float w2, float w3, const float* v) {
    float p0, p1, p2, p3;
    asm(
        "v_mul_f32 %0, %8, %18\n\t"
        "v_mul_f32 %1, %9, %18\n\t"
        "v_mul_f32 %2, %10, %18\n\t"
        "v_mul_f32 %3, %11, %18\n\t"
        "v_cvt_pk_bf16_f32 %0, 0, %0\n\t"
        "v_cvt_pk_bf16_f32 %1, 0, %1\n\t"
        "v_cvt_pk_bf16_f32 %2, 0, %2\n\t"
        "v_cvt_pk_bf16_f32 %3, 0, %3\n\t"
        "v_mul_f32 %4, %9, %17\n\t"
        "v_mul_f32 %5, %10, %17\n\t"
        "v_mul_f32 %6, %11, %17\n\t"
        "v_mul_f32 %7, %12, %17\n\t"
        "v_cvt_pk_bf16_f32 %4, 0, %4\n\t"
        "v_cvt_pk_bf16_f32 %5, 0, %5\n\t"
        "v_cvt_pk_bf16_f32 %6, 0, %6\n\t"
        "v_cvt_pk_bf16_f32 %7, 0, %7\n\t"
        "v_add_f32 %0, %0, %4\n\t"
        "v_add_f32 %1, %1, %5\n\t"
        "v_add_f32 %2, %2, %6\n\t"
        "v_add_f32 %3, %3, %7\n\t"
        "v_cvt_pk_bf16_f32 %0, 0, %0\n\t"
        "v_cvt_pk_bf16_f32 %1, 0, %1\n\t"
        "v_cvt_pk_bf16_f32 %2, 0, %2\n\t"
        "v_cvt_pk_bf16_f32 %3, 0, %3\n\t"
        "v_mul_f32 %4, %10, %16\n\t"
        "v_mul_f32 %5, %11, %16\n\t"
        "v_mul_f32 %6, %12, %16\n\t"
        "v_mul_f32 %7, %13, %16\n\t"
        "v_cvt_pk_bf16_f32 %4, 0, %4\n\t"
        "v_cvt_pk_bf16_f32 %5, 0, %5\n\t"
        "v_cvt_pk_bf16_f32 %6, 0, %6\n\t"
        "v_cvt_pk_bf16_f32 %7, 0, %7\n\t"
        "v_add_f32 %0, %0, %4\n\t"
        "v_add_f32 %1, %1, %5\n\t"
        "v_add_f32 %2, %2, %6\n\t"
        "v_add_f32 %3, %3, %7\n\t"
        "v_cvt_pk_bf16_f32 %0, 0, %0\n\t"
        "v_cvt_pk_bf16_f32 %1, 0, %1\n\t"
        "v_cvt_pk_bf16_f32 %2, 0, %2\n\t"
        "v_cvt_pk_bf16_f32 %3, 0, %3\n\t"
        "v_mul_f32 %4, %11, %15\n\t"
        "v_mul_f32 %5, %12, %15\n\t"
        "v_mul_f32 %6, %13, %15\n\t"
        "v_mul_f32 %7, %14, %15\n\t"
        "v_cvt_pk_bf16_f32 %4, 0, %4\n\t"
        "v_cvt_pk_bf16_f32 %5, 0, %5\n\t"
        "v_cvt_pk_bf16_f32 %6, 0, %6\n\t"
        "v_cvt_pk_bf16_f32 %7, 0, %7\n\t"
        "v_add_f32 %0, %0, %4\n\t"
        "v_add_f32 %1, %1, %5\n\t"
        "v_add_f32 %2, %2, %6\n\t"
        "v_add_f32 %3, %3, %7\n\t"
        "v_cvt_pk_bf16_f32 %0, 0, %0\n\t"
        "v_cvt_pk_bf16_f32 %1, 0, %1\n\t"
        "v_cvt_pk_bf16_f32 %2, 0, %2\n\t"
        "v_cvt_pk_bf16_f32 %3, 0, %3\n\t"
        "v_mul_f32 %0, %0, %19\n\t"
        "v_mul_f32 %1, %1, %20\n\t"
        "v_mul_f32 %2, %2, %21\n\t"
        "v_mul_f32 %3, %3, %22\n\t"
        : "=&v"(t[0]), "=&v"(t[1]), "=&v"(t[2]), "=&v"(t[3]), "=&v"(p0), "=&v"(p1), "=&v"(p2), "=&v"(p3)
        : "v"(e[0]), "v"(e[1]), "v"(e[2]), "v"(e[3]), "v"(e[4]), "v"(e[5]), "v"(e[6]), "v"(w0), "v"(w1), "v"(w2), "v"(w3), "v"(v[0]), "v"(v[1]), "v"(v[2]), "v"(v[3]));
}
__device__ __forceinline__ void lb_post(unsigned myaddr, unsigned seq) { asm volatile("ds_write_b32 %0, %1" :: "v"(myaddr), "v"(seq) : "memory"); }
__device__ __forceinline__ void lb_wait(unsigned addr, unsigned seq) {
    unsigned v;
    asm volatile("s_branch 2f\n1:\n\ts_sleep 1\n2:\n\tds_read_b32 %0, %1\n\ts_waitcnt lgkmcnt(0)\n\tv_cmp_gt_u32 vcc, %2, %0\n\ts_cbranch_vccnz 1b" : "=&v"(v) : "v"(addr), "v"(seq) : "vcc", "memory");
}

constexpr size_t WS_H = 0, WS_KVT = (size_t)10 << 20, WS_ZT = (size_t)15 << 20, WS_E = (size_t)20 << 20, WS_FLAG = WS_E + (256 << 10);
#define HP 65536
#define KP 8192
#define ZP 4096
constexpr int FL_KV = 0, FL_Z = 65536, FL_H = 131072, FL_EPOCH = 196608;
constexpr unsigned SPIN_CAP = 1u << 17;
#define ABL 0
#define CSPLIT 1
#define MERGE 3
#define CB 13
#define PAUSE 0
#define QLATE 0
#define CVPRIO 0
#define STG_SEL (idx & 3)
#define SP0 3
#define SDP 1
#define LP0 3
#define LDP 2
#define KVS 5
#define KVU 2
#define QIT 2
#define ZB 3
#define OS 2
#define MPRIO 2

constexpr int G1STAGE = 32768;
constexpr int L_KS = 131072, L_VS = 147456;
constexpr int CST = 4288;
constexpr int UOFF = 4 * CST, UPL = 768;
constexpr int upl(int c) { return c * UPL + (c >> 1) * 32; }
constexpr int CHSZ = UOFF + 8 * UPL + 192;
constexpr int L_AGG = 4 * CHSZ, L_SY = L_AGG + 4096;
constexpr int L_HT = 98304, L_ES = 114688;
constexpr int L_C = 98304, G2STAGE = 16384;
constexpr int L_ZI = 0;
constexpr int L_Z2 = 20480;
constexpr int L_Y = 40960, L_Y2 = 131072;
static_assert(L_SY + 64 <= L_HT, "lds");
static_assert(L_Y + 32768 <= L_AGG && L_Z2 + 64 * 264 <= L_Y, "lds");

__device__ __forceinline__ int img_off(int r, int c) { return (r >> 1) * 256 + ((((r & 1) * 8 + c) ^ ((r >> 1) & 15)) << 4); }

constexpr int PF = 2;
constexpr int g1_upto(int g) {
    int n = 16;
    for (int t = 0; t <= g; ++t) {
        int sb = (t + PF) / 4, sub = (t + PF) % 4, allowed = sb >= 1 ? 4 * (sb + 3) + sub + 1 : 16;
        if (allowed > 64) allowed = 64;
        const bool pause = t >= 4 * KVS - PF && t < 4 * KVS - PF + PAUSE;
        if (!pause) n = n + 2 < allowed ? n + 2 : allowed;
    }
    return n;
}
constexpr int g1_cnt(int s) { return g1_upto(4 * s - PF - 1) - 4 * (s + 1); }
static_assert(g1_upto(49) == 64, "schedule");
constexpr int AW = 8;
constexpr int g2_dma(int g) { return ((g + PF) / 4 >= 1 && (g + PF) / 4 + 3 < 16 && (g + PF) % 4 < 2) ? 1 : 0; }
constexpr int g2_a(int g) { return g + AW < 64 ? 1 : 0; }
constexpr int g2_ops(int g) { return g2_dma(g) + g2_a(g); }
constexpr int g2_cnt_stage(int s) {
    int g = 4 * s - PF, t1 = 4 * (s - 3) - PF + 1, c = g2_a(t1);
    for (int t = t1 + 1; t < g; ++t) c += g2_ops(t);
    return c;
}
constexpr int g2_cnt_a(int g) {
    int c = 0;
    if (g < AW) { c = AW - 1 - g; for (int t = 0; t < g; ++t) c += g2_ops(t); }
    else { for (int t = g - AW + 1; t < g; ++t) c += g2_ops(t); }
    return c;
}

struct Args {
    const char *q, *k, *v, *sk, *ssmk, *ssmD, *x, *lre, *lim, *bre, *bim, *cre, *cim, *logdt, *dskip;
    char *out, *ws;
};

__global__ __launch_bounds__(768, 3) void fused(Args a) {
    __shared__ __attribute__((aligned(1024))) char shm[163840];
    const char* pc0;
    int code_a, code_b;
    asm volatile("s_getpc_b64 %0\n.Lfk_code_0:\n\ts_mov_b32 %1, .Lfk_code_a-.Lfk_code_0\n\ts_mov_b32 %2, .Lfk_code_b-.Lfk_code_0" : "=s"(pc0), "=s"(code_a), "=s"(code_b));
    char* const ws = a.ws;
    TS_DECL TS(0);
    const int tid = threadIdx.x, lane = tid & 63, wid = __builtin_amdgcn_readfirstlane(tid >> 6);
    const int b = blockIdx.x, xcd = b & 7, idx = b >> 3;
    const int tm = (xcd >> 1) * 4 + (idx >> 3), tn = (xcd & 1) * 8 + (idx & 7);
    const int wm = wid & 3, wn = (wid >> 2) & 1, l31 = lane & 31, lh = lane >> 5;
    const unsigned lb = (unsigned)(uintptr_t)shm;
    const char* const flg = ws + WS_FLAG;
    const unsigned l16 = lane * 16;
    const int rot2 = (2 * tm) & 15;
    typedef __attribute__((ext_vector_type(4))) short v4i16;

    if (wid >= 8) {
        const int chn = xcd * 128 + idx * 4 + wm;
        const unsigned cb = lb + wm * CHSZ;
        const int r = lane & 15, g4 = lane >> 4;
        const unsigned fpub = xcd * 256 + idx * 8, fpoll = xcd * 256 + l31 * 8;
        __builtin_amdgcn_s_setprio(3);
        u32x2 ep, fl, kw[2]; unsigned hal[2], dskl; u32x4 sw[4], uu[4];
        unsigned cpf = 0;
        if (wm == 3) {
            if (lane < 16 && (idx * 16 + lane) * 128 < (code_a > code_b ? code_a : code_b)) GLD_B32(cpf, (idx * 16 + lane) * 128, pc0, 0);
        }
        {
            const unsigned ho = (idx ? idx * 64 - 4 + g4 : 0) * 2048 + xcd * 256 + wm * 64 + r * 2;
            gld_u16_<0>(hal[0], ho, a.k); gld_u16_<32>(hal[1], ho, a.k);
            GLD_B64_SC1(ep, 0u * lane, flg + FL_EPOCH, 0);
            const unsigned c0 = xcd * 128 + wm * 32 + r;
            GLD_B64(kw[0], c0 * 8, a.sk, 0); GLD_B64(kw[1], c0 * 8, a.sk, 128);
            GLD_U16(dskl, 0u * lane, a.ssmD + chn * 2);
        }
        TS(1);
        WAIT_V(0);
        BAR();
        TS(2);
        TIE(ep); TIE(kw[0]); TIE(kw[1]); TIE(hal[0]); TIE(hal[1]); TIE(dskl); TIE(cpf);
        const unsigned e0 = __builtin_amdgcn_readfirstlane(ep[0]), e1 = __builtin_amdgcn_readfirstlane(ep[1]);
        const unsigned tagA = e0 + 1, tagB = e1 + (tagA == 0 ? 1u : 0u);
        const u32x2 tag = {tagA, tagB};
        u32x4 kvo[4];
        {
            const int q = r >> 2, pp = r & 3;
            u32x2 kt[4][3], vt[4][2];
            unsigned hm[2][3];
#pragma unroll
            for (int u = 0; u < 4; ++u) {
                const int p = u >> 1, oh = u & 1;
                auto tr = [&](int base, int row) -> u32x2 {
                    const int rr = row + q, ch = (wm * 4 + p * 2 + (pp >> 1)) ^ (2 * (q | (((rr >> 3) & 1) << 2)));
                    return __builtin_bit_cast(u32x2, __builtin_amdgcn_ds_read_tr16_b64_v4i16(LDSP(v4i16, lb + base + rr * 256 + ch * 16 + (pp & 1) * 8)));
                };
                const int row0 = (g4 + 4 * oh) * 8;
                kt[u][0] = tr(L_KS, (oh == 0 && g4 == 0) ? 0 : row0 - 4);
                kt[u][1] = tr(L_KS, row0); kt[u][2] = tr(L_KS, row0 + 4); vt[u][0] = tr(L_VS, row0); vt[u][1] = tr(L_VS, row0 + 4);
                if (oh == 0) { hm[p][0] = __shfl(hal[p], r + 16); hm[p][1] = __shfl(hal[p], r + 32); hm[p][2] = __shfl(hal[p], r + 48); }
            }
            SB();
            const int sl = ((lane & 3) * 16 + (lane >> 2)) * 4;
#pragma unroll
            for (int u = 0; u < 4; ++u) {
                const int p = u >> 1, oh = u & 1;
                u32x2 k0 = kt[u][0];
                const u32x2 k1 = kt[u][1], k2 = kt[u][2], v0 = vt[u][0], v1 = vt[u][1];
                if (oh == 0 && g4 == 0) k0 = idx ? u32x2{hm[p][0] << 16, hm[p][1] | (hm[p][2] << 16)} : u32x2{0u, 0u};
                const float w0 = bflo(kw[p][0]), w1 = bfhi(kw[p][0]), w2 = bflo(kw[p][1]), w3 = bfhi(kw[p][1]);
                const float e[11] = {bfhi(k0[0]), bflo(k0[1]), bfhi(k0[1]), bflo(k1[0]), bfhi(k1[0]), bflo(k1[1]), bfhi(k1[1]), bflo(k2[0]), bfhi(k2[0]), bflo(k2[1]), bfhi(k2[1])};
                const float vf[8] = {bflo(v0[0]), bfhi(v0[0]), bflo(v0[1]), bfhi(v0[1]), bflo(v1[0]), bfhi(v1[0]), bflo(v1[1]), bfhi(v1[1])};
                float t[8];
                if (ABL & 2) { for (int i = 0; i < 8; ++i) t[i] = e[i]; } else
                kv4(t, e, w0, w1, w2, w3, vf);
                if (u > KVU) { TIE(t[0]); BAR(); }
                if (!(ABL & 2)) kv4(t + 4, e + 4, w0, w1, w2, w3, vf + 4);
                const u32x4 o = {pack2(t[7], t[6]), pack2(t[5], t[4]), pack2(t[3], t[2]), pack2(t[1], t[0])};
#pragma unroll
                for (int j = 0; j < 4; ++j) kvo[u][j] = __builtin_amdgcn_ds_bpermute(sl, o[j]);
                SB();
                if (u >= KVU) BAR();
            }
#pragma unroll
            for (int u = 0; u < 4; ++u) TIE(kvo[u]);
        }
        TS(3);
        u32x2 qv[8];
        const char* krow = a.ssmk + (size_t)chn * 4096;
        const char* qs = a.q + (size_t)(idx * 64 + wm * 16) * 2048 + xcd * 256;
        {
            const int ph = STG_SEL, ps = SP0 + ph * SDP, pl = LP0 + ph * LDP;
            const char* kvd = ws + WS_KVT + (size_t)(xcd * 64 + wm * 16) * KP + idx * 256;
            int nb1 = 1 + 2 * (3 - KVU);
#pragma unroll 1
            for (; nb1 <= ps; ++nb1) BAR();
            static_for<0, 4>([&](auto uc) {
                constexpr int u = decltype(uc)::value;
                GST_B128_SC1((lane >> 3) * KP + (lane & 7) * 16, kvo[u], kvd + (u >> 1) * 8 * KP, (u & 1) * 128);
            });
#pragma unroll 1
            for (; nb1 <= pl; ++nb1) {
                if (nb1 == ps + 4) WAIT_V(0);
                BAR();
                if (nb1 == ps + 4 && wm == 0 && lane == 0) GST_B64_SC1(fpub, tag, flg + FL_KV);
            }
            GLD_B128_NT(sw[0], l16, krow, 0); GLD_B128_NT(sw[1], l16, krow, 1024); GLD_B128_NT(sw[2], l16, krow, 2048); GLD_B128_NT(sw[3], l16, krow, 3072);
            static_for<0, 8>([&](auto jc) { constexpr int j = decltype(jc)::value; GLD_B64_NT(qv[j], lh * 2048 + l31 * 8, qs + j * 4096, 0); });
#pragma unroll 1
            for (; nb1 <= 12; ++nb1) {
                if (nb1 == ps + 4) WAIT_V(12);
                BAR();
                if (nb1 == ps + 4 && wm == 0 && lane == 0) GST_B64_SC1(fpub, tag, flg + FL_KV);
            }
        }
        if (ABL & 1024) fl = tag; else GLD_B64_SC1(fl, fpoll, flg + FL_KV, 0);
        BAR();
        WAIT_V(0); TIE(fl);
        const char* usrc = ws + WS_KVT + (size_t)(xcd * 64 + idx * 2 + (wm >> 1)) * KP + (wm & 1) * 4096;
        if (!(ABL & 16)) { GLD_B128_SC1(uu[0], l16, usrc, 0); GLD_B128_SC1(uu[1], l16, usrc, 1024); GLD_B128_SC1(uu[2], l16, usrc, 2048); GLD_B128_SC1(uu[3], l16, usrc, 3072); }
        if (!(MERGE & 1)) {
            GLD_B128_NT(sw[0], l16, krow, 0); GLD_B128_NT(sw[1], l16, krow, 1024); GLD_B128_NT(sw[2], l16, krow, 2048); GLD_B128_NT(sw[3], l16, krow, 3072);
        }
        if (CB == 14) BAR();
        {
            const char* cs = ((wm & 2) ? a.cim : a.cre) + (size_t)(tn * 64 + (wm & 1) * 32) * 2048;
#pragma unroll
            for (int i = 0; i < 4; ++i) {
                const int pair = i * 4 + g4, v = r ^ pair;
                const unsigned o = (pair * 2 + (v >> 3)) * 2048 + (v & 7) * 16;
                DMA(lb + L_C + 2 * G2STAGE + wm * 4096 + i * 1024, o, cs + (rot2 << 7));
                DMA(lb + L_C + 3 * G2STAGE + wm * 4096 + i * 1024, o, cs + (rot2 << 7) + 128);
                if (i == CSPLIT) BAR();
            }
        }
        if (CB == 13) BAR();
        TS(4);
        const unsigned ud = lb + ((wm & 2) + ((lane >> 2) & 1)) * CHSZ + UOFF + upl(((lane >> 3) & 1) * 4 + (lane & 3)) + (16 + (wm & 1) * 16 + g4) * 16;
        {
            const u32x4 z4 = {0, 0, 0, 0};
            if (lane < 16) *LDSP(unsigned, lb + L_SY + lane * 4) = 0;
            if (lane < 32) *LDSP(u32x4, cb + (lane >> 3) * CST + (lane & 7) * 16) = z4;
            *LDSP(u32x4, cb + UOFF + upl(g4) + r * 16) = z4;
            *LDSP(u32x4, cb + UOFF + upl(4 + g4) + r * 16) = z4;
        }
        WAIT_L(0);
        TS(5);
        BAR();
        TS(11);
        WAIT_V(8);
        TS(12);
        if (!(ABL & 32)) {
#pragma unroll
            for (int it = 0; it < 4; ++it) TIE(sw[it]);
#pragma unroll
            for (int it = 0; it < 4; ++it) {
                const unsigned q = cb + 128 + it * 1024 + l16;
                const unsigned d0 = sw[it][0], d1 = sw[it][1], d2 = sw[it][2], d3 = sw[it][3];
                const unsigned o0 = it ? __builtin_amdgcn_readlane(sw[it ? it - 1 : 0][2], 63) : 0u, o1 = it ? __builtin_amdgcn_readlane(sw[it ? it - 1 : 0][3], 63) : 0u;
                const unsigned x0 = __builtin_amdgcn_update_dpp(o0, d2, 0x138, 0xf, 0xf, false), x1 = __builtin_amdgcn_update_dpp(o1, d3, 0x138, 0xf, 0xf, false);
                const unsigned a0 = __builtin_amdgcn_alignbyte(d0, x1, 2), a1 = __builtin_amdgcn_alignbyte(d1, d0, 2), a2 = __builtin_amdgcn_alignbyte(d2, d1, 2),
                               a3 = __builtin_amdgcn_alignbyte(d3, d2, 2);
                *LDSP(u32x4, q) = sw[it];
                *LDSP(u32x4, q + CST) = u32x4{a0, a1, a2, a3};
                *LDSP(u32x4, q + 2 * CST) = u32x4{x1, d0, d1, d2};
                *LDSP(u32x4, q + 3 * CST) = u32x4{__builtin_amdgcn_alignbyte(x1, x0, 2), a0, a1, a2};
                if (it == 3 && lane == 63) {
                    *LDSP(u32x2, q + 16 + CST) = u32x2{d3 >> 16, 0u};
                    *LDSP(u32x2, q + 16 + 2 * CST) = u32x2{d3, 0u};
                    *LDSP(u32x2, q + 16 + 3 * CST) = u32x2{a3, d3 >> 16};
                }
            }
        }
        TS(13);
#pragma unroll
        for (int it = 0; it < 4; ++it) TIE(uu[it]);
        if (!__all(fl[0] == tagA && fl[1] == tagB)) {
            for (unsigned spin = 0; spin < SPIN_CAP; ++spin) {
                GLD_B64_SC1(fl, fpoll, flg + FL_KV, 0);
                WAIT_V(0); TIE(fl);
                if (__all(fl[0] == tagA && fl[1] == tagB)) break;
                __builtin_amdgcn_s_sleep(8);
            }
            GLD_B128_SC1(uu[0], l16, usrc, 0); GLD_B128_SC1(uu[1], l16, usrc, 1024); GLD_B128_SC1(uu[2], l16, usrc, 2048); GLD_B128_SC1(uu[3], l16, usrc, 3072);
            WAIT_V(0);
#pragma unroll
            for (int it = 0; it < 4; ++it) TIE(uu[it]);
        }
#pragma unroll
        for (int it = 0; it < 4; ++it) *LDSP(u32x4, ud + it * 64) = uu[it];
        lb_post(lb + L_SY + 32 + wm * 4, 1); lb_wait(lb + L_SY + 32 + (wm ^ 1) * 4, 1);
        __builtin_amdgcn_s_setprio(CVPRIO);
        TS(6);
        f32x4 ca[4][2];
        {
            unsigned kb = cb + ((3 - r) & 3) * CST + 2 * (64 + r - 8 * g4 - 7 + ((3 - r) & 3)) - 64;
            unsigned ub = cb + UOFF + upl(g4) + r * 16;
#pragma unroll
            for (int i = 0; i < 4; ++i) { ca[i][0] = f32x4{0, 0, 0, 0}; ca[i][1] = f32x4{0, 0, 0, 0}; }
            s16x4 FA[2][8][2]; bf16x8 FB[2][2];
#define CV_A(h, q, e) DSR64(FA[h][((q) + 2) & 7][e], kb, 32 * ((q) + 2) + 8 * (e) + 2048 * (h))
#define CV_B(s, ct) DSR128(FB[(s) & 1][ct], ub, (1 - ((s) & 1)) * upl(4) + (16 + 16 * (ct) - ((s) >> 1)) * 16)
            static_for<0, 8>([&](auto ic) { constexpr int n = decltype(ic)::value; CV_A((n >> 1) & 1, -2 + (n >> 2), n & 1); });
            WAIT_L(4);
            CV_B(0, 0); CV_B(0, 1);
            static_for<0, 8>([&](auto ic) { constexpr int n = decltype(ic)::value; CV_A((n >> 1) & 1, (n >> 2), n & 1); });
            auto body = [&]() {
                static_for<0, 4>([&](auto ic) {
                    constexpr int s = decltype(ic)::value;
                    static_for<0, 12>([&](auto mc) {
                        constexpr int m = decltype(mc)::value, bi = m / 3, k = m % 3, sl = (2 * s + bi) & 7;
                        if constexpr (m == 0) { WAIT_L(8); TIE(FB[s & 1][0]); TIE(FB[s & 1][1]); }
                        if constexpr (m == 6) WAIT_L(10);
                        if constexpr (m == 9) WAIT_L(9);
                        if constexpr (k == 0) { TIE(FA[0][sl][0]); TIE(FA[0][sl][1]); TIE(FA[1][sl][0]); TIE(FA[1][sl][1]); }
                        const bf16x8 fa = __builtin_shufflevector(FA[k == 2][sl][0], FA[k == 2][sl][1], 0, 1, 2, 3, 4, 5, 6, 7);
                        ca[bi][k != 1] = __builtin_amdgcn_mfma_f32_16x16x32_bf16(fa, FB[s & 1][k == 0], ca[bi][k != 1], 0, 0, 0);
                        SB();
                        if constexpr (m < 2) CV_B(s + 1, m);
                        else if constexpr (m < 10) CV_A(((m - 2) >> 1) & 1, 2 * (s + 1) + ((m - 2) >> 2), (m - 2) & 1);
                        SB();
                    });
                });
                kb += 256; ub -= 32;
            };
#pragma unroll 1
            for (int it = 0; it < QIT; ++it) body();
            {
                WAIT_V(0);
                lb_post(lb + L_SY + 32 + wm * 4, 2);
                if (!(MERGE & 2)) static_for<0, 8>([&](auto jc) { constexpr int j = decltype(jc)::value; GLD_B64_NT(qv[j], lh * 2048 + l31 * 8, qs + j * 4096, 0); });
            }
#pragma unroll 1
            for (int it = QIT; it < 8; ++it) body();
            WAIT_L(0);
#pragma unroll
            for (int i = 0; i < 4; ++i) { TIE(FA[0][i][0]); TIE(FA[0][i][1]); TIE(FA[1][i][0]); TIE(FA[1][i][1]); }
            TIE(FB[0][0]); TIE(FB[0][1]);
#undef CV_A
#undef CV_B
        }
        TS(7);
        __builtin_amdgcn_s_setprio(3);
        int nb = 0;
#define HB() do { if (nb < 15) BAR(); ++nb; } while (0)
        {
            const float Dd = bf2f(dskl);
            const char* zd = ws + WS_ZT + (size_t)chn * ZP;
            u32x2 kvr[2][4];
#pragma unroll
            for (int ct = 0; ct < 2; ++ct)
#pragma unroll
                for (int bi = 0; bi < 4; ++bi)
                    kvr[ct][bi] = *LDSP(u32x2, cb + UOFF + upl(2 * bi + (g4 >> 1)) + (16 + 16 * ct + r) * 16 + (1 - (g4 & 1)) * 8);
#pragma unroll
            for (int ct = 0; ct < 2; ++ct)
#pragma unroll
                for (int bi = 0; bi < 4; ++bi) {
                    const f32x4 y = ca[bi][ct];
                    const u32x2 kq = kvr[ct][bi];
                    *LDSP(u32x2, cb + ct * 2304 + r * 144 + bi * 32 + g4 * 8) =
                        u32x2{pack2(rb1(y[0]) + rb1(bfhi(kq[1]) * Dd), rb1(y[1]) + rb1(bflo(kq[1]) * Dd)), pack2(rb1(y[2]) + rb1(bfhi(kq[0]) * Dd), rb1(y[3]) + rb1(bflo(kq[0]) * Dd))};
                }
            if (ZB >= 3) HB();
            u32x4 t[4];
#pragma unroll
            for (int it = 0; it < 4; ++it) t[it] = *LDSP(u32x4, cb + (it * 8 + (lane >> 3)) * 144 + (lane & 7) * 16);
            if (!(ABL & 128)) { GST_B128_SC1(l16, t[0], zd, 0); GST_B128_SC1(l16, t[1], zd, 1024); GST_B128_SC1(l16, t[2], zd, 2048); GST_B128_SC1(l16, t[3], zd, 3072); }
            else { TIE(t[0]); TIE(t[1]); TIE(t[2]); TIE(t[3]); }
        }
        if (ZB >= 2) HB();
        if (ZB >= 1) HB();
        WAIT_V(0);
        lb_post(lb + L_SY + 32 + wm * 4, 3);
        if (wm == 0) {
            lb_wait(lb + L_SY + 32 + (lane & 3) * 4, 3);
            if (!(ABL & 1024) && lane == 0) GST_B64_SC1(fpub, tag, flg + FL_Z);
        }
        TS(8);
        HB(); HB(); HB();
        if (!(ABL & 1024) && wm == 0) GLD_B64_SC1(fl, fpoll, flg + FL_Z, 0);
        HB(); HB();
        if (wm == 0) { WAIT_V(0); TIE(fl); lb_post(lb + L_SY + 48, __all(fl[0] == tagA && fl[1] == tagB) ? 2 : 1); }
        lb_wait(lb + L_SY + 48, 1);
        if (__builtin_amdgcn_readfirstlane(*LDSP(unsigned, lb + L_SY + 48)) != 2) {
            while (nb < 15) { BAR(); ++nb; }
            for (unsigned spin = 0; spin < SPIN_CAP; ++spin) {
                GLD_B64_SC1(fl, fpoll, flg + FL_Z, 0);
                WAIT_V(0); TIE(fl);
                if (__all(fl[0] == tagA && fl[1] == tagB)) break;
                __builtin_amdgcn_s_sleep(8);
            }
        }
        u32x4 zv[4];
        {
            const char* zsrc = ws + WS_ZT + (size_t)(xcd * 128 + wm * 32) * ZP + idx * 128;
#pragma unroll
            for (int it = 0; it < 4; ++it) if (!(ABL & 256)) GLD_B128_SC1(zv[it], (lane >> 3) * ZP + (lane & 7) * 16, zsrc + it * 8 * ZP, 0);
        }
        HB(); HB();
        WAIT_V(0);
        TS(9);
        {
            const unsigned zi = lb + L_ZI + wm * 5120;
#pragma unroll
            for (int it = 0; it < 4; ++it) { TIE(zv[it]); *LDSP(u32x4, zi + ((lane >> 3) + 8 * it) * 160 + (lane & 7) * 16) = zv[it]; }
            v4i16 z[8];
#pragma unroll
            for (int n = 0; n < 8; ++n) z[n] = __builtin_amdgcn_ds_read_tr16_b64_v4i16(LDSP(v4i16, zi + ((n & 1) * 16 + (lane >> 2)) * 160 + (n >> 1) * 32 + (lane & 3) * 8));
            HB();
#pragma unroll
            for (int n = 0; n < 8; ++n) *LDSP(v4i16, lb + L_Z2 + ((n >> 1) * 16 + r) * 264 + wm * 64 + (n & 1) * 32 + g4 * 8) = z[n];
            lb_post(lb + L_SY + 32 + wm * 4, 4);
        }
        HB();
        lb_wait(lb + L_SY + 32 + (lane & 3) * 4, 4);
        char* const oa = a.out + (size_t)(idx * 64 + wm * 16) * 8192 + xcd * 512;
        u32x2 zz[8];
#pragma unroll
        for (int n = 0; n < 8; ++n) zz[n] = *LDSP(u32x2, lb + L_Z2 + (wm * 16 + n * 2 + lh) * 264 + l31 * 8);
#pragma unroll
        for (int n = 0; n < 8; ++n) {
            TIE(qv[n]);
            const u32x2 z = zz[n];
            const f32x4 o = {rb1(bflo(qv[n][0]) * bflo(z[0])), rb1(bfhi(qv[n][0]) * bfhi(z[0])), rb1(bflo(qv[n][1]) * bflo(z[1])), rb1(bfhi(qv[n][1]) * bfhi(z[1]))};
            if (!(ABL & 512)) GST_B128_NT(lh * 8192 + l31 * 16, o, oa + n * 16384, 0); else { f32x4 o2 = o; TIE(o2); }
            if (n < 7 && (n + 1) % OS == 0) HB();
        }
#undef HB
        asm volatile(".Lfk_code_a:");
        TS(10);
        TS_FLUSH();
        return;
    }

    unsigned c_dt, c_re, c_im;
    u32x2 ep, fl;
    {
        GLD_B64_SC1(ep, 0u * lane, flg + FL_EPOCH, 0);
        const unsigned po = (tn * 64 + wn * 32 + l31) * 2;
        GLD_U16(c_dt, po, a.logdt); GLD_U16(c_re, po, a.lre); GLD_U16(c_im, po, a.lim);
    }
    {
        const int g4 = lane >> 4;
        const unsigned src = g4 * 2048 + (((lane & 15) ^ (2 * g4)) << 4);
        const char* ks = (wn ? a.v : a.k) + (size_t)(idx * 64 + wm * 16) * 2048 + xcd * 256;
#pragma unroll
        for (int i = 0; i < 4; ++i) { if (ABL & 1) break; dma_nt_(lb + L_KS + wid * 4096 + i * 1024, src ^ ((i >> 1) * 128), ks + i * 8192); }
    }
    unsigned dv[4];
#pragma unroll
    for (int i = 0; i < 4; ++i) {
        const int pair = i * 4 + (lane >> 4), v = (lane & 15) ^ pair;
        dv[i] = (pair * 2 + (v >> 3)) * 2048 + (v & 7) * 16;
    }
    const char* g1src = wid < 4 ? a.x + (size_t)(tm * 128 + wid * 32) * 2048
                                : ((wid & 1) ? a.bim : a.bre) + (size_t)(tn * 64 + ((wid >> 1) & 1) * 32) * 2048;
    const int rot = (b * 5) & 15;
    const unsigned g1dst = lb + wid * 4096;
#define G1_ISSUE(st, i) DMA(g1dst + ((st) & 3) * G1STAGE + (i) * 1024, dv[i], g1src + ((((st) + rot) & 15) << 7))
    G1_ISSUE(0, 0); G1_ISSUE(0, 1);
    TS(14);
    float Lr, Li, cr, ci, L8r, L8i, L16r, L16i, L128r, L128i, Qr, Qi, Wr, Wi;
    unsigned tagA, tagB;
    {
        WAIT_V((ABL & 1) ? 2 : 6);
        TIE(c_dt); TIE(c_re); TIE(c_im); TIE(ep);
        const float dt = rbf(expf(bf2f(c_dt)));
        const float lr = bf2f(c_re), li = bf2f(c_im);
        const float e = expf(dt * lr);
        float sn, cs;
        sincosf(dt * li, &sn, &cs);
        Lr = e * cs; Li = e * sn;
        const float nr = Lr - 1.f, ni = Li, den = lr * lr + li * li;
        cr = (nr * lr + ni * li) / den; ci = (ni * lr - nr * li) / den;
        float pr = Lr, pi = Li, tr, ti;
        Qr = cr; Qi = ci; Wr = 1.f; Wi = 0.f;
        const int run = 2 * wm + lh;
#pragma unroll
        for (int it = 1; it <= 10; ++it) {
            cmul(pr, pi, pr, pi, tr, ti); pr = tr; pi = ti;
            if (it == 3) { L8r = pr; L8i = pi; }
            if (it == 4) { L16r = pr; L16i = pi; }
            if (it == 7) { L128r = pr; L128i = pi; }
            if (it >= 4 && it <= 6) { cmul(Qr, Qi, pr, pi, tr, ti); if ((run >> (it - 4)) & 1) { Qr = tr; Qi = ti; } }
            if (it >= 8) { cmul(Wr, Wi, pr, pi, tr, ti); if (!((run >> (it - 8)) & 1)) { Wr = tr; Wi = ti; } }
        }
        TIE(Lr); TIE(Li); TIE(cr); TIE(ci); TIE(L8r); TIE(L8i); TIE(L16r); TIE(L16i); TIE(L128r); TIE(L128i); TIE(Qr); TIE(Qi); TIE(Wr); TIE(Wi);
        const unsigned e0 = __builtin_amdgcn_readfirstlane(ep[0]), e1 = __builtin_amdgcn_readfirstlane(ep[1]);
        tagA = e0 + 1; tagB = (e1 + (tagA == 0 ? 1u : 0u));
    }
    const unsigned tagG = tagA ^ tagB ^ 0x9E3779B9u;
    const u32x2 tag = {tagA, tagB};
    TS(1);
    WAIT_V(2);
    BAR();
    static_for<2, 16>([&](auto ic) { constexpr int n = decltype(ic)::value; G1_ISSUE(n >> 2, n & 3); });
    TS(15);

    f32x16 acc[2];
    {
#pragma unroll
        for (int j = 0; j < 2; ++j)
#pragma unroll
            for (int r = 0; r < 16; ++r) acc[j][r] = 0.f;
        const int prow = 16 * ((l31 >> 2) & 1) + 4 * (l31 >> 3) + (l31 & 3);
        const int aoff = img_off(wm * 32 + prow, lh), boff = img_off(128 + wn * 64 + l31, lh);
        unsigned aad[4], bad[4];
#pragma unroll
        for (int ks = 0; ks < 4; ++ks) { aad[ks] = lb + (aoff ^ (ks * 32)); bad[ks] = lb + (boff ^ (ks * 32)); }
        bf16x8 fa[PF + 1], fb[PF + 1][2];
        WAIT_V(12); BAR();
        TS(2);
#define G1_FRAGS(g) do { constexpr int bo_ = (((g) >> 2) & 3) * G1STAGE, hi_ = bo_ >= 65536 ? 65536 : 0; \
        DSR128(fa[(g) % (PF + 1)], aad[(g) & 3] + hi_, bo_ - hi_); \
        DSR128(fb[(g) % (PF + 1)][0], bad[(g) & 3] + hi_, bo_ - hi_); DSR128(fb[(g) % (PF + 1)][1], bad[(g) & 3] + hi_, bo_ - hi_ + 4096); } while (0)
        static_for<0, PF>([&](auto ic) { constexpr int g = decltype(ic)::value; G1_FRAGS(g); });
        static_for<0, 64>([&](auto ic) {
            constexpr int g = decltype(ic)::value, gn = g + PF;
            if constexpr ((gn & 3) == 0 && gn < 64) { static_assert(g1_cnt(gn >> 2) >= 0, "schedule"); WAIT_V(g1_cnt(gn >> 2)); WAIT_L(0); BAR(); if constexpr (((gn >> 2) & 3) == 0 || gn >= 52) TS(gn >= 52 ? 23 + (gn >> 2) - 13 : 16 + (gn >> 4)); }
            if constexpr (gn < 64) G1_FRAGS(gn);
            SB();
            WAIT_L((63 - g < PF ? 63 - g : PF) * 3);
            TIE(fa[g % (PF + 1)]); TIE(fb[g % (PF + 1)][0]); TIE(fb[g % (PF + 1)][1]);
            acc[0] = __builtin_amdgcn_mfma_f32_32x32x16_bf16(fa[g % (PF + 1)], fb[g % (PF + 1)][0], acc[0], 0, 0, 0);
            SB();
            constexpr int n0 = g1_upto(g - 1), n1 = g1_upto(g);
            if constexpr (n1 > n0) G1_ISSUE(n0 >> 2, n0 & 3);
            SB();
            acc[1] = __builtin_amdgcn_mfma_f32_32x32x16_bf16(fa[g % (PF + 1)], fb[g % (PF + 1)][1], acc[1], 0, 0, 0);
            SB();
            if constexpr (n1 > n0 + 1) { G1_ISSUE((n0 + 1) >> 2, (n0 + 1) & 3); SB(); }
        });
#undef G1_FRAGS
    }
    TS(3);
    __builtin_amdgcn_s_setprio(MPRIO);
    float ur[16], ui[16];
    float hr, hi;
    unsigned pl8 = lb + L_AGG + (wn * 32 + l31) * 8;
    const unsigned pmine = lb + L_SY + wid * 4, pall = lb + L_SY + (lane & 7) * 4;
    const int run = wm * 2 + lh;
    const float nLi = -Li;
    TIE(pl8);
    {
        asm volatile("s_nop 7\n\ts_nop 3" : "+v"(acc[0]), "+v"(acc[1]));
#pragma unroll
        for (int r = 0; r < 16; ++r) { ur[r] = acc[0][r]; ui[r] = acc[1][r]; }
        float gr = ur[0], gi = ui[0], fr = ur[8], fi = ui[8];
#pragma unroll
        for (int r = 1; r < 8; ++r) {
            float nr, ni; cmad(nr, ni, Lr, Li, nLi, gr, gi, ur[r], ui[r]); gr = nr; gi = ni;
            cmad(nr, ni, Lr, Li, nLi, fr, fi, ur[8 + r], ui[8 + r]); fr = nr; fi = ni;
        }
        cmac(fr, fi, L8r, L8i, -L8i, gr, gi);
        *LDSP(f32x2, pl8 + run * 512) = f32x2{fr, fi};
        WAIT_L(0); BAR();
        f32x2 e[8];
#pragma unroll
        for (int j = 0; j < 8; ++j) e[j] = *LDSP(f32x2, pl8 + j * 512);
        const float nL16i = -L16i;
#pragma unroll
        for (int j = 1; j < 8; ++j) { float er = e[j][0], ei = e[j][1]; cmac(er, ei, L16r, L16i, nL16i, e[j - 1][0], e[j - 1][1]); e[j] = f32x2{er, ei}; }
        if (wm == 0 && lh == 0) {
            const u32x4 gq = {__float_as_uint(e[7][0]), tagA, __float_as_uint(e[7][1]), tagG};
            GST_B128_SC1((tm * 1024 + tn * 64 + wn * 32 + l31) * 16, gq, ws + WS_E, 0);
        }
        float cinr = 0.f, cini = 0.f;
#pragma unroll
        for (int j = 0; j < 7; ++j) { if (j + 1 == run) { cinr = e[j][0]; cini = e[j][1]; } }
        TS(4);
        const float ncr = -cr, nci = -ci;
        mul4(hr, hi, cr, nci, ncr, nci, cinr, cini);
#pragma unroll
        for (int r = 0; r < 16; ++r) { float nr, ni; mul4(nr, ni, cr, nci, ncr, nci, ur[r], ui[r]); ur[r] = nr; ui[r] = ni; TIE(ur[r]); TIE(ui[r]); }
        TIE(hr); TIE(hi);
    }
    unsigned hw[2], ta[2];
    {
        const unsigned ht = lb + L_HT + wid * 2048;
#pragma unroll
        for (int j = 0; j < 2; ++j) {
            const int row = 8 * lh + 4 * j + ((lane & 15) >> 2), chunk = 2 * ((lane >> 4) & 1) + ((lane & 3) >> 1);
            ta[j] = ht + row * 64 + ((chunk ^ ((row >> 1) & 3)) << 4) + 8 * (lane & 1);
            hw[j] = ht + l31 * 64 + (((lh * 2 + j) ^ ((l31 >> 1) & 3)) << 4);
            TIE(ta[j]); TIE(hw[j]);
        }
    }
    {
        const int j0 = 2 * run - (16 - tm);
        float pr = 0.f, pi = 0.f;
        float nL128i = -L128i, nWi = -Wi;
        TIE(nL128i); TIE(nWi);
        if (4 * wm + 3 >= 16 - tm) {
            u32x4 eg[2];
            const unsigned eo0 = (j0 < 0 ? 0 : j0) * 16384 + (tn * 64 + wn * 32 + l31) * 16, eo1 = (j0 + 1 < 0 ? 0 : j0 + 1) * 16384 + (tn * 64 + wn * 32 + l31) * 16;
#pragma unroll 1
            for (unsigned spin = 0; spin < SPIN_CAP; ++spin) {
                GLD_B128_SC1(eg[0], eo0, ws + WS_E, 0); GLD_B128_SC1(eg[1], eo1, ws + WS_E, 0);
                WAIT_V(0); TIE(eg[0]); TIE(eg[1]);
                if (__all(eg[0][1] == tagA && eg[0][3] == tagG && eg[1][1] == tagA && eg[1][3] == tagG)) break;
                __builtin_amdgcn_s_sleep(2);
            }
            float e1r = j0 + 1 >= 0 ? __uint_as_float(eg[1][0]) : 0.f, e1i = j0 + 1 >= 0 ? __uint_as_float(eg[1][2]) : 0.f;
            const float e0r = j0 >= 0 ? __uint_as_float(eg[0][0]) : 0.f, e0i = j0 >= 0 ? __uint_as_float(eg[0][2]) : 0.f;
            cmac(e1r, e1i, L128r, L128i, nL128i, e0r, e0i);
            cmul4(pr, pi, Wr, Wi, nWi, e1r, e1i);
        }
        TS(5);
        *LDSP(f32x2, pl8 + (L_ES - L_AGG) + run * 512) = f32x2{pr, pi};
        lb_post(pmine, 1); lb_wait(pall, 1);
    }
    TS(6);
    {
        f32x2 e[8];
#pragma unroll
        for (int j = 0; j < 8; ++j) e[j] = *LDSP(f32x2, pl8 + (L_ES - L_AGG) + j * 512);
        const f32x2 G = ((e[0] + e[1]) + (e[2] + e[3])) + ((e[4] + e[5]) + (e[6] + e[7]));
        mac4(hr, hi, Qr, -Qi, -Qr, -Qi, G[0], G[1]);
#pragma unroll
        for (int r = 0; r < 16; ++r) { cmac(ur[r], ui[r], Lr, nLi, Li, hr, hi); hr = ur[r]; hi = ui[r]; }
        const char* hd = ws + WS_H + (size_t)((tm * 4 + wm) * 2) * HP + (tn * 4 + wn * 2) * 1024;
        v4i16 t[2][2][2];
#pragma unroll
        for (int c = 0; c < 2; ++c) {
#pragma unroll
            for (int e2 = 0; e2 < 2; ++e2) {
                const float* o = (c ? ui : ur) + 8 * e2;
                *LDSP(u32x4, hw[e2]) = u32x4{pack2(o[0], o[1]), pack2(o[2], o[3]), pack2(o[4], o[5]), pack2(o[6], o[7])};
            }
#pragma unroll
            for (int hf = 0; hf < 2; ++hf)
#pragma unroll
                for (int j = 0; j < 2; ++j) t[c][hf][j] = __builtin_amdgcn_ds_read_tr16_b64_v4i16(LDSP(v4i16, ta[j] + hf * 1024));
        }
        static_for<0, 4>([&](auto ic) {
            constexpr int n = decltype(ic)::value;
            const bf16x8 f = __builtin_shufflevector(t[n >> 1][n & 1][0], t[n >> 1][n & 1][1], 0, 1, 2, 3, 4, 5, 6, 7);
            GST_B128_SC1(l16, f, hd + (n >> 1) * HP, (n & 1) * 1024);
        });
    }
    TS(7);
    bf16x8 fa2[AW];
    u32x2 xv[4], dsv;
    const char* Aw = ws + WS_H + (size_t)((tm * 4 + wm) * 2 + wn) * HP;
    const char* g2src = (wn ? a.cim : a.cre) + (size_t)(tn * 64 + wm * 16) * 2048;
    const unsigned g2dst = lb + L_C + wid * 2048;
    unsigned g2v[2];
#pragma unroll
    for (int i = 0; i < 2; ++i) {
        const int pair = i * 4 + (lane >> 4), v = (lane & 15) ^ pair ^ ((wm & 1) * 8);
        g2v[i] = (pair * 2 + (v >> 3)) * 2048 + (v & 7) * 16;
    }
#define G2_ISSUE(st, i) DMA(g2dst + (((st) + 2) & 3) * G2STAGE + (i) * 1024, g2v[i], g2src + ((((st) + rot2) & 15) << 7))
#define G2_A(g) GLD_B128_SC1(fa2[(g) % AW], l16, Aw + (((((g) >> 2) + rot2) & 15) << 12), ((g) & 3) * 1024)
    {
        const char* xs = a.x + (size_t)(tm * 128 + wm * 32) * 2048 + tn * 128 + wn * 64;
#pragma unroll
        for (int it = 0; it < 4; ++it) GLD_B64(xv[it], (lane >> 3) * 2048 + (lane & 7) * 8, xs + it * 16384, 0);
        GLD_B64(dsv, (lane & 7) * 8, a.dskip + tn * 128 + wn * 64, 0);
        WAIT_V(5);
        lb_post(pmine, 2); lb_wait(pall, 2);
        if (wid == 0 && lane < 16) GST_B64_SC1((tm * 16 + lane) * 128 + tn * 8, tag, flg + FL_H);
        TS(8);
        static_for<4, 8>([&](auto ic) { constexpr int n = decltype(ic)::value; G2_ISSUE(n >> 1, n & 1); });
        if (wid == 0) {
#pragma unroll 1
            for (unsigned spin = 0; spin < SPIN_CAP; ++spin) {
                GLD_B64_SC1(fl, (tm * 16 + tn) * 128 + (lane & 15) * 8, flg + FL_H, 0);
                WAIT_V(0); TIE(fl);
                if (__all(fl[0] == tagA && fl[1] == tagB)) break;
            }
            lb_post(pmine, 3);
            if (b == 255 && lane == 0) GST_B64_SC1(0u * lane, tag, flg + FL_EPOCH);
        } else lb_wait(lb + L_SY, 3);
        static_for<0, AW>([&](auto ic) { constexpr int g = decltype(ic)::value; G2_A(g); });
        lb_wait(lb + L_SY + 32 + (lane & 3) * 4, 2);
    }
    TS(9);
    __builtin_amdgcn_s_setprio(0);
    {
#pragma unroll
        for (int j = 0; j < 2; ++j)
#pragma unroll
            for (int r = 0; r < 16; ++r) acc[j][r] = 0.f;
        const int boff = img_off(wn * 64 + l31, lh);
        unsigned bad[4];
#pragma unroll
        for (int ks = 0; ks < 4; ++ks) bad[ks] = lb + L_C + (boff ^ (ks * 32));
        bf16x8 fb[PF + 1][2];
#define G2_FRAGS(g) do { constexpr int bo_ = ((((g) >> 2) + 2) & 3) * G2STAGE; \
        DSR128(fb[(g) % (PF + 1)][0], bad[(g) & 3], bo_); DSR128(fb[(g) % (PF + 1)][1], bad[(g) & 3], bo_ + 4096); } while (0)
        static_for<0, PF>([&](auto ic) { constexpr int g = decltype(ic)::value; G2_FRAGS(g); });
        static_for<0, 64>([&](auto ic) {
            constexpr int g = decltype(ic)::value, gn = g + PF;
            if constexpr ((gn & 3) == 0 && gn < 64) {
                if constexpr ((gn >> 2) >= 4) WAIT_V(g2_cnt_stage(gn >> 2));
                WAIT_L(0); BAR();
                if constexpr (g == 2) TS(10);
                if constexpr (g == 18 || g == 34 || g == 50) TS(26 + (g >> 4));
            }
            if constexpr (gn < 64) G2_FRAGS(gn);
            SB();
            asm volatile("s_waitcnt vmcnt(%0) lgkmcnt(%1)" ::"n"(g2_cnt_a(g)), "n"((63 - g < PF ? 63 - g : PF) * 2) : "memory");
            TIE(fa2[g % AW]); TIE(fb[g % (PF + 1)][0]); TIE(fb[g % (PF + 1)][1]);
            acc[0] = __builtin_amdgcn_mfma_f32_32x32x16_bf16(fb[g % (PF + 1)][0], fa2[g % AW], acc[0], 0, 0, 0);
            SB();
            if constexpr (g2_dma(g)) G2_ISSUE(gn / 4 + 3, gn & 3);
            SB();
            acc[1] = __builtin_amdgcn_mfma_f32_32x32x16_bf16(fb[g % (PF + 1)][1], fa2[g % AW], acc[1], 0, 0, 0);
            SB();
            if constexpr (g2_a(g)) G2_A(g + AW);
            if constexpr (g == 58) lb_wait(lb + L_SY + 32 + (lane & 3) * 4, 3);
            SB();
        });
#undef G2_FRAGS
    }
    TS(11);
    {
        const unsigned wo = l31 * 128, sx = (l31 & 7) << 4;
        const f32x16 snd = wn ? acc[0] : acc[1], own = wn ? acc[1] : acc[0];
#pragma unroll
        for (int rq = 0; rq < 4; ++rq) *LDSP(f32x4, lb + L_Y + wid * 4096 + wo + ((rq * 32 + lh * 16) ^ sx)) = f32x4{snd[4 * rq], snd[4 * rq + 1], snd[4 * rq + 2], snd[4 * rq + 3]};
        lb_post(pmine, 4);
#pragma unroll
        for (int rq = 0; rq < 4; ++rq) *LDSP(f32x4, lb + L_Y2 + wid * 4096 + wo + ((rq * 32 + lh * 16) ^ sx)) = f32x4{own[4 * rq], own[4 * rq + 1], own[4 * rq + 2], own[4 * rq + 3]};
        TIE(dsv);
#pragma unroll
        for (int it = 0; it < 4; ++it) TIE(xv[it]);
        const float ds0 = bflo(dsv[0]), ds1 = bfhi(dsv[0]), ds2 = bflo(dsv[1]), ds3 = bfhi(dsv[1]);
        const unsigned ro = (lane >> 3) * 128 + (((lane & 7) ^ (lane >> 3)) << 4);
        f32x4 y[4];
#pragma unroll
        for (int it = 0; it < 4; ++it) {
            const f32x4 w = *LDSP(f32x4, lb + L_Y2 + wid * 4096 + it * 1024 + ro);
            y[it] = f32x4{2.f * w[0] + rb1(bflo(xv[it][0]) * ds0), 2.f * w[1] + rb1(bfhi(xv[it][0]) * ds1), 2.f * w[2] + rb1(bflo(xv[it][1]) * ds2), 2.f * w[3] + rb1(bfhi(xv[it][1]) * ds3)};
            TIE(y[it]);
        }
        lb_wait(lb + L_SY + (wid ^ 4) * 4, 4);
        char* const ob = a.out + (size_t)(tm * 128 + wm * 32) * 8192 + 4096 + tn * 256 + wn * 128;
        f32x4 w[4];
#pragma unroll
        for (int it = 0; it < 4; ++it) w[it] = *LDSP(f32x4, lb + L_Y + (wid ^ 4) * 4096 + it * 1024 + ro);
#pragma unroll
        for (int it = 0; it < 4; ++it) {
            const f32x4 o = {2.f * w[it][0] + y[it][0], 2.f * w[it][1] + y[it][1], 2.f * w[it][2] + y[it][2], 2.f * w[it][3] + y[it][3]};
            GST_B128_NT((lane >> 3) * 8192 + (lane & 7) * 16, o, ob + it * 65536, 0);
        }
    }
    asm volatile(".Lfk_code_b:");
    TS(12);
    TS_FLUSH();
}

}

extern "C" void kernel_launch(void* const* d_in, const int* in_sizes, int n_in, void* d_out, int out_size, void* d_ws, size_t ws_size,
                              hipStream_t stream) {
    (void)in_sizes; (void)n_in; (void)out_size; (void)ws_size;
    fk::Args a;
    a.q = (const char*)d_in[0]; a.k = (const char*)d_in[1]; a.v = (const char*)d_in[2]; a.sk = (const char*)d_in[3];
    a.ssmk = (const char*)d_in[4]; a.ssmD = (const char*)d_in[5]; a.x = (const char*)d_in[6]; a.lre = (const char*)d_in[7];
    a.lim = (const char*)d_in[8]; a.bre = (const char*)d_in[9]; a.bim = (const char*)d_in[10]; a.cre = (const char*)d_in[11];
    a.cim = (const char*)d_in[12]; a.logdt = (const char*)d_in[13]; a.dskip = (const char*)d_in[14];
    a.out = (char*)d_out; a.ws = (char*)d_ws;
    hipLaunchKernelGGL(fk::fused, dim3(256), dim3(768), 0, stream, a);
}
```

```cpp
#define GATE_AT 1
#define GATE_X 2000
#include <hip/hip_runtime.h>
#include <stdint.h>
#include <type_traits>

namespace fk {

constexpr int S = 2048, D = 1024, P = 1024;

typedef __attribute__((ext_vector_type(8))) short bf16x8;
typedef __attribute__((ext_vector_type(4))) short s16x4;
typedef __attribute__((ext_vector_type(16))) float f32x16;
typedef __attribute__((ext_vector_type(4))) float f32x4;
typedef __attribute__((ext_vector_type(2))) float f32x2;
typedef __attribute__((ext_vector_type(4))) unsigned u32x4;
typedef __attribute__((ext_vector_type(2))) unsigned u32x2;
#define LDSP(T, a) ((__attribute__((address_space(3))) T*)(uintptr_t)(unsigned)(a))

__device__ __forceinline__ float bf2f(unsigned v) { return __uint_as_float(v << 16); }
__device__ __forceinline__ float bflo(unsigned w) { return __uint_as_float(w << 16); }
__device__ __forceinline__ float bfhi(unsigned w) { return __uint_as_float(w & 0xffff0000u); }
__device__ __forceinline__ uint16_t f2bf(float f) { __bf16 b = (__bf16)f; return __builtin_bit_cast(uint16_t, b); }
__device__ __forceinline__ float rbf(float f) { return bf2f(f2bf(f)); }
typedef __attribute__((ext_vector_type(2))) __bf16 bf16x2;
__device__ __forceinline__ unsigned pack2(float lo, float hi) { return __builtin_bit_cast(unsigned, __builtin_convertvector(f32x2{lo, hi}, bf16x2)); }
__device__ __forceinline__ float rb1(float x) { return __uint_as_float(pack2(0.f, x)); }
__device__ __forceinline__ void cmac(float& xr, float& xi, float ar, float ai, float nai, float hr, float hi) {
    asm("v_fmac_f32 %0, %2, %5\n\tv_fmac_f32 %1, %2, %6\n\tv_fmac_f32 %0, %4, %6\n\tv_fmac_f32 %1, %3, %5" : "+v"(xr), "+v"(xi) : "v"(ar), "v"(ai), "v"(nai), "v"(hr), "v"(hi));
}
__device__ __forceinline__ void cmad(float& o_r, float& o_i, float ar, float ai, float nai, float hr, float hi, float er, float ei) {
    asm("v_fma_f32 %0, %2, %5, %7\n\tv_fma_f32 %1, %2, %6, %8\n\tv_fmac_f32 %0, %4, %6\n\tv_fmac_f32 %1, %3, %5" : "=&v"(o_r), "=&v"(o_i) : "v"(ar), "v"(ai), "v"(nai), "v"(hr), "v"(hi), "v"(er), "v"(ei));
}
__device__ __forceinline__ void mac4(float& x, float& y, float a0, float a1, float b0, float b1, float h0, float h1) {
    asm("v_fmac_f32 %0, %2, %6\n\tv_fmac_f32 %1, %4, %7\n\tv_fmac_f32 %0, %3, %7\n\tv_fmac_f32 %1, %5, %6" : "+v"(x), "+v"(y) : "v"(a0), "v"(a1), "v"(b0), "v"(b1), "v"(h0), "v"(h1));
}
__device__ __forceinline__ void mul4(float& x, float& y, float a0, float a1, float b0, float b1, float h0, float h1) {
    asm("v_mul_f32 %0, %2, %6\n\tv_mul_f32 %1, %4, %7\n\tv_fmac_f32 %0, %3, %7\n\tv_fmac_f32 %1, %5, %6" : "=&v"(x), "=&v"(y) : "v"(a0), "v"(a1), "v"(b0), "v"(b1), "v"(h0), "v"(h1));
}
__device__ __forceinline__ void cmul4(float& o_r, float& o_i, float ar, float ai, float nai, float hr, float hi) {
    asm("v_mul_f32 %0, %2, %5\n\tv_mul_f32 %1, %2, %6\n\tv_fmac_f32 %0, %4, %6\n\tv_fmac_f32 %1, %3, %5" : "=&v"(o_r), "=&v"(o_i) : "v"(ar), "v"(ai), "v"(nai), "v"(hr), "v"(hi));
}
__device__ __forceinline__ void cmul(float ar, float ai, float br, float bi, float& cr, float& ci) { cr = ar * br - ai * bi; ci = ar * bi + ai * br; }

template <int I, int N, class F>
__device__ __forceinline__ void static_for(F&& f) { if constexpr (I < N) { f(std::integral_constant<int, I>{}); static_for<I + 1, N>(f); } }

#define TS_DECL
#define TS(i)
#define TS_FLUSH()

#define WAIT_V(n) asm volatile("s_waitcnt vmcnt(%0)" ::"n"((n) > 63 ? 63 : (n)) : "memory")
#define WAIT_L(n) asm volatile("s_waitcnt lgkmcnt(%0)" ::"n"((n) > 15 ? 15 : (n)) : "memory")
#define BAR() asm volatile("s_barrier" ::: "memory")
#define SB() __builtin_amdgcn_sched_barrier(0)
template <class T> __device__ __forceinline__ void tie_(T& x) { asm volatile("" : "+v"(x)); }
#define TIE(x) tie_(x)
#define DEF_LD(name, ins) template <int OFF, class T> __device__ __forceinline__ void name(T& dst, unsigned voff, const char* sb) { \
    asm volatile(ins : "=v"(dst) : "v"(voff), "s"(sb), "n"(OFF) : "memory"); }
DEF_LD(gld_u16_, "global_load_ushort %0, %1, %2 offset:%3")
DEF_LD(gld_b32_, "global_load_dword %0, %1, %2 offset:%3")
DEF_LD(gld_b64_, "global_load_dwordx2 %0, %1, %2 offset:%3")
DEF_LD(gld_b128_, "global_load_dwordx4 %0, %1, %2 offset:%3")
DEF_LD(gld_b64_nt_, "global_load_dwordx2 %0, %1, %2 offset:%3 nt")
DEF_LD(gld_b128_nt_, "global_load_dwordx4 %0, %1, %2 offset:%3 nt")
DEF_LD(gld_b64_sc1_, "global_load_dwordx2 %0, %1, %2 offset:%3 sc1")
DEF_LD(gld_b128_sc1_, "global_load_dwordx4 %0, %1, %2 offset:%3 sc1")
#define DEF_ST(name, ins) template <int OFF, class T> __device__ __forceinline__ void name(unsigned voff, const T& v, const char* sb) { \
    asm volatile(ins :: "v"(voff), "v"(v), "s"(sb), "n"(OFF) : "memory"); }
DEF_ST(gst_b128_, "global_store_dwordx4 %0, %1, %2 offset:%3\n\ts_nop 1")
DEF_ST(gst_b128_nt_, "global_store_dwordx4 %0, %1, %2 offset:%3 nt\n\ts_nop 1")
DEF_ST(gst_b128_sc1_, "global_store_dwordx4 %0, %1, %2 offset:%3 sc1\n\ts_nop 1")
DEF_ST(gst_b64_sc1_, "global_store_dwordx2 %0, %1, %2 offset:%3 sc1")
#define GLD_U16(dst, voff, sb) gld_u16_<0>(dst, voff, sb)
#define GLD_B32(dst, voff, sb, off) gld_b32_<(off)>(dst, voff, sb)
#define GLD_B64(dst, voff, sb, off) gld_b64_<(off)>(dst, voff, sb)
#define GLD_B128(dst, voff, sb, off) gld_b128_<(off)>(dst, voff, sb)
#define GLD_B64_NT(dst, voff, sb, off) gld_b64_nt_<(off)>(dst, voff, sb)
#define GLD_B128_NT(dst, voff, sb, off) gld_b128_nt_<(off)>(dst, voff, sb)
#define GLD_B64_SC1(dst, voff, sb, off) gld_b64_sc1_<(off)>(dst, voff, sb)
#define GLD_B128_SC1(dst, voff, sb, off) gld_b128_sc1_<(off)>(dst, voff, sb)
#define GST_B128(voff, v, sb, off) gst_b128_<(off)>(voff, v, sb)
#define GST_B128_NT(voff, v, sb, off) gst_b128_nt_<(off)>(voff, v, sb)
#define GST_B128_SC1(voff, v, sb, off) gst_b128_sc1_<(off)>(voff, v, sb)
#define GST_B64_SC1(voff, v, sb) gst_b64_sc1_<0>(voff, v, sb)
__device__ __forceinline__ void dma_(unsigned ldsaddr, unsigned voff, const char* sb) {
    asm volatile("s_mov_b32 m0, %0\n\ts_nop 0\n\tglobal_load_lds_dwordx4 %1, %2" :: "s"(ldsaddr), "v"(voff), "s"(sb) : "memory"); }
__device__ __forceinline__ void dma_nt_(unsigned ldsaddr, unsigned voff, const char* sb) {
    asm volatile("s_mov_b32 m0, %0\n\ts_nop 0\n\tglobal_load_lds_dwordx4 %1, %2 nt" :: "s"(ldsaddr), "v"(voff), "s"(sb) : "memory"); }
#define DMA(ldsaddr, voff, sb) dma_(ldsaddr, voff, sb)
template <int OFF, class T> __device__ __forceinline__ void dsr128_(T& dst, unsigned addr) { asm volatile("ds_read_b128 %0, %1 offset:%2" : "=v"(dst) : "v"(addr), "n"(OFF)); }
template <int OFF, class T> __device__ __forceinline__ void dsr64_(T& dst, unsigned addr) { asm volatile("ds_read_b64 %0, %1 offset:%2" : "=v"(dst) : "v"(addr), "n"(OFF)); }
#define DSR128(dst, addr, off) dsr128_<(off)>(dst, addr)
#define DSR64(dst, addr, off) dsr64_<(off)>(dst, addr)

__device__ __forceinline__ void kv4(float* t, const float* e, float w0, float w1, float w2, float w3, const float* v) {
    float p0, p1, p2, p3;
    asm(
        "v_mul_f32 %0, %8, %18\n\t"
        "v_mul_f32 %1, %9, %18\n\t"
        "v_mul_f32 %2, %10, %18\n\t"
        "v_mul_f32 %3, %11, %18\n\t"
        "v_cvt_pk_bf16_f32 %0, 0, %0\n\t"
        "v_cvt_pk_bf16_f32 %1, 0, %1\n\t"
        "v_cvt_pk_bf16_f32 %2, 0, %2\n\t"
        "v_cvt_pk_bf16_f32 %3, 0, %3\n\t"
        "v_mul_f32 %4, %9, %17\n\t"
        "v_mul_f32 %5, %10, %17\n\t"
        "v_mul_f32 %6, %11, %17\n\t"
        "v_mul_f32 %7, %12, %17\n\t"
        "v_cvt_pk_bf16_f32 %4, 0, %4\n\t"
        "v_cvt_pk_bf16_f32 %5, 0, %5\n\t"
        "v_cvt_pk_bf16_f32 %6, 0, %6\n\t"
        "v_cvt_pk_bf16_f32 %7, 0, %7\n\t"
        "v_add_f32 %0, %0, %4\n\t"
        "v_add_f32 %1, %1, %5\n\t"
        "v_add_f32 %2, %2, %6\n\t"
        "v_add_f32 %3, %3, %7\n\t"
        "v_cvt_pk_bf16_f32 %0, 0, %0\n\t"
        "v_cvt_pk_bf16_f32 %1, 0, %1\n\t"
        "v_cvt_pk_bf16_f32 %2, 0, %2\n\t"
        "v_cvt_pk_bf16_f32 %3, 0, %3\n\t"
        "v_mul_f32 %4, %10, %16\n\t"
        "v_mul_f32 %5, %11, %16\n\t"
        "v_mul_f32 %6, %12, %16\n\t"
        "v_mul_f32 %7, %13, %16\n\t"
        "v_cvt_pk_bf16_f32 %4, 0, %4\n\t"
        "v_cvt_pk_bf16_f32 %5, 0, %5\n\t"
        "v_cvt_pk_bf16_f32 %6, 0, %6\n\t"
        "v_cvt_pk_bf16_f32 %7, 0, %7\n\t"
        "v_add_f32 %0, %0, %4\n\t"
        "v_add_f32 %1, %1, %5\n\t"
        "v_add_f32 %2, %2, %6\n\t"
        "v_add_f32 %3, %3, %7\n\t"
        "v_cvt_pk_bf16_f32 %0, 0, %0\n\t"
        "v_cvt_pk_bf16_f32 %1, 0, %1\n\t"
        "v_cvt_pk_bf16_f32 %2, 0, %2\n\t"
        "v_cvt_pk_bf16_f32 %3, 0, %3\n\t"
        "v_mul_f32 %4, %11, %15\n\t"
        "v_mul_f32 %5, %12, %15\n\t"
        "v_mul_f32 %6, %13, %15\n\t"
        "v_mul_f32 %7, %14, %15\n\t"
        "v_cvt_pk_bf16_f32 %4, 0, %4\n\t"
        "v_cvt_pk_bf16_f32 %5, 0, %5\n\t"
        "v_cvt_pk_bf16_f32 %6, 0, %6\n\t"
        "v_cvt_pk_bf16_f32 %7, 0, %7\n\t"
        "v_add_f32 %0, %0, %4\n\t"
        "v_add_f32 %1, %1, %5\n\t"
        "v_add_f32 %2, %2, %6\n\t"
        "v_add_f32 %3, %3, %7\n\t"
        "v_cvt_pk_bf16_f32 %0, 0, %0\n\t"
        "v_cvt_pk_bf16_f32 %1, 0, %1\n\t"
        "v_cvt_pk_bf16_f32 %2, 0, %2\n\t"
        "v_cvt_pk_bf16_f32 %3, 0, %3\n\t"
        "v_mul_f32 %0, %0, %19\n\t"
        "v_mul_f32 %1, %1, %20\n\t"
        "v_mul_f32 %2, %2, %21\n\t"
        "v_mul_f32 %3, %3, %22\n\t"
        : "=&v"(t[0]), "=&v"(t[1]), "=&v"(t[2]), "=&v"(t[3]), "=&v"(p0), "=&v"(p1), "=&v"(p2), "=&v"(p3)
        : "v"(e[0]), "v"(e[1]), "v"(e[2]), "v"(e[3]), "v"(e[4]), "v"(e[5]), "v"(e[6]), "v"(w0), "v"(w1), "v"(w2), "v"(w3), "v"(v[0]), "v"(v[1]), "v"(v[2]), "v"(v[3]));
}
__device__ __forceinline__ void lb_post(unsigned myaddr, unsigned seq) { asm volatile("ds_write_b32 %0, %1" :: "v"(myaddr), "v"(seq) : "memory"); }
__device__ __forceinline__ void lb_wait(unsigned addr, unsigned seq) {
    unsigned v;
    asm volatile("s_branch 2f\n1:\n\ts_sleep 1\n2:\n\tds_read_b32 %0, %1\n\ts_waitcnt lgkmcnt(0)\n\tv_cmp_gt_u32 vcc, %2, %0\n\ts_cbranch_vccnz 1b" : "=&v"(v) : "v"(addr), "v"(seq) : "vcc", "memory");
}

constexpr size_t WS_H = 0, WS_KVT = (size_t)10 << 20, WS_ZT = (size_t)15 << 20, WS_E = (size_t)20 << 20, WS_FLAG = WS_E + (256 << 10);
#define HP 65536
#define KP 8192
#define ZP 4096
constexpr int FL_KV = 0, FL_Z = 65536, FL_H = 131072, FL_EPOCH = 196608;
constexpr unsigned SPIN_CAP = 1u << 17;
#define ABL 0
#define CSPLIT 1
#define MERGE 3
#define CB 13
#define PAUSE 0
#define QLATE 0
#define CVPRIO 0
#define STG_SEL (idx & 3)
#define SP0 3
#define SDP 1
#define LP0 3
#define LDP 2
#define KVS 5
#define KVU 2
#define QIT 2
#define ZB 3
#define OS 2
#define MPRIO 2

constexpr int G1STAGE = 32768;
constexpr int L_KS = 131072, L_VS = 147456;
constexpr int CST = 4288;
constexpr int UOFF = 4 * CST, UPL = 768;
constexpr int upl(int c) { return c * UPL + (c >> 1) * 32; }
constexpr int CHSZ = UOFF + 8 * UPL + 192;
constexpr int L_AGG = 4 * CHSZ, L_SY = L_AGG + 4096;
constexpr int L_HT = 98304, L_ES = 114688;
constexpr int L_C = 98304, G2STAGE = 16384;
constexpr int L_ZI = 0;
constexpr int L_Z2 = 20480;
constexpr int L_Y = 40960, L_Y2 = 131072;
static_assert(L_SY + 64 <= L_HT, "lds");
static_assert(L_Y + 32768 <= L_AGG && L_Z2 + 64 * 264 <= L_Y, "lds");

__device__ __forceinline__ int img_off(int r, int c) { return (r >> 1) * 256 + ((((r & 1) * 8 + c) ^ ((r >> 1) & 15)) << 4); }

constexpr int PF = 2;
constexpr int g1_upto(int g) {
    int n = 16;
    for (int t = 0; t <= g; ++t) {
        int sb = (t + PF) / 4, sub = (t + PF) % 4, allowed = sb >= 1 ? 4 * (sb + 3) + sub + 1 : 16;
        if (allowed > 64) allowed = 64;
        const bool pause = t >= 4 * KVS - PF && t < 4 * KVS - PF + PAUSE;
        if (!pause) n = n + 2 < allowed ? n + 2 : allowed;
    }
    return n;
}
constexpr int g1_cnt(int s) { return g1_upto(4 * s - PF - 1) - 4 * (s + 1); }
static_assert(g1_upto(49) == 64, "schedule");
constexpr int AW = 8;
constexpr int g2_dma(int g) { return ((g + PF) / 4 >= 1 && (g + PF) / 4 + 3 < 16 && (g + PF) % 4 < 2) ? 1 : 0; }
constexpr int g2_a(int g) { return g + AW < 64 ? 1 : 0; }
constexpr int g2_ops(int g) { return g2_dma(g) + g2_a(g); }
constexpr int g2_cnt_stage(int s) {
    int g = 4 * s - PF, t1 = 4 * (s - 3) - PF + 1, c = g2_a(t1);
    for (int t = t1 + 1; t < g; ++t) c += g2_ops(t);
    return c;
}
constexpr int g2_cnt_a(int g) {
    int c = 0;
    if (g < AW) { c = AW - 1 - g; for (int t = 0; t < g; ++t) c += g2_ops(t); }
    else { for (int t = g - AW + 1; t < g; ++t) c += g2_ops(t); }
    return c;
}

struct Args {
    const char *q, *k, *v, *sk, *ssmk, *ssmD, *x, *lre, *lim, *bre, *bim, *cre, *cim, *logdt, *dskip;
    char *out, *ws;
};

__global__ __launch_bounds__(768, 3) void fused(Args a) {
    __shared__ __attribute__((aligned(1024))) char shm[163840];
    const char* pc0;
    int code_a, code_b;
    asm volatile("s_getpc_b64 %0\n.Lfk_code_0:\n\ts_mov_b32 %1, .Lfk_code_a-.Lfk_code_0\n\ts_mov_b32 %2, .Lfk_code_b-.Lfk_code_0" : "=s"(pc0), "=s"(code_a), "=s"(code_b));
    char* const ws = a.ws;
    const unsigned long long gate_t0 = __builtin_amdgcn_s_memrealtime();
#define GATE(i) do { if (GATE_AT == (i)) { while (__builtin_amdgcn_s_memrealtime() - gate_t0 < GATE_X) __builtin_amdgcn_s_sleep(1); __builtin_amdgcn_s_waitcnt(0xC07F); } } while (0)
    __builtin_amdgcn_s_waitcnt(0xC07F);
    GATE(0);
    TS_DECL TS(0);
    const int tid = threadIdx.x, lane = tid & 63, wid = __builtin_amdgcn_readfirstlane(tid >> 6);
    const int b = blockIdx.x, xcd = b & 7, idx = b >> 3;
    const int tm = (xcd >> 1) * 4 + (idx >> 3), tn = (xcd & 1) * 8 + (idx & 7);
    const int wm = wid & 3, wn = (wid >> 2) & 1, l31 = lane & 31, lh = lane >> 5;
    const unsigned lb = (unsigned)(uintptr_t)shm;
    const char* const flg = ws + WS_FLAG;
    const unsigned l16 = lane * 16;
    const int rot2 = (2 * tm) & 15;
    typedef __attribute__((ext_vector_type(4))) short v4i16;

    if (wid >= 8) {
        const int chn = xcd * 128 + idx * 4 + wm;
        const unsigned cb = lb + wm * CHSZ;
        const int r = lane & 15, g4 = lane >> 4;
        const unsigned fpub = xcd * 256 + idx * 8, fpoll = xcd * 256 + l31 * 8;
        __builtin_amdgcn_s_setprio(3);
        u32x2 ep, fl, kw[2]; unsigned hal[2], dskl; u32x4 sw[4], uu[4];
        unsigned cpf = 0;
        if (wm == 3) {
            if (lane < 16 && (idx * 16 + lane) * 128 < (code_a > code_b ? code_a : code_b)) GLD_B32(cpf, (idx * 16 + lane) * 128, pc0, 0);
        }
        {
            const unsigned ho = (idx ? idx * 64 - 4 + g4 : 0) * 2048 + xcd * 256 + wm * 64 + r * 2;
            gld_u16_<0>(hal[0], ho, a.k); gld_u16_<32>(hal[1], ho, a.k);
            GLD_B64_SC1(ep, 0u * lane, flg + FL_EPOCH, 0);
            const unsigned c0 = xcd * 128 + wm * 32 + r;
            GLD_B64(kw[0], c0 * 8, a.sk, 0); GLD_B64(kw[1], c0 * 8, a.sk, 128);
            GLD_U16(dskl, 0u * lane, a.ssmD + chn * 2);
        }
        TS(1);
        WAIT_V(0);
        BAR();
        TS(2);
        TIE(ep); TIE(kw[0]); TIE(kw[1]); TIE(hal[0]); TIE(hal[1]); TIE(dskl); TIE(cpf);
        const unsigned e0 = __builtin_amdgcn_readfirstlane(ep[0]), e1 = __builtin_amdgcn_readfirstlane(ep[1]);
        const unsigned tagA = e0 + 1, tagB = e1 + (tagA == 0 ? 1u : 0u);
        const u32x2 tag = {tagA, tagB};
        u32x4 kvo[4];
        {
            const int q = r >> 2, pp = r & 3;
            u32x2 kt[4][3], vt[4][2];
            unsigned hm[2][3];
#pragma unroll
            for (int u = 0; u < 4; ++u) {
                const int p = u >> 1, oh = u & 1;
                auto tr = [&](int base, int row) -> u32x2 {
                    const int rr = row + q, ch = (wm * 4 + p * 2 + (pp >> 1)) ^ (2 * (q | (((rr >> 3) & 1) << 2)));
                    return __builtin_bit_cast(u32x2, __builtin_amdgcn_ds_read_tr16_b64_v4i16(LDSP(v4i16, lb + base + rr * 256 + ch * 16 + (pp & 1) * 8)));
                };
                const int row0 = (g4 + 4 * oh) * 8;
                kt[u][0] = tr(L_KS, (oh == 0 && g4 == 0) ? 0 : row0 - 4);
                kt[u][1] = tr(L_KS, row0); kt[u][2] = tr(L_KS, row0 + 4); vt[u][0] = tr(L_VS, row0); vt[u][1] = tr(L_VS, row0 + 4);
                if (oh == 0) { hm[p][0] = __shfl(hal[p], r + 16); hm[p][1] = __shfl(hal[p], r + 32); hm[p][2] = __shfl(hal[p], r + 48); }
            }
            SB();
            const int sl = ((lane & 3) * 16 + (lane >> 2)) * 4;
#pragma unroll
            for (int u = 0; u < 4; ++u) {
                const int p = u >> 1, oh = u & 1;
                u32x2 k0 = kt[u][0];
                const u32x2 k1 = kt[u][1], k2 = kt[u][2], v0 = vt[u][0], v1 = vt[u][1];
                if (oh == 0 && g4 == 0) k0 = idx ? u32x2{hm[p][0] << 16, hm[p][1] | (hm[p][2] << 16)} : u32x2{0u, 0u};
                const float w0 = bflo(kw[p][0]), w1 = bfhi(kw[p][0]), w2 = bflo(kw[p][1]), w3 = bfhi(kw[p][1]);
                const float e[11] = {bfhi(k0[0]), bflo(k0[1]), bfhi(k0[1]), bflo(k1[0]), bfhi(k1[0]), bflo(k1[1]), bfhi(k1[1]), bflo(k2[0]), bfhi(k2[0]), bflo(k2[1]), bfhi(k2[1])};
                const float vf[8] = {bflo(v0[0]), bfhi(v0[0]), bflo(v0[1]), bfhi(v0[1]), bflo(v1[0]), bfhi(v1[0]), bflo(v1[1]), bfhi(v1[1])};
                float t[8];
                if (ABL & 2) { for (int i = 0; i < 8; ++i) t[i] = e[i]; } else
                kv4(t, e, w0, w1, w2, w3, vf);
                if (u > KVU) { TIE(t[0]); BAR(); }
                if (!(ABL & 2)) kv4(t + 4, e + 4, w0, w1, w2, w3, vf + 4);
                const u32x4 o = {pack2(t[7], t[6]), pack2(t[5], t[4]), pack2(t[3], t[2]), pack2(t[1], t[0])};
#pragma unroll
                for (int j = 0; j < 4; ++j) kvo[u][j] = __builtin_amdgcn_ds_bpermute(sl, o[j]);
                SB();
                if (u >= KVU) BAR();
            }
#pragma unroll
            for (int u = 0; u < 4; ++u) TIE(kvo[u]);
        }
        TS(3);
        u32x2 qv[8];
        const char* krow = a.ssmk + (size_t)chn * 4096;
        const char* qs = a.q + (size_t)(idx * 64 + wm * 16) * 2048 + xcd * 256;
        {
            const int ph = STG_SEL, ps = SP0 + ph * SDP, pl = LP0 + ph * LDP;
            const char* kvd = ws + WS_KVT + (size_t)(xcd * 64 + wm * 16) * KP + idx * 256;
            int nb1 = 1 + 2 * (3 - KVU);
#pragma unroll 1
            for (; nb1 <= ps; ++nb1) BAR();
            static_for<0, 4>([&](auto uc) {
                constexpr int u = decltype(uc)::value;
                GST_B128_SC1((lane >> 3) * KP + (lane & 7) * 16, kvo[u], kvd + (u >> 1) * 8 * KP, (u & 1) * 128);
            });
#pragma unroll 1
            for (; nb1 <= pl; ++nb1) {
                if (nb1 == ps + 4) WAIT_V(0);
                BAR();
                if (nb1 == ps + 4 && wm == 0 && lane == 0) GST_B64_SC1(fpub, tag, flg + FL_KV);
            }
            GLD_B128_NT(sw[0], l16, krow, 0); GLD_B128_NT(sw[1], l16, krow, 1024); GLD_B128_NT(sw[2], l16, krow, 2048); GLD_B128_NT(sw[3], l16, krow, 3072);
            static_for<0, 8>([&](auto jc) { constexpr int j = decltype(jc)::value; GLD_B64_NT(qv[j], lh * 2048 + l31 * 8, qs + j * 4096, 0); });
#pragma unroll 1
            for (; nb1 <= 12; ++nb1) {
                if (nb1 == ps + 4) WAIT_V(12);
                BAR();
                if (nb1 == ps + 4 && wm == 0 && lane == 0) GST_B64_SC1(fpub, tag, flg + FL_KV);
            }
        }
        if (ABL & 1024) fl = tag; else GLD_B64_SC1(fl, fpoll, flg + FL_KV, 0);
        BAR();
        WAIT_V(0); TIE(fl);
        const char* usrc = ws + WS_KVT + (size_t)(xcd * 64 + idx * 2 + (wm >> 1)) * KP + (wm & 1) * 4096;
        if (!(ABL & 16)) { GLD_B128_SC1(uu[0], l16, usrc, 0); GLD_B128_SC1(uu[1], l16, usrc, 1024); GLD_B128_SC1(uu[2], l16, usrc, 2048); GLD_B128_SC1(uu[3], l16, usrc, 3072); }
        if (!(MERGE & 1)) {
            GLD_B128_NT(sw[0], l16, krow, 0); GLD_B128_NT(sw[1], l16, krow, 1024); GLD_B128_NT(sw[2], l16, krow, 2048); GLD_B128_NT(sw[3], l16, krow, 3072);
        }
        if (CB == 14) BAR();
        {
            const char* cs = ((wm & 2) ? a.cim : a.cre) + (size_t)(tn * 64 + (wm & 1) * 32) * 2048;
#pragma unroll
            for (int i = 0; i < 4; ++i) {
                const int pair = i * 4 + g4, v = r ^ pair;
                const unsigned o = (pair * 2 + (v >> 3)) * 2048 + (v & 7) * 16;
                DMA(lb + L_C + 2 * G2STAGE + wm * 4096 + i * 1024, o, cs + (rot2 << 7));
                DMA(lb + L_C + 3 * G2STAGE + wm * 4096 + i * 1024, o, cs + (rot2 << 7) + 128);
                if (i == CSPLIT) BAR();
            }
        }
        if (CB == 13) BAR();
        TS(4);
        const unsigned ud = lb + ((wm & 2) + ((lane >> 2) & 1)) * CHSZ + UOFF + upl(((lane >> 3) & 1) * 4 + (lane & 3)) + (16 + (wm & 1) * 16 + g4) * 16;
        {
            const u32x4 z4 = {0, 0, 0, 0};
            if (lane < 16) *LDSP(unsigned, lb + L_SY + lane * 4) = 0;
            if (lane < 32) *LDSP(u32x4, cb + (lane >> 3) * CST + (lane & 7) * 16) = z4;
            *LDSP(u32x4, cb + UOFF + upl(g4) + r * 16) = z4;
            *LDSP(u32x4, cb + UOFF + upl(4 + g4) + r * 16) = z4;
        }
        WAIT_L(0);
        TS(5);
        BAR();
        TS(11);
        WAIT_V(8);
        TS(12);
        if (!(ABL & 32)) {
#pragma unroll
            for (int it = 0; it < 4; ++it) TIE(sw[it]);
#pragma unroll
            for (int it = 0; it < 4; ++it) {
                const unsigned q = cb + 128 + it * 1024 + l16;
                const unsigned d0 = sw[it][0], d1 = sw[it][1], d2 = sw[it][2], d3 = sw[it][3];
                const unsigned o0 = it ? __builtin_amdgcn_readlane(sw[it ? it - 1 : 0][2], 63) : 0u, o1 = it ? __builtin_amdgcn_readlane(sw[it ? it - 1 : 0][3], 63) : 0u;
                const unsigned x0 = __builtin_amdgcn_update_dpp(o0, d2, 0x138, 0xf, 0xf, false), x1 = __builtin_amdgcn_update_dpp(o1, d3, 0x138, 0xf, 0xf, false);
                const unsigned a0 = __builtin_amdgcn_alignbyte(d0, x1, 2), a1 = __builtin_amdgcn_alignbyte(d1, d0, 2), a2 = __builtin_amdgcn_alignbyte(d2, d1, 2),
                               a3 = __builtin_amdgcn_alignbyte(d3, d2, 2);
                *LDSP(u32x4, q) = sw[it];
                *LDSP(u32x4, q + CST) = u32x4{a0, a1, a2, a3};
                *LDSP(u32x4, q + 2 * CST) = u32x4{x1, d0, d1, d2};
                *LDSP(u32x4, q + 3 * CST) = u32x4{__builtin_amdgcn_alignbyte(x1, x0, 2), a0, a1, a2};
                if (it == 3 && lane == 63) {
                    *LDSP(u32x2, q + 16 + CST) = u32x2{d3 >> 16, 0u};
                    *LDSP(u32x2, q + 16 + 2 * CST) = u32x2{d3, 0u};
                    *LDSP(u32x2, q + 16 + 3 * CST) = u32x2{a3, d3 >> 16};
                }
            }
        }
        TS(13);
#pragma unroll
        for (int it = 0; it < 4; ++it) TIE(uu[it]);
        if (!__all(fl[0] == tagA && fl[1] == tagB)) {
            for (unsigned spin = 0; spin < SPIN_CAP; ++spin) {
                GLD_B64_SC1(fl, fpoll, flg + FL_KV, 0);
                WAIT_V(0); TIE(fl);
                if (__all(fl[0] == tagA && fl[1] == tagB)) break;
                __builtin_amdgcn_s_sleep(8);
            }
            GLD_B128_SC1(uu[0], l16, usrc, 0); GLD_B128_SC1(uu[1], l16, usrc, 1024); GLD_B128_SC1(uu[2], l16, usrc, 2048); GLD_B128_SC1(uu[3], l16, usrc, 3072);
            WAIT_V(0);
#pragma unroll
            for (int it = 0; it < 4; ++it) TIE(uu[it]);
        }
#pragma unroll
        for (int it = 0; it < 4; ++it) *LDSP(u32x4, ud + it * 64) = uu[it];
        lb_post(lb + L_SY + 32 + wm * 4, 1); lb_wait(lb + L_SY + 32 + (wm ^ 1) * 4, 1);
        __builtin_amdgcn_s_setprio(CVPRIO);
        TS(6);
        f32x4 ca[4][2];
        {
            unsigned kb = cb + ((3 - r) & 3) * CST + 2 * (64 + r - 8 * g4 - 7 + ((3 - r) & 3)) - 64;
            unsigned ub = cb + UOFF + upl(g4) + r * 16;
#pragma unroll
            for (int i = 0; i < 4; ++i) { ca[i][0] = f32x4{0, 0, 0, 0}; ca[i][1] = f32x4{0, 0, 0, 0}; }
            s16x4 FA[2][8][2]; bf16x8 FB[2][2];
#define CV_A(h, q, e) DSR64(FA[h][((q) + 2) & 7][e], kb, 32 * ((q) + 2) + 8 * (e) + 2048 * (h))
#define CV_B(s, ct) DSR128(FB[(s) & 1][ct], ub, (1 - ((s) & 1)) * upl(4) + (16 + 16 * (ct) - ((s) >> 1)) * 16)
            static_for<0, 8>([&](auto ic) { constexpr int n = decltype(ic)::value; CV_A((n >> 1) & 1, -2 + (n >> 2), n & 1); });
            WAIT_L(4);
            CV_B(0, 0); CV_B(0, 1);
            static_for<0, 8>([&](auto ic) { constexpr int n = decltype(ic)::value; CV_A((n >> 1) & 1, (n >> 2), n & 1); });
            auto body = [&]() {
                static_for<0, 4>([&](auto ic) {
                    constexpr int s = decltype(ic)::value;
                    static_for<0, 12>([&](auto mc) {
                        constexpr int m = decltype(mc)::value, bi = m / 3, k = m % 3, sl = (2 * s + bi) & 7;
                        if constexpr (m == 0) { WAIT_L(8); TIE(FB[s & 1][0]); TIE(FB[s & 1][1]); }
                        if constexpr (m == 6) WAIT_L(10);
                        if constexpr (m == 9) WAIT_L(9);
                        if constexpr (k == 0) { TIE(FA[0][sl][0]); TIE(FA[0][sl][1]); TIE(FA[1][sl][0]); TIE(FA[1][sl][1]); }
                        const bf16x8 fa = __builtin_shufflevector(FA[k == 2][sl][0], FA[k == 2][sl][1], 0, 1, 2, 3, 4, 5, 6, 7);
                        ca[bi][k != 1] = __builtin_amdgcn_mfma_f32_16x16x32_bf16(fa, FB[s & 1][k == 0], ca[bi][k != 1], 0, 0, 0);
                        SB();
                        if constexpr (m < 2) CV_B(s + 1, m);
                        else if constexpr (m < 10) CV_A(((m - 2) >> 1) & 1, 2 * (s + 1) + ((m - 2) >> 2), (m - 2) & 1);
                        SB();
                    });
                });
                kb += 256; ub -= 32;
            };
#pragma unroll 1
            for (int it = 0; it < QIT; ++it) body();
            {
                WAIT_V(0);
                lb_post(lb + L_SY + 32 + wm * 4, 2);
                if (!(MERGE & 2)) static_for<0, 8>([&](auto jc) { constexpr int j = decltype(jc)::value; GLD_B64_NT(qv[j], lh * 2048 + l31 * 8, qs + j * 4096, 0); });
            }
#pragma unroll 1
            for (int it = QIT; it < 8; ++it) body();
            WAIT_L(0);
#pragma unroll
            for (int i = 0; i < 4; ++i) { TIE(FA[0][i][0]); TIE(FA[0][i][1]); TIE(FA[1][i][0]); TIE(FA[1][i][1]); }
            TIE(FB[0][0]); TIE(FB[0][1]);
#undef CV_A
#undef CV_B
        }
        TS(7);
        __builtin_amdgcn_s_setprio(3);
        int nb = 0;
#define HB() do { if (nb < 15) BAR(); ++nb; } while (0)
        {
            const float Dd = bf2f(dskl);
            const char* zd = ws + WS_ZT + (size_t)chn * ZP;
            u32x2 kvr[2][4];
#pragma unroll
            for (int ct = 0; ct < 2; ++ct)
#pragma unroll
                for (int bi = 0; bi < 4; ++bi)
                    kvr[ct][bi] = *LDSP(u32x2, cb + UOFF + upl(2 * bi + (g4 >> 1)) + (16 + 16 * ct + r) * 16 + (1 - (g4 & 1)) * 8);
#pragma unroll
            for (int ct = 0; ct < 2; ++ct)
#pragma unroll
                for (int bi = 0; bi < 4; ++bi) {
                    const f32x4 y = ca[bi][ct];
                    const u32x2 kq = kvr[ct][bi];
                    *LDSP(u32x2, cb + ct * 2304 + r * 144 + bi * 32 + g4 * 8) =
                        u32x2{pack2(rb1(y[0]) + rb1(bfhi(kq[1]) * Dd), rb1(y[1]) + rb1(bflo(kq[1]) * Dd)), pack2(rb1(y[2]) + rb1(bfhi(kq[0]) * Dd), rb1(y[3]) + rb1(bflo(kq[0]) * Dd))};
                }
            if (ZB >= 3) HB();
            u32x4 t[4];
#pragma unroll
            for (int it = 0; it < 4; ++it) t[it] = *LDSP(u32x4, cb + (it * 8 + (lane >> 3)) * 144 + (lane & 7) * 16);
            if (!(ABL & 128)) { GST_B128_SC1(l16, t[0], zd, 0); GST_B128_SC1(l16, t[1], zd, 1024); GST_B128_SC1(l16, t[2], zd, 2048); GST_B128_SC1(l16, t[3], zd, 3072); }
            else { TIE(t[0]); TIE(t[1]); TIE(t[2]); TIE(t[3]); }
        }
        if (ZB >= 2) HB();
        if (ZB >= 1) HB();
        WAIT_V(0);
        lb_post(lb + L_SY + 32 + wm * 4, 3);
        if (wm == 0) {
            lb_wait(lb + L_SY + 32 + (lane & 3) * 4, 3);
            if (!(ABL & 1024) && lane == 0) GST_B64_SC1(fpub, tag, flg + FL_Z);
        }
        TS(8);
        HB(); HB(); HB();
        if (!(ABL & 1024) && wm == 0) GLD_B64_SC1(fl, fpoll, flg + FL_Z, 0);
        HB(); HB();
        if (wm == 0) { WAIT_V(0); TIE(fl); lb_post(lb + L_SY + 48, __all(fl[0] == tagA && fl[1] == tagB) ? 2 : 1); }
        lb_wait(lb + L_SY + 48, 1);
        if (__builtin_amdgcn_readfirstlane(*LDSP(unsigned, lb + L_SY + 48)) != 2) {
            while (nb < 15) { BAR(); ++nb; }
            for (unsigned spin = 0; spin < SPIN_CAP; ++spin) {
                GLD_B64_SC1(fl, fpoll, flg + FL_Z, 0);
                WAIT_V(0); TIE(fl);
                if (__all(fl[0] == tagA && fl[1] == tagB)) break;
                __builtin_amdgcn_s_sleep(8);
            }
        }
        u32x4 zv[4];
        {
            const char* zsrc = ws + WS_ZT + (size_t)(xcd * 128 + wm * 32) * ZP + idx * 128;
#pragma unroll
            for (int it = 0; it < 4; ++it) if (!(ABL & 256)) GLD_B128_SC1(zv[it], (lane >> 3) * ZP + (lane & 7) * 16, zsrc + it * 8 * ZP, 0);
        }
        HB(); HB();
        WAIT_V(0);
        TS(9);
        {
            const unsigned zi = lb + L_ZI + wm * 5120;
#pragma unroll
            for (int it = 0; it < 4; ++it) { TIE(zv[it]); *LDSP(u32x4, zi + ((lane >> 3) + 8 * it) * 160 + (lane & 7) * 16) = zv[it]; }
            v4i16 z[8];
#pragma unroll
            for (int n = 0; n < 8; ++n) z[n] = __builtin_amdgcn_ds_read_tr16_b64_v4i16(LDSP(v4i16, zi + ((n & 1) * 16 + (lane >> 2)) * 160 + (n >> 1) * 32 + (lane & 3) * 8));
            HB();
#pragma unroll
            for (int n = 0; n < 8; ++n) *LDSP(v4i16, lb + L_Z2 + ((n >> 1) * 16 + r) * 264 + wm * 64 + (n & 1) * 32 + g4 * 8) = z[n];
            lb_post(lb + L_SY + 32 + wm * 4, 4);
        }
        HB();
        lb_wait(lb + L_SY + 32 + (lane & 3) * 4, 4);
        char* const oa = a.out + (size_t)(idx * 64 + wm * 16) * 8192 + xcd * 512;
        u32x2 zz[8];
#pragma unroll
        for (int n = 0; n < 8; ++n) zz[n] = *LDSP(u32x2, lb + L_Z2 + (wm * 16 + n * 2 + lh) * 264 + l31 * 8);
#pragma unroll
        for (int n = 0; n < 8; ++n) {
            TIE(qv[n]);
            const u32x2 z = zz[n];
            const f32x4 o = {rb1(bflo(qv[n][0]) * bflo(z[0])), rb1(bfhi(qv[n][0]) * bfhi(z[0])), rb1(bflo(qv[n][1]) * bflo(z[1])), rb1(bfhi(qv[n][1]) * bfhi(z[1]))};
            if (!(ABL & 512)) GST_B128_NT(lh * 8192 + l31 * 16, o, oa + n * 16384, 0); else { f32x4 o2 = o; TIE(o2); }
            if (n < 7 && (n + 1) % OS == 0) HB();
        }
#undef HB
        asm volatile(".Lfk_code_a:");
        TS(10);
        TS_FLUSH();
        return;
    }

    unsigned c_dt, c_re, c_im;
    u32x2 ep, fl;
    {
        GLD_B64_SC1(ep, 0u * lane, flg + FL_EPOCH, 0);
        const unsigned po = (tn * 64 + wn * 32 + l31) * 2;
        GLD_U16(c_dt, po, a.logdt); GLD_U16(c_re, po, a.lre); GLD_U16(c_im, po, a.lim);
    }
    {
        const int g4 = lane >> 4;
        const unsigned src = g4 * 2048 + (((lane & 15) ^ (2 * g4)) << 4);
        const char* ks = (wn ? a.v : a.k) + (size_t)(idx * 64 + wm * 16) * 2048 + xcd * 256;
#pragma unroll
        for (int i = 0; i < 4; ++i) { if (ABL & 1) break; dma_nt_(lb + L_KS + wid * 4096 + i * 1024, src ^ ((i >> 1) * 128), ks + i * 8192); }
    }
    unsigned dv[4];
#pragma unroll
    for (int i = 0; i < 4; ++i) {
        const int pair = i * 4 + (lane >> 4), v = (lane & 15) ^ pair;
        dv[i] = (pair * 2 + (v >> 3)) * 2048 + (v & 7) * 16;
    }
    const char* g1src = wid < 4 ? a.x + (size_t)(tm * 128 + wid * 32) * 2048
                                : ((wid & 1) ? a.bim : a.bre) + (size_t)(tn * 64 + ((wid >> 1) & 1) * 32) * 2048;
    const int rot = (b * 5) & 15;
    const unsigned g1dst = lb + wid * 4096;
#define G1_ISSUE(st, i) DMA(g1dst + ((st) & 3) * G1STAGE + (i) * 1024, dv[i], g1src + ((((st) + rot) & 15) << 7))
    G1_ISSUE(0, 0); G1_ISSUE(0, 1);
    TS(14);
    float Lr, Li, cr, ci, L8r, L8i, L16r, L16i, L128r, L128i, Qr, Qi, Wr, Wi;
    unsigned tagA, tagB;
    {
        WAIT_V((ABL & 1) ? 2 : 6);
        TIE(c_dt); TIE(c_re); TIE(c_im); TIE(ep);
        const float dt = rbf(expf(bf2f(c_dt)));
        const float lr = bf2f(c_re), li = bf2f(c_im);
        const float e = expf(dt * lr);
        float sn, cs;
        sincosf(dt * li, &sn, &cs);
        Lr = e * cs; Li = e * sn;
        const float nr = Lr - 1.f, ni = Li, den = lr * lr + li * li;
        cr = (nr * lr + ni * li) / den; ci = (ni * lr - nr * li) / den;
        float pr = Lr, pi = Li, tr, ti;
        Qr = cr; Qi = ci; Wr = 1.f; Wi = 0.f;
        const int run = 2 * wm + lh;
#pragma unroll
        for (int it = 1; it <= 10; ++it) {
            cmul(pr, pi, pr, pi, tr, ti); pr = tr; pi = ti;
            if (it == 3) { L8r = pr; L8i = pi; }
            if (it == 4) { L16r = pr; L16i = pi; }
            if (it == 7) { L128r = pr; L128i = pi; }
            if (it >= 4 && it <= 6) { cmul(Qr, Qi, pr, pi, tr, ti); if ((run >> (it - 4)) & 1) { Qr = tr; Qi = ti; } }
            if (it >= 8) { cmul(Wr, Wi, pr, pi, tr, ti); if (!((run >> (it - 8)) & 1)) { Wr = tr; Wi = ti; } }
        }
        TIE(Lr); TIE(Li); TIE(cr); TIE(ci); TIE(L8r); TIE(L8i); TIE(L16r); TIE(L16i); TIE(L128r); TIE(L128i); TIE(Qr); TIE(Qi); TIE(Wr); TIE(Wi);
        const unsigned e0 = __builtin_amdgcn_readfirstlane(ep[0]), e1 = __builtin_amdgcn_readfirstlane(ep[1]);
        tagA = e0 + 1; tagB = (e1 + (tagA == 0 ? 1u : 0u));
    }
    const unsigned tagG = tagA ^ tagB ^ 0x9E3779B9u;
    const u32x2 tag = {tagA, tagB};
    TS(1);
    WAIT_V(2);
    BAR();
    static_for<2, 16>([&](auto ic) { constexpr int n = decltype(ic)::value; G1_ISSUE(n >> 2, n & 3); });
    TS(15);

    f32x16 acc[2];
    {
#pragma unroll
        for (int j = 0; j < 2; ++j)
#pragma unroll
            for (int r = 0; r < 16; ++r) acc[j][r] = 0.f;
        const int prow = 16 * ((l31 >> 2) & 1) + 4 * (l31 >> 3) + (l31 & 3);
        const int aoff = img_off(wm * 32 + prow, lh), boff = img_off(128 + wn * 64 + l31, lh);
        unsigned aad[4], bad[4];
#pragma unroll
        for (int ks = 0; ks < 4; ++ks) { aad[ks] = lb + (aoff ^ (ks * 32)); bad[ks] = lb + (boff ^ (ks * 32)); }
        bf16x8 fa[PF + 1], fb[PF + 1][2];
        WAIT_V(12); GATE(1); BAR();
        TS(2);
#define G1_FRAGS(g) do { constexpr int bo_ = (((g) >> 2) & 3) * G1STAGE, hi_ = bo_ >= 65536 ? 65536 : 0; \
        DSR128(fa[(g) % (PF + 1)], aad[(g) & 3] + hi_, bo_ - hi_); \
        DSR128(fb[(g) % (PF + 1)][0], bad[(g) & 3] + hi_, bo_ - hi_); DSR128(fb[(g) % (PF + 1)][1], bad[(g) & 3] + hi_, bo_ - hi_ + 4096); } while (0)
        static_for<0, PF>([&](auto ic) { constexpr int g = decltype(ic)::value; G1_FRAGS(g); });
        static_for<0, 64>([&](auto ic) {
            constexpr int g = decltype(ic)::value, gn = g + PF;
            if constexpr ((gn & 3) == 0 && gn < 64) { static_assert(g1_cnt(gn >> 2) >= 0, "schedule"); WAIT_V(g1_cnt(gn >> 2)); WAIT_L(0); BAR(); if constexpr (((gn >> 2) & 3) == 0 || gn >= 52) TS(gn >= 52 ? 23 + (gn >> 2) - 13 : 16 + (gn >> 4)); }
            if constexpr (gn < 64) G1_FRAGS(gn);
            SB();
            WAIT_L((63 - g < PF ? 63 - g : PF) * 3);
            TIE(fa[g % (PF + 1)]); TIE(fb[g % (PF + 1)][0]); TIE(fb[g % (PF + 1)][1]);
            acc[0] = __builtin_amdgcn_mfma_f32_32x32x16_bf16(fa[g % (PF + 1)], fb[g % (PF + 1)][0], acc[0], 0, 0, 0);
            SB();
            constexpr int n0 = g1_upto(g - 1), n1 = g1_upto(g);
            if constexpr (n1 > n0) G1_ISSUE(n0 >> 2, n0 & 3);
            SB();
            acc[1] = __builtin_amdgcn_mfma_f32_32x32x16_bf16(fa[g % (PF + 1)], fb[g % (PF + 1)][1], acc[1], 0, 0, 0);
            SB();
            if constexpr (n1 > n0 + 1) { G1_ISSUE((n0 + 1) >> 2, (n0 + 1) & 3); SB(); }
        });
#undef G1_FRAGS
    }
    GATE(2);
    TS(3);
    __builtin_amdgcn_s_setprio(MPRIO);
    float ur[16], ui[16];
    float hr, hi;
    unsigned pl8 = lb + L_AGG + (wn * 32 + l31) * 8;
    const unsigned pmine = lb + L_SY + wid * 4, pall = lb + L_SY + (lane & 7) * 4;
    const int run = wm * 2 + lh;
    const float nLi = -Li;
    TIE(pl8);
    {
        asm volatile("s_nop 7\n\ts_nop 3" : "+v"(acc[0]), "+v"(acc[1]));
#pragma unroll
        for (int r = 0; r < 16; ++r) { ur[r] = acc[0][r]; ui[r] = acc[1][r]; }
        float gr = ur[0], gi = ui[0], fr = ur[8], fi = ui[8];
#pragma unroll
        for (int r = 1; r < 8; ++r) {
            float nr, ni; cmad(nr, ni, Lr, Li, nLi, gr, gi, ur[r], ui[r]); gr = nr; gi = ni;
            cmad(nr, ni, Lr, Li, nLi, fr, fi, ur[8 + r], ui[8 + r]); fr = nr; fi = ni;
        }
        cmac(fr, fi, L8r, L8i, -L8i, gr, gi);
        *LDSP(f32x2, pl8 + run * 512) = f32x2{fr, fi};
        WAIT_L(0); BAR();
        f32x2 e[8];
#pragma unroll
        for (int j = 0; j < 8; ++j) e[j] = *LDSP(f32x2, pl8 + j * 512);
        const float nL16i = -L16i;
#pragma unroll
        for (int j = 1; j < 8; ++j) { float er = e[j][0], ei = e[j][1]; cmac(er, ei, L16r, L16i, nL16i, e[j - 1][0], e[j - 1][1]); e[j] = f32x2{er, ei}; }
        if (wm == 0 && lh == 0) {
            const u32x4 gq = {__float_as_uint(e[7][0]), tagA, __float_as_uint(e[7][1]), tagG};
            GST_B128_SC1((tm * 1024 + tn * 64 + wn * 32 + l31) * 16, gq, ws + WS_E, 0);
        }
        float cinr = 0.f, cini = 0.f;
#pragma unroll
        for (int j = 0; j < 7; ++j) { if (j + 1 == run) { cinr = e[j][0]; cini = e[j][1]; } }
        TS(4);
        const float ncr = -cr, nci = -ci;
        mul4(hr, hi, cr, nci, ncr, nci, cinr, cini);
#pragma unroll
        for (int r = 0; r < 16; ++r) { float nr, ni; mul4(nr, ni, cr, nci, ncr, nci, ur[r], ui[r]); ur[r] = nr; ui[r] = ni; TIE(ur[r]); TIE(ui[r]); }
        TIE(hr); TIE(hi);
    }
    unsigned hw[2], ta[2];
    {
        const unsigned ht = lb + L_HT + wid * 2048;
#pragma unroll
        for (int j = 0; j < 2; ++j) {
            const int row = 8 * lh + 4 * j + ((lane & 15) >> 2), chunk = 2 * ((lane >> 4) & 1) + ((lane & 3) >> 1);
            ta[j] = ht + row * 64 + ((chunk ^ ((row >> 1) & 3)) << 4) + 8 * (lane & 1);
            hw[j] = ht + l31 * 64 + (((lh * 2 + j) ^ ((l31 >> 1) & 3)) << 4);
            TIE(ta[j]); TIE(hw[j]);
        }
    }
    {
        const int j0 = 2 * run - (16 - tm);
        float pr = 0.f, pi = 0.f;
        float nL128i = -L128i, nWi = -Wi;
        TIE(nL128i); TIE(nWi);
        if (4 * wm + 3 >= 16 - tm) {
            u32x4 eg[2];
            const unsigned eo0 = (j0 < 0 ? 0 : j0) * 16384 + (tn * 64 + wn * 32 + l31) * 16, eo1 = (j0 + 1 < 0 ? 0 : j0 + 1) * 16384 + (tn * 64 + wn * 32 + l31) * 16;
#pragma unroll 1
            for (unsigned spin = 0; spin < SPIN_CAP; ++spin) {
                GLD_B128_SC1(eg[0], eo0, ws + WS_E, 0); GLD_B128_SC1(eg[1], eo1, ws + WS_E, 0);
                WAIT_V(0); TIE(eg[0]); TIE(eg[1]);
                if (__all(eg[0][1] == tagA && eg[0][3] == tagG && eg[1][1] == tagA && eg[1][3] == tagG)) break;
                __builtin_amdgcn_s_sleep(2);
            }
            float e1r = j0 + 1 >= 0 ? __uint_as_float(eg[1][0]) : 0.f, e1i = j0 + 1 >= 0 ? __uint_as_float(eg[1][2]) : 0.f;
            const float e0r = j0 >= 0 ? __uint_as_float(eg[0][0]) : 0.f, e0i = j0 >= 0 ? __uint_as_float(eg[0][2]) : 0.f;
            cmac(e1r, e1i, L128r, L128i, nL128i, e0r, e0i);
            cmul4(pr, pi, Wr, Wi, nWi, e1r, e1i);
        }
        TS(5);
        *LDSP(f32x2, pl8 + (L_ES - L_AGG) + run * 512) = f32x2{pr, pi};
        lb_post(pmine, 1); lb_wait(pall, 1);
    }
    TS(6);
    {
        f32x2 e[8];
#pragma unroll
        for (int j = 0; j < 8; ++j) e[j] = *LDSP(f32x2, pl8 + (L_ES - L_AGG) + j * 512);
        const f32x2 G = ((e[0] + e[1]) + (e[2] + e[3])) + ((e[4] + e[5]) + (e[6] + e[7]));
        mac4(hr, hi, Qr, -Qi, -Qr, -Qi, G[0], G[1]);
#pragma unroll
        for (int r = 0; r < 16; ++r) { cmac(ur[r], ui[r], Lr, nLi, Li, hr, hi); hr = ur[r]; hi = ui[r]; }
        const char* hd = ws + WS_H + (size_t)((tm * 4 + wm) * 2) * HP + (tn * 4 + wn * 2) * 1024;
        v4i16 t[2][2][2];
#pragma unroll
        for (int c = 0; c < 2; ++c) {
#pragma unroll
            for (int e2 = 0; e2 < 2; ++e2) {
                const float* o = (c ? ui : ur) + 8 * e2;
                *LDSP(u32x4, hw[e2]) = u32x4{pack2(o[0], o[1]), pack2(o[2], o[3]), pack2(o[4], o[5]), pack2(o[6], o[7])};
            }
#pragma unroll
            for (int hf = 0; hf < 2; ++hf)
#pragma unroll
                for (int j = 0; j < 2; ++j) t[c][hf][j] = __builtin_amdgcn_ds_read_tr16_b64_v4i16(LDSP(v4i16, ta[j] + hf * 1024));
        }
        static_for<0, 4>([&](auto ic) {
            constexpr int n = decltype(ic)::value;
            const bf16x8 f = __builtin_shufflevector(t[n >> 1][n & 1][0], t[n >> 1][n & 1][1], 0, 1, 2, 3, 4, 5, 6, 7);
            GST_B128_SC1(l16, f, hd + (n >> 1) * HP, (n & 1) * 1024);
        });
    }
    TS(7);
    bf16x8 fa2[AW];
    u32x2 xv[4], dsv;
    const char* Aw = ws + WS_H + (size_t)((tm * 4 + wm) * 2 + wn) * HP;
    const char* g2src = (wn ? a.cim : a.cre) + (size_t)(tn * 64 + wm * 16) * 2048;
    const unsigned g2dst = lb + L_C + wid * 2048;
    unsigned g2v[2];
#pragma unroll
    for (int i = 0; i < 2; ++i) {
        const int pair = i * 4 + (lane >> 4), v = (lane & 15) ^ pair ^ ((wm & 1) * 8);
        g2v[i] = (pair * 2 + (v >> 3)) * 2048 + (v & 7) * 16;
    }
#define G2_ISSUE(st, i) DMA(g2dst + (((st) + 2) & 3) * G2STAGE + (i) * 1024, g2v[i], g2src + ((((st) + rot2) & 15) << 7))
#define G2_A(g) GLD_B128_SC1(fa2[(g) % AW], l16, Aw + (((((g) >> 2) + rot2) & 15) << 12), ((g) & 3) * 1024)
    {
        const char* xs = a.x + (size_t)(tm * 128 + wm * 32) * 2048 + tn * 128 + wn * 64;
#pragma unroll
        for (int it = 0; it < 4; ++it) GLD_B64(xv[it], (lane >> 3) * 2048 + (lane & 7) * 8, xs + it * 16384, 0);
        GLD_B64(dsv, (lane & 7) * 8, a.dskip + tn * 128 + wn * 64, 0);
        WAIT_V(5);
        lb_post(pmine, 2); lb_wait(pall, 2);
        if (wid == 0 && lane < 16) GST_B64_SC1((tm * 16 + lane) * 128 + tn * 8, tag, flg + FL_H);
        TS(8);
        static_for<4, 8>([&](auto ic) { constexpr int n = decltype(ic)::value; G2_ISSUE(n >> 1, n & 1); });
        if (wid == 0) {
#pragma unroll 1
            for (unsigned spin = 0; spin < SPIN_CAP; ++spin) {
                GLD_B64_SC1(fl, (tm * 16 + tn) * 128 + (lane & 15) * 8, flg + FL_H, 0);
                WAIT_V(0); TIE(fl);
                if (__all(fl[0] == tagA && fl[1] == tagB)) break;
            }
            lb_post(pmine, 3);
            if (b == 255 && lane == 0) GST_B64_SC1(0u * lane, tag, flg + FL_EPOCH);
        } else lb_wait(lb + L_SY, 3);
        static_for<0, AW>([&](auto ic) { constexpr int g = decltype(ic)::value; G2_A(g); });
        lb_wait(lb + L_SY + 32 + (lane & 3) * 4, 2);
    }
    GATE(3);
    TS(9);
    __builtin_amdgcn_s_setprio(0);
    {
#pragma unroll
        for (int j = 0; j < 2; ++j)
#pragma unroll
            for (int r = 0; r < 16; ++r) acc[j][r] = 0.f;
        const int boff = img_off(wn * 64 + l31, lh);
        unsigned bad[4];
#pragma unroll
        for (int ks = 0; ks < 4; ++ks) bad[ks] = lb + L_C + (boff ^ (ks * 32));
        bf16x8 fb[PF + 1][2];
#define G2_FRAGS(g) do { constexpr int bo_ = ((((g) >> 2) + 2) & 3) * G2STAGE; \
        DSR128(fb[(g) % (PF + 1)][0], bad[(g) & 3], bo_); DSR128(fb[(g) % (PF + 1)][1], bad[(g) & 3], bo_ + 4096); } while (0)
        static_for<0, PF>([&](auto ic) { constexpr int g = decltype(ic)::value; G2_FRAGS(g); });
        static_for<0, 64>([&](auto ic) {
            constexpr int g = decltype(ic)::value, gn = g + PF;
            if constexpr ((gn & 3) == 0 && gn < 64) {
                if constexpr ((gn >> 2) >= 4) WAIT_V(g2_cnt_stage(gn >> 2));
                WAIT_L(0); BAR();
                if constexpr (g == 2) TS(10);
                if constexpr (g == 18 || g == 34 || g == 50) TS(26 + (g >> 4));
            }
            if constexpr (gn < 64) G2_FRAGS(gn);
            SB();
            asm volatile("s_waitcnt vmcnt(%0) lgkmcnt(%1)" ::"n"(g2_cnt_a(g)), "n"((63 - g < PF ? 63 - g : PF) * 2) : "memory");
            TIE(fa2[g % AW]); TIE(fb[g % (PF + 1)][0]); TIE(fb[g % (PF + 1)][1]);
            acc[0] = __builtin_amdgcn_mfma_f32_32x32x16_bf16(fb[g % (PF + 1)][0], fa2[g % AW], acc[0], 0, 0, 0);
            SB();
            if constexpr (g2_dma(g)) G2_ISSUE(gn / 4 + 3, gn & 3);
            SB();
            acc[1] = __builtin_amdgcn_mfma_f32_32x32x16_bf16(fb[g % (PF + 1)][1], fa2[g % AW], acc[1], 0, 0, 0);
            SB();
            if constexpr (g2_a(g)) G2_A(g + AW);
            if constexpr (g == 58) lb_wait(lb + L_SY + 32 + (lane & 3) * 4, 3);
            SB();
        });
#undef G2_FRAGS
    }
    GATE(4);
    TS(11);
    {
        const unsigned wo = l31 * 128, sx = (l31 & 7) << 4;
        const f32x16 snd = wn ? acc[0] : acc[1], own = wn ? acc[1] : acc[0];
#pragma unroll
        for (int rq = 0; rq < 4; ++rq) *LDSP(f32x4, lb + L_Y + wid * 4096 + wo + ((rq * 32 + lh * 16) ^ sx)) = f32x4{snd[4 * rq], snd[4 * rq + 1], snd[4 * rq + 2], snd[4 * rq + 3]};
        lb_post(pmine, 4);
#pragma unroll
        for (int rq = 0; rq < 4; ++rq) *LDSP(f32x4, lb + L_Y2 + wid * 4096 + wo + ((rq * 32 + lh * 16) ^ sx)) = f32x4{own[4 * rq], own[4 * rq + 1], own[4 * rq + 2], own[4 * rq + 3]};
        TIE(dsv);
#pragma unroll
        for (int it = 0; it < 4; ++it) TIE(xv[it]);
        const float ds0 = bflo(dsv[0]), ds1 = bfhi(dsv[0]), ds2 = bflo(dsv[1]), ds3 = bfhi(dsv[1]);
        const unsigned ro = (lane >> 3) * 128 + (((lane & 7) ^ (lane >> 3)) << 4);
        f32x4 y[4];
#pragma unroll
        for (int it = 0; it < 4; ++it) {
            const f32x4 w = *LDSP(f32x4, lb + L_Y2 + wid * 4096 + it * 1024 + ro);
            y[it] = f32x4{2.f * w[0] + rb1(bflo(xv[it][0]) * ds0), 2.f * w[1] + rb1(bfhi(xv[it][0]) * ds1), 2.f * w[2] + rb1(bflo(xv[it][1]) * ds2), 2.f * w[3] + rb1(bfhi(xv[it][1]) * ds3)};
            TIE(y[it]);
        }
        lb_wait(lb + L_SY + (wid ^ 4) * 4, 4);
        char* const ob = a.out + (size_t)(tm * 128 + wm * 32) * 8192 + 4096 + tn * 256 + wn * 128;
        f32x4 w[4];
#pragma unroll
        for (int it = 0; it < 4; ++it) w[it] = *LDSP(f32x4, lb + L_Y + (wid ^ 4) * 4096 + it * 1024 + ro);
#pragma unroll
        for (int it = 0; it < 4; ++it) {
            const f32x4 o = {2.f * w[it][0] + y[it][0], 2.f * w[it][1] + y[it][1], 2.f * w[it][2] + y[it][2], 2.f * w[it][3] + y[it][3]};
            GST_B128_NT((lane >> 3) * 8192 + (lane & 7) * 16, o, ob + it * 65536, 0);
        }
    }
    GATE(5);
    asm volatile(".Lfk_code_b:");
    TS(12);
    TS_FLUSH();
}

}

extern "C" void kernel_launch(void* const* d_in, const int* in_sizes, int n_in, void* d_out, int out_size, void* d_ws, size_t ws_size,
                              hipStream_t stream) {
    (void)in_sizes; (void)n_in; (void)out_size; (void)ws_size;
    fk::Args a;
    a.q = (const char*)d_in[0]; a.k = (const char*)d_in[1]; a.v = (const char*)d_in[2]; a.sk = (const char*)d_in[3];
    a.ssmk = (const char*)d_in[4]; a.ssmD = (const char*)d_in[5]; a.x = (const char*)d_in[6]; a.lre = (const char*)d_in[7];
    a.lim = (const char*)d_in[8]; a.bre = (const char*)d_in[9]; a.bim = (const char*)d_in[10]; a.cre = (const char*)d_in[11];
    a.cim = (const char*)d_in[12]; a.logdt = (const char*)d_in[13]; a.dskip = (const char*)d_in[14];
    a.out = (char*)d_out; a.ws = (char*)d_ws;
    hipLaunchKernelGGL(fk::fused, dim3(256), dim3(768), 0, stream, a);
}
```

```cpp
#define FV 10
#include <hip/hip_runtime.h>
#include <stdint.h>
#include <type_traits>

namespace fk {

constexpr int S = 2048, D = 1024, P = 1024;

typedef __attribute__((ext_vector_type(8))) short bf16x8;
typedef __attribute__((ext_vector_type(4))) short s16x4;
typedef __attribute__((ext_vector_type(16))) float f32x16;
typedef __attribute__((ext_vector_type(4))) float f32x4;
typedef __attribute__((ext_vector_type(2))) float f32x2;
typedef __attribute__((ext_vector_type(4))) unsigned u32x4;
typedef __attribute__((ext_vector_type(2))) unsigned u32x2;
#define LDSP(T, a) ((__attribute__((address_space(3))) T*)(uintptr_t)(unsigned)(a))

__device__ __forceinline__ float bf2f(unsigned v) { return __uint_as_float(v << 16); }
__device__ __forceinline__ float bflo(unsigned w) { return __uint_as_float(w << 16); }
__device__ __forceinline__ float bfhi(unsigned w) { return __uint_as_float(w & 0xffff0000u); }
__device__ __forceinline__ uint16_t f2bf(float f) { __bf16 b = (__bf16)f; return __builtin_bit_cast(uint16_t, b); }
__device__ __forceinline__ float rbf(float f) { return bf2f(f2bf(f)); }
typedef __attribute__((ext_vector_type(2))) __bf16 bf16x2;
__device__ __forceinline__ unsigned pack2(float lo, float hi) { return __builtin_bit_cast(unsigned, __builtin_convertvector(f32x2{lo, hi}, bf16x2)); }
__device__ __forceinline__ float rb1(float x) { return __uint_as_float(pack2(0.f, x)); }
__device__ __forceinline__ void cmac(float& xr, float& xi, float ar, float ai, float nai, float hr, float hi) {
    asm("v_fmac_f32 %0, %2, %5\n\tv_fmac_f32 %1, %2, %6\n\tv_fmac_f32 %0, %4, %6\n\tv_fmac_f32 %1, %3, %5" : "+v"(xr), "+v"(xi) : "v"(ar), "v"(ai), "v"(nai), "v"(hr), "v"(hi));
}
__device__ __forceinline__ void cmad(float& o_r, float& o_i, float ar, float ai, float nai, float hr, float hi, float er, float ei) {
    asm("v_fma_f32 %0, %2, %5, %7\n\tv_fma_f32 %1, %2, %6, %8\n\tv_fmac_f32 %0, %4, %6\n\tv_fmac_f32 %1, %3, %5" : "=&v"(o_r), "=&v"(o_i) : "v"(ar), "v"(ai), "v"(nai), "v"(hr), "v"(hi), "v"(er), "v"(ei));
}
__device__ __forceinline__ void mac4(float& x, float& y, float a0, float a1, float b0, float b1, float h0, float h1) {
    asm("v_fmac_f32 %0, %2, %6\n\tv_fmac_f32 %1, %4, %7\n\tv_fmac_f32 %0, %3, %7\n\tv_fmac_f32 %1, %5, %6" : "+v"(x), "+v"(y) : "v"(a0), "v"(a1), "v"(b0), "v"(b1), "v"(h0), "v"(h1));
}
__device__ __forceinline__ void mul4(float& x, float& y, float a0, float a1, float b0, float b1, float h0, float h1) {
    asm("v_mul_f32 %0, %2, %6\n\tv_mul_f32 %1, %4, %7\n\tv_fmac_f32 %0, %3, %7\n\tv_fmac_f32 %1, %5, %6" : "=&v"(x), "=&v"(y) : "v"(a0), "v"(a1), "v"(b0), "v"(b1), "v"(h0), "v"(h1));
}
__device__ __forceinline__ void cmul4(float& o_r, float& o_i, float ar, float ai, float nai, float hr, float hi) {
    asm("v_mul_f32 %0, %2, %5\n\tv_mul_f32 %1, %2, %6\n\tv_fmac_f32 %0, %4, %6\n\tv_fmac_f32 %1, %3, %5" : "=&v"(o_r), "=&v"(o_i) : "v"(ar), "v"(ai), "v"(nai), "v"(hr), "v"(hi));
}
__device__ __forceinline__ void cmul(float ar, float ai, float br, float bi, float& cr, float& ci) { cr = ar * br - ai * bi; ci = ar * bi + ai * br; }

template <int I, int N, class F>
__device__ __forceinline__ void static_for(F&& f) { if constexpr (I < N) { f(std::integral_constant<int, I>{}); static_for<I + 1, N>(f); } }

#define TS_DECL
#define TS(i)
#define TS_FLUSH()

#define WAIT_V(n) asm volatile("s_waitcnt vmcnt(%0)" ::"n"((n) > 63 ? 63 : (n)) : "memory")
#define WAIT_L(n) asm volatile("s_waitcnt lgkmcnt(%0)" ::"n"((n) > 15 ? 15 : (n)) : "memory")
#define BAR() asm volatile("s_barrier" ::: "memory")
#define SB() __builtin_amdgcn_sched_barrier(0)
template <class T> __device__ __forceinline__ void tie_(T& x) { asm volatile("" : "+v"(x)); }
#define TIE(x) tie_(x)
#define DEF_LD(name, ins) template <int OFF, class T> __device__ __forceinline__ void name(T& dst, unsigned voff, const char* sb) { \
    asm volatile(ins : "=v"(dst) : "v"(voff), "s"(sb), "n"(OFF) : "memory"); }
DEF_LD(gld_u16_, "global_load_ushort %0, %1, %2 offset:%3")
DEF_LD(gld_b32_, "global_load_dword %0, %1, %2 offset:%3")
DEF_LD(gld_b64_, "global_load_dwordx2 %0, %1, %2 offset:%3")
DEF_LD(gld_b128_, "global_load_dwordx4 %0, %1, %2 offset:%3")
DEF_LD(gld_b64_nt_, "global_load_dwordx2 %0, %1, %2 offset:%3 nt")
DEF_LD(gld_b128_nt_, "global_load_dwordx4 %0, %1, %2 offset:%3 nt")
DEF_LD(gld_b64_sc1_, "global_load_dwordx2 %0, %1, %2 offset:%3 sc1")
DEF_LD(gld_b128_sc1_, "global_load_dwordx4 %0, %1, %2 offset:%3 sc1")
#define DEF_ST(name, ins) template <int OFF, class T> __device__ __forceinline__ void name(unsigned voff, const T& v, const char* sb) { \
    asm volatile(ins :: "v"(voff), "v"(v), "s"(sb), "n"(OFF) : "memory"); }
DEF_ST(gst_b128_, "global_store_dwordx4 %0, %1, %2 offset:%3\n\ts_nop 1")
DEF_ST(gst_b128_nt_, "global_store_dwordx4 %0, %1, %2 offset:%3 nt\n\ts_nop 1")
DEF_ST(gst_b128_sc1_, "global_store_dwordx4 %0, %1, %2 offset:%3 sc1\n\ts_nop 1")
DEF_ST(gst_b64_sc1_, "global_store_dwordx2 %0, %1, %2 offset:%3 sc1")
#define GLD_U16(dst, voff, sb) gld_u16_<0>(dst, voff, sb)
#define GLD_B32(dst, voff, sb, off) gld_b32_<(off)>(dst, voff, sb)
#define GLD_B64(dst, voff, sb, off) gld_b64_<(off)>(dst, voff, sb)
#define GLD_B128(dst, voff, sb, off) gld_b128_<(off)>(dst, voff, sb)
#define GLD_B64_NT(dst, voff, sb, off) gld_b64_nt_<(off)>(dst, voff, sb)
#define GLD_B128_NT(dst, voff, sb, off) gld_b128_nt_<(off)>(dst, voff, sb)
#define GLD_B64_SC1(dst, voff, sb, off) gld_b64_sc1_<(off)>(dst, voff, sb)
#define GLD_B128_SC1(dst, voff, sb, off) gld_b128_sc1_<(off)>(dst, voff, sb)
#define GST_B128(voff, v, sb, off) gst_b128_<(off)>(voff, v, sb)
#define GST_B128_NT(voff, v, sb, off) gst_b128_nt_<(off)>(voff, v, sb)
#define GST_B128_SC1(voff, v, sb, off) gst_b128_sc1_<(off)>(voff, v, sb)
#define GST_B64_SC1(voff, v, sb) gst_b64_sc1_<0>(voff, v, sb)
__device__ __forceinline__ void dma_(unsigned ldsaddr, unsigned voff, const char* sb) {
    asm volatile("s_mov_b32 m0, %0\n\ts_nop 0\n\tglobal_load_lds_dwordx4 %1, %2" :: "s"(ldsaddr), "v"(voff), "s"(sb) : "memory"); }
__device__ __forceinline__ void dma_nt_(unsigned ldsaddr, unsigned voff, const char* sb) {
    asm volatile("s_mov_b32 m0, %0\n\ts_nop 0\n\tglobal_load_lds_dwordx4 %1, %2 nt" :: "s"(ldsaddr), "v"(voff), "s"(sb) : "memory"); }
#define DMA(ldsaddr, voff, sb) dma_(ldsaddr, voff, sb)
template <int OFF, class T> __device__ __forceinline__ void dsr128_(T& dst, unsigned addr) { asm volatile("ds_read_b128 %0, %1 offset:%2" : "=v"(dst) : "v"(addr), "n"(OFF)); }
template <int OFF, class T> __device__ __forceinline__ void dsr64_(T& dst, unsigned addr) { asm volatile("ds_read_b64 %0, %1 offset:%2" : "=v"(dst) : "v"(addr), "n"(OFF)); }
#define DSR128(dst, addr, off) dsr128_<(off)>(dst, addr)
#define DSR64(dst, addr, off) dsr64_<(off)>(dst, addr)

__device__ __forceinline__ void kv4(float* t, const float* e, float w0, float w1, float w2, float w3, const float* v) {
    float p0, p1, p2, p3;
    asm(
        "v_mul_f32 %0, %8, %18\n\t"
        "v_mul_f32 %1, %9, %18\n\t"
        "v_mul_f32 %2, %10, %18\n\t"
        "v_mul_f32 %3, %11, %18\n\t"
        "v_cvt_pk_bf16_f32 %0, 0, %0\n\t"
        "v_cvt_pk_bf16_f32 %1, 0, %1\n\t"
        "v_cvt_pk_bf16_f32 %2, 0, %2\n\t"
        "v_cvt_pk_bf16_f32 %3, 0, %3\n\t"
        "v_mul_f32 %4, %9, %17\n\t"
        "v_mul_f32 %5, %10, %17\n\t"
        "v_mul_f32 %6, %11, %17\n\t"
        "v_mul_f32 %7, %12, %17\n\t"
        "v_cvt_pk_bf16_f32 %4, 0, %4\n\t"
        "v_cvt_pk_bf16_f32 %5, 0, %5\n\t"
        "v_cvt_pk_bf16_f32 %6, 0, %6\n\t"
        "v_cvt_pk_bf16_f32 %7, 0, %7\n\t"
        "v_add_f32 %0, %0, %4\n\t"
        "v_add_f32 %1, %1, %5\n\t"
        "v_add_f32 %2, %2, %6\n\t"
        "v_add_f32 %3, %3, %7\n\t"
        "v_cvt_pk_bf16_f32 %0, 0, %0\n\t"
        "v_cvt_pk_bf16_f32 %1, 0, %1\n\t"
        "v_cvt_pk_bf16_f32 %2, 0, %2\n\t"
        "v_cvt_pk_bf16_f32 %3, 0, %3\n\t"
        "v_mul_f32 %4, %10, %16\n\t"
        "v_mul_f32 %5, %11, %16\n\t"
        "v_mul_f32 %6, %12, %16\n\t"
        "v_mul_f32 %7, %13, %16\n\t"
        "v_cvt_pk_bf16_f32 %4, 0, %4\n\t"
        "v_cvt_pk_bf16_f32 %5, 0, %5\n\t"
        "v_cvt_pk_bf16_f32 %6, 0, %6\n\t"
        "v_cvt_pk_bf16_f32 %7, 0, %7\n\t"
        "v_add_f32 %0, %0, %4\n\t"
        "v_add_f32 %1, %1, %5\n\t"
        "v_add_f32 %2, %2, %6\n\t"
        "v_add_f32 %3, %3, %7\n\t"
        "v_cvt_pk_bf16_f32 %0, 0, %0\n\t"
        "v_cvt_pk_bf16_f32 %1, 0, %1\n\t"
        "v_cvt_pk_bf16_f32 %2, 0, %2\n\t"
        "v_cvt_pk_bf16_f32 %3, 0, %3\n\t"
        "v_mul_f32 %4, %11, %15\n\t"
        "v_mul_f32 %5, %12, %15\n\t"
        "v_mul_f32 %6, %13, %15\n\t"
        "v_mul_f32 %7, %14, %15\n\t"
        "v_cvt_pk_bf16_f32 %4, 0, %4\n\t"
        "v_cvt_pk_bf16_f32 %5, 0, %5\n\t"
        "v_cvt_pk_bf16_f32 %6, 0, %6\n\t"
        "v_cvt_pk_bf16_f32 %7, 0, %7\n\t"
        "v_add_f32 %0, %0, %4\n\t"
        "v_add_f32 %1, %1, %5\n\t"
        "v_add_f32 %2, %2, %6\n\t"
        "v_add_f32 %3, %3, %7\n\t"
        "v_cvt_pk_bf16_f32 %0, 0, %0\n\t"
        "v_cvt_pk_bf16_f32 %1, 0, %1\n\t"
        "v_cvt_pk_bf16_f32 %2, 0, %2\n\t"
        "v_cvt_pk_bf16_f32 %3, 0, %3\n\t"
        "v_mul_f32 %0, %0, %19\n\t"
        "v_mul_f32 %1, %1, %20\n\t"
        "v_mul_f32 %2, %2, %21\n\t"
        "v_mul_f32 %3, %3, %22\n\t"
        : "=&v"(t[0]), "=&v"(t[1]), "=&v"(t[2]), "=&v"(t[3]), "=&v"(p0), "=&v"(p1), "=&v"(p2), "=&v"(p3)
        : "v"(e[0]), "v"(e[1]), "v"(e[2]), "v"(e[3]), "v"(e[4]), "v"(e[5]), "v"(e[6]), "v"(w0), "v"(w1), "v"(w2), "v"(w3), "v"(v[0]), "v"(v[1]), "v"(v[2]), "v"(v[3]));
}
__device__ __forceinline__ void lb_post(unsigned myaddr, unsigned seq) { asm volatile("ds_write_b32 %0, %1" :: "v"(myaddr), "v"(seq) : "memory"); }
__device__ __forceinline__ void lb_wait(unsigned addr, unsigned seq) {
    unsigned v;
    asm volatile("s_branch 2f\n1:\n\ts_sleep 1\n2:\n\tds_read_b32 %0, %1\n\ts_waitcnt lgkmcnt(0)\n\tv_cmp_gt_u32 vcc, %2, %0\n\ts_cbranch_vccnz 1b" : "=&v"(v) : "v"(addr), "v"(seq) : "vcc", "memory");
}

constexpr size_t WS_H = 0, WS_KVT = (size_t)10 << 20, WS_ZT = (size_t)15 << 20, WS_E = (size_t)20 << 20, WS_FLAG = WS_E + (256 << 10);
#define HP 65536
#define KP 8192
#define ZP 4096
constexpr int FL_KV = 0, FL_Z = 65536, FL_H = 131072, FL_EPOCH = 196608;
constexpr unsigned SPIN_CAP = 1u << 17;
#define ABL 0
#define CSPLIT 1
#define MERGE 3
#define CB 13
#define PAUSE 0
#define QLATE 0
#define CVPRIO 0
#define STG_SEL (idx & 3)
#define SP0 3
#define SDP 1
#define LP0 3
#define LDP 2
#define KVS 5
#define KVU 2
#define QIT 2
#define ZB 3
#define OS 2
#define MPRIO 2

constexpr int G1STAGE = 32768;
constexpr int L_KS = 131072, L_VS = 147456;
constexpr int CST = 4288;
constexpr int UOFF = 4 * CST, UPL = 768;
constexpr int upl(int c) { return c * UPL + (c >> 1) * 32; }
constexpr int CHSZ = UOFF + 8 * UPL + 192;
constexpr int L_AGG = 4 * CHSZ, L_SY = L_AGG + 4096;
constexpr int L_HT = 98304, L_ES = 114688;
constexpr int L_C = 98304, G2STAGE = 16384;
constexpr int L_ZI = 0;
constexpr int L_Z2 = 20480;
constexpr int L_Y = 40960, L_Y2 = 131072;
static_assert(L_SY + 64 <= L_HT, "lds");
static_assert(L_Y + 32768 <= L_AGG && L_Z2 + 64 * 264 <= L_Y, "lds");

__device__ __forceinline__ int img_off(int r, int c) { return (r >> 1) * 256 + ((((r & 1) * 8 + c) ^ ((r >> 1) & 15)) << 4); }

constexpr int PF = 2;
constexpr int g1_upto(int g) {
    int n = 16;
    for (int t = 0; t <= g; ++t) {
        int sb = (t + PF) / 4, sub = (t + PF) % 4, allowed = sb >= 1 ? 4 * (sb + 3) + sub + 1 : 16;
        if (allowed > 64) allowed = 64;
        const bool pause = t >= 4 * KVS - PF && t < 4 * KVS - PF + PAUSE;
        if (!pause) n = n + 2 < allowed ? n + 2 : allowed;
    }
    return n;
}
constexpr int g1_cnt(int s) { return g1_upto(4 * s - PF - 1) - 4 * (s + 1); }
static_assert(g1_upto(49) == 64, "schedule");
constexpr int AW = 8;
constexpr int g2_dma(int g) { return ((g + PF) / 4 >= 1 && (g + PF) / 4 + 3 < 16 && (g + PF) % 4 < 2) ? 1 : 0; }
constexpr int g2_a(int g) { return g + AW < 64 ? 1 : 0; }
constexpr int g2_ops(int g) { return g2_dma(g) + g2_a(g); }
constexpr int g2_cnt_stage(int s) {
    int g = 4 * s - PF, t1 = 4 * (s - 3) - PF + 1, c = g2_a(t1);
    for (int t = t1 + 1; t < g; ++t) c += g2_ops(t);
    return c;
}
constexpr int g2_cnt_a(int g) {
    int c = 0;
    if (g < AW) { c = AW - 1 - g; for (int t = 0; t < g; ++t) c += g2_ops(t); }
    else { for (int t = g - AW + 1; t < g; ++t) c += g2_ops(t); }
    return c;
}

struct Args {
    const char *q, *k, *v, *sk, *ssmk, *ssmD, *x, *lre, *lim, *bre, *bim, *cre, *cim, *logdt, *dskip;
    char *out, *ws;
};

__global__ __launch_bounds__(768, 3) void fused(Args a_) {
    __shared__ __attribute__((aligned(1024))) char shm[163840];
    unsigned long long f_ta, f_tb;
    const __attribute__((address_space(4))) Args* ap = (const __attribute__((address_space(4))) Args*)__builtin_amdgcn_kernarg_segment_ptr();
    asm volatile("s_memrealtime %0\n\ts_waitcnt lgkmcnt(0)" : "=s"(f_ta), "+s"(ap));
    Args a;
    a.q = ap->q; a.k = ap->k; a.v = ap->v; a.sk = ap->sk; a.ssmk = ap->ssmk; a.ssmD = ap->ssmD; a.x = ap->x; a.lre = ap->lre; a.lim = ap->lim;
    a.bre = ap->bre; a.bim = ap->bim; a.cre = ap->cre; a.cim = ap->cim; a.logdt = ap->logdt; a.dskip = ap->dskip; a.out = ap->out; a.ws = ap->ws;
    asm volatile("s_memrealtime %0\n\ts_waitcnt lgkmcnt(0)" : "=s"(f_tb), "+s"(a.q), "+s"(a.lim), "+s"(a.ws));
    const unsigned f_t0 = (FV == 6 || FV == 7) ? (unsigned)f_tb : (unsigned)f_ta;
    const char* pc0;
    int code_a, code_b;
    asm volatile("s_getpc_b64 %0\n.Lfk_code_0:\n\ts_mov_b32 %1, .Lfk_code_a-.Lfk_code_0\n\ts_mov_b32 %2, .Lfk_code_b-.Lfk_code_0" : "=s"(pc0), "=s"(code_a), "=s"(code_b));
    char* const ws = a.ws;
    TS_DECL TS(0);
#define F_END() do { const unsigned f_t1 = (unsigned)__builtin_amdgcn_s_memrealtime(); __builtin_amdgcn_s_waitcnt(0xC07F); \
    if ((threadIdx.x & 63) == 0) *(u32x2*)(ws + ((size_t)64 << 20) + (blockIdx.x * 12 + (threadIdx.x >> 6)) * 8) = u32x2{f_t0, f_t1}; } while (0)
    const int tid = threadIdx.x, lane = tid & 63, wid = __builtin_amdgcn_readfirstlane(tid >> 6);
    const int b = blockIdx.x, xcd = b & 7, idx = b >> 3;
    const int tm = (xcd >> 1) * 4 + (idx >> 3), tn = (xcd & 1) * 8 + (idx & 7);
    const int wm = wid & 3, wn = (wid >> 2) & 1, l31 = lane & 31, lh = lane >> 5;
    const unsigned lb = (unsigned)(uintptr_t)shm;
    const char* const flg = ws + WS_FLAG;
    const unsigned l16 = lane * 16;
    const int rot2 = (2 * tm) & 15;
    typedef __attribute__((ext_vector_type(4))) short v4i16;

    if (wid >= 8) {
        const int chn = xcd * 128 + idx * 4 + wm;
        const unsigned cb = lb + wm * CHSZ;
        const int r = lane & 15, g4 = lane >> 4;
        const unsigned fpub = xcd * 256 + idx * 8, fpoll = xcd * 256 + l31 * 8;
        __builtin_amdgcn_s_setprio(3);
        u32x2 ep, fl, kw[2]; unsigned hal[2], dskl; u32x4 sw[4], uu[4];
        unsigned cpf = 0;
        if (wm == 3) {
            if (lane < 16 && (idx * 16 + lane) * 128 < (code_a > code_b ? code_a : code_b)) GLD_B32(cpf, (idx * 16 + lane) * 128, pc0, 0);
        }
        {
            const unsigned ho = (idx ? idx * 64 - 4 + g4 : 0) * 2048 + xcd * 256 + wm * 64 + r * 2;
            gld_u16_<0>(hal[0], ho, a.k); gld_u16_<32>(hal[1], ho, a.k);
            GLD_B64_SC1(ep, 0u * lane, flg + FL_EPOCH, 0);
            const unsigned c0 = xcd * 128 + wm * 32 + r;
            GLD_B64(kw[0], c0 * 8, a.sk, 0); GLD_B64(kw[1], c0 * 8, a.sk, 128);
            GLD_U16(dskl, 0u * lane, a.ssmD + chn * 2);
        }
        TS(1);
        WAIT_V(0);
        BAR();
        TS(2);
        TIE(ep); TIE(kw[0]); TIE(kw[1]); TIE(hal[0]); TIE(hal[1]); TIE(dskl); TIE(cpf);
        const unsigned e0 = __builtin_amdgcn_readfirstlane(ep[0]), e1 = __builtin_amdgcn_readfirstlane(ep[1]);
        const unsigned tagA = e0 + 1, tagB = e1 + (tagA == 0 ? 1u : 0u);
        const u32x2 tag = {tagA, tagB};
        u32x4 kvo[4];
        {
            const int q = r >> 2, pp = r & 3;
            u32x2 kt[4][3], vt[4][2];
            unsigned hm[2][3];
#pragma unroll
            for (int u = 0; u < 4; ++u) {
                const int p = u >> 1, oh = u & 1;
                auto tr = [&](int base, int row) -> u32x2 {
                    const int rr = row + q, ch = (wm * 4 + p * 2 + (pp >> 1)) ^ (2 * (q | (((rr >> 3) & 1) << 2)));
                    return __builtin_bit_cast(u32x2, __builtin_amdgcn_ds_read_tr16_b64_v4i16(LDSP(v4i16, lb + base + rr * 256 + ch * 16 + (pp & 1) * 8)));
                };
                const int row0 = (g4 + 4 * oh) * 8;
                kt[u][0] = tr(L_KS, (oh == 0 && g4 == 0) ? 0 : row0 - 4);
                kt[u][1] = tr(L_KS, row0); kt[u][2] = tr(L_KS, row0 + 4); vt[u][0] = tr(L_VS, row0); vt[u][1] = tr(L_VS, row0 + 4);
                if (oh == 0) { hm[p][0] = __shfl(hal[p], r + 16); hm[p][1] = __shfl(hal[p], r + 32); hm[p][2] = __shfl(hal[p], r + 48); }
            }
            SB();
            const int sl = ((lane & 3) * 16 + (lane >> 2)) * 4;
#pragma unroll
            for (int u = 0; u < 4; ++u) {
                const int p = u >> 1, oh = u & 1;
                u32x2 k0 = kt[u][0];
                const u32x2 k1 = kt[u][1], k2 = kt[u][2], v0 = vt[u][0], v1 = vt[u][1];
                if (oh == 0 && g4 == 0) k0 = idx ? u32x2{hm[p][0] << 16, hm[p][1] | (hm[p][2] << 16)} : u32x2{0u, 0u};
                const float w0 = bflo(kw[p][0]), w1 = bfhi(kw[p][0]), w2 = bflo(kw[p][1]), w3 = bfhi(kw[p][1]);
                const float e[11] = {bfhi(k0[0]), bflo(k0[1]), bfhi(k0[1]), bflo(k1[0]), bfhi(k1[0]), bflo(k1[1]), bfhi(k1[1]), bflo(k2[0]), bfhi(k2[0]), bflo(k2[1]), bfhi(k2[1])};
                const float vf[8] = {bflo(v0[0]), bfhi(v0[0]), bflo(v0[1]), bfhi(v0[1]), bflo(v1[0]), bfhi(v1[0]), bflo(v1[1]), bfhi(v1[1])};
                float t[8];
                if (ABL & 2) { for (int i = 0; i < 8; ++i) t[i] = e[i]; } else
                kv4(t, e, w0, w1, w2, w3, vf);
                if (u > KVU) { TIE(t[0]); BAR(); }
                if (!(ABL & 2)) kv4(t + 4, e + 4, w0, w1, w2, w3, vf + 4);
                const u32x4 o = {pack2(t[7], t[6]), pack2(t[5], t[4]), pack2(t[3], t[2]), pack2(t[1], t[0])};
#pragma unroll
                for (int j = 0; j < 4; ++j) kvo[u][j] = __builtin_amdgcn_ds_bpermute(sl, o[j]);
                SB();
                if (u >= KVU) BAR();
            }
#pragma unroll
            for (int u = 0; u < 4; ++u) TIE(kvo[u]);
        }
        TS(3);
        u32x2 qv[8];
        const char* krow = a.ssmk + (size_t)chn * 4096;
        const char* qs = a.q + (size_t)(idx * 64 + wm * 16) * 2048 + xcd * 256;
        {
            const int ph = STG_SEL, ps = SP0 + ph * SDP, pl = LP0 + ph * LDP;
            const char* kvd = ws + WS_KVT + (size_t)(xcd * 64 + wm * 16) * KP + idx * 256;
            int nb1 = 1 + 2 * (3 - KVU);
#pragma unroll 1
            for (; nb1 <= ps; ++nb1) BAR();
            static_for<0, 4>([&](auto uc) {
                constexpr int u = decltype(uc)::value;
                GST_B128_SC1((lane >> 3) * KP + (lane & 7) * 16, kvo[u], kvd + (u >> 1) * 8 * KP, (u & 1) * 128);
            });
#pragma unroll 1
            for (; nb1 <= pl; ++nb1) {
                if (nb1 == ps + 4) WAIT_V(0);
                BAR();
                if (nb1 == ps + 4 && wm == 0 && lane == 0) GST_B64_SC1(fpub, tag, flg + FL_KV);
            }
            GLD_B128_NT(sw[0], l16, krow, 0); GLD_B128_NT(sw[1], l16, krow, 1024); GLD_B128_NT(sw[2], l16, krow, 2048); GLD_B128_NT(sw[3], l16, krow, 3072);
            static_for<0, 8>([&](auto jc) { constexpr int j = decltype(jc)::value; GLD_B64_NT(qv[j], lh * 2048 + l31 * 8, qs + j * 4096, 0); });
#pragma unroll 1
            for (; nb1 <= 12; ++nb1) {
                if (nb1 == ps + 4) WAIT_V(12);
                BAR();
                if (nb1 == ps + 4 && wm == 0 && lane == 0) GST_B64_SC1(fpub, tag, flg + FL_KV);
            }
        }
        if (ABL & 1024) fl = tag; else GLD_B64_SC1(fl, fpoll, flg + FL_KV, 0);
        BAR();
        WAIT_V(0); TIE(fl);
        const char* usrc = ws + WS_KVT + (size_t)(xcd * 64 + idx * 2 + (wm >> 1)) * KP + (wm & 1) * 4096;
        if (!(ABL & 16)) { GLD_B128_SC1(uu[0], l16, usrc, 0); GLD_B128_SC1(uu[1], l16, usrc, 1024); GLD_B128_SC1(uu[2], l16, usrc, 2048); GLD_B128_SC1(uu[3], l16, usrc, 3072); }
        if (!(MERGE & 1)) {
            GLD_B128_NT(sw[0], l16, krow, 0); GLD_B128_NT(sw[1], l16, krow, 1024); GLD_B128_NT(sw[2], l16, krow, 2048); GLD_B128_NT(sw[3], l16, krow, 3072);
        }
        if (CB == 14) BAR();
        {
            const char* cs = ((wm & 2) ? a.cim : a.cre) + (size_t)(tn * 64 + (wm & 1) * 32) * 2048;
#pragma unroll
            for (int i = 0; i < 4; ++i) {
                const int pair = i * 4 + g4, v = r ^ pair;
                const unsigned o = (pair * 2 + (v >> 3)) * 2048 + (v & 7) * 16;
                DMA(lb + L_C + 2 * G2STAGE + wm * 4096 + i * 1024, o, cs + (rot2 << 7));
                DMA(lb + L_C + 3 * G2STAGE + wm * 4096 + i * 1024, o, cs + (rot2 << 7) + 128);
                if (i == CSPLIT) BAR();
            }
        }
        if (CB == 13) BAR();
        TS(4);
        const unsigned ud = lb + ((wm & 2) + ((lane >> 2) & 1)) * CHSZ + UOFF + upl(((lane >> 3) & 1) * 4 + (lane & 3)) + (16 + (wm & 1) * 16 + g4) * 16;
        {
            const u32x4 z4 = {0, 0, 0, 0};
            if (lane < 16) *LDSP(unsigned, lb + L_SY + lane * 4) = 0;
            if (lane < 32) *LDSP(u32x4, cb + (lane >> 3) * CST + (lane & 7) * 16) = z4;
            *LDSP(u32x4, cb + UOFF + upl(g4) + r * 16) = z4;
            *LDSP(u32x4, cb + UOFF + upl(4 + g4) + r * 16) = z4;
        }
        WAIT_L(0);
        TS(5);
        BAR();
        TS(11);
        WAIT_V(8);
        TS(12);
        if (!(ABL & 32)) {
#pragma unroll
            for (int it = 0; it < 4; ++it) TIE(sw[it]);
#pragma unroll
            for (int it = 0; it < 4; ++it) {
                const unsigned q = cb + 128 + it * 1024 + l16;
                const unsigned d0 = sw[it][0], d1 = sw[it][1], d2 = sw[it][2], d3 = sw[it][3];
                const unsigned o0 = it ? __builtin_amdgcn_readlane(sw[it ? it - 1 : 0][2], 63) : 0u, o1 = it ? __builtin_amdgcn_readlane(sw[it ? it - 1 : 0][3], 63) : 0u;
                const unsigned x0 = __builtin_amdgcn_update_dpp(o0, d2, 0x138, 0xf, 0xf, false), x1 = __builtin_amdgcn_update_dpp(o1, d3, 0x138, 0xf, 0xf, false);
                const unsigned a0 = __builtin_amdgcn_alignbyte(d0, x1, 2), a1 = __builtin_amdgcn_alignbyte(d1, d0, 2), a2 = __builtin_amdgcn_alignbyte(d2, d1, 2),
                               a3 = __builtin_amdgcn_alignbyte(d3, d2, 2);
                *LDSP(u32x4, q) = sw[it];
                *LDSP(u32x4, q + CST) = u32x4{a0, a1, a2, a3};
                *LDSP(u32x4, q + 2 * CST) = u32x4{x1, d0, d1, d2};
                *LDSP(u32x4, q + 3 * CST) = u32x4{__builtin_amdgcn_alignbyte(x1, x0, 2), a0, a1, a2};
                if (it == 3 && lane == 63) {
                    *LDSP(u32x2, q + 16 + CST) = u32x2{d3 >> 16, 0u};
                    *LDSP(u32x2, q + 16 + 2 * CST) = u32x2{d3, 0u};
                    *LDSP(u32x2, q + 16 + 3 * CST) = u32x2{a3, d3 >> 16};
                }
            }
        }
        TS(13);
#pragma unroll
        for (int it = 0; it < 4; ++it) TIE(uu[it]);
        if (!__all(fl[0] == tagA && fl[1] == tagB)) {
            for (unsigned spin = 0; spin < SPIN_CAP; ++spin) {
                GLD_B64_SC1(fl, fpoll, flg + FL_KV, 0);
                WAIT_V(0); TIE(fl);
                if (__all(fl[0] == tagA && fl[1] == tagB)) break;
                __builtin_amdgcn_s_sleep(8);
            }
            GLD_B128_SC1(uu[0], l16, usrc, 0); GLD_B128_SC1(uu[1], l16, usrc, 1024); GLD_B128_SC1(uu[2], l16, usrc, 2048); GLD_B128_SC1(uu[3], l16, usrc, 3072);
            WAIT_V(0);
#pragma unroll
            for (int it = 0; it < 4; ++it) TIE(uu[it]);
        }
#pragma unroll
        for (int it = 0; it < 4; ++it) *LDSP(u32x4, ud + it * 64) = uu[it];
        lb_post(lb + L_SY + 32 + wm * 4, 1); lb_wait(lb + L_SY + 32 + (wm ^ 1) * 4, 1);
        __builtin_amdgcn_s_setprio(CVPRIO);
        TS(6);
        f32x4 ca[4][2];
        {
            unsigned kb = cb + ((3 - r) & 3) * CST + 2 * (64 + r - 8 * g4 - 7 + ((3 - r) & 3)) - 64;
            unsigned ub = cb + UOFF + upl(g4) + r * 16;
#pragma unroll
            for (int i = 0; i < 4; ++i) { ca[i][0] = f32x4{0, 0, 0, 0}; ca[i][1] = f32x4{0, 0, 0, 0}; }
            s16x4 FA[2][8][2]; bf16x8 FB[2][2];
#define CV_A(h, q, e) DSR64(FA[h][((q) + 2) & 7][e], kb, 32 * ((q) + 2) + 8 * (e) + 2048 * (h))
#define CV_B(s, ct) DSR128(FB[(s) & 1][ct], ub, (1 - ((s) & 1)) * upl(4) + (16 + 16 * (ct) - ((s) >> 1)) * 16)
            static_for<0, 8>([&](auto ic) { constexpr int n = decltype(ic)::value; CV_A((n >> 1) & 1, -2 + (n >> 2), n & 1); });
            WAIT_L(4);
            CV_B(0, 0); CV_B(0, 1);
            static_for<0, 8>([&](auto ic) { constexpr int n = decltype(ic)::value; CV_A((n >> 1) & 1, (n >> 2), n & 1); });
            auto body = [&]() {
                static_for<0, 4>([&](auto ic) {
                    constexpr int s = decltype(ic)::value;
                    static_for<0, 12>([&](auto mc) {
                        constexpr int m = decltype(mc)::value, bi = m / 3, k = m % 3, sl = (2 * s + bi) & 7;
                        if constexpr (m == 0) { WAIT_L(8); TIE(FB[s & 1][0]); TIE(FB[s & 1][1]); }
                        if constexpr (m == 6) WAIT_L(10);
                        if constexpr (m == 9) WAIT_L(9);
                        if constexpr (k == 0) { TIE(FA[0][sl][0]); TIE(FA[0][sl][1]); TIE(FA[1][sl][0]); TIE(FA[1][sl][1]); }
                        const bf16x8 fa = __builtin_shufflevector(FA[k == 2][sl][0], FA[k == 2][sl][1], 0, 1, 2, 3, 4, 5, 6, 7);
                        ca[bi][k != 1] = __builtin_amdgcn_mfma_f32_16x16x32_bf16(fa, FB[s & 1][k == 0], ca[bi][k != 1], 0, 0, 0);
                        SB();
                        if constexpr (m < 2) CV_B(s + 1, m);
                        else if constexpr (m < 10) CV_A(((m - 2) >> 1) & 1, 2 * (s + 1) + ((m - 2) >> 2), (m - 2) & 1);
                        SB();
                    });
                });
                kb += 256; ub -= 32;
            };
#pragma unroll 1
            for (int it = 0; it < QIT; ++it) body();
            {
                WAIT_V(0);
                lb_post(lb + L_SY + 32 + wm * 4, 2);
                if (!(MERGE & 2)) static_for<0, 8>([&](auto jc) { constexpr int j = decltype(jc)::value; GLD_B64_NT(qv[j], lh * 2048 + l31 * 8, qs + j * 4096, 0); });
            }
#pragma unroll 1
            for (int it = QIT; it < 8; ++it) body();
            WAIT_L(0);
#pragma unroll
            for (int i = 0; i < 4; ++i) { TIE(FA[0][i][0]); TIE(FA[0][i][1]); TIE(FA[1][i][0]); TIE(FA[1][i][1]); }
            TIE(FB[0][0]); TIE(FB[0][1]);
#undef CV_A
#undef CV_B
        }
        TS(7);
        __builtin_amdgcn_s_setprio(3);
        int nb = 0;
#define HB() do { if (nb < 15) BAR(); ++nb; } while (0)
        {
            const float Dd = bf2f(dskl);
            const char* zd = ws + WS_ZT + (size_t)chn * ZP;
            u32x2 kvr[2][4];
#pragma unroll
            for (int ct = 0; ct < 2; ++ct)
#pragma unroll
                for (int bi = 0; bi < 4; ++bi)
                    kvr[ct][bi] = *LDSP(u32x2, cb + UOFF + upl(2 * bi + (g4 >> 1)) + (16 + 16 * ct + r) * 16 + (1 - (g4 & 1)) * 8);
#pragma unroll
            for (int ct = 0; ct < 2; ++ct)
#pragma unroll
                for (int bi = 0; bi < 4; ++bi) {
                    const f32x4 y = ca[bi][ct];
                    const u32x2 kq = kvr[ct][bi];
                    *LDSP(u32x2, cb + ct * 2304 + r * 144 + bi * 32 + g4 * 8) =
                        u32x2{pack2(rb1(y[0]) + rb1(bfhi(kq[1]) * Dd), rb1(y[1]) + rb1(bflo(kq[1]) * Dd)), pack2(rb1(y[2]) + rb1(bfhi(kq[0]) * Dd), rb1(y[3]) + rb1(bflo(kq[0]) * Dd))};
                }
            if (ZB >= 3) HB();
            u32x4 t[4];
#pragma unroll
            for (int it = 0; it < 4; ++it) t[it] = *LDSP(u32x4, cb + (it * 8 + (lane >> 3)) * 144 + (lane & 7) * 16);
            if (!(ABL & 128)) { GST_B128_SC1(l16, t[0], zd, 0); GST_B128_SC1(l16, t[1], zd, 1024); GST_B128_SC1(l16, t[2], zd, 2048); GST_B128_SC1(l16, t[3], zd, 3072); }
            else { TIE(t[0]); TIE(t[1]); TIE(t[2]); TIE(t[3]); }
        }
        if (ZB >= 2) HB();
        if (ZB >= 1) HB();
        WAIT_V(0);
        lb_post(lb + L_SY + 32 + wm * 4, 3);
        if (wm == 0) {
            lb_wait(lb + L_SY + 32 + (lane & 3) * 4, 3);
            if (!(ABL & 1024) && lane == 0) GST_B64_SC1(fpub, tag, flg + FL_Z);
        }
        TS(8);
        HB(); HB(); HB();
        if (!(ABL & 1024) && wm == 0) GLD_B64_SC1(fl, fpoll, flg + FL_Z, 0);
        HB(); HB();
        if (wm == 0) { WAIT_V(0); TIE(fl); lb_post(lb + L_SY + 48, __all(fl[0] == tagA && fl[1] == tagB) ? 2 : 1); }
        lb_wait(lb + L_SY + 48, 1);
        if (__builtin_amdgcn_readfirstlane(*LDSP(unsigned, lb + L_SY + 48)) != 2) {
            while (nb < 15) { BAR(); ++nb; }
            for (unsigned spin = 0; spin < SPIN_CAP; ++spin) {
                GLD_B64_SC1(fl, fpoll, flg + FL_Z, 0);
                WAIT_V(0); TIE(fl);
                if (__all(fl[0] == tagA && fl[1] == tagB)) break;
                __builtin_amdgcn_s_sleep(8);
            }
        }
        u32x4 zv[4];
        {
            const char* zsrc = ws + WS_ZT + (size_t)(xcd * 128 + wm * 32) * ZP + idx * 128;
#pragma unroll
            for (int it = 0; it < 4; ++it) if (!(ABL & 256)) GLD_B128_SC1(zv[it], (lane >> 3) * ZP + (lane & 7) * 16, zsrc + it * 8 * ZP, 0);
        }
        HB(); HB();
        WAIT_V(0);
        TS(9);
        {
            const unsigned zi = lb + L_ZI + wm * 5120;
#pragma unroll
            for (int it = 0; it < 4; ++it) { TIE(zv[it]); *LDSP(u32x4, zi + ((lane >> 3) + 8 * it) * 160 + (lane & 7) * 16) = zv[it]; }
            v4i16 z[8];
#pragma unroll
            for (int n = 0; n < 8; ++n) z[n] = __builtin_amdgcn_ds_read_tr16_b64_v4i16(LDSP(v4i16, zi + ((n & 1) * 16 + (lane >> 2)) * 160 + (n >> 1) * 32 + (lane & 3) * 8));
            HB();
#pragma unroll
            for (int n = 0; n < 8; ++n) *LDSP(v4i16, lb + L_Z2 + ((n >> 1) * 16 + r) * 264 + wm * 64 + (n & 1) * 32 + g4 * 8) = z[n];
            lb_post(lb + L_SY + 32 + wm * 4, 4);
        }
        HB();
        lb_wait(lb + L_SY + 32 + (lane & 3) * 4, 4);
        char* const oa = a.out + (size_t)(idx * 64 + wm * 16) * 8192 + xcd * 512;
        u32x2 zz[8];
#pragma unroll
        for (int n = 0; n < 8; ++n) zz[n] = *LDSP(u32x2, lb + L_Z2 + (wm * 16 + n * 2 + lh) * 264 + l31 * 8);
#pragma unroll
        for (int n = 0; n < 8; ++n) {
            TIE(qv[n]);
            const u32x2 z = zz[n];
            const f32x4 o = {rb1(bflo(qv[n][0]) * bflo(z[0])), rb1(bfhi(qv[n][0]) * bfhi(z[0])), rb1(bflo(qv[n][1]) * bflo(z[1])), rb1(bfhi(qv[n][1]) * bfhi(z[1]))};
            if (!(ABL & 512)) GST_B128_NT(lh * 8192 + l31 * 16, o, oa + n * 16384, 0); else { f32x4 o2 = o; TIE(o2); }
            if (n < 7 && (n + 1) % OS == 0) HB();
        }
#undef HB
        asm volatile(".Lfk_code_a:");
        F_END();
        TS(10);
        TS_FLUSH();
        return;
    }

    unsigned c_dt, c_re, c_im;
    u32x2 ep, fl;
    {
        GLD_B64_SC1(ep, 0u * lane, flg + FL_EPOCH, 0);
        const unsigned po = (tn * 64 + wn * 32 + l31) * 2;
        GLD_U16(c_dt, po, a.logdt); GLD_U16(c_re, po, a.lre); GLD_U16(c_im, po, a.lim);
    }
    {
        const int g4 = lane >> 4;
        const unsigned src = g4 * 2048 + (((lane & 15) ^ (2 * g4)) << 4);
        const char* ks = (wn ? a.v : a.k) + (size_t)(idx * 64 + wm * 16) * 2048 + xcd * 256;
#pragma unroll
        for (int i = 0; i < 4; ++i) { if (ABL & 1) break; dma_nt_(lb + L_KS + wid * 4096 + i * 1024, src ^ ((i >> 1) * 128), ks + i * 8192); }
    }
    unsigned dv[4];
#pragma unroll
    for (int i = 0; i < 4; ++i) {
        const int pair = i * 4 + (lane >> 4), v = (lane & 15) ^ pair;
        dv[i] = (pair * 2 + (v >> 3)) * 2048 + (v & 7) * 16;
    }
    const char* g1src = wid < 4 ? a.x + (size_t)(tm * 128 + wid * 32) * 2048
                                : ((wid & 1) ? a.bim : a.bre) + (size_t)(tn * 64 + ((wid >> 1) & 1) * 32) * 2048;
    const int rot = (b * 5) & 15;
    const unsigned g1dst = lb + wid * 4096;
#define G1_ISSUE(st, i) DMA(g1dst + ((st) & 3) * G1STAGE + (i) * 1024, dv[i], g1src + ((((st) + rot) & 15) << 7))
    G1_ISSUE(0, 0); G1_ISSUE(0, 1);
    TS(14);
    float Lr, Li, cr, ci, L8r, L8i, L16r, L16i, L128r, L128i, Qr, Qi, Wr, Wi;
    unsigned tagA, tagB;
    {
        WAIT_V((ABL & 1) ? 2 : 6);
        TIE(c_dt); TIE(c_re); TIE(c_im); TIE(ep);
        const float dt = rbf(expf(bf2f(c_dt)));
        const float lr = bf2f(c_re), li = bf2f(c_im);
        const float e = expf(dt * lr);
        float sn, cs;
        sincosf(dt * li, &sn, &cs);
        Lr = e * cs; Li = e * sn;
        const float nr = Lr - 1.f, ni = Li, den = lr * lr + li * li;
        cr = (nr * lr + ni * li) / den; ci = (ni * lr - nr * li) / den;
        float pr = Lr, pi = Li, tr, ti;
        Qr = cr; Qi = ci; Wr = 1.f; Wi = 0.f;
        const int run = 2 * wm + lh;
#pragma unroll
        for (int it = 1; it <= 10; ++it) {
            cmul(pr, pi, pr, pi, tr, ti); pr = tr; pi = ti;
            if (it == 3) { L8r = pr; L8i = pi; }
            if (it == 4) { L16r = pr; L16i = pi; }
            if (it == 7) { L128r = pr; L128i = pi; }
            if (it >= 4 && it <= 6) { cmul(Qr, Qi, pr, pi, tr, ti); if ((run >> (it - 4)) & 1) { Qr = tr; Qi = ti; } }
            if (it >= 8) { cmul(Wr, Wi, pr, pi, tr, ti); if (!((run >> (it - 8)) & 1)) { Wr = tr; Wi = ti; } }
        }
        TIE(Lr); TIE(Li); TIE(cr); TIE(ci); TIE(L8r); TIE(L8i); TIE(L16r); TIE(L16i); TIE(L128r); TIE(L128i); TIE(Qr); TIE(Qi); TIE(Wr); TIE(Wi);
        const unsigned e0 = __builtin_amdgcn_readfirstlane(ep[0]), e1 = __builtin_amdgcn_readfirstlane(ep[1]);
        tagA = e0 + 1; tagB = (e1 + (tagA == 0 ? 1u : 0u));
    }
    const unsigned tagG = tagA ^ tagB ^ 0x9E3779B9u;
    const u32x2 tag = {tagA, tagB};
    TS(1);
    WAIT_V(2);
    BAR();
    static_for<2, 16>([&](auto ic) { constexpr int n = decltype(ic)::value; G1_ISSUE(n >> 2, n & 3); });
    TS(15);

    f32x16 acc[2];
    {
#pragma unroll
        for (int j = 0; j < 2; ++j)
#pragma unroll
            for (int r = 0; r < 16; ++r) acc[j][r] = 0.f;
        const int prow = 16 * ((l31 >> 2) & 1) + 4 * (l31 >> 3) + (l31 & 3);
        const int aoff = img_off(wm * 32 + prow, lh), boff = img_off(128 + wn * 64 + l31, lh);
        unsigned aad[4], bad[4];
#pragma unroll
        for (int ks = 0; ks < 4; ++ks) { aad[ks] = lb + (aoff ^ (ks * 32)); bad[ks] = lb + (boff ^ (ks * 32)); }
        bf16x8 fa[PF + 1], fb[PF + 1][2];
        WAIT_V(12); BAR();
        TS(2);
#define G1_FRAGS(g) do { constexpr int bo_ = (((g) >> 2) & 3) * G1STAGE, hi_ = bo_ >= 65536 ? 65536 : 0; \
        DSR128(fa[(g) % (PF + 1)], aad[(g) & 3] + hi_, bo_ - hi_); \
        DSR128(fb[(g) % (PF + 1)][0], bad[(g) & 3] + hi_, bo_ - hi_); DSR128(fb[(g) % (PF + 1)][1], bad[(g) & 3] + hi_, bo_ - hi_ + 4096); } while (0)
        static_for<0, PF>([&](auto ic) { constexpr int g = decltype(ic)::value; G1_FRAGS(g); });
        static_for<0, 64>([&](auto ic) {
            constexpr int g = decltype(ic)::value, gn = g + PF;
            if constexpr ((gn & 3) == 0 && gn < 64) { static_assert(g1_cnt(gn >> 2) >= 0, "schedule"); WAIT_V(g1_cnt(gn >> 2)); WAIT_L(0); BAR(); if constexpr (((gn >> 2) & 3) == 0 || gn >= 52) TS(gn >= 52 ? 23 + (gn >> 2) - 13 : 16 + (gn >> 4)); }
            if constexpr (gn < 64) G1_FRAGS(gn);
            SB();
            WAIT_L((63 - g < PF ? 63 - g : PF) * 3);
            TIE(fa[g % (PF + 1)]); TIE(fb[g % (PF + 1)][0]); TIE(fb[g % (PF + 1)][1]);
            acc[0] = __builtin_amdgcn_mfma_f32_32x32x16_bf16(fa[g % (PF + 1)], fb[g % (PF + 1)][0], acc[0], 0, 0, 0);
            SB();
            constexpr int n0 = g1_upto(g - 1), n1 = g1_upto(g);
            if constexpr (n1 > n0) G1_ISSUE(n0 >> 2, n0 & 3);
            SB();
            acc[1] = __builtin_amdgcn_mfma_f32_32x32x16_bf16(fa[g % (PF + 1)], fb[g % (PF + 1)][1], acc[1], 0, 0, 0);
            SB();
            if constexpr (n1 > n0 + 1) { G1_ISSUE((n0 + 1) >> 2, (n0 + 1) & 3); SB(); }
        });
#undef G1_FRAGS
    }
    TS(3);
    __builtin_amdgcn_s_setprio(MPRIO);
    float ur[16], ui[16];
    float hr, hi;
    unsigned pl8 = lb + L_AGG + (wn * 32 + l31) * 8;
    const unsigned pmine = lb + L_SY + wid * 4, pall = lb + L_SY + (lane & 7) * 4;
    const int run = wm * 2 + lh;
    const float nLi = -Li;
    TIE(pl8);
    {
        asm volatile("s_nop 7\n\ts_nop 3" : "+v"(acc[0]), "+v"(acc[1]));
#pragma unroll
        for (int r = 0; r < 16; ++r) { ur[r] = acc[0][r]; ui[r] = acc[1][r]; }
        float gr = ur[0], gi = ui[0], fr = ur[8], fi = ui[8];
#pragma unroll
        for (int r = 1; r < 8; ++r) {
            float nr, ni; cmad(nr, ni, Lr, Li, nLi, gr, gi, ur[r], ui[r]); gr = nr; gi = ni;
            cmad(nr, ni, Lr, Li, nLi, fr, fi, ur[8 + r], ui[8 + r]); fr = nr; fi = ni;
        }
        cmac(fr, fi, L8r, L8i, -L8i, gr, gi);
        *LDSP(f32x2, pl8 + run * 512) = f32x2{fr, fi};
        WAIT_L(0); BAR();
        f32x2 e[8];
#pragma unroll
        for (int j = 0; j < 8; ++j) e[j] = *LDSP(f32x2, pl8 + j * 512);
        const float nL16i = -L16i;
#pragma unroll
        for (int j = 1; j < 8; ++j) { float er = e[j][0], ei = e[j][1]; cmac(er, ei, L16r, L16i, nL16i, e[j - 1][0], e[j - 1][1]); e[j] = f32x2{er, ei}; }
        if (wm == 0 && lh == 0) {
            const u32x4 gq = {__float_as_uint(e[7][0]), tagA, __float_as_uint(e[7][1]), tagG};
            GST_B128_SC1((tm * 1024 + tn * 64 + wn * 32 + l31) * 16, gq, ws + WS_E, 0);
        }
        float cinr = 0.f, cini = 0.f;
#pragma unroll
        for (int j = 0; j < 7; ++j) { if (j + 1 == run) { cinr = e[j][0]; cini = e[j][1]; } }
        TS(4);
        const float ncr = -cr, nci = -ci;
        mul4(hr, hi, cr, nci, ncr, nci, cinr, cini);
#pragma unroll
        for (int r = 0; r < 16; ++r) { float nr, ni; mul4(nr, ni, cr, nci, ncr, nci, ur[r], ui[r]); ur[r] = nr; ui[r] = ni; TIE(ur[r]); TIE(ui[r]); }
        TIE(hr); TIE(hi);
    }
    unsigned hw[2], ta[2];
    {
        const unsigned ht = lb + L_HT + wid * 2048;
#pragma unroll
        for (int j = 0; j < 2; ++j) {
            const int row = 8 * lh + 4 * j + ((lane & 15) >> 2), chunk = 2 * ((lane >> 4) & 1) + ((lane & 3) >> 1);
            ta[j] = ht + row * 64 + ((chunk ^ ((row >> 1) & 3)) << 4) + 8 * (lane & 1);
            hw[j] = ht + l31 * 64 + (((lh * 2 + j) ^ ((l31 >> 1) & 3)) << 4);
            TIE(ta[j]); TIE(hw[j]);
        }
    }
    {
        const int j0 = 2 * run - (16 - tm);
        float pr = 0.f, pi = 0.f;
        float nL128i = -L128i, nWi = -Wi;
        TIE(nL128i); TIE(nWi);
        if (4 * wm + 3 >= 16 - tm) {
            u32x4 eg[2];
            const unsigned eo0 = (j0 < 0 ? 0 : j0) * 16384 + (tn * 64 + wn * 32 + l31) * 16, eo1 = (j0 + 1 < 0 ? 0 : j0 + 1) * 16384 + (tn * 64 + wn * 32 + l31) * 16;
#pragma unroll 1
            for (unsigned spin = 0; spin < SPIN_CAP; ++spin) {
                GLD_B128_SC1(eg[0], eo0, ws + WS_E, 0); GLD_B128_SC1(eg[1], eo1, ws + WS_E, 0);
                WAIT_V(0); TIE(eg[0]); TIE(eg[1]);
                if (__all(eg[0][1] == tagA && eg[0][3] == tagG && eg[1][1] == tagA && eg[1][3] == tagG)) break;
                __builtin_amdgcn_s_sleep(2);
            }
            float e1r = j0 + 1 >= 0 ? __uint_as_float(eg[1][0]) : 0.f, e1i = j0 + 1 >= 0 ? __uint_as_float(eg[1][2]) : 0.f;
            const float e0r = j0 >= 0 ? __uint_as_float(eg[0][0]) : 0.f, e0i = j0 >= 0 ? __uint_as_float(eg[0][2]) : 0.f;
            cmac(e1r, e1i, L128r, L128i, nL128i, e0r, e0i);
            cmul4(pr, pi, Wr, Wi, nWi, e1r, e1i);
        }
        TS(5);
        *LDSP(f32x2, pl8 + (L_ES - L_AGG) + run * 512) = f32x2{pr, pi};
        lb_post(pmine, 1); lb_wait(pall, 1);
    }
    TS(6);
    {
        f32x2 e[8];
#pragma unroll
        for (int j = 0; j < 8; ++j) e[j] = *LDSP(f32x2, pl8 + (L_ES - L_AGG) + j * 512);
        const f32x2 G = ((e[0] + e[1]) + (e[2] + e[3])) + ((e[4] + e[5]) + (e[6] + e[7]));
        mac4(hr, hi, Qr, -Qi, -Qr, -Qi, G[0], G[1]);
#pragma unroll
        for (int r = 0; r < 16; ++r) { cmac(ur[r], ui[r], Lr, nLi, Li, hr, hi); hr = ur[r]; hi = ui[r]; }
        const char* hd = ws + WS_H + (size_t)((tm * 4 + wm) * 2) * HP + (tn * 4 + wn * 2) * 1024;
        v4i16 t[2][2][2];
#pragma unroll
        for (int c = 0; c < 2; ++c) {
#pragma unroll
            for (int e2 = 0; e2 < 2; ++e2) {
                const float* o = (c ? ui : ur) + 8 * e2;
                *LDSP(u32x4, hw[e2]) = u32x4{pack2(o[0], o[1]), pack2(o[2], o[3]), pack2(o[4], o[5]), pack2(o[6], o[7])};
            }
#pragma unroll
            for (int hf = 0; hf < 2; ++hf)
#pragma unroll
                for (int j = 0; j < 2; ++j) t[c][hf][j] = __builtin_amdgcn_ds_read_tr16_b64_v4i16(LDSP(v4i16, ta[j] + hf * 1024));
        }
        static_for<0, 4>([&](auto ic) {
            constexpr int n = decltype(ic)::value;
            const bf16x8 f = __builtin_shufflevector(t[n >> 1][n & 1][0], t[n >> 1][n & 1][1], 0, 1, 2, 3, 4, 5, 6, 7);
            GST_B128_SC1(l16, f, hd + (n >> 1) * HP, (n & 1) * 1024);
        });
    }
    TS(7);
    bf16x8 fa2[AW];
    u32x2 xv[4], dsv;
    const char* Aw = ws + WS_H + (size_t)((tm * 4 + wm) * 2 + wn) * HP;
    const char* g2src = (wn ? a.cim : a.cre) + (size_t)(tn * 64 + wm * 16) * 2048;
    const unsigned g2dst = lb + L_C + wid * 2048;
    unsigned g2v[2];
#pragma unroll
    for (int i = 0; i < 2; ++i) {
        const int pair = i * 4 + (lane >> 4), v = (lane & 15) ^ pair ^ ((wm & 1) * 8);
        g2v[i] = (pair * 2 + (v >> 3)) * 2048 + (v & 7) * 16;
    }
#define G2_ISSUE(st, i) DMA(g2dst + (((st) + 2) & 3) * G2STAGE + (i) * 1024, g2v[i], g2src + ((((st) + rot2) & 15) << 7))
#define G2_A(g) GLD_B128_SC1(fa2[(g) % AW], l16, Aw + (((((g) >> 2) + rot2) & 15) << 12), ((g) & 3) * 1024)
    {
        const char* xs = a.x + (size_t)(tm * 128 + wm * 32) * 2048 + tn * 128 + wn * 64;
#pragma unroll
        for (int it = 0; it < 4; ++it) GLD_B64(xv[it], (lane >> 3) * 2048 + (lane & 7) * 8, xs + it * 16384, 0);
        GLD_B64(dsv, (lane & 7) * 8, a.dskip + tn * 128 + wn * 64, 0);
        WAIT_V(5);
        lb_post(pmine, 2); lb_wait(pall, 2);
        if (wid == 0 && lane < 16) GST_B64_SC1((tm * 16 + lane) * 128 + tn * 8, tag, flg + FL_H);
        TS(8);
        static_for<4, 8>([&](auto ic) { constexpr int n = decltype(ic)::value; G2_ISSUE(n >> 1, n & 1); });
        if (wid == 0) {
#pragma unroll 1
            for (unsigned spin = 0; spin < SPIN_CAP; ++spin) {
                GLD_B64_SC1(fl, (tm * 16 + tn) * 128 + (lane & 15) * 8, flg + FL_H, 0);
                WAIT_V(0); TIE(fl);
                if (__all(fl[0] == tagA && fl[1] == tagB)) break;
            }
            lb_post(pmine, 3);
            if (b == 255 && lane == 0) GST_B64_SC1(0u * lane, tag, flg + FL_EPOCH);
        } else lb_wait(lb + L_SY, 3);
        static_for<0, AW>([&](auto ic) { constexpr int g = decltype(ic)::value; G2_A(g); });
        lb_wait(lb + L_SY + 32 + (lane & 3) * 4, 2);
    }
    TS(9);
    __builtin_amdgcn_s_setprio(0);
    {
#pragma unroll
        for (int j = 0; j < 2; ++j)
#pragma unroll
            for (int r = 0; r < 16; ++r) acc[j][r] = 0.f;
        const int boff = img_off(wn * 64 + l31, lh);
        unsigned bad[4];
#pragma unroll
        for (int ks = 0; ks < 4; ++ks) bad[ks] = lb + L_C + (boff ^ (ks * 32));
        bf16x8 fb[PF + 1][2];
#define G2_FRAGS(g) do { constexpr int bo_ = ((((g) >> 2) + 2) & 3) * G2STAGE; \
        DSR128(fb[(g) % (PF + 1)][0], bad[(g) & 3], bo_); DSR128(fb[(g) % (PF + 1)][1], bad[(g) & 3], bo_ + 4096); } while (0)
        static_for<0, PF>([&](auto ic) { constexpr int g = decltype(ic)::value; G2_FRAGS(g); });
        static_for<0, 64>([&](auto ic) {
            constexpr int g = decltype(ic)::value, gn = g + PF;
            if constexpr ((gn & 3) == 0 && gn < 64) {
                if constexpr ((gn >> 2) >= 4) WAIT_V(g2_cnt_stage(gn >> 2));
                WAIT_L(0); BAR();
                if constexpr (g == 2) TS(10);
                if constexpr (g == 18 || g == 34 || g == 50) TS(26 + (g >> 4));
            }
            if constexpr (gn < 64) G2_FRAGS(gn);
            SB();
            asm volatile("s_waitcnt vmcnt(%0) lgkmcnt(%1)" ::"n"(g2_cnt_a(g)), "n"((63 - g < PF ? 63 - g : PF) * 2) : "memory");
            TIE(fa2[g % AW]); TIE(fb[g % (PF + 1)][0]); TIE(fb[g % (PF + 1)][1]);
            acc[0] = __builtin_amdgcn_mfma_f32_32x32x16_bf16(fb[g % (PF + 1)][0], fa2[g % AW], acc[0], 0, 0, 0);
            SB();
            if constexpr (g2_dma(g)) G2_ISSUE(gn / 4 + 3, gn & 3);
            SB();
            acc[1] = __builtin_amdgcn_mfma_f32_32x32x16_bf16(fb[g % (PF + 1)][1], fa2[g % AW], acc[1], 0, 0, 0);
            SB();
            if constexpr (g2_a(g)) G2_A(g + AW);
            if constexpr (g == 58) lb_wait(lb + L_SY + 32 + (lane & 3) * 4, 3);
            SB();
        });
#undef G2_FRAGS
    }
    TS(11);
    {
        const unsigned wo = l31 * 128, sx = (l31 & 7) << 4;
        const f32x16 snd = wn ? acc[0] : acc[1], own = wn ? acc[1] : acc[0];
#pragma unroll
        for (int rq = 0; rq < 4; ++rq) *LDSP(f32x4, lb + L_Y + wid * 4096 + wo + ((rq * 32 + lh * 16) ^ sx)) = f32x4{snd[4 * rq], snd[4 * rq + 1], snd[4 * rq + 2], snd[4 * rq + 3]};
        lb_post(pmine, 4);
#pragma unroll
        for (int rq = 0; rq < 4; ++rq) *LDSP(f32x4, lb + L_Y2 + wid * 4096 + wo + ((rq * 32 + lh * 16) ^ sx)) = f32x4{own[4 * rq], own[4 * rq + 1], own[4 * rq + 2], own[4 * rq + 3]};
        TIE(dsv);
#pragma unroll
        for (int it = 0; it < 4; ++it) TIE(xv[it]);
        const float ds0 = bflo(dsv[0]), ds1 = bfhi(dsv[0]), ds2 = bflo(dsv[1]), ds3 = bfhi(dsv[1]);
        const unsigned ro = (lane >> 3) * 128 + (((lane & 7) ^ (lane >> 3)) << 4);
        f32x4 y[4];
#pragma unroll
        for (int it = 0; it < 4; ++it) {
            const f32x4 w = *LDSP(f32x4, lb + L_Y2 + wid * 4096 + it * 1024 + ro);
            y[it] = f32x4{2.f * w[0] + rb1(bflo(xv[it][0]) * ds0), 2.f * w[1] + rb1(bfhi(xv[it][0]) * ds1), 2.f * w[2] + rb1(bflo(xv[it][1]) * ds2), 2.f * w[3] + rb1(bfhi(xv[it][1]) * ds3)};
            TIE(y[it]);
        }
        lb_wait(lb + L_SY + (wid ^ 4) * 4, 4);
        char* const ob = a.out + (size_t)(tm * 128 + wm * 32) * 8192 + 4096 + tn * 256 + wn * 128;
        f32x4 w[4];
#pragma unroll
        for (int it = 0; it < 4; ++it) w[it] = *LDSP(f32x4, lb + L_Y + (wid ^ 4) * 4096 + it * 1024 + ro);
#pragma unroll
        for (int it = 0; it < 4; ++it) {
            const f32x4 o = {2.f * w[it][0] + y[it][0], 2.f * w[it][1] + y[it][1], 2.f * w[it][2] + y[it][2], 2.f * w[it][3] + y[it][3]};
            GST_B128_NT((lane >> 3) * 8192 + (lane & 7) * 16, o, ob + it * 65536, 0);
        }
    }
    asm volatile(".Lfk_code_b:");
    F_END();
    TS(12);
    TS_FLUSH();
}

__global__ __launch_bounds__(64) void pre_k(char* ws_) {
    unsigned long long f_ta;
    char* const __attribute__((address_space(4)))* ap = (char* const __attribute__((address_space(4)))*)__builtin_amdgcn_kernarg_segment_ptr();
    asm volatile("s_memrealtime %0\n\ts_waitcnt lgkmcnt(0)" : "=s"(f_ta), "+s"(ap));
    char* ws = *ap;
    if (threadIdx.x == 0 && blockIdx.x == 0) *(unsigned*)(ws + ((size_t)65 << 20)) = (unsigned)f_ta;
}
__global__ __launch_bounds__(256) void gate_k(char* ws) {
    __shared__ unsigned mx[4];
    const unsigned now = (unsigned)__builtin_amdgcn_s_memrealtime();
    if (threadIdx.x < 4) mx[threadIdx.x] = (threadIdx.x & 1) ? 0xffffffffu : 0u;
    __syncthreads();
    unsigned s0 = 0, s1 = 0xffffffffu, e0 = 0, e1 = 0xffffffffu;
    for (int w = 0; w < 12; ++w) {
        const u32x2 t = *(const u32x2*)(ws + ((size_t)64 << 20) + (threadIdx.x * 12 + w) * 8);
        const unsigned ds = now - t[0], de = now - t[1];
        s0 = max(s0, ds); s1 = min(s1, ds); e0 = max(e0, de); e1 = min(e1, de);
    }
    atomicMax(&mx[0], s0); atomicMin(&mx[1], s1); atomicMax(&mx[2], e0); atomicMin(&mx[3], e1);
    __syncthreads();
    unsigned ref, len;
    if (FV == 1 || FV == 6 || FV == 9) { ref = now - mx[0]; len = 10000; }
    else if (FV == 2 || FV == 7) { ref = now - mx[1]; len = 10000; }
    else if (FV == 3) { ref = now - mx[3]; len = 5000; }
    else if (FV == 5) { ref = now - mx[2]; len = 5000; }
    else if (FV == 8) { ref = *(const unsigned*)(ws + ((size_t)65 << 20)); len = 10000; if (now - ref > 20000) { ref = now; len = 0; } }
    else if (FV == 10) { ref = now; len = 0; }
    else { ref = now; len = 5000; }
    if (mx[0] > 20000) { ref = now; len = 0; }
    while ((unsigned)__builtin_amdgcn_s_memrealtime() - ref < len) __builtin_amdgcn_s_sleep(1);
}
}

extern "C" void kernel_launch(void* const* d_in, const int* in_sizes, int n_in, void* d_out, int out_size, void* d_ws, size_t ws_size,
                              hipStream_t stream) {
    (void)in_sizes; (void)n_in; (void)out_size; (void)ws_size;
    fk::Args a;
    a.q = (const char*)d_in[0]; a.k = (const char*)d_in[1]; a.v = (const char*)d_in[2]; a.sk = (const char*)d_in[3];
    a.ssmk = (const char*)d_in[4]; a.ssmD = (const char*)d_in[5]; a.x = (const char*)d_in[6]; a.lre = (const char*)d_in[7];
    a.lim = (const char*)d_in[8]; a.bre = (const char*)d_in[9]; a.bim = (const char*)d_in[10]; a.cre = (const char*)d_in[11];
    a.cim = (const char*)d_in[12]; a.logdt = (const char*)d_in[13]; a.dskip = (const char*)d_in[14];
    a.out = (char*)d_out; a.ws = (char*)d_ws;
    if (FV >= 8) hipLaunchKernelGGL(fk::pre_k, dim3(256), dim3(64), 0, stream, (char*)d_ws);
    hipLaunchKernelGGL(fk::fused, dim3(256), dim3(768), 0, stream, a);
    if (FV != 10) hipLaunchKernelGGL(fk::gate_k, dim3(1), dim3(256), 0, stream, (char*)d_ws);
}
```

```cpp
#include <hip/hip_runtime.h>
#include <stdint.h>
#include <type_traits>

namespace fk {

constexpr int S = 2048, D = 1024, P = 1024;

typedef __attribute__((ext_vector_type(8))) short bf16x8;
typedef __attribute__((ext_vector_type(4))) short s16x4;
typedef __attribute__((ext_vector_type(16))) float f32x16;
typedef __attribute__((ext_vector_type(4))) float f32x4;
typedef __attribute__((ext_vector_type(2))) float f32x2;
typedef __attribute__((ext_vector_type(4))) unsigned u32x4;
typedef __attribute__((ext_vector_type(2))) unsigned u32x2;
#define LDSP(T, a) ((__attribute__((address_space(3))) T*)(uintptr_t)(unsigned)(a))

__device__ __forceinline__ float bf2f(unsigned v) { return __uint_as_float(v << 16); }
__device__ __forceinline__ float bflo(unsigned w) { return __uint_as_float(w << 16); }
__device__ __forceinline__ float bfhi(unsigned w) { return __uint_as_float(w & 0xffff0000u); }
__device__ __forceinline__ uint16_t f2bf(float f) { __bf16 b = (__bf16)f; return __builtin_bit_cast(uint16_t, b); }
__device__ __forceinline__ float rbf(float f) { return bf2f(f2bf(f)); }
typedef __attribute__((ext_vector_type(2))) __bf16 bf16x2;
__device__ __forceinline__ unsigned pack2(float lo, float hi) { return __builtin_bit_cast(unsigned, __builtin_convertvector(f32x2{lo, hi}, bf16x2)); }
__device__ __forceinline__ float rb1(float x) { return __uint_as_float(pack2(0.f, x)); }
__device__ __forceinline__ void cmac(float& xr, float& xi, float ar, float ai, float nai, float hr, float hi) {
    asm("v_fmac_f32 %0, %2, %5\n\tv_fmac_f32 %1, %2, %6\n\tv_fmac_f32 %0, %4, %6\n\tv_fmac_f32 %1, %3, %5" : "+v"(xr), "+v"(xi) : "v"(ar), "v"(ai), "v"(nai), "v"(hr), "v"(hi));
}
__device__ __forceinline__ void cmad(float& o_r, float& o_i, float ar, float ai, float nai, float hr, float hi, float er, float ei) {
    asm("v_fma_f32 %0, %2, %5, %7\n\tv_fma_f32 %1, %2, %6, %8\n\tv_fmac_f32 %0, %4, %6\n\tv_fmac_f32 %1, %3, %5" : "=&v"(o_r), "=&v"(o_i) : "v"(ar), "v"(ai), "v"(nai), "v"(hr), "v"(hi), "v"(er), "v"(ei));
}
__device__ __forceinline__ void mac4(float& x, float& y, float a0, float a1, float b0, float b1, float h0, float h1) {
    asm("v_fmac_f32 %0, %2, %6\n\tv_fmac_f32 %1, %4, %7\n\tv_fmac_f32 %0, %3, %7\n\tv_fmac_f32 %1, %5, %6" : "+v"(x), "+v"(y) : "v"(a0), "v"(a1), "v"(b0), "v"(b1), "v"(h0), "v"(h1));
}
__device__ __forceinline__ void mul4(float& x, float& y, float a0, float a1, float b0, float b1, float h0, float h1) {
    asm("v_mul_f32 %0, %2, %6\n\tv_mul_f32 %1, %4, %7\n\tv_fmac_f32 %0, %3, %7\n\tv_fmac_f32 %1, %5, %6" : "=&v"(x), "=&v"(y) : "v"(a0), "v"(a1), "v"(b0), "v"(b1), "v"(h0), "v"(h1));
}
__device__ __forceinline__ void cmul4(float& o_r, float& o_i, float ar, float ai, float nai, float hr, float hi) {
    asm("v_mul_f32 %0, %2, %5\n\tv_mul_f32 %1, %2, %6\n\tv_fmac_f32 %0, %4, %6\n\tv_fmac_f32 %1, %3, %5" : "=&v"(o_r), "=&v"(o_i) : "v"(ar), "v"(ai), "v"(nai), "v"(hr), "v"(hi));
}
__device__ __forceinline__ void cmul(float ar, float ai, float br, float bi, float& cr, float& ci) { cr = ar * br - ai * bi; ci = ar * bi + ai * br; }

template <int I, int N, class F>
__device__ __forceinline__ void static_for(F&& f) { if constexpr (I < N) { f(std::integral_constant<int, I>{}); static_for<I + 1, N>(f); } }

#define TS_DECL
#define TS(i)
#define TS_FLUSH()

#define WAIT_V(n) asm volatile("s_waitcnt vmcnt(%0)" ::"n"((n) > 63 ? 63 : (n)) : "memory")
#define WAIT_L(n) asm volatile("s_waitcnt lgkmcnt(%0)" ::"n"((n) > 15 ? 15 : (n)) : "memory")
#define BAR() asm volatile("s_barrier" ::: "memory")
#define SB() __builtin_amdgcn_sched_barrier(0)
template <class T> __device__ __forceinline__ void tie_(T& x) { asm volatile("" : "+v"(x)); }
#define TIE(x) tie_(x)
#define DEF_LD(name, ins) template <int OFF, class T> __device__ __forceinline__ void name(T& dst, unsigned voff, const char* sb) { \
    asm volatile(ins : "=v"(dst) : "v"(voff), "s"(sb), "n"(OFF) : "memory"); }
DEF_LD(gld_u16_, "global_load_ushort %0, %1, %2 offset:%3")
DEF_LD(gld_b32_, "global_load_dword %0, %1, %2 offset:%3")
DEF_LD(gld_b64_, "global_load_dwordx2 %0, %1, %2 offset:%3")
DEF_LD(gld_b128_, "global_load_dwordx4 %0, %1, %2 offset:%3")
DEF_LD(gld_b64_nt_, "global_load_dwordx2 %0, %1, %2 offset:%3 nt")
DEF_LD(gld_b128_nt_, "global_load_dwordx4 %0, %1, %2 offset:%3 nt")
DEF_LD(gld_b64_sc1_, "global_load_dwordx2 %0, %1, %2 offset:%3 sc1")
DEF_LD(gld_b128_sc1_, "global_load_dwordx4 %0, %1, %2 offset:%3 sc1")
#define DEF_ST(name, ins) template <int OFF, class T> __device__ __forceinline__ void name(unsigned voff, const T& v, const char* sb) { \
    asm volatile(ins :: "v"(voff), "v"(v), "s"(sb), "n"(OFF) : "memory"); }
DEF_ST(gst_b128_, "global_store_dwordx4 %0, %1, %2 offset:%3\n\ts_nop 1")
DEF_ST(gst_b128_nt_, "global_store_dwordx4 %0, %1, %2 offset:%3 nt\n\ts_nop 1")
DEF_ST(gst_b128_sc1_, "global_store_dwordx4 %0, %1, %2 offset:%3 sc1\n\ts_nop 1")
DEF_ST(gst_b64_sc1_, "global_store_dwordx2 %0, %1, %2 offset:%3 sc1")
#define GLD_U16(dst, voff, sb) gld_u16_<0>(dst, voff, sb)
#define GLD_B32(dst, voff, sb, off) gld_b32_<(off)>(dst, voff, sb)
#define GLD_B64(dst, voff, sb, off) gld_b64_<(off)>(dst, voff, sb)
#define GLD_B128(dst, voff, sb, off) gld_b128_<(off)>(dst, voff, sb)
#define GLD_B64_NT(dst, voff, sb, off) gld_b64_nt_<(off)>(dst, voff, sb)
#define GLD_B128_NT(dst, voff, sb, off) gld_b128_nt_<(off)>(dst, voff, sb)
#define GLD_B64_SC1(dst, voff, sb, off) gld_b64_sc1_<(off)>(dst, voff, sb)
#define GLD_B128_SC1(dst, voff, sb, off) gld_b128_sc1_<(off)>(dst, voff, sb)
#define GST_B128(voff, v, sb, off) gst_b128_<(off)>(voff, v, sb)
#define GST_B128_NT(voff, v, sb, off) gst_b128_nt_<(off)>(voff, v, sb)
#define GST_B128_SC1(voff, v, sb, off) gst_b128_sc1_<(off)>(voff, v, sb)
#define GST_B64_SC1(voff, v, sb) gst_b64_sc1_<0>(voff, v, sb)
__device__ __forceinline__ void dma_(unsigned ldsaddr, unsigned voff, const char* sb) {
    asm volatile("s_mov_b32 m0, %0\n\ts_nop 0\n\tglobal_load_lds_dwordx4 %1, %2" :: "s"(ldsaddr), "v"(voff), "s"(sb) : "memory"); }
__device__ __forceinline__ void dma_nt_(unsigned ldsaddr, unsigned voff, const char* sb) {
    asm volatile("s_mov_b32 m0, %0\n\ts_nop 0\n\tglobal_load_lds_dwordx4 %1, %2 nt" :: "s"(ldsaddr), "v"(voff), "s"(sb) : "memory"); }
#define DMA(ldsaddr, voff, sb) dma_(ldsaddr, voff, sb)
template <int OFF, class T> __device__ __forceinline__ void dsr128_(T& dst, unsigned addr) { asm volatile("ds_read_b128 %0, %1 offset:%2" : "=v"(dst) : "v"(addr), "n"(OFF)); }
template <int OFF, class T> __device__ __forceinline__ void dsr64_(T& dst, unsigned addr) { asm volatile("ds_read_b64 %0, %1 offset:%2" : "=v"(dst) : "v"(addr), "n"(OFF)); }
#define DSR128(dst, addr, off) dsr128_<(off)>(dst, addr)
#define DSR64(dst, addr, off) dsr64_<(off)>(dst, addr)

__device__ __forceinline__ void kv4(float* t, const float* e, float w0, float w1, float w2, float w3, const float* v) {
    float p0, p1, p2, p3;
    asm(
        "v_mul_f32 %0, %8, %18\n\t"
        "v_mul_f32 %1, %9, %18\n\t"
        "v_mul_f32 %2, %10, %18\n\t"
        "v_mul_f32 %3, %11, %18\n\t"
        "v_cvt_pk_bf16_f32 %0, 0, %0\n\t"
        "v_cvt_pk_bf16_f32 %1, 0, %1\n\t"
        "v_cvt_pk_bf16_f32 %2, 0, %2\n\t"
        "v_cvt_pk_bf16_f32 %3, 0, %3\n\t"
        "v_mul_f32 %4, %9, %17\n\t"
        "v_mul_f32 %5, %10, %17\n\t"
        "v_mul_f32 %6, %11, %17\n\t"
        "v_mul_f32 %7, %12, %17\n\t"
        "v_cvt_pk_bf16_f32 %4, 0, %4\n\t"
        "v_cvt_pk_bf16_f32 %5, 0, %5\n\t"
        "v_cvt_pk_bf16_f32 %6, 0, %6\n\t"
        "v_cvt_pk_bf16_f32 %7, 0, %7\n\t"
        "v_add_f32 %0, %0, %4\n\t"
        "v_add_f32 %1, %1, %5\n\t"
        "v_add_f32 %2, %2, %6\n\t"
        "v_add_f32 %3, %3, %7\n\t"
        "v_cvt_pk_bf16_f32 %0, 0, %0\n\t"
        "v_cvt_pk_bf16_f32 %1, 0, %1\n\t"
        "v_cvt_pk_bf16_f32 %2, 0, %2\n\t"
        "v_cvt_pk_bf16_f32 %3, 0, %3\n\t"
        "v_mul_f32 %4, %10, %16\n\t"
        "v_mul_f32 %5, %11, %16\n\t"
        "v_mul_f32 %6, %12, %16\n\t"
        "v_mul_f32 %7, %13, %16\n\t"
        "v_cvt_pk_bf16_f32 %4, 0, %4\n\t"
        "v_cvt_pk_bf16_f32 %5, 0, %5\n\t"
        "v_cvt_pk_bf16_f32 %6, 0, %6\n\t"
        "v_cvt_pk_bf16_f32 %7, 0, %7\n\t"
        "v_add_f32 %0, %0, %4\n\t"
        "v_add_f32 %1, %1, %5\n\t"
        "v_add_f32 %2, %2, %6\n\t"
        "v_add_f32 %3, %3, %7\n\t"
        "v_cvt_pk_bf16_f32 %0, 0, %0\n\t"
        "v_cvt_pk_bf16_f32 %1, 0, %1\n\t"
        "v_cvt_pk_bf16_f32 %2, 0, %2\n\t"
        "v_cvt_pk_bf16_f32 %3, 0, %3\n\t"
        "v_mul_f32 %4, %11, %15\n\t"
        "v_mul_f32 %5, %12, %15\n\t"
        "v_mul_f32 %6, %13, %15\n\t"
        "v_mul_f32 %7, %14, %15\n\t"
        "v_cvt_pk_bf16_f32 %4, 0, %4\n\t"
        "v_cvt_pk_bf16_f32 %5, 0, %5\n\t"
        "v_cvt_pk_bf16_f32 %6, 0, %6\n\t"
        "v_cvt_pk_bf16_f32 %7, 0, %7\n\t"
        "v_add_f32 %0, %0, %4\n\t"
        "v_add_f32 %1, %1, %5\n\t"
        "v_add_f32 %2, %2, %6\n\t"
        "v_add_f32 %3, %3, %7\n\t"
        "v_cvt_pk_bf16_f32 %0, 0, %0\n\t"
        "v_cvt_pk_bf16_f32 %1, 0, %1\n\t"
        "v_cvt_pk_bf16_f32 %2, 0, %2\n\t"
        "v_cvt_pk_bf16_f32 %3, 0, %3\n\t"
        "v_mul_f32 %0, %0, %19\n\t"
        "v_mul_f32 %1, %1, %20\n\t"
        "v_mul_f32 %2, %2, %21\n\t"
        "v_mul_f32 %3, %3, %22\n\t"
        : "=&v"(t[0]), "=&v"(t[1]), "=&v"(t[2]), "=&v"(t[3]), "=&v"(p0), "=&v"(p1), "=&v"(p2), "=&v"(p3)
        : "v"(e[0]), "v"(e[1]), "v"(e[2]), "v"(e[3]), "v"(e[4]), "v"(e[5]), "v"(e[6]), "v"(w0), "v"(w1), "v"(w2), "v"(w3), "v"(v[0]), "v"(v[1]), "v"(v[2]), "v"(v[3]));
}
__device__ __forceinline__ void lb_post(unsigned myaddr, unsigned seq) { asm volatile("ds_write_b32 %0, %1" :: "v"(myaddr), "v"(seq) : "memory"); }
__device__ __forceinline__ void lb_wait(unsigned addr, unsigned seq) {
    unsigned v;
    asm volatile("s_branch 2f\n1:\n\ts_sleep 1\n2:\n\tds_read_b32 %0, %1\n\ts_waitcnt lgkmcnt(0)\n\tv_cmp_gt_u32 vcc, %2, %0\n\ts_cbranch_vccnz 1b" : "=&v"(v) : "v"(addr), "v"(seq) : "vcc", "memory");
}

constexpr size_t WS_H = 0, WS_KVT = (size_t)10 << 20, WS_ZT = (size_t)15 << 20, WS_E = (size_t)20 << 20, WS_FLAG = WS_E + (256 << 10);
#define HP 65536
#define KP 8192
#define ZP 4096
constexpr int FL_KV = 0, FL_Z = 65536, FL_H = 131072, FL_EPOCH = 196608;
constexpr unsigned SPIN_CAP = 1u << 17;
#define ABL 0
#define CSPLIT 1
#define MERGE 3
#define CB 13
#define PAUSE 0
#define QLATE 0
#define CVPRIO 0
#define STG_SEL (idx & 3)
#define SP0 3
#define SDP 1
#define LP0 3
#define LDP 2
#define KVS 5
#define KVU 2
#define QIT 1
#define ZB 3
#define OS 2
#define MPRIO 2

#define ROT 0x80A5
#define R2 0x41
#define CPF 0
#define PRE 4
#define PRO 12
#define G1NT 0
#define G1NTS 16
#define TMIX 0
#define TLBT 0
#define CDEF 0
#define CVLATE 1
#define FPRIO 3
#define YS 12
#define DP0 1
#define DP1 5
#define AWB 1
constexpr int G1STAGE = 32768;
constexpr int L_KS = 131072, L_VS = 147456;
constexpr int CST = 4288;
constexpr int UOFF = 4 * CST, UPL = 2304;
constexpr int CHSZ = UOFF + 2 * UPL + 64;
constexpr int L_AGG = 4 * CHSZ, L_ES = L_AGG + 4096, L_SY = L_ES + 4096;
constexpr int L_HT = 98304;
constexpr int L_C = 98304, G2STAGE = 16384;
constexpr int L_ZI = 0;
constexpr int L_Z2 = 20480;
constexpr int L_Y = 40960, L_Y2 = 131072;
static_assert(L_SY + 64 <= L_HT, "lds");
static_assert(L_Y + 32768 <= L_AGG && L_Z2 + 64 * 264 <= L_Y, "lds");

__device__ __forceinline__ int img_off(int r, int c) { return (r >> 1) * 256 + ((((r & 1) * 8 + c) ^ ((r >> 1) & 15)) << 4); }

constexpr int g1_upto(int g) {
    int n = PRO;
    for (int t = 0; t <= g; ++t) {
        int sb = (t + 1) / 2, sub = (t + 1) % 2, allowed = sb >= 1 ? 4 * (sb + 3) + 2 * (sub + 1) : 16;
        if (allowed > 64) allowed = 64;
        n = n + 4 < allowed ? n + 4 : allowed;
    }
    return n;
}
constexpr int g1_cnt(int s) { return g1_upto(2 * s - 2) - 4 * (s + 1); }
static_assert(g1_upto(24) == 64, "schedule");
constexpr int AW = 8;
constexpr bool g2_dma(int g) { return (g + 1) / 2 >= 2 && (g + 1) / 2 + 3 < 16; }
constexpr int g2_cnt(int id, int at) {
    int n = -1000;
    for (int f = 4; f < 4 + AW; ++f) { ++n; if (f == id) n = 0; }
    for (int f = 4; f < at; ++f) {
        if ((f & 1) && g2_dma(f >> 1)) { ++n; if (100 + (f >> 1) == id) n = 0; }
        if (f + AW < 64) { ++n; if (f + AW == id) n = 0; }
    }
    return n;
}

constexpr int cv_m(int o) { return (o & 1) ? 7 - (o >> 1) : o >> 1; }
constexpr int cv_first(int o) { int p = 0; for (int i = 0; i < o; ++i) p += 8 - cv_m(i); return p; }
constexpr int cv_grp(int p) { int o = 0; while (cv_first(o + 1) <= p) ++o; return o; }
constexpr int cv_read(int p) {
    int slot[40] = {};
    for (int i = 0; i < 40; ++i) slot[i] = -1;
    slot[0] = 2 * 3 + 1;
    for (int o = 0; o < 8; ++o) {
        int q = cv_first(o + 1) - 1;
        for (int h = 0; h < 2; ++h) { while (slot[q] >= 0) ++q; slot[q] = 2 * (o + 4) + h; }
    }
    const int bs[8] = {1, 3, 5, 11, 12, 13, 19, 21};
    for (int jb = 0; jb < 8; ++jb) slot[bs[jb]] = 32 + jb;
    return slot[p];
}
static_assert(cv_read(35) == 22 && cv_read(36) == 23 && cv_read(37) == -1 && cv_read(10) == 11 && cv_read(15) == 12 && cv_read(18) == 15 && cv_read(24) == 17, "conv schedule");
constexpr int cv_wait(int o) {
    int n = 0;
    for (int q = cv_first(o) - 1;; --q) {
        const int rd = cv_read(q < 0 ? q + 36 : q);
        if (rd == 2 * (o + (q < 0 ? 8 : 0)) + 1) return n;
        if (rd >= 0) ++n;
    }
}

struct Args {
    const char *q, *k, *v, *sk, *ssmk, *ssmD, *x, *lre, *lim, *bre, *bim, *cre, *cim, *logdt, *dskip;
    char *out, *ws;
};

__global__ __launch_bounds__(768, 3) void fused(Args a) {
    __shared__ __attribute__((aligned(1024))) char shm[163840];
    const char* pc0;
    int code_a, code_b;
    asm volatile("s_getpc_b64 %0\n.Lfk_code_0:\n\ts_mov_b32 %1, .Lfk_code_a-.Lfk_code_0\n\ts_mov_b32 %2, .Lfk_code_b-.Lfk_code_0" : "=s"(pc0), "=s"(code_a), "=s"(code_b));
    char* const ws = a.ws;
    TS_DECL TS(0);
    const int tid = threadIdx.x, lane = tid & 63, wid = __builtin_amdgcn_readfirstlane(tid >> 6);
    const int b = blockIdx.x, xcd = b & 7, idx = b >> 3;
    const int tm = TMIX ? (idx >> 3) * 4 + (xcd >> 1) : (xcd >> 1) * 4 + (idx >> 3), tn = (xcd & 1) * 8 + (idx & 7);
    const int wm = wid & 3, wn = (wid >> 2) & 1, l31 = lane & 31, lh = lane >> 5;
    const unsigned lb = (unsigned)(uintptr_t)shm;
    const char* const flg = ws + WS_FLAG;
    const unsigned l16 = lane * 16;
    const int rot2 = (((R2 >> 4) & 15) * (idx >> 3) + (R2 & 15) * (xcd >> 1) + ((R2 >> 8) & 15) * (xcd & 1) + (R2 >> 12) * (idx & 7)) & 15;
    unsigned slo, shi;
    {
        const unsigned long long id = 0xFEDCBA9876543210ull, seq = rot2 ? (id >> (4 * rot2)) | (id << (64 - 4 * rot2)) : id;
        const int es = (tn - rot2) & 15;
        const unsigned long long lo = (1ull << (4 * es)) - 1, tab = ((seq & lo) << 4) | (seq & ~((lo << 4) | 15)) | tn;
        slo = (unsigned)tab; shi = (unsigned)(tab >> 32);
    }
#define SLICE(st) ((((st) < 8 ? slo : shi) >> (4 * ((st) & 7))) & 15)
    typedef __attribute__((ext_vector_type(4))) short v4i16;

    if (wid >= 8) {
        const int chn = xcd * 128 + idx * 4 + wm;
        const unsigned cb = lb + wm * CHSZ;
        const int r = lane & 15, g4 = lane >> 4;
        const unsigned fpub = xcd * 256 + idx * 8, fpoll = xcd * 256 + l31 * 8;
        __builtin_amdgcn_s_setprio(3);
        u32x2 ep, fl, kw[2]; unsigned hal[2], dskl; u32x4 sw[4], uu[4];
        unsigned cpf = 0;
        if (wm == 3) {
            if (lane < 16 && (idx * 16 + lane) * 128 < (code_a > code_b ? code_a : code_b)) GLD_B32(cpf, (idx * 16 + lane) * 128, pc0, 0);
        }
        {
            const unsigned ho = (idx ? idx * 64 - 4 + g4 : 0) * 2048 + xcd * 256 + wm * 64 + r * 2;
            gld_u16_<0>(hal[0], ho, a.k); gld_u16_<32>(hal[1], ho, a.k);
            GLD_B64_SC1(ep, 0u * lane, flg + FL_EPOCH, 0);
            const unsigned c0 = xcd * 128 + wm * 32 + r;
            GLD_B64(kw[0], c0 * 8, a.sk, 0); GLD_B64(kw[1], c0 * 8, a.sk, 128);
            GLD_U16(dskl, 0u * lane, a.ssmD + chn * 2);
        }
        TS(1);
        WAIT_V(0);
        BAR();
        TS(2);
        TIE(ep); TIE(kw[0]); TIE(kw[1]); TIE(hal[0]); TIE(hal[1]); TIE(dskl); TIE(cpf);
        const unsigned e0 = __builtin_amdgcn_readfirstlane(ep[0]), e1 = __builtin_amdgcn_readfirstlane(ep[1]);
        const unsigned tagA = e0 + 1, tagB = e1 + (tagA == 0 ? 1u : 0u);
        const u32x2 tag = {tagA, tagB};
        u32x4 kvo[4];
        {
            const int q = r >> 2, pp = r & 3;
            u32x2 kt[4][3], vt[4][2];
            unsigned hm[2][3];
#pragma unroll
            for (int u = 0; u < 4; ++u) {
                const int p = u >> 1, oh = u & 1;
                auto tr = [&](int base, int row) -> u32x2 {
                    const int rr = row + q, ch = (wm * 4 + p * 2 + (pp >> 1)) ^ (2 * (q | (((rr >> 3) & 1) << 2)));
                    return __builtin_bit_cast(u32x2, __builtin_amdgcn_ds_read_tr16_b64_v4i16(LDSP(v4i16, lb + base + rr * 256 + ch * 16 + (pp & 1) * 8)));
                };
                const int row0 = (g4 + 4 * oh) * 8;
                kt[u][0] = tr(L_KS, (oh == 0 && g4 == 0) ? 0 : row0 - 4);
                kt[u][1] = tr(L_KS, row0); kt[u][2] = tr(L_KS, row0 + 4); vt[u][0] = tr(L_VS, row0); vt[u][1] = tr(L_VS, row0 + 4);
                if (oh == 0) { hm[p][0] = __shfl(hal[p], r + 16); hm[p][1] = __shfl(hal[p], r + 32); hm[p][2] = __shfl(hal[p], r + 48); }
            }
            SB();
            TS(14);
            const int sl = ((lane & 3) * 16 + (lane >> 2)) * 4;
#pragma unroll
            for (int u = 0; u < 4; ++u) {
                const int p = u >> 1, oh = u & 1;
                u32x2 k0 = kt[u][0];
                const u32x2 k1 = kt[u][1], k2 = kt[u][2], v0 = vt[u][0], v1 = vt[u][1];
                if (oh == 0 && g4 == 0) k0 = idx ? u32x2{hm[p][0] << 16, hm[p][1] | (hm[p][2] << 16)} : u32x2{0u, 0u};
                const float w0 = bflo(kw[p][0]), w1 = bfhi(kw[p][0]), w2 = bflo(kw[p][1]), w3 = bfhi(kw[p][1]);
                const float e[11] = {bfhi(k0[0]), bflo(k0[1]), bfhi(k0[1]), bflo(k1[0]), bfhi(k1[0]), bflo(k1[1]), bfhi(k1[1]), bflo(k2[0]), bfhi(k2[0]), bflo(k2[1]), bfhi(k2[1])};
                const float vf[8] = {bflo(v0[0]), bfhi(v0[0]), bflo(v0[1]), bfhi(v0[1]), bflo(v1[0]), bfhi(v1[0]), bflo(v1[1]), bfhi(v1[1])};
                float t[8];
                if (ABL & 2) { for (int i = 0; i < 8; ++i) t[i] = e[i]; } else
                kv4(t, e, w0, w1, w2, w3, vf);
                if (u > KVU) { TIE(t[0]); BAR(); }
                if (!(ABL & 2)) kv4(t + 4, e + 4, w0, w1, w2, w3, vf + 4);
                const u32x4 o = {pack2(t[7], t[6]), pack2(t[5], t[4]), pack2(t[3], t[2]), pack2(t[1], t[0])};
#pragma unroll
                for (int j = 0; j < 4; ++j) kvo[u][j] = __builtin_amdgcn_ds_bpermute(sl, o[j]);
                SB();
                if (u == 0) TS(15); if (u == 1) TS(16); if (u == 2) TS(17); if (u == 3) TS(18);
                if (u >= KVU) BAR();
            }
#pragma unroll
            for (int u = 0; u < 4; ++u) TIE(kvo[u]);
        }
        TS(3);
        u32x2 qv[8];
        const char* krow = a.ssmk + (size_t)chn * 4096;
        const char* qs = a.q + (size_t)(idx * 64 + wm * 16) * 2048 + xcd * 256;
        {
            const int ph = STG_SEL, ps = SP0 + ph * SDP, pl = LP0 + ph * LDP;
            const char* kvd = ws + WS_KVT + (size_t)(xcd * 64 + wm * 16) * KP + idx * 256;
            int nb1 = 1 + 2 * (3 - KVU);
#pragma unroll 1
            for (; nb1 <= ps; ++nb1) BAR();
            static_for<0, 4>([&](auto uc) {
                constexpr int u = decltype(uc)::value;
                GST_B128_SC1((lane >> 3) * KP + (lane & 7) * 16, kvo[u], kvd + (u >> 1) * 8 * KP, (u & 1) * 128);
            });
#pragma unroll 1
            for (; nb1 <= pl; ++nb1) {
                if (nb1 == ps + 4) WAIT_V(0);
                BAR();
                if (nb1 == ps + 4 && wm == 0 && lane == 0) GST_B64_SC1(fpub, tag, flg + FL_KV);
            }
            if (MERGE & 1) { GLD_B128_NT(sw[0], l16, krow, 0); GLD_B128_NT(sw[1], l16, krow, 1024); GLD_B128_NT(sw[2], l16, krow, 2048); GLD_B128_NT(sw[3], l16, krow, 3072); }
            if (MERGE & 2) static_for<0, 8>([&](auto jc) { constexpr int j = decltype(jc)::value; GLD_B64_NT(qv[j], lh * 2048 + l31 * 8, qs + j * 4096, 0); });
#pragma unroll 1
            for (; nb1 <= 12; ++nb1) {
                if (nb1 == ps + 4) WAIT_V(((MERGE & 1) ? 4 : 0) + ((MERGE & 2) ? 8 : 0));
                BAR();
                if (nb1 == ps + 4 && wm == 0 && lane == 0) GST_B64_SC1(fpub, tag, flg + FL_KV);
            }
        }
        if (ABL & 1024) fl = tag; else GLD_B64_SC1(fl, fpoll, flg + FL_KV, 0);
        BAR();
        WAIT_V(0); TIE(fl);
        const char* usrc = ws + WS_KVT + (size_t)(xcd * 64 + idx * 2 + (wm >> 1)) * KP + (wm & 1) * 4096;
        if (!(ABL & 16)) { GLD_B128_SC1(uu[0], l16, usrc, 0); GLD_B128_SC1(uu[1], l16, usrc, 1024); GLD_B128_SC1(uu[2], l16, usrc, 2048); GLD_B128_SC1(uu[3], l16, usrc, 3072); }
        if (!(MERGE & 1)) {
            GLD_B128_NT(sw[0], l16, krow, 0); GLD_B128_NT(sw[1], l16, krow, 1024); GLD_B128_NT(sw[2], l16, krow, 2048); GLD_B128_NT(sw[3], l16, krow, 3072);
        }
        if (CB == 14) BAR();
        {
            const char* cs = ((wm & 2) ? a.cim : a.cre) + (size_t)(tn * 64 + (wm & 1) * 2) * 2048;
#pragma unroll
            for (int i = 0; i < 4; ++i) {
                const int pair = i * 4 + g4, v = r ^ pair, row = pair * 2 + (v >> 3);
                const unsigned o = ((row & 15) * 4 + (row >> 4)) * 2048 + (v & 7) * 16;
                DMA(lb + L_C + 2 * G2STAGE + wm * 4096 + i * 1024, o, cs + (SLICE(0) << 7));
                DMA(lb + L_C + 3 * G2STAGE + wm * 4096 + i * 1024, o, cs + (SLICE(1) << 7));
                if (i == CSPLIT) BAR();
            }
        }
        if (CB == 13) BAR();
        TS(4);
        const unsigned ud = lb + ((wm & 2) + ((lane >> 2) & 1)) * CHSZ + UOFF + (lane & 1) * UPL + (16 + (wm & 1) * 64 + g4 * 4 + ((lane >> 3) & 1) * 2 + ((lane >> 1) & 1)) * 16;
        {
            const u32x4 z4 = {0, 0, 0, 0};
            if (lane < 16) *LDSP(unsigned, lb + L_SY + lane * 4) = 0;
            *LDSP(u32x4, lane < 32 ? cb + (lane >> 3) * CST + (lane & 7) * 16 : cb + UOFF + (g4 & 1) * UPL + r * 16) = z4;
        }
        WAIT_L(0);
        TS(5);
        BAR();
        __builtin_amdgcn_s_setprio(FPRIO);
        TS(11);
        WAIT_V(8);
        TS(12);
        if (!(ABL & 32)) {
#pragma unroll
            for (int it = 0; it < 4; ++it) TIE(sw[it]);
#pragma unroll
            for (int it = 0; it < 4; ++it) {
                const unsigned q = cb + 128 + it * 1024 + l16;
                const unsigned d0 = sw[it][0], d1 = sw[it][1], d2 = sw[it][2], d3 = sw[it][3];
                const unsigned o0 = it ? __builtin_amdgcn_readlane(sw[it ? it - 1 : 0][2], 63) : 0u, o1 = it ? __builtin_amdgcn_readlane(sw[it ? it - 1 : 0][3], 63) : 0u;
                const unsigned x0 = __builtin_amdgcn_update_dpp(o0, d2, 0x138, 0xf, 0xf, false), x1 = __builtin_amdgcn_update_dpp(o1, d3, 0x138, 0xf, 0xf, false);
                const unsigned a0 = __builtin_amdgcn_alignbyte(d0, x1, 2), a1 = __builtin_amdgcn_alignbyte(d1, d0, 2), a2 = __builtin_amdgcn_alignbyte(d2, d1, 2),
                               a3 = __builtin_amdgcn_alignbyte(d3, d2, 2);
                *LDSP(u32x4, q) = sw[it];
                *LDSP(u32x4, q + CST) = u32x4{a0, a1, a2, a3};
                *LDSP(u32x4, q + 2 * CST) = u32x4{x1, d0, d1, d2};
                *LDSP(u32x4, q + 3 * CST) = u32x4{__builtin_amdgcn_alignbyte(x1, x0, 2), a0, a1, a2};
                if (it == 3 && lane == 63) {
                    *LDSP(u32x2, q + 16 + CST) = u32x2{d3 >> 16, 0u};
                    *LDSP(u32x2, q + 16 + 2 * CST) = u32x2{d3, 0u};
                    *LDSP(u32x2, q + 16 + 3 * CST) = u32x2{a3, d3 >> 16};
                }
            }
        }
        TS(13);
#pragma unroll
        for (int it = 0; it < 4; ++it) TIE(uu[it]);
        if (!__all(fl[0] == tagA && fl[1] == tagB)) {
            for (unsigned spin = 0; spin < SPIN_CAP; ++spin) {
                GLD_B64_SC1(fl, fpoll, flg + FL_KV, 0);
                WAIT_V(0); TIE(fl);
                if (__all(fl[0] == tagA && fl[1] == tagB)) break;
                __builtin_amdgcn_s_sleep(8);
            }
            GLD_B128_SC1(uu[0], l16, usrc, 0); GLD_B128_SC1(uu[1], l16, usrc, 1024); GLD_B128_SC1(uu[2], l16, usrc, 2048); GLD_B128_SC1(uu[3], l16, usrc, 3072);
            WAIT_V(0);
#pragma unroll
            for (int it = 0; it < 4; ++it) TIE(uu[it]);
        }
#pragma unroll
        for (int it = 0; it < 4; ++it) *LDSP(u32x4, ud + it * 256) = uu[it];
        lb_post(lb + L_SY + 32 + wm * 4, 1); lb_wait(lb + L_SY + 32 + (wm ^ 1) * 4, 1);
        __builtin_amdgcn_s_setprio(CVPRIO);
        TS(6);
        f32x4 ca[8];
        {
            unsigned kb = cb + ((3 - r) & 3) * CST + 2 * (64 + 16 + r - 8 * g4 - 7 + ((3 - r) & 3));
            unsigned ub = cb + UOFF + (g4 & 1) * UPL + (15 + r + (g4 >> 1)) * 16 - 64;
#pragma unroll
            for (int i = 0; i < 8; ++i) ca[i] = f32x4{0, 0, 0, 0};
            s16x4 FA[4][2]; bf16x8 FB[2][8];
#define CV_A(s, o, e) DSR64(FA[(o) & 3][e], kb, 64 * ((s) + ((o) >> 3)) + 512 * cv_m((o) & 7) + 8 * (e))
#define CV_B(s, jb) DSR128(FB[(s) & 1][jb], ub, 64 + 256 * (jb) - 32 * (s))
            static_for<0, 8>([&](auto ic) { constexpr int n = decltype(ic)::value; CV_B(0, n); CV_A(0, n >> 1, n & 1); });
            WAIT_L(0);
            auto body = [&]() {
                static_for<0, 72>([&](auto ic) {
                    constexpr int s = decltype(ic)::value / 36, p = decltype(ic)::value % 36, o = cv_grp(p), m = cv_m(o), jb = p - cv_first(o);
                    if constexpr (jb == 0) { WAIT_L(cv_wait(o)); TIE(FA[o & 3][0]); TIE(FA[o & 3][1]); }
                    if constexpr (p == 0) { for (int i = 0; i < 8; ++i) TIE(FB[s][i]); }
                    const bf16x8 fa = __builtin_shufflevector(FA[o & 3][0], FA[o & 3][1], 0, 1, 2, 3, 4, 5, 6, 7);
                    ca[jb + m] = __builtin_amdgcn_mfma_f32_16x16x32_bf16(fa, FB[s][jb], ca[jb + m], 0, 0, 0);
                    SB();
                    constexpr int rd = cv_read(p);
                    if constexpr (rd >= 32) CV_B(s + 1, rd - 32);
                    else if constexpr (rd >= 0) CV_A(s, rd >> 1, rd & 1);
                    SB();
                });
                kb += 128; ub -= 64;
            };
#pragma unroll 1
            for (int it = 0; it < QIT; ++it) body();
            {
                WAIT_V(0); TIE(cpf);
                lb_post(lb + L_SY + 32 + wm * 4, 2);
                WAIT_L(0);
                if (CVLATE) lb_wait(lb + L_SY + (lane & 7) * 4, 2);
            }
#pragma unroll 1
            for (int it = QIT; it < 4; ++it) body();
            WAIT_L(0);
#pragma unroll
            for (int i = 0; i < 4; ++i) { TIE(FA[i][0]); TIE(FA[i][1]); }
#pragma unroll
            for (int i = 0; i < 8; ++i) TIE(FB[0][i]);
#undef CV_A
#undef CV_B
        }
        TS(7);
        __builtin_amdgcn_s_setprio(3);
        if ((MERGE & 6) == 4) static_for<0, 8>([&](auto jc) { constexpr int j = decltype(jc)::value; GLD_B64_NT(qv[j], lh * 2048 + l31 * 8, qs + j * 4096, 0); });
        int nb = 1;
#define HB() do { if (nb < 15) BAR(); ++nb; } while (0)
        {
            const float Dd = bf2f(dskl);
            const char* zd = ws + WS_ZT + (size_t)chn * ZP;
            u32x2 kvr[8];
#pragma unroll
            for (int jt = 0; jt < 8; ++jt) kvr[jt] = *LDSP(u32x2, cb + UOFF + (g4 >> 1) * UPL + (16 + 16 * jt + r) * 16 + (1 - (g4 & 1)) * 8);
#pragma unroll
            for (int jt = 0; jt < 8; ++jt) {
                const f32x4 y = ca[jt];
                const u32x2 kq = kvr[jt];
                *LDSP(u32x2, cb + (jt * 4 + (r >> 2)) * 144 + (r & 3) * 32 + g4 * 8) =
                    u32x2{pack2(rb1(y[0]) + rb1(bfhi(kq[1]) * Dd), rb1(y[1]) + rb1(bflo(kq[1]) * Dd)), pack2(rb1(y[2]) + rb1(bfhi(kq[0]) * Dd), rb1(y[3]) + rb1(bflo(kq[0]) * Dd))};
            }
            if (ZB >= 3) HB();
            u32x4 t[4];
#pragma unroll
            for (int it = 0; it < 4; ++it) t[it] = *LDSP(u32x4, cb + (it * 8 + (lane >> 3)) * 144 + (lane & 7) * 16);
            if (!(ABL & 128)) { GST_B128_SC1(l16, t[0], zd, 0); GST_B128_SC1(l16, t[1], zd, 1024); GST_B128_SC1(l16, t[2], zd, 2048); GST_B128_SC1(l16, t[3], zd, 3072); }
            else { TIE(t[0]); TIE(t[1]); TIE(t[2]); TIE(t[3]); }
        }
        if (ZB >= 2) HB();
        if (ZB >= 1) HB();
        WAIT_V(0);
        lb_post(lb + L_SY + 32 + wm * 4, 3);
        if (wm == 0) {
            lb_wait(lb + L_SY + 32 + (lane & 3) * 4, 3);
            if (!(ABL & 1024) && lane == 0) GST_B64_SC1(fpub, tag, flg + FL_Z);
        }
        TS(8);
        HB(); HB(); HB();
        if (!(ABL & 1024) && wm == 0) GLD_B64_SC1(fl, fpoll, flg + FL_Z, 0);
        HB(); HB();
        if (wm == 0) { WAIT_V(0); TIE(fl); lb_post(lb + L_SY + 48, __all(fl[0] == tagA && fl[1] == tagB) ? 2 : 1); }
        lb_wait(lb + L_SY + 48, 1);
        if (__builtin_amdgcn_readfirstlane(*LDSP(unsigned, lb + L_SY + 48)) != 2) {
            while (nb < 15) { BAR(); ++nb; }
            for (unsigned spin = 0; spin < SPIN_CAP; ++spin) {
                GLD_B64_SC1(fl, fpoll, flg + FL_Z, 0);
                WAIT_V(0); TIE(fl);
                if (__all(fl[0] == tagA && fl[1] == tagB)) break;
                __builtin_amdgcn_s_sleep(8);
            }
        }
        u32x4 zv[4];
        {
            const char* zsrc = ws + WS_ZT + (size_t)(xcd * 128 + wm * 32) * ZP + idx * 128;
#pragma unroll
            for (int it = 0; it < 4; ++it) if (!(ABL & 256)) GLD_B128_SC1(zv[it], (lane >> 3) * ZP + (lane & 7) * 16, zsrc + it * 8 * ZP, 0);
        }
        HB(); HB();
        WAIT_V(0);
        TS(9);
        {
            const unsigned zi = lb + L_ZI + wm * 5120;
#pragma unroll
            for (int it = 0; it < 4; ++it) { TIE(zv[it]); *LDSP(u32x4, zi + ((lane >> 3) + 8 * it) * 160 + (lane & 7) * 16) = zv[it]; }
            v4i16 z[8];
#pragma unroll
            for (int n = 0; n < 8; ++n) z[n] = __builtin_amdgcn_ds_read_tr16_b64_v4i16(LDSP(v4i16, zi + ((n & 1) * 16 + (lane >> 2)) * 160 + (n >> 1) * 32 + (lane & 3) * 8));
            HB();
#pragma unroll
            for (int n = 0; n < 8; ++n) *LDSP(v4i16, lb + L_Z2 + ((n >> 1) * 16 + r) * 264 + wm * 64 + (n & 1) * 32 + g4 * 8) = z[n];
            lb_post(lb + L_SY + 32 + wm * 4, 4);
        }
        HB();
        lb_wait(lb + L_SY + 32 + (lane & 3) * 4, 4);
        char* const oa = a.out + (size_t)(idx * 64 + wm * 16) * 8192 + xcd * 512;
        u32x2 zz[8];
#pragma unroll
        for (int n = 0; n < 8; ++n) zz[n] = *LDSP(u32x2, lb + L_Z2 + (wm * 16 + n * 2 + lh) * 264 + l31 * 8);
#pragma unroll
        for (int n = 0; n < 8; ++n) {
            TIE(qv[n]);
            const u32x2 z = zz[n];
            const f32x4 o = {rb1(bflo(qv[n][0]) * bflo(z[0])), rb1(bfhi(qv[n][0]) * bfhi(z[0])), rb1(bflo(qv[n][1]) * bflo(z[1])), rb1(bfhi(qv[n][1]) * bfhi(z[1]))};
            if (!(ABL & 512)) GST_B128_NT(lh * 8192 + l31 * 16, o, oa + n * 16384, 0); else { f32x4 o2 = o; TIE(o2); }
            if (n < 7 && (n + 1) % OS == 0) HB();
        }
#undef HB
        asm volatile(".Lfk_code_a:");
        TS(10);
        TS_FLUSH();
        return;
    }

    const int l15 = lane & 15, g4 = lane >> 4, sq = 2 * wn + (wm & 1), st = sq * 16 + l15, run = (wm >> 1) * 4 + g4;
    unsigned c_dt, c_re, c_im;
    u32x2 ep, fl;
    {
        GLD_B64_SC1(ep, 0u * lane, flg + FL_EPOCH, 0);
        const unsigned po = (tn * 64 + st) * 2;
        GLD_U16(c_dt, po, a.logdt); GLD_U16(c_re, po, a.lre); GLD_U16(c_im, po, a.lim);
    }
    {
        const unsigned src = g4 * 2048 + (((lane & 15) ^ (2 * g4)) << 4);
        const char* ks = (wn ? a.v : a.k) + (size_t)(idx * 64 + wm * 16) * 2048 + xcd * 256;
#pragma unroll
        for (int i = 0; i < 4; ++i) { if (ABL & 1) break; dma_nt_(lb + L_KS + wid * 4096 + i * 1024, src ^ ((i >> 1) * 128), ks + i * 8192); }
    }
    unsigned dv[4];
#pragma unroll
    for (int i = 0; i < 4; ++i) {
        const int pair = i * 4 + g4, v = l15 ^ pair, row = pair * 2 + (v >> 3);
        dv[i] = (wid < 4 ? 16 * ((row >> 2) & 3) + 4 * (row >> 4) + (row & 3) : row) * 2048 + (v & 7) * 16;
    }
    const char* g1src = wid < 4 ? a.x + (size_t)(tm * 128 + (wid >> 1) * 64 + (wid & 1) * 8) * 2048
                                : ((wid & 1) ? a.bim : a.bre) + (size_t)(tn * 64 + ((wid >> 1) & 1) * 32) * 2048;
    const int rot = (((ROT >> 12) & 15) * (idx & 7) + ((ROT >> 8) & 15) * (idx >> 3) + ((ROT >> 4) & 15) * (xcd >> 1) + (ROT & 15) * (xcd & 1)) & 15;
    const unsigned g1dst = lb + wid * 4096;
#define G1_ISSUE(st, i) do { if (((G1NT >> (wid >> 2)) & 1) && (st) < G1NTS) dma_nt_(g1dst + ((st) & 3) * G1STAGE + (i) * 1024, dv[i], g1src + ((((st) + rot) & 15) << 7)); \
    else DMA(g1dst + ((st) & 3) * G1STAGE + (i) * 1024, dv[i], g1src + ((((st) + rot) & 15) << 7)); } while (0)
    static_for<0, PRE>([&](auto ic) { constexpr int n = decltype(ic)::value; G1_ISSUE(n >> 2, n & 3); });
    TS(14);
    float Lr, Li, cr, ci, L8r, L8i, L16r, L16i, L128r, L128i, Qr, Qi, Wr, Wi;
    unsigned tagA, tagB;
    {
        WAIT_V((ABL & 1) ? PRE : 4 + PRE);
        TIE(c_dt); TIE(c_re); TIE(c_im); TIE(ep);
        const float dt = rbf(expf(bf2f(c_dt)));
        const float lr = bf2f(c_re), li = bf2f(c_im);
        const float e = expf(dt * lr);
        float sn, cs;
        sincosf(dt * li, &sn, &cs);
        Lr = e * cs; Li = e * sn;
        const float nr = Lr - 1.f, ni = Li, den = lr * lr + li * li;
        cr = (nr * lr + ni * li) / den; ci = (ni * lr - nr * li) / den;
        float pr = Lr, pi = Li, tr, ti;
        Qr = cr; Qi = ci; Wr = 1.f; Wi = 0.f;
#pragma unroll
        for (int it = 1; it <= 10; ++it) {
            cmul(pr, pi, pr, pi, tr, ti); pr = tr; pi = ti;
            if (it == 3) { L8r = pr; L8i = pi; }
            if (it == 4) { L16r = pr; L16i = pi; }
            if (it == 7) { L128r = pr; L128i = pi; }
            if (it >= 4 && it <= 6) { cmul(Qr, Qi, pr, pi, tr, ti); if ((run >> (it - 4)) & 1) { Qr = tr; Qi = ti; } }
            if (it >= 8) { cmul(Wr, Wi, pr, pi, tr, ti); if (!((run >> (it - 8)) & 1)) { Wr = tr; Wi = ti; } }
        }
        TIE(Lr); TIE(Li); TIE(cr); TIE(ci); TIE(L8r); TIE(L8i); TIE(L16r); TIE(L16i); TIE(L128r); TIE(L128i); TIE(Qr); TIE(Qi); TIE(Wr); TIE(Wi);
        const unsigned e0 = __builtin_amdgcn_readfirstlane(ep[0]), e1 = __builtin_amdgcn_readfirstlane(ep[1]);
        tagA = e0 + 1; tagB = (e1 + (tagA == 0 ? 1u : 0u));
    }
    const unsigned tagG = tagA ^ tagB ^ 0x9E3779B9u;
    const u32x2 tag = {tagA, tagB};
    TS(1);
    WAIT_V(PRE);
    BAR();
    static_for<PRE, PRO>([&](auto ic) { constexpr int n = decltype(ic)::value; G1_ISSUE(n >> 2, n & 3); });
    TS(15);

    f32x4 acc[4][2];
    {
#pragma unroll
        for (int i = 0; i < 4; ++i) { acc[i][0] = f32x4{0, 0, 0, 0}; acc[i][1] = f32x4{0, 0, 0, 0}; }
        const int aoff = img_off((wm >> 1) * 64 + l15, g4), boff = img_off(128 + wn * 64 + (wm & 1) * 16 + l15, g4);
        unsigned aad[2][2], bad[2];
#pragma unroll
        for (int ks = 0; ks < 2; ++ks) { aad[ks][0] = lb + (aoff ^ (ks * 64)); aad[ks][1] = lb + (aoff ^ (ks * 64) ^ 128); bad[ks] = lb + (boff ^ (ks * 64)); }
        bf16x8 fa[2][4], fb[2][2];
        WAIT_V(PRO - 4); BAR();
        TS(2);
#define G1_FRAG(g, n) do { constexpr int bo_ = (((g) >> 1) & 3) * G1STAGE, hi_ = bo_ >= 65536 ? 65536 : 0, x_ = (n) < 3 ? (n) >> 1 : (n) - 2; \
        if constexpr ((n) == 0 || (n) == 2) DSR128(fb[(g) & 1][x_], bad[(g) & 1] + hi_, bo_ - hi_ + x_ * 4096); \
        else DSR128(fa[(g) & 1][x_], aad[(g) & 1][x_ & 1] + hi_, bo_ - hi_ + x_ * 2048); } while (0)
        static_for<0, 6>([&](auto ic) { constexpr int n = decltype(ic)::value; G1_FRAG(0, n); });
        static_for<0, 32>([&](auto ic) {
            constexpr int g = decltype(ic)::value, gn = g + 1, n0 = g1_upto(g - 1), nd = g1_upto(g) - n0;
            constexpr bool bar = (gn & 1) == 0 && gn < 32;
            if constexpr (bar) {
                static_assert(g1_cnt(gn >> 1) >= 0, "schedule");
                WAIT_V(g1_cnt(gn >> 1)); WAIT_L(0); BAR();
                if constexpr (((gn >> 1) & 3) == 0 || gn >= 26) TS(gn >= 26 ? 23 + (gn >> 1) - 13 : 16 + (gn >> 3));
#pragma unroll
                for (int i = 0; i < 4; ++i) TIE(fa[g & 1][i]);
                TIE(fb[g & 1][0]); TIE(fb[g & 1][1]);
            }
            static_for<0, 8>([&](auto mc) {
                constexpr int m = decltype(mc)::value, i = m >> 1, j = m & 1;
                if constexpr (!bar && (i == 0 || j == 0)) {
                    WAIT_L(5 - (i == 0 ? j + 1 : i + 2) + (gn < 32 ? (m < 6 ? m : 6) : 0));
                    TIE(fa[g & 1][i]); TIE(fb[g & 1][j]);
                }
                acc[i][j] = __builtin_amdgcn_mfma_f32_16x16x32_bf16(fa[g & 1][i], fb[g & 1][j], acc[i][j], 0, 0, 0);
                SB();
                if constexpr (gn < 32 && m < 6) G1_FRAG(gn, m);
                constexpr int d = nd > 2 ? ((m & 1) ? m >> 1 : -1) : (m == DP0 ? 0 : m == DP1 ? 1 : -1);
                if constexpr (d >= 0 && d < nd) G1_ISSUE((n0 + d) >> 2, (n0 + d) & 3);
                SB();
            });
        });
#undef G1_FRAG
    }
    TS(3);
    __builtin_amdgcn_s_setprio(MPRIO);
    float ur[16], ui[16];
    float hr, hi;
    unsigned pl8 = lb + L_AGG + st * 8;
    const unsigned pmine = lb + L_SY + wid * 4, pall = lb + L_SY + (lane & 7) * 4;
    const float nLi = -Li;
    TIE(pl8);
    {
        asm volatile("s_nop 7" : "+v"(acc[0][0]), "+v"(acc[0][1]), "+v"(acc[1][0]), "+v"(acc[1][1]), "+v"(acc[2][0]), "+v"(acc[2][1]), "+v"(acc[3][0]), "+v"(acc[3][1]));
#pragma unroll
        for (int r = 0; r < 16; ++r) { ur[r] = acc[r >> 2][0][r & 3]; ui[r] = acc[r >> 2][1][r & 3]; }
        float gr = ur[0], gi = ui[0], fr = ur[8], fi = ui[8];
#pragma unroll
        for (int r = 1; r < 8; ++r) {
            float nr, ni; cmad(nr, ni, Lr, Li, nLi, gr, gi, ur[r], ui[r]); gr = nr; gi = ni;
            cmad(nr, ni, Lr, Li, nLi, fr, fi, ur[8 + r], ui[8 + r]); fr = nr; fi = ni;
        }
        cmac(fr, fi, L8r, L8i, -L8i, gr, gi);
        *LDSP(f32x2, pl8 + run * 512) = f32x2{fr, fi};
        WAIT_L(0); BAR();
        f32x2 e[8];
#pragma unroll
        for (int j = 0; j < 8; ++j) e[j] = *LDSP(f32x2, pl8 + j * 512);
        const float nL16i = -L16i;
#pragma unroll
        for (int j = 1; j < 8; ++j) { float er = e[j][0], ei = e[j][1]; cmac(er, ei, L16r, L16i, nL16i, e[j - 1][0], e[j - 1][1]); e[j] = f32x2{er, ei}; }
        if (run == 0) {
            const u32x4 gq = {__float_as_uint(e[7][0]), tagA, __float_as_uint(e[7][1]), tagG};
            GST_B128_SC1((tm * 1024 + tn * 64 + st) * 16, gq, ws + WS_E, 0);
        }
        float cinr = 0.f, cini = 0.f;
#pragma unroll
        for (int j = 0; j < 7; ++j) { if (j + 1 == run) { cinr = e[j][0]; cini = e[j][1]; } }
        TS(4);
        const float ncr = -cr, nci = -ci;
        mul4(hr, hi, cr, nci, ncr, nci, cinr, cini);
#pragma unroll
        for (int r = 0; r < 16; ++r) { float nr, ni; mul4(nr, ni, cr, nci, ncr, nci, ur[r], ui[r]); ur[r] = nr; ui[r] = ni; TIE(ur[r]); TIE(ui[r]); }
        TIE(hr); TIE(hi);
    }
    unsigned hw[2], ta[2];
    {
        const unsigned ht = lb + L_HT + wid * 4096;
#pragma unroll
        for (int j = 0; j < 2; ++j) {
            const int pr = (8 * (g4 & 1) + 4 * j + (l15 >> 2)) ^ ((g4 & 1) * 4), pw = l15 ^ ((l15 >> 3) * 4);
            ta[j] = ht + (g4 >> 1) * 512 + pr * 32 + ((((l15 >> 1) ^ (pr >> 2)) & 1) << 4) + 8 * (lane & 1);
            hw[j] = ht + g4 * 1024 + pw * 32 + (((j ^ (pw >> 2)) & 1) << 4);
            TIE(ta[j]); TIE(hw[j]);
        }
    }
    {
        const int j0 = 2 * run - (16 - tm);
        float pr = 0.f, pi = 0.f;
        float nL128i = -L128i, nWi = -Wi;
        TIE(nL128i); TIE(nWi);
        if (8 * (wm >> 1) + 7 >= 16 - tm) {
            u32x4 eg[2];
            const unsigned eo0 = (j0 < 0 ? 0 : j0) * 16384 + (tn * 64 + st) * 16, eo1 = (j0 + 1 < 0 ? 0 : j0 + 1) * 16384 + (tn * 64 + st) * 16;
#pragma unroll 1
            for (unsigned spin = 0; spin < SPIN_CAP; ++spin) {
                GLD_B128_SC1(eg[0], eo0, ws + WS_E, 0); GLD_B128_SC1(eg[1], eo1, ws + WS_E, 0);
                WAIT_V(0); TIE(eg[0]); TIE(eg[1]);
                if (__all(eg[0][1] == tagA && eg[0][3] == tagG && eg[1][1] == tagA && eg[1][3] == tagG)) break;
                __builtin_amdgcn_s_sleep(2);
            }
            float e1r = j0 + 1 >= 0 ? __uint_as_float(eg[1][0]) : 0.f, e1i = j0 + 1 >= 0 ? __uint_as_float(eg[1][2]) : 0.f;
            const float e0r = j0 >= 0 ? __uint_as_float(eg[0][0]) : 0.f, e0i = j0 >= 0 ? __uint_as_float(eg[0][2]) : 0.f;
            cmac(e1r, e1i, L128r, L128i, nL128i, e0r, e0i);
            cmul4(pr, pi, Wr, Wi, nWi, e1r, e1i);
        }
        TS(5);
        *LDSP(f32x2, pl8 + (L_ES - L_AGG) + run * 512) = f32x2{pr, pi};
        lb_post(pmine, 1); lb_wait(pall, 1);
    }
    TS(6);
    {
        f32x2 e[8];
#pragma unroll
        for (int j = 0; j < 8; ++j) e[j] = *LDSP(f32x2, pl8 + (L_ES - L_AGG) + j * 512);
        const f32x2 G = ((e[0] + e[1]) + (e[2] + e[3])) + ((e[4] + e[5]) + (e[6] + e[7]));
        mac4(hr, hi, Qr, -Qi, -Qr, -Qi, G[0], G[1]);
#pragma unroll
        for (int r = 0; r < 16; ++r) { cmac(ur[r], ui[r], Lr, nLi, Li, hr, hi); hr = ur[r]; hi = ui[r]; }
        const char* hd = ws + WS_H + (size_t)((tm * 4 + (wm & 2)) * 2 + wn) * HP + (tn * 4 + (wm & 1) * 2) * 1024;
#pragma unroll
        for (int e2 = 0; e2 < 2; ++e2)
#pragma unroll
            for (int c = 0; c < 2; ++c) {
                const float* o = (c ? ui : ur) + 8 * e2;
                *LDSP(u32x4, hw[e2] + c * 512) = u32x4{pack2(o[0], o[1]), pack2(o[2], o[3]), pack2(o[4], o[5]), pack2(o[6], o[7])};
            }
        v4i16 t[4][2];
#pragma unroll
        for (int n = 0; n < 4; ++n)
#pragma unroll
            for (int j = 0; j < 2; ++j) t[n][j] = __builtin_amdgcn_ds_read_tr16_b64_v4i16(LDSP(v4i16, ta[j] + n * 1024));
        lb_post(pmine, 2);
        static_for<0, 4>([&](auto ic) {
            constexpr int n = decltype(ic)::value;
            const bf16x8 f = __builtin_shufflevector(t[n][0], t[n][1], 0, 1, 2, 3, 4, 5, 6, 7);
            GST_B128_SC1(l16, f, hd + (n >> 1) * 2 * HP + ((n & 1) ^ wn) * 1024, 0);
        });
    }
    TS(7);
    bf16x8 fa2[AW];
    u32x2 xv[4], dsv;
    const char* Aw = ws + WS_H + (size_t)((tm * 4 + wm) * 2 + wn) * HP;
    const char* g2src = (wn ? a.cim : a.cre) + (size_t)(tn * 64 + wm) * 2048;
    const unsigned g2dst = lb + L_C + wid * 2048;
    unsigned g2v[2];
#pragma unroll
    for (int i = 0; i < 2; ++i) {
        const int pair = i * 4 + (lane >> 4), v = (lane & 15) ^ pair ^ ((wm & 1) * 8);
        g2v[i] = (pair * 2 + (v >> 3)) * 8192 + (v & 7) * 16;
    }
    unsigned cad[2][2];
#pragma unroll
    for (int ks = 0; ks < 2; ++ks)
#pragma unroll
        for (int i = 0; i < 2; ++i) cad[ks][i] = lb + L_C + (img_off((g4 >> 1) * 64 + l15, 4 * wn + (g4 & 1)) ^ (ks * 32) ^ (i * 128));
#define G2_ISSUE(st, i) DMA(g2dst + (((st) + 2) & 3) * G2STAGE + (i) * 1024, g2v[i], g2src + (SLICE(st) << 7))
#define G2_A(g) GLD_B128_SC1(fa2[(g) % AW], l16, Aw + (SLICE((g) >> 2) << 12), ((g) & 3) * 1024)
    {
        const char* xs = a.x + (size_t)(tm * 128 + wm * 32 + wn * 16) * 2048 + tn * 128;
        static_for<0, 4>([&](auto ic) { constexpr int it = decltype(ic)::value; GLD_B64(xv[it], g4 * 8192 + l15 * 8, xs + (it >> 1) * 4096, (it & 1) * 2048); });
        GLD_B64(dsv, l15 * 8, a.dskip + tn * 128, 0);
        {
            lb_wait(lb + L_SY + (wid ^ 1) * 4, 2);
            bf16x8 fo[4];
#pragma unroll
            for (int n = 0; n < 4; ++n) {
                const int o = (n >> 1) * 4096 + ((n & 1) ^ wn) * 1024 - (wm & 1) * 2048;
                fo[n] = __builtin_shufflevector(__builtin_amdgcn_ds_read_tr16_b64_v4i16(LDSP(v4i16, ta[0] + o)), __builtin_amdgcn_ds_read_tr16_b64_v4i16(LDSP(v4i16, ta[1] + o)), 0, 1, 2, 3, 4, 5, 6, 7);
            }
            lb_wait(lb + L_SY + 32 + (lane & 3) * 4, 2);
#pragma unroll
            for (int ks = 0; ks < 2; ++ks)
#pragma unroll
                for (int i = 0; i < 4; ++i) {
                    const bf16x8 c = *LDSP(bf16x8, cad[ks][i & 1] + 2 * G2STAGE + i * 2048);
#pragma unroll
                    for (int j = 0; j < 2; ++j) acc[i][j] = __builtin_amdgcn_mfma_f32_16x16x32_bf16(fo[2 * ks + j], c, ks ? acc[i][j] : f32x4{0, 0, 0, 0}, 0, 0, 0);
                }
#pragma unroll
            for (int i = 0; i < 4; ++i) { TIE(acc[i][0]); TIE(acc[i][1]); }
        }
        TS(16);
        WAIT_V(5);
        lb_post(pmine, 3); lb_wait(pall, 3);
        if (wid == 0 && lane < 16) GST_B64_SC1((tm * 16 + lane) * 128 + tn * 8, tag, flg + FL_H);
        TS(8);
        static_for<4, 10>([&](auto ic) { constexpr int n = decltype(ic)::value; G2_ISSUE(n >> 1, n & 1); });
        TS(13);
        if (wid == 0) {
#pragma unroll 1
            for (unsigned spin = 0; spin < SPIN_CAP; ++spin) {
                GLD_B64_SC1(fl, (tm * 16 + tn) * 128 + (lane & 15) * 8, flg + FL_H, 0);
                WAIT_V(0); TIE(fl);
                if (__all(fl[0] == tagA && fl[1] == tagB)) break;
            }
            lb_post(pmine, 4);
            if (b == 255 && lane == 0) GST_B64_SC1(0u * lane, tag, flg + FL_EPOCH);
        } else lb_wait(lb + L_SY, 4);
        TS(20);
        static_for<4, 4 + AW>([&](auto ic) { constexpr int g = decltype(ic)::value; G2_A(g); });
    }
    TS(9);
    __builtin_amdgcn_s_setprio(0);
    f32x4 y[4];
    {
        bf16x8 fb[2][4];
#define G2_FRAG(g, i) DSR128(fb[(g) & 1][i], cad[(g) & 1][(i) & 1], ((((g) >> 1) + 2) & 3) * G2STAGE + (i) * 2048)
        static_for<0, 4>([&](auto ic) { constexpr int i = decltype(ic)::value; G2_FRAG(2, i); });
        static_for<2, 32>([&](auto ic) {
            constexpr int g = decltype(ic)::value, gn = g + 1;
            constexpr bool bar = (gn & 1) == 0 && gn < 32;
            if constexpr (bar) {
                constexpr int ca = g2_cnt(2 * g, 2 * g), cs = gn >= 10 ? g2_cnt(100 + gn - 6, 2 * g) : 63;
                static_assert(cs >= 0, "schedule");
                WAIT_V(AWB && ca < cs ? ca : cs); WAIT_L(0); BAR();
                if constexpr (!AWB) WAIT_V(ca);
                if constexpr (g == 3) TS(10);
                if constexpr (g == 9 || g == 17 || g == 25) TS(26 + (g >> 3));
#pragma unroll
                for (int i = 0; i < 4; ++i) TIE(fb[g & 1][i]);
                TIE(fa2[(2 * g) % AW]);
            }
            static_for<0, 8>([&](auto mc) {
                constexpr int m = decltype(mc)::value, i = m & 3, j = m >> 2, f = 2 * g + j;
                static_assert(g2_cnt(f, f) >= 0, "schedule");
                if constexpr (i == 0 && (j || !bar)) { WAIT_V(g2_cnt(f, f)); TIE(fa2[f % AW]); }
                if constexpr (!bar && j == 0) { WAIT_L(gn < 32 ? 3 : 3 - i); TIE(fb[g & 1][i]); }
                acc[i][j] = __builtin_amdgcn_mfma_f32_16x16x32_bf16(fa2[f % AW], fb[g & 1][i], acc[i][j], 0, 0, 0);
                SB();
                if constexpr (gn < 32 && m < 4) G2_FRAG(gn, m);
                if constexpr (m == 5 && g2_dma(g)) G2_ISSUE(gn / 2 + 3, gn & 1);
                if constexpr (i == 3 && f + AW < 64) G2_A(f + AW);
                if constexpr (g >= YS && g < YS + 4 && m == 6) {
                    constexpr int it = g - YS;
                    TIE(xv[it]); TIE(dsv);
                    y[it] = f32x4{rb1(bflo(xv[it][0]) * bflo(dsv[0])), rb1(bfhi(xv[it][0]) * bfhi(dsv[0])), rb1(bflo(xv[it][1]) * bflo(dsv[1])), rb1(bfhi(xv[it][1]) * bfhi(dsv[1]))};
                    TIE(y[it]);
                }
                SB();
            });
        });
#undef G2_FRAG
    }
    TS(11);
    {
#pragma unroll
        for (int i = 0; i < 4; ++i) *LDSP(f32x4, lb + L_Y2 + wid * 4096 + l16 + i * 1024) = acc[i][1];
        lb_post(pmine, 5);
#pragma unroll
        for (int r = 0; r < 4; ++r) {
            y[r] = f32x4{2.f * acc[0][0][r] + y[r][0], 2.f * acc[1][0][r] + y[r][1], 2.f * acc[2][0][r] + y[r][2], 2.f * acc[3][0][r] + y[r][3]};
            TIE(y[r]);
        }
        lb_wait(lb + L_SY + (wid ^ 4) * 4, 5);
        char* const ob = a.out + (size_t)(tm * 128 + wm * 32 + wn * 16) * 8192 + 4096 + tn * 256;
        f32x4 w[4];
#pragma unroll
        for (int i = 0; i < 4; ++i) w[i] = *LDSP(f32x4, lb + L_Y2 + (wid ^ 4) * 4096 + l16 + i * 1024);
#pragma unroll
        for (int r = 0; r < 4; ++r) {
            const f32x4 o = {2.f * w[0][r] + y[r][0], 2.f * w[1][r] + y[r][1], 2.f * w[2][r] + y[r][2], 2.f * w[3][r] + y[r][3]};
            GST_B128_NT(g4 * 32768 + l15 * 16, o, ob + r * 8192, 0);
        }
    }
    asm volatile(".Lfk_code_b:");
    TS(12);
    TS_FLUSH();
}

}

extern "C" void kernel_launch(void* const* d_in, const int* in_sizes, int n_in, void* d_out, int out_size, void* d_ws, size_t ws_size,
                              hipStream_t stream) {
    (void)in_sizes; (void)n_in; (void)out_size; (void)ws_size;
    fk::Args a;
    a.q = (const char*)d_in[0]; a.k = (const char*)d_in[1]; a.v = (const char*)d_in[2]; a.sk = (const char*)d_in[3];
    a.ssmk = (const char*)d_in[4]; a.ssmD = (const char*)d_in[5]; a.x = (const char*)d_in[6]; a.lre = (const char*)d_in[7];
    a.lim = (const char*)d_in[8]; a.bre = (const char*)d_in[9]; a.bim = (const char*)d_in[10]; a.cre = (const char*)d_in[11];
    a.cim = (const char*)d_in[12]; a.logdt = (const char*)d_in[13]; a.dskip = (const char*)d_in[14];
    a.out = (char*)d_out; a.ws = (char*)d_ws;
    hipLaunchKernelGGL(fk::fused, dim3(256), dim3(768), 0, stream, a);
}
```

```cpp
#include <hip/hip_runtime.h>
#include <stdint.h>
#include <type_traits>

namespace fk {

constexpr int S = 2048, D = 1024, P = 1024;

typedef __attribute__((ext_vector_type(8))) short bf16x8;
typedef __attribute__((ext_vector_type(4))) short s16x4;
typedef __attribute__((ext_vector_type(16))) float f32x16;
typedef __attribute__((ext_vector_type(4))) float f32x4;
typedef __attribute__((ext_vector_type(2))) float f32x2;
typedef __attribute__((ext_vector_type(4))) unsigned u32x4;
typedef __attribute__((ext_vector_type(2))) unsigned u32x2;
#define LDSP(T, a) ((__attribute__((address_space(3))) T*)(uintptr_t)(unsigned)(a))

__device__ __forceinline__ float bf2f(unsigned v) { return __uint_as_float(v << 16); }
__device__ __forceinline__ float bflo(unsigned w) { return __uint_as_float(w << 16); }
__device__ __forceinline__ float bfhi(unsigned w) { return __uint_as_float(w & 0xffff0000u); }
__device__ __forceinline__ uint16_t f2bf(float f) { __bf16 b = (__bf16)f; return __builtin_bit_cast(uint16_t, b); }
__device__ __forceinline__ float rbf(float f) { return bf2f(f2bf(f)); }
typedef __attribute__((ext_vector_type(2))) __bf16 bf16x2;
__device__ __forceinline__ unsigned pack2(float lo, float hi) { return __builtin_bit_cast(unsigned, __builtin_convertvector(f32x2{lo, hi}, bf16x2)); }
__device__ __forceinline__ float rb1(float x) { return __uint_as_float(pack2(0.f, x)); }
__device__ __forceinline__ void cmac(float& xr, float& xi, float ar, float ai, float nai, float hr, float hi) {
    asm("v_fmac_f32 %0, %2, %5\n\tv_fmac_f32 %1, %2, %6\n\tv_fmac_f32 %0, %4, %6\n\tv_fmac_f32 %1, %3, %5" : "+v"(xr), "+v"(xi) : "v"(ar), "v"(ai), "v"(nai), "v"(hr), "v"(hi));
}
__device__ __forceinline__ void cmad(float& o_r, float& o_i, float ar, float ai, float nai, float hr, float hi, float er, float ei) {
    asm("v_fma_f32 %0, %2, %5, %7\n\tv_fma_f32 %1, %2, %6, %8\n\tv_fmac_f32 %0, %4, %6\n\tv_fmac_f32 %1, %3, %5" : "=&v"(o_r), "=&v"(o_i) : "v"(ar), "v"(ai), "v"(nai), "v"(hr), "v"(hi), "v"(er), "v"(ei));
}
__device__ __forceinline__ void mac4(float& x, float& y, float a0, float a1, float b0, float b1, float h0, float h1) {
    asm("v_fmac_f32 %0, %2, %6\n\tv_fmac_f32 %1, %4, %7\n\tv_fmac_f32 %0, %3, %7\n\tv_fmac_f32 %1, %5, %6" : "+v"(x), "+v"(y) : "v"(a0), "v"(a1), "v"(b0), "v"(b1), "v"(h0), "v"(h1));
}
__device__ __forceinline__ void mul4(float& x, float& y, float a0, float a1, float b0, float b1, float h0, float h1) {
    asm("v_mul_f32 %0, %2, %6\n\tv_mul_f32 %1, %4, %7\n\tv_fmac_f32 %0, %3, %7\n\tv_fmac_f32 %1, %5, %6" : "=&v"(x), "=&v"(y) : "v"(a0), "v"(a1), "v"(b0), "v"(b1), "v"(h0), "v"(h1));
}
__device__ __forceinline__ void cmul4(float& o_r, float& o_i, float ar, float ai, float nai, float hr, float hi) {
    asm("v_mul_f32 %0, %2, %5\n\tv_mul_f32 %1, %2, %6\n\tv_fmac_f32 %0, %4, %6\n\tv_fmac_f32 %1, %3, %5" : "=&v"(o_r), "=&v"(o_i) : "v"(ar), "v"(ai), "v"(nai), "v"(hr), "v"(hi));
}
__device__ __forceinline__ void cmul(float ar, float ai, float br, float bi, float& cr, float& ci) { cr = ar * br - ai * bi; ci = ar * bi + ai * br; }

#define PK_LO " op_sel:[0,0,0] op_sel_hi:[0,1,1]"
#define PK_HI " op_sel:[1,0,0] op_sel_hi:[1,1,1]"
#define PK_NEG " neg_lo:[1,0,0] neg_hi:[1,0,0]"
__device__ __forceinline__ void pk_cmad(f32x2& o_r, f32x2& o_i, f32x2 a, f32x2 hr, f32x2 hi, f32x2 er, f32x2 ei) {
    asm("v_pk_fma_f32 %0, %2, %3, %5" PK_LO "\n\tv_pk_fma_f32 %1, %2, %4, %6" PK_LO "\n\tv_pk_fma_f32 %0, %2, %4, %0" PK_HI PK_NEG "\n\tv_pk_fma_f32 %1, %2, %3, %1" PK_HI
        : "=&v"(o_r), "=&v"(o_i) : "v"(a), "v"(hr), "v"(hi), "v"(er), "v"(ei));
}
__device__ __forceinline__ void pk_cmac_conj(f32x2& xr, f32x2& xi, f32x2 a, f32x2 hr, f32x2 hi) {
    asm("v_pk_fma_f32 %0, %2, %3, %0" PK_LO "\n\tv_pk_fma_f32 %1, %2, %4, %1" PK_LO "\n\tv_pk_fma_f32 %0, %2, %4, %0" PK_HI "\n\tv_pk_fma_f32 %1, %2, %3, %1" PK_HI PK_NEG
        : "+v"(xr), "+v"(xi) : "v"(a), "v"(hr), "v"(hi));
}
__device__ __forceinline__ void pk_conj_mul(f32x2& o_r, f32x2& o_i, f32x2 a, f32x2 hr, f32x2 hi) {
    asm("v_pk_mul_f32 %0, %2, %3 op_sel:[0,0] op_sel_hi:[0,1]\n\tv_pk_mul_f32 %1, %2, %4 op_sel:[0,0] op_sel_hi:[0,1] neg_lo:[1,0] neg_hi:[1,0]\n\t"
        "v_pk_fma_f32 %0, %2, %4, %0" PK_HI PK_NEG "\n\tv_pk_fma_f32 %1, %2, %3, %1" PK_HI PK_NEG : "=&v"(o_r), "=&v"(o_i) : "v"(a), "v"(hr), "v"(hi));
}
__device__ __forceinline__ void pk_conj_mac(f32x2& xr, f32x2& xi, f32x2 a, f32x2 hr, f32x2 hi) {
    asm("v_pk_fma_f32 %0, %2, %3, %0" PK_LO "\n\tv_pk_fma_f32 %1, %2, %4, %1" PK_LO PK_NEG "\n\tv_pk_fma_f32 %0, %2, %4, %0" PK_HI PK_NEG "\n\tv_pk_fma_f32 %1, %2, %3, %1" PK_HI PK_NEG
        : "+v"(xr), "+v"(xi) : "v"(a), "v"(hr), "v"(hi));
}
__device__ __forceinline__ void cmac2(f32x2& x, f32x2 a, f32x2 h) {
    asm("v_pk_fma_f32 %0, %1, %2, %0" PK_LO "\n\tv_pk_fma_f32 %0, %1, %2, %0 op_sel:[1,1,0] op_sel_hi:[1,0,1] neg_lo:[1,0,0]" : "+v"(x) : "v"(a), "v"(h));
}
__device__ __forceinline__ float sum4(float x) {
    const auto p = __builtin_amdgcn_permlane32_swap(__float_as_uint(x), __float_as_uint(x), false, false);
    x = __uint_as_float(p[0]) + __uint_as_float(p[1]);
    const auto q = __builtin_amdgcn_permlane16_swap(__float_as_uint(x), __float_as_uint(x), false, false);
    return __uint_as_float(q[0]) + __uint_as_float(q[1]);
}

template <int I, int N, class F>
__device__ __forceinline__ void static_for(F&& f) { if constexpr (I < N) { f(std::integral_constant<int, I>{}); static_for<I + 1, N>(f); } }

#define TS_DECL
#define TS0a()
#define TS0b()
#define TSE(i)
#define TS(i)
#define TS_FLUSH()

#define WAIT_V(n) asm volatile("s_waitcnt vmcnt(%0)" ::"n"((n) > 63 ? 63 : (n)) : "memory")
#define WAIT_L(n) asm volatile("s_waitcnt lgkmcnt(%0)" ::"n"((n) > 15 ? 15 : (n)) : "memory")
#define BAR() asm volatile("s_barrier" ::: "memory")
#define SB() __builtin_amdgcn_sched_barrier(0)
template <class T> __device__ __forceinline__ void tie_(T& x) { asm volatile("" : "+v"(x)); }
#define TIE(x) tie_(x)
__device__ __forceinline__ unsigned take(unsigned x) { unsigned y; asm volatile("v_mov_b32 %0, %1" : "=v"(y) : "v"(x)); return y; }
#define DEF_LD(name, ins) template <int OFF, class T> __device__ __forceinline__ void name(T& dst, unsigned voff, const char* sb) { \
    asm volatile(ins : "=v"(dst) : "v"(voff), "s"(sb), "n"(OFF) : "memory"); }
DEF_LD(gld_u16_, "global_load_ushort %0, %1, %2 offset:%3")
DEF_LD(gld_b32_, "global_load_dword %0, %1, %2 offset:%3")
DEF_LD(gld_b64_, "global_load_dwordx2 %0, %1, %2 offset:%3")
DEF_LD(gld_b128_, "global_load_dwordx4 %0, %1, %2 offset:%3")
DEF_LD(gld_b64_nt_, "global_load_dwordx2 %0, %1, %2 offset:%3 nt")
DEF_LD(gld_b128_nt_, "global_load_dwordx4 %0, %1, %2 offset:%3 nt")
DEF_LD(gld_b64_sc1_, "global_load_dwordx2 %0, %1, %2 offset:%3 sc1")
DEF_LD(gld_b128_sc1_, "global_load_dwordx4 %0, %1, %2 offset:%3 sc1")
#define DEF_ST(name, ins) template <int OFF, class T> __device__ __forceinline__ void name(unsigned voff, const T& v, const char* sb) { \
    asm volatile(ins :: "v"(voff), "v"(v), "s"(sb), "n"(OFF) : "memory"); }
DEF_ST(gst_b128_, "global_store_dwordx4 %0, %1, %2 offset:%3\n\ts_nop 1")
DEF_ST(gst_b128_nt_, "global_store_dwordx4 %0, %1, %2 offset:%3 nt\n\ts_nop 1")
DEF_ST(gst_b128_sc1_, "global_store_dwordx4 %0, %1, %2 offset:%3 sc1\n\ts_nop 1")
DEF_ST(gst_b64_sc1_, "global_store_dwordx2 %0, %1, %2 offset:%3 sc1")
#define GLD_U16(dst, voff, sb) gld_u16_<0>(dst, voff, sb)
#define GLD_B32(dst, voff, sb, off) gld_b32_<(off)>(dst, voff, sb)
#define GLD_B64(dst, voff, sb, off) gld_b64_<(off)>(dst, voff, sb)
#define GLD_B128(dst, voff, sb, off) gld_b128_<(off)>(dst, voff, sb)
#define GLD_B64_NT(dst, voff, sb, off) gld_b64_nt_<(off)>(dst, voff, sb)
#define GLD_B128_NT(dst, voff, sb, off) gld_b128_nt_<(off)>(dst, voff, sb)
#define GLD_B64_SC1(dst, voff, sb, off) gld_b64_sc1_<(off)>(dst, voff, sb)
#define GLD_B128_SC1(dst, voff, sb, off) gld_b128_sc1_<(off)>(dst, voff, sb)
#define GST_B128(voff, v, sb, off) gst_b128_<(off)>(voff, v, sb)
#define GST_B128_NT(voff, v, sb, off) gst_b128_nt_<(off)>(voff, v, sb)
#define GST_B128_SC1(voff, v, sb, off) gst_b128_sc1_<(off)>(voff, v, sb)
#define GST_B64_SC1(voff, v, sb) gst_b64_sc1_<0>(voff, v, sb)
__device__ __forceinline__ void dma_(unsigned ldsaddr, unsigned voff, const char* sb) {
    asm volatile("s_mov_b32 m0, %0\n\ts_nop 0\n\tglobal_load_lds_dwordx4 %1, %2" :: "s"(ldsaddr), "v"(voff), "s"(sb) : "memory"); }
__device__ __forceinline__ void dma_nt_(unsigned ldsaddr, unsigned voff, const char* sb) {
    asm volatile("s_mov_b32 m0, %0\n\ts_nop 0\n\tglobal_load_lds_dwordx4 %1, %2 nt" :: "s"(ldsaddr), "v"(voff), "s"(sb) : "memory"); }
#define DMA(ldsaddr, voff, sb) dma_(ldsaddr, voff, sb)
template <int OFF, class T> __device__ __forceinline__ void dsr128_(T& dst, unsigned addr) { asm volatile("ds_read_b128 %0, %1 offset:%2" : "=v"(dst) : "v"(addr), "n"(OFF)); }
template <int OFF, class T> __device__ __forceinline__ void dsr64_(T& dst, unsigned addr) { asm volatile("ds_read_b64 %0, %1 offset:%2" : "=v"(dst) : "v"(addr), "n"(OFF)); }
#define DSR128(dst, addr, off) dsr128_<(off)>(dst, addr)
#define DSR64(dst, addr, off) dsr64_<(off)>(dst, addr)

__device__ __forceinline__ void kv4(unsigned& o01, unsigned& o23, unsigned ka, unsigned kb, unsigned kc, unsigned kd, const unsigned* w, unsigned v0, unsigned v1) {
    unsigned t1, t3, p0, p1, p2, p3;
    asm(
        "v_dot2_f32_bf16 %0, %8, %19, 0\n\tv_dot2_f32_bf16 %1, %9, %18, 0\n\tv_dot2_f32_bf16 %2, %9, %19, 0\n\tv_dot2_f32_bf16 %3, %10, %18, 0\n\t"
        "v_dot2_f32_bf16 %4, %9, %16, 0\n\tv_dot2_f32_bf16 %5, %9, %17, 0\n\tv_dot2_f32_bf16 %6, %10, %16, 0\n\tv_dot2_f32_bf16 %7, %10, %17, 0\n\t"
        "v_cvt_pk_bf16_f32 %0, %0, %4\n\tv_cvt_pk_bf16_f32 %1, %1, %5\n\tv_cvt_pk_bf16_f32 %2, %2, %6\n\tv_cvt_pk_bf16_f32 %3, %3, %7\n\t"
        "v_dot2_f32_bf16 %4, %9, %15, 0\n\tv_dot2_f32_bf16 %5, %10, %14, 0\n\tv_dot2_f32_bf16 %6, %10, %15, 0\n\tv_dot2_f32_bf16 %7, %11, %14, 0\n\t"
        "v_dot2_f32_bf16 %0, %0, %22, 0\n\tv_dot2_f32_bf16 %1, %1, %22, 0\n\tv_dot2_f32_bf16 %2, %2, %22, 0\n\tv_dot2_f32_bf16 %3, %3, %22, 0\n\t"
        "v_cvt_pk_bf16_f32 %0, %0, %4\n\tv_cvt_pk_bf16_f32 %1, %1, %5\n\tv_cvt_pk_bf16_f32 %2, %2, %6\n\tv_cvt_pk_bf16_f32 %3, %3, %7\n\t"
        "v_dot2_f32_bf16 %4, %10, %12, 0\n\tv_dot2_f32_bf16 %5, %10, %13, 0\n\tv_dot2_f32_bf16 %6, %11, %12, 0\n\tv_dot2_f32_bf16 %7, %11, %13, 0\n\t"
        "v_dot2_f32_bf16 %0, %0, %22, 0\n\tv_dot2_f32_bf16 %1, %1, %22, 0\n\tv_dot2_f32_bf16 %2, %2, %22, 0\n\tv_dot2_f32_bf16 %3, %3, %22, 0\n\t"
        "v_cvt_pk_bf16_f32 %0, %0, %4\n\tv_cvt_pk_bf16_f32 %1, %1, %5\n\tv_cvt_pk_bf16_f32 %2, %2, %6\n\tv_cvt_pk_bf16_f32 %3, %3, %7\n\t"
        "v_dot2_f32_bf16 %0, %0, %22, 0\n\tv_dot2_f32_bf16 %1, %1, %22, 0\n\tv_dot2_f32_bf16 %2, %2, %22, 0\n\tv_dot2_f32_bf16 %3, %3, %22, 0\n\t"
        "v_cvt_pk_bf16_f32 %0, %0, 0\n\tv_cvt_pk_bf16_f32 %1, 0, %1\n\tv_cvt_pk_bf16_f32 %2, %2, 0\n\tv_cvt_pk_bf16_f32 %3, 0, %3\n\t"
        "v_dot2_f32_bf16 %0, %0, %20, 0\n\tv_dot2_f32_bf16 %1, %1, %20, 0\n\tv_dot2_f32_bf16 %2, %2, %21, 0\n\tv_dot2_f32_bf16 %3, %3, %21, 0\n\t"
        "s_nop 1\n\tv_cvt_pk_bf16_f32 %0, %1, %0\n\tv_cvt_pk_bf16_f32 %2, %3, %2\n\t"
        : "=&v"(o01), "=&v"(t1), "=&v"(o23), "=&v"(t3), "=&v"(p0), "=&v"(p1), "=&v"(p2), "=&v"(p3)
        : "v"(ka), "v"(kb), "v"(kc), "v"(kd), "v"(w[0]), "v"(w[1]), "v"(w[2]), "v"(w[3]), "v"(w[4]), "v"(w[5]), "v"(w[6]), "v"(w[7]), "v"(v0), "v"(v1), "s"(0x3f803f80u));
}
__device__ __forceinline__ void lb_post(unsigned myaddr, unsigned seq) { asm volatile("ds_write_b32 %0, %1" :: "v"(myaddr), "v"(seq) : "memory"); }
__device__ __forceinline__ void lb_wait(unsigned addr, unsigned seq) {
    unsigned v;
    asm volatile("s_branch 2f\n1:\n\ts_sleep 1\n2:\n\tds_read_b32 %0, %1\n\ts_waitcnt lgkmcnt(0)\n\tv_cmp_gt_u32 vcc, %2, %0\n\ts_cbranch_vccnz 1b" : "=&v"(v) : "v"(addr), "v"(seq) : "vcc", "memory");
}

constexpr size_t WS_H = 0, WS_KVT = (size_t)10 << 20, WS_ZT = (size_t)15 << 20, WS_E = (size_t)20 << 20, WS_FLAG = WS_E + (512 << 10);
#define HP 65536
#define KP 8192
#define ZP 4096
constexpr int FL_KV = 0, FL_Z = 65536, FL_H = 131072, FL_EPOCH = 196608;
constexpr unsigned SPIN_CAP = 1u << 17;
#define ABL 0
#define CSPLIT 1
#define MERGE 3
#define CB 13
#define PAUSE 0
#define QLATE 0
#define CVPRIO 0
#define STG_SEL (idx & 3)
#define SP0 3
#define SDP 1
#define LP0 3
#define LDP 2
#define KVS 5
#define KVU 2
#define QIT 1
#define ZB 3
#define OS 2
#define MPRIO 2

#define ROT 0x80A5
#define R2 0x41
#define CPF 0
#define PRE 4
#define PRO 12
#define G1NT 0
#define G1NTS 16
#define TMIX 0
#define TLBT 0
#define CDEF 0
#define CVLATE 1
#define FPRIO 3
#define YS 12
#define DP0 1
#define DP1 5
#define AWB 1
#define PK 0
#define PU 13
#define PC0 13
#define PC1 14
#define PQ 0
#define SPF 0
#define SPH 0
#define NB1 4
#define EP 1
#define ESL 0
#define CFB 0
#define P1 1
#define P0 1
constexpr int it_cnt(int p, int k, int last) {
    const int pos[6] = {PK, PU, PC0, PC1, PQ, PU - 1}, n[6] = {4, 4, 4, 4, 8, 1};
    int c = 0;
    for (int i = 0; i < 6; ++i) if (pos[i] <= last && (pos[i] > p || (pos[i] == p && i > k))) c += n[i];
    return c;
}
constexpr int G1STAGE = 32768;
constexpr int g1_slot(int s) { return (NB1 == 5 ? (s + 3) % 5 : s & 3) * G1STAGE; }
constexpr int L_KS = NB1 == 5 ? 65536 : 131072, L_VS = L_KS + 16384;
constexpr int CST = 4288;
constexpr int UOFF = 4 * CST, UPL = 2304;
constexpr int CHSZ = UOFF + 2 * UPL + 64;
constexpr int L_AGG = 4 * CHSZ, L_ES = L_AGG + 15 * 512, L_SY = L_ES + 2048;
constexpr int L_HT = 98304;
constexpr int L_C = 98304, G2STAGE = 16384;
constexpr int L_ZI = 0;
constexpr int L_Z2 = 20480;
constexpr int L_Y = 40960, L_Y2 = 131072;
static_assert(L_SY + 64 <= L_HT, "lds");
static_assert(L_Y + 32768 <= L_AGG && L_Z2 + 64 * 264 <= L_Y, "lds");

__device__ __forceinline__ int img_off(int r, int c) { return (r >> 1) * 256 + ((((r & 1) * 8 + c) ^ ((r >> 1) & 15)) << 4); }

constexpr int g1_upto(int g) {
    int n = PRO;
    for (int t = 0; t <= g; ++t) {
        int sb = (t + 1) / 2, sub = (t + 1) % 2, allowed = sb >= 1 ? 4 * (sb + NB1 - 1) + 2 * (sub + 1) : 4 * NB1;
        if (allowed > 64) allowed = 64;
        n = n + 4 < allowed ? n + 4 : allowed;
    }
    return n;
}
constexpr int g1_cnt(int s) { return g1_upto(2 * s - 2) - 4 * (s + 1); }
static_assert(g1_upto(32 - 2 * NB1) == 64, "schedule");
constexpr int AW = 8;
constexpr bool g2_dma(int g) { return (g + 1) / 2 >= 2 && (g + 1) / 2 + 3 < 16; }
constexpr int g2_cnt(int id, int at) {
    int n = -1000;
    for (int f = 4; f < 4 + AW; ++f) { ++n; if (f == id) n = 0; }
    for (int f = 4; f < at; ++f) {
        if ((f & 1) && g2_dma(f >> 1)) { ++n; if (100 + (f >> 1) == id) n = 0; }
        if (f + AW < 64) { ++n; if (f + AW == id) n = 0; }
    }
    return n;
}

constexpr int cv_m(int o) { return (o & 1) ? 7 - (o >> 1) : o >> 1; }
constexpr int cv_first(int o) { int p = 0; for (int i = 0; i < o; ++i) p += 8 - cv_m(i); return p; }
constexpr int cv_grp(int p) { int o = 0; while (cv_first(o + 1) <= p) ++o; return o; }
constexpr int cv_read(int p) {
    int slot[40] = {};
    for (int i = 0; i < 40; ++i) slot[i] = -1;
    slot[0] = 2 * 3 + 1;
    for (int o = 0; o < 8; ++o) {
        int q = cv_first(o + 1) - 1;
        for (int h = 0; h < 2; ++h) { while (slot[q] >= 0) ++q; slot[q] = 2 * (o + 4) + h; }
    }
    const int bs[8] = {1, 3, 5, 11, 12, 13, 19, 21};
    for (int jb = 0; jb < 8; ++jb) slot[bs[jb]] = 32 + jb;
    return slot[p];
}
static_assert(cv_read(35) == 22 && cv_read(36) == 23 && cv_read(37) == -1 && cv_read(10) == 11 && cv_read(15) == 12 && cv_read(18) == 15 && cv_read(24) == 17, "conv schedule");
constexpr int cv_wait(int o) {
    int n = 0;
    for (int q = cv_first(o) - 1;; --q) {
        const int rd = cv_read(q < 0 ? q + 36 : q);
        if (rd == 2 * (o + (q < 0 ? 8 : 0)) + 1) return n;
        if (rd >= 0) ++n;
    }
}

struct Args {
    const char *q, *k, *v, *sk, *ssmk, *ssmD, *x, *lre, *lim, *bre, *bim, *cre, *cim, *logdt, *dskip;
    char *out, *ws;
};

typedef __attribute__((ext_vector_type(8))) unsigned long long u64x8;
__global__ __launch_bounds__(768, 3) void fused(Args) {
    __shared__ __attribute__((aligned(1024))) char shm[163840];
    TS0a();
    u64x8 ka0, ka1; unsigned long long ka2;
    const char* pc0;
    int code_n;
    asm volatile("s_load_dwordx16 %0, %5, 0x0\n\ts_load_dwordx16 %1, %5, 0x40\n\ts_load_dwordx2 %2, %5, 0x80\n\t"
                 "s_getpc_b64 %3\n.Lfk_code_0:\n\ts_mov_b32 %4, .Lfk_code_a-.Lfk_code_0-4\n\ts_max_i32 %4, %4, .Lfk_code_b-.Lfk_code_0-4"
                 : "=&s"(ka0), "=&s"(ka1), "=&s"(ka2), "=&s"(pc0), "=&s"(code_n) : "s"(__builtin_amdgcn_kernarg_segment_ptr()) : "scc");
    const int tid = threadIdx.x, lane = tid & 63, wid = __builtin_amdgcn_readfirstlane(tid >> 6);
    const int b = blockIdx.x, xcd = b & 7, idx = b >> 3;
    unsigned cpf;
    asm volatile("s_cmp_lt_u32 %1, 8\n\ts_cbranch_scc1 1f\n\ts_setprio 3\n\ts_cmp_lg_u32 %1, 8\n\ts_cbranch_scc1 1f\n\t"
                 "v_lshl_add_u32 %0, %2, 7, %3\n\tv_min_i32 %0, %4, %0\n\ts_mov_b64 exec, 0xffff\n\tglobal_load_dword %0, %0, %5\n\ts_mov_b64 exec, -1\n1:"
                 : "=&v"(cpf) : "s"(wid), "v"(lane), "s"(idx * 2048), "s"(code_n), "s"(pc0) : "scc", "memory");
    TS_DECL
    const int tm = (xcd >> 1) * 4 + (idx >> 3), tn = (xcd & 1) * 8 + (idx & 7);
    const int wm = wid & 3, wn = (wid >> 2) & 1, l31 = lane & 31, lh = lane >> 5;
    const unsigned lb = (unsigned)(uintptr_t)shm;
    const unsigned l16 = lane * 16;
    const int l15 = lane & 15, g4 = lane >> 4, sq = 2 * wn + (wm & 1), st = sq * 16 + l15, run = (wm >> 1) * 4 + g4;
    unsigned so, kvv[4];
    const int trow = idx * 64 * 2048 + xcd * 256;
    if (wid < 8) {
        so = (tn * 64 + sq * 16) * 2 + (l15 & 7) * 4;
        kvv[0] = trow + wm * 32768 + g4 * 2048 + ((l15 ^ (2 * g4)) << 4);
        kvv[1] = kvv[0] + 8192; kvv[2] = (kvv[0] ^ 128) + 16384; kvv[3] = kvv[2] + 8192;
#pragma unroll
        for (int i = 0; i < 4; ++i) TIE(kvv[i]);
    } else {
        so = trow + (idx ? -4 * 2048 : 0) + g4 * 2048 + wm * 64 + l15 * 4;
        asm volatile("v_writelane_b32 %0, %1, 2" : "+v"(so) : "s"((xcd * 64 + idx * 2 + (wm >> 1)) * 4));
        kvv[0] = (xcd * 128 + wm * 32 + l31) * 8;
        TIE(kvv[0]);
    }
    unsigned g1dst = lb + wid * 4096;
    asm volatile("v_writelane_b32 %4, 0, 0\n\tv_writelane_b32 %4, 4, 1\n\ts_waitcnt lgkmcnt(0)" : "+s"(ka0), "+s"(ka1), "+s"(ka2), "+s"(g1dst), "+v"(so));
    Args a;
    a.q = (const char*)ka0[0]; a.k = (const char*)ka0[1]; a.v = (const char*)ka0[2]; a.sk = (const char*)ka0[3]; a.ssmk = (const char*)ka0[4]; a.ssmD = (const char*)ka0[5];
    a.x = (const char*)ka0[6]; a.lre = (const char*)ka0[7]; a.lim = (const char*)ka1[0]; a.bre = (const char*)ka1[1]; a.bim = (const char*)ka1[2]; a.cre = (const char*)ka1[3];
    a.cim = (const char*)ka1[4]; a.logdt = (const char*)ka1[5]; a.dskip = (const char*)ka1[6]; a.out = (char*)ka1[7]; a.ws = (char*)ka2;
    char* const ws = a.ws;
    const char* const flg = ws + WS_FLAG;
    TS(26);
    unsigned sm; u32x2 kw0;
    {
        unsigned long long p;
        asm volatile("s_cmp_gt_u32 %[w], 7\n\ts_cbranch_scc0 1f\n\tv_mov_b64 %2, %[k]\n\ts_mov_b64 exec, 7\n\tv_mov_b64 %2, %[d]\n\ts_branch 4f\n1:\n\t"
                     "v_mov_b64 %2, %[c3]\n\ts_mov_b32 exec_hi, 0xffff\n\tv_mov_b64 %2, %[c2]\n\ts_mov_b32 exec_hi, 0\n\tv_mov_b64 %2, %[c1]\n4:\n\t"
                     "s_mov_b64 exec, 3\n\tv_mov_b64 %2, %[e]\n\ts_mov_b64 exec, -1\n\t"
                     "v_mad_u64_u32 %2, vcc, %[o], 1, %2\n\tglobal_load_dword %0, %2, off sc1\n\ts_cbranch_scc0 2f\n\tglobal_load_dwordx2 %1, %[ko], %[sk]\n\ts_branch 3f\n2:\n\t"
                     "s_add_u32 m0, %[m], %[n0]\n\ts_nop 0\n\tglobal_load_lds_dwordx4 %[ko], %[kv] nt\n\ts_add_u32 m0, %[m], %[n0]+1024\n\ts_nop 0\n\tglobal_load_lds_dwordx4 %[k1], %[kv] nt\n\t"
                     "s_add_u32 m0, %[m], %[n0]+2048\n\ts_nop 0\n\tglobal_load_lds_dwordx4 %[k2], %[kv] nt\n\ts_add_u32 m0, %[m], %[n0]+3072\n\ts_nop 0\n\tglobal_load_lds_dwordx4 %[k3], %[kv] nt\n3:"
                     : "=&v"(sm), "=&v"(kw0), "=&v"(p)
                     : [k] "s"(a.k), [w] "s"(wid), [c1] "s"(a.logdt), [c2] "s"(a.lre), [c3] "s"(a.lim), [d] "s"(a.ssmD), [e] "s"(flg + FL_EPOCH), [o] "v"(so), [sk] "s"(a.sk),
                       [ko] "v"(kvv[0]), [k1] "v"(kvv[1]), [k2] "v"(kvv[2]), [k3] "v"(kvv[3]), [kv] "s"(wn ? a.v : a.k), [m] "s"(g1dst), [n0] "n"(L_KS)
                     : "scc", "vcc", "memory");
    }
    TS0b();
    TS(27);
    if (P1) BAR();
#define TAKE_TAG(w) const unsigned tagA = __builtin_amdgcn_readlane(w, 0) + 1, tagB = __builtin_amdgcn_readlane(w, 1) + (tagA == 0 ? 1u : 0u); \
    const u32x2 tag = {tagA, tagB}
#define SLICE_TABLE() unsigned slo, shi; { int ix = idx; asm volatile("" : "+s"(ix)); \
        const int rot2 = (((R2 >> 4) & 15) * (ix >> 3) + (R2 & 15) * (xcd >> 1)) & 15, es = (tn - rot2) & 15; \
        const unsigned long long id = 0xFEDCBA9876543210ull, seq = rot2 ? (id >> (4 * rot2)) | (id << (64 - 4 * rot2)) : id; \
        const unsigned long long lo = (1ull << (4 * es)) - 1, tab = ((seq & lo) << 4) | (seq & ~((lo << 4) | 15)) | tn; \
        slo = (unsigned)tab; shi = (unsigned)(tab >> 32); asm volatile("" : "+s"(slo), "+s"(shi)); }
#define SLICE(st) ((((st) < 8 ? slo : shi) >> (4 * ((st) & 7))) & 15)
    typedef __attribute__((ext_vector_type(4))) short v4i16;
    int role = wid;
    asm volatile("" : "+s"(role));

    if (role >= 8) {
        const int chn = xcd * 128 + idx * 4 + wm;
        const unsigned cb = lb + wm * CHSZ;
        const int r = l15;
        const unsigned fpub = xcd * 256 + idx * 8, fpoll = xcd * 256 + l31 * 8;
        u32x2 fl; u32x4 sw[4], uu[4];
        SLICE_TABLE();
        TS(1);
        WAIT_V(0);
        asm volatile("" :: "v"(cpf));
        const unsigned hal = take(sm);
        TAKE_TAG(hal);
        const unsigned dskl = (__builtin_amdgcn_readlane(hal, 2) >> ((wm & 1) * 16)) & 0xffffu;
        u32x4 kvo[4];
        {
            unsigned kw[2][8], ta[2], th[2];
            u32x2 hm[2];
#pragma unroll
            for (int j = 0; j < 2; ++j) {
                const unsigned x = take(kw0[j]);
                const auto y = __builtin_amdgcn_permlane16_swap(x, x, false, false);
#pragma unroll
                for (int p = 0; p < 2; ++p) { kw[p][4 * j] = y[p] & 0xffffu; kw[p][4 * j + 1] = y[p] << 16; kw[p][4 * j + 2] = y[p] >> 16; kw[p][4 * j + 3] = y[p] & 0xffff0000u; }
            }
#pragma unroll
            for (int p = 0; p < 2; ++p) {
                unsigned h[3];
#pragma unroll
                for (int j = 0; j < 3; ++j) h[j] = (__shfl(hal, p * 8 + (r >> 1) + 16 * (j + 1)) >> ((r & 1) * 16)) & 0xffffu;
                hm[p] = idx ? u32x2{h[0] << 16, h[1] | (h[2] << 16)} : u32x2{0u, 0u};
                const int q = r >> 2, pp = r & 3;
                ta[p] = lb + L_KS + (8 * g4 + q) * 256 + (((wm * 4 + p * 2 + (pp >> 1)) ^ (2 * (q | ((g4 & 1) << 2)))) << 4) + (pp & 1) * 8;
                th[p] = (ta[p] ^ 128) - 1024;
                TIE(hm[p]); TIE(ta[p]); TIE(th[p]);
#pragma unroll
                for (int j = 0; j < 8; ++j) TIE(kw[p][j]);
            }
            int sl = ((lane & 3) * 16 + (lane >> 2)) * 4;
            TIE(sl);
            BAR();
            TS(2);
            u32x2 kt[4][3], vt[4][2];
#pragma unroll
            for (int u = 0; u < 4; ++u) {
                const int p = u >> 1, o = (u & 1) * 8192;
                auto tr = [&](unsigned ad) -> u32x2 { return __builtin_bit_cast(u32x2, __builtin_amdgcn_ds_read_tr16_b64_v4i16(LDSP(v4i16, ad))); };
                kt[u][0] = tr(th[p] + o); kt[u][1] = tr(ta[p] + o); vt[u][0] = tr(ta[p] + o + 16384); kt[u][2] = tr(ta[p] + o + 1024); vt[u][1] = tr(ta[p] + o + 17408);
                SB();
            }
            TS(14);
#pragma unroll
            for (int u = 0; u < 4; ++u) {
                const int p = u >> 1, oh = u & 1;
                const u32x2 k0 = oh == 0 && g4 == 0 ? hm[p] : kt[u][0];
                unsigned o[4];
                kv4(o[3], o[2], k0[0], k0[1], kt[u][1][0], kt[u][1][1], kw[p], vt[u][0][0], vt[u][0][1]);
                if (u > KVU) { TIE(o[3]); BAR(); }
                kv4(o[1], o[0], kt[u][1][0], kt[u][1][1], kt[u][2][0], kt[u][2][1], kw[p], vt[u][1][0], vt[u][1][1]);
                if (u == 0) { TIE(o[0]); TIE(o[2]); WAIT_L(0); }
#pragma unroll
                for (int j = 0; j < 4; ++j) kvo[u][j] = __builtin_amdgcn_ds_bpermute(sl, o[j]);
                SB();
                if (u == 0) TS(15); if (u == 1) TS(16); if (u == 2) TS(17); if (u == 3) TS(18);
                if (u >= KVU) BAR();
            }
#pragma unroll
            for (int u = 0; u < 4; ++u) TIE(kvo[u]);
        }
        TS(3);
        u32x2 qv[8];
        const char* krow = a.ssmk + (size_t)chn * 4096;
        const char* qs = a.q + (size_t)(idx * 64 + wm * 16) * 2048 + xcd * 256;
        const char* usrc = ws + WS_KVT + (size_t)(xcd * 64 + idx * 2 + (wm >> 1)) * KP + (wm & 1) * 4096;
        const char* cs = ((wm & 2) ? a.cim : a.cre) + (size_t)(tn * 64 + (wm & 1) * 2) * 2048;
        unsigned co[4];
#pragma unroll
        for (int i = 0; i < 4; ++i) {
            const int pair = i * 4 + g4, v = r ^ pair, row = pair * 2 + (v >> 3);
            co[i] = ((row & 15) * 4 + (row >> 4)) * 2048 + (v & 7) * 16;
        }
        bool kv_ok = false;
#pragma unroll
        for (int it = 0; it < 4; ++it) asm volatile("" : "=v"(uu[it]));
        auto items = [&](auto pc) {
            constexpr int p = decltype(pc)::value;
            if constexpr (PK == p) { GLD_B128_NT(sw[0], l16, krow, 0); GLD_B128_NT(sw[1], l16, krow, 1024); GLD_B128_NT(sw[2], l16, krow, 2048); GLD_B128_NT(sw[3], l16, krow, 3072); }
            if constexpr (PU == p) {
                WAIT_V(PK == p ? 4 : 0); TIE(fl);
                kv_ok = __all(fl[0] == tagA && fl[1] == tagB);
                const char* us = kv_ok ? usrc : krow;
                GLD_B128_SC1(uu[0], l16, us, 0); GLD_B128_SC1(uu[1], l16, us, 1024); GLD_B128_SC1(uu[2], l16, us, 2048); GLD_B128_SC1(uu[3], l16, us, 3072);
            }
            if constexpr (PC0 == p) { for (int i = 0; i < 4; ++i) DMA(lb + L_C + 2 * G2STAGE + wm * 4096 + i * 1024, co[i], cs + (SLICE(0) << 7)); }
            if constexpr (PC1 == p) { for (int i = 0; i < 4; ++i) DMA(lb + L_C + 3 * G2STAGE + wm * 4096 + i * 1024, co[i], cs + (SLICE(1) << 7)); }
            if constexpr (PQ == p) static_for<0, 8>([&](auto jc) { constexpr int j = decltype(jc)::value; GLD_B64_NT(qv[j], lh * 2048 + l31 * 8, qs + j * 4096, 0); });
            if constexpr (PU == p + 1) GLD_B64_SC1(fl, fpoll, flg + FL_KV, 0);
        };
#define ITEMS(p) items(std::integral_constant<int, p>{})
        {
            const int ph = STG_SEL, ps = SP0 + ph * SDP, pl = LP0 + ph * LDP;
            const char* kvd = ws + WS_KVT + (size_t)(xcd * 64 + wm * 16) * KP + idx * 256;
            int nb1 = 1 + 2 * (3 - KVU);
#pragma unroll 1
            for (; nb1 <= ps; ++nb1) BAR();
            static_for<0, 4>([&](auto uc) {
                constexpr int u = decltype(uc)::value;
                GST_B128_SC1((lane >> 3) * KP + (lane & 7) * 16, kvo[u], kvd + (u >> 1) * 8 * KP, (u & 1) * 128);
            });
#pragma unroll 1
            for (; nb1 <= pl; ++nb1) {
                if (nb1 == ps + 4) WAIT_V(0);
                BAR();
                if (nb1 == ps + 4 && wm == 0 && lane == 0) GST_B64_SC1(fpub, tag, flg + FL_KV);
            }
            ITEMS(0);
#pragma unroll 1
            for (; nb1 <= 11; ++nb1) {
                if (nb1 == ps + 4) WAIT_V(it_cnt(0, -1, 0));
                BAR();
                if (nb1 == ps + 4 && wm == 0 && lane == 0) GST_B64_SC1(fpub, tag, flg + FL_KV);
            }
        }
        ITEMS(11);
        BAR();
        ITEMS(12);
        BAR();
        ITEMS(13);
        BAR();
        ITEMS(14);
        BAR();
        ITEMS(15);
        TS(4);
        const unsigned ud = lb + ((wm & 2) + ((lane >> 2) & 1)) * CHSZ + UOFF + (lane & 1) * UPL + (16 + (wm & 1) * 64 + g4 * 4 + ((lane >> 3) & 1) * 2 + ((lane >> 1) & 1)) * 16;
        {
            const u32x4 z4 = {0, 0, 0, 0};
            if (lane < 16) *LDSP(unsigned, lb + L_SY + lane * 4) = 0;
            if (lane < 56) *LDSP(u32x4, lb + L_AGG + wm * 896 + l16) = z4;
            *LDSP(u32x4, lane < 32 ? cb + (lane >> 3) * CST + (lane & 7) * 16 : cb + UOFF + (g4 & 1) * UPL + r * 16) = z4;
        }
        WAIT_L(0);
        TS(5);
        BAR();
        __builtin_amdgcn_s_setprio(FPRIO);
        ITEMS(16);
        TS(11);
        WAIT_V(it_cnt(PK, 0, 16));
        TS(12);
        if (!(ABL & 32)) {
#pragma unroll
            for (int it = 0; it < 4; ++it) TIE(sw[it]);
#pragma unroll
            for (int it = 0; it < 4; ++it) {
                const unsigned q = cb + 128 + it * 1024 + l16;
                const unsigned d0 = sw[it][0], d1 = sw[it][1], d2 = sw[it][2], d3 = sw[it][3];
                const unsigned o0 = it ? __builtin_amdgcn_readlane(sw[it ? it - 1 : 0][2], 63) : 0u, o1 = it ? __builtin_amdgcn_readlane(sw[it ? it - 1 : 0][3], 63) : 0u;
                const unsigned x0 = __builtin_amdgcn_update_dpp(o0, d2, 0x138, 0xf, 0xf, false), x1 = __builtin_amdgcn_update_dpp(o1, d3, 0x138, 0xf, 0xf, false);
                const unsigned a0 = __builtin_amdgcn_alignbyte(d0, x1, 2), a1 = __builtin_amdgcn_alignbyte(d1, d0, 2), a2 = __builtin_amdgcn_alignbyte(d2, d1, 2),
                               a3 = __builtin_amdgcn_alignbyte(d3, d2, 2);
                *LDSP(u32x4, q) = sw[it];
                *LDSP(u32x4, q + CST) = u32x4{a0, a1, a2, a3};
                *LDSP(u32x4, q + 2 * CST) = u32x4{x1, d0, d1, d2};
                *LDSP(u32x4, q + 3 * CST) = u32x4{__builtin_amdgcn_alignbyte(x1, x0, 2), a0, a1, a2};
                if (it == 3 && lane == 63) {
                    *LDSP(u32x2, q + 16 + CST) = u32x2{d3 >> 16, 0u};
                    *LDSP(u32x2, q + 16 + 2 * CST) = u32x2{d3, 0u};
                    *LDSP(u32x2, q + 16 + 3 * CST) = u32x2{a3, d3 >> 16};
                }
            }
        }
        TS(13);
        WAIT_V(it_cnt(PU, 1, 16));
#pragma unroll
        for (int it = 0; it < 4; ++it) TIE(uu[it]);
        if (!kv_ok) {
            for (unsigned spin = 0; spin < SPIN_CAP; ++spin) {
                GLD_B64_SC1(fl, fpoll, flg + FL_KV, 0);
                WAIT_V(0); TIE(fl);
                if (__all(fl[0] == tagA && fl[1] == tagB)) break;
                __builtin_amdgcn_s_sleep(8);
            }
            GLD_B128_SC1(uu[0], l16, usrc, 0); GLD_B128_SC1(uu[1], l16, usrc, 1024); GLD_B128_SC1(uu[2], l16, usrc, 2048); GLD_B128_SC1(uu[3], l16, usrc, 3072);
            WAIT_V(0);
#pragma unroll
            for (int it = 0; it < 4; ++it) TIE(uu[it]);
        }
#pragma unroll
        for (int it = 0; it < 4; ++it) *LDSP(u32x4, ud + it * 256) = uu[it];
        lb_post(lb + L_SY + 32 + wm * 4, 1); lb_wait(lb + L_SY + 32 + (wm ^ 1) * 4, 1);
        __builtin_amdgcn_s_setprio(CVPRIO);
        TS(6);
        f32x4 ca[8];
        {
            unsigned kb = cb + ((3 - r) & 3) * CST + 2 * (64 + 16 + r - 8 * g4 - 7 + ((3 - r) & 3));
            unsigned ub = cb + UOFF + (g4 & 1) * UPL + (15 + r + (g4 >> 1)) * 16 - 64;
#pragma unroll
            for (int i = 0; i < 8; ++i) ca[i] = f32x4{0, 0, 0, 0};
            s16x4 FA[4][2]; bf16x8 FB[2][8];
#define CV_A(s, o, e) DSR64(FA[(o) & 3][e], kb, 64 * ((s) + ((o) >> 3)) + 512 * cv_m((o) & 7) + 8 * (e))
#define CV_B(s, jb) DSR128(FB[(s) & 1][jb], ub, 64 + 256 * (jb) - 32 * (s))
            static_for<0, 8>([&](auto ic) { constexpr int n = decltype(ic)::value; CV_B(0, n); CV_A(0, n >> 1, n & 1); });
            WAIT_L(0);
            auto body = [&]() {
                static_for<0, 72>([&](auto ic) {
                    constexpr int s = decltype(ic)::value / 36, p = decltype(ic)::value % 36, o = cv_grp(p), m = cv_m(o), jb = p - cv_first(o);
                    if constexpr (jb == 0) { WAIT_L(cv_wait(o)); TIE(FA[o & 3][0]); TIE(FA[o & 3][1]); }
                    if constexpr (p == 0) { for (int i = 0; i < 8; ++i) TIE(FB[s][i]); }
                    const bf16x8 fa = __builtin_shufflevector(FA[o & 3][0], FA[o & 3][1], 0, 1, 2, 3, 4, 5, 6, 7);
                    ca[jb + m] = __builtin_amdgcn_mfma_f32_16x16x32_bf16(fa, FB[s][jb], ca[jb + m], 0, 0, 0);
                    SB();
                    constexpr int rd = cv_read(p);
                    if constexpr (rd >= 32) CV_B(s + 1, rd - 32);
                    else if constexpr (rd >= 0) CV_A(s, rd >> 1, rd & 1);
                    SB();
                });
                kb += 128; ub -= 64;
            };
#pragma unroll 1
            for (int it = 0; it < QIT; ++it) body();
            {
                WAIT_V(0);
                lb_post(lb + L_SY + 32 + wm * 4, 2);
                WAIT_L(0);
                if (PQ == 17) { lb_wait(lb + L_SY + (lane & 7) * 4, 1); ITEMS(17); }
                if (CVLATE) lb_wait(lb + L_SY + (lane & 7) * 4, 2);
            }
#pragma unroll 1
            for (int it = QIT; it < 4; ++it) body();
            WAIT_L(0);
#pragma unroll
            for (int i = 0; i < 4; ++i) { TIE(FA[i][0]); TIE(FA[i][1]); }
#pragma unroll
            for (int i = 0; i < 8; ++i) TIE(FB[0][i]);
#undef CV_A
#undef CV_B
        }
        TS(7);
        __builtin_amdgcn_s_setprio(3);
        int nb = 1;
#define HB() do { if (nb < 15) BAR(); ++nb; } while (0)
        {
            const float Dd = bf2f(dskl);
            const char* zd = ws + WS_ZT + (size_t)chn * ZP;
            u32x2 kvr[8];
#pragma unroll
            for (int jt = 0; jt < 8; ++jt) kvr[jt] = *LDSP(u32x2, cb + UOFF + (g4 >> 1) * UPL + (16 + 16 * jt + r) * 16 + (1 - (g4 & 1)) * 8);
#pragma unroll
            for (int jt = 0; jt < 8; ++jt) {
                const f32x4 y = ca[jt];
                const u32x2 kq = kvr[jt];
                *LDSP(u32x2, cb + (jt * 4 + (r >> 2)) * 144 + (r & 3) * 32 + g4 * 8) =
                    u32x2{pack2(rb1(y[0]) + rb1(bfhi(kq[1]) * Dd), rb1(y[1]) + rb1(bflo(kq[1]) * Dd)), pack2(rb1(y[2]) + rb1(bfhi(kq[0]) * Dd), rb1(y[3]) + rb1(bflo(kq[0]) * Dd))};
            }
            if (ZB >= 3) HB();
            u32x4 t[4];
#pragma unroll
            for (int it = 0; it < 4; ++it) t[it] = *LDSP(u32x4, cb + (it * 8 + (lane >> 3)) * 144 + (lane & 7) * 16);
            if (!(ABL & 128)) { GST_B128_SC1(l16, t[0], zd, 0); GST_B128_SC1(l16, t[1], zd, 1024); GST_B128_SC1(l16, t[2], zd, 2048); GST_B128_SC1(l16, t[3], zd, 3072); }
            else { TIE(t[0]); TIE(t[1]); TIE(t[2]); TIE(t[3]); }
        }
        if (ZB >= 2) HB();
        if (ZB >= 1) HB();
        WAIT_V(0);
        lb_post(lb + L_SY + 32 + wm * 4, 3);
        if (wm == 0) {
            lb_wait(lb + L_SY + 32 + (lane & 3) * 4, 3);
            if (!(ABL & 1024) && lane == 0) GST_B64_SC1(fpub, tag, flg + FL_Z);
        }
        TS(8);
        HB(); HB(); HB();
        if (!(ABL & 1024) && wm == 0) GLD_B64_SC1(fl, fpoll, flg + FL_Z, 0);
        HB(); HB();
        if (wm == 0) { WAIT_V(0); TIE(fl); lb_post(lb + L_SY + 48, __all(fl[0] == tagA && fl[1] == tagB) ? 2 : 1); }
        lb_wait(lb + L_SY + 48, 1);
        if (__builtin_amdgcn_readfirstlane(*LDSP(unsigned, lb + L_SY + 48)) != 2) {
            while (nb < 15) { BAR(); ++nb; }
            for (unsigned spin = 0; spin < SPIN_CAP; ++spin) {
                GLD_B64_SC1(fl, fpoll, flg + FL_Z, 0);
                WAIT_V(0); TIE(fl);
                if (__all(fl[0] == tagA && fl[1] == tagB)) break;
                __builtin_amdgcn_s_sleep(8);
            }
        }
        u32x4 zv[4];
        {
            ITEMS(18);
            const char* zsrc = ws + WS_ZT + (size_t)(xcd * 128 + wm * 32) * ZP + idx * 128;
#pragma unroll
            for (int it = 0; it < 4; ++it) if (!(ABL & 256)) GLD_B128_SC1(zv[it], (lane >> 3) * ZP + (lane & 7) * 16, zsrc + it * 8 * ZP, 0);
        }
        HB(); HB();
        WAIT_V(0);
        TS(9);
        {
            const unsigned zi = lb + L_ZI + wm * 5120;
#pragma unroll
            for (int it = 0; it < 4; ++it) { TIE(zv[it]); *LDSP(u32x4, zi + ((lane >> 3) + 8 * it) * 160 + (lane & 7) * 16) = zv[it]; }
            v4i16 z[8];
#pragma unroll
            for (int n = 0; n < 8; ++n) z[n] = __builtin_amdgcn_ds_read_tr16_b64_v4i16(LDSP(v4i16, zi + ((n & 1) * 16 + (lane >> 2)) * 160 + (n >> 1) * 32 + (lane & 3) * 8));
            HB();
#pragma unroll
            for (int n = 0; n < 8; ++n) *LDSP(v4i16, lb + L_Z2 + ((n >> 1) * 16 + r) * 264 + wm * 64 + (n & 1) * 32 + g4 * 8) = z[n];
            lb_post(lb + L_SY + 32 + wm * 4, 4);
        }
        HB();
        lb_wait(lb + L_SY + 32 + (lane & 3) * 4, 4);
        char* const oa = a.out + (size_t)(idx * 64 + wm * 16) * 8192 + xcd * 512;
        u32x2 zz[8];
#pragma unroll
        for (int n = 0; n < 8; ++n) zz[n] = *LDSP(u32x2, lb + L_Z2 + (wm * 16 + n * 2 + lh) * 264 + l31 * 8);
#pragma unroll
        for (int n = 0; n < 8; ++n) {
            TIE(qv[n]);
            const u32x2 z = zz[n];
            const f32x4 o = {rb1(bflo(qv[n][0]) * bflo(z[0])), rb1(bfhi(qv[n][0]) * bfhi(z[0])), rb1(bflo(qv[n][1]) * bflo(z[1])), rb1(bfhi(qv[n][1]) * bfhi(z[1]))};
            if (!(ABL & 512)) GST_B128_NT(lh * 8192 + l31 * 16, o, oa + n * 16384, 0); else { f32x4 o2 = o; TIE(o2); }
            if (n < 7 && (n + 1) % OS == 0) HB();
        }
#undef HB
        asm volatile(".Lfk_code_a:");
        TSE(10);
        TS_FLUSH();
        return;
    }

    u32x2 fl;
    unsigned dv[4];
    {
        const int v = l15 ^ g4, hi = v >> 3;
        const int r0 = wn ? tn * 64 + (wm >> 1) * 32 : tm * 128 + (wm >> 1) * 64 + (wm & 1) * 4;
        const unsigned d0 = (r0 + ((g4 * (wn ? 2 : 9)) & (wn ? 6 : 17))) * 2048 + (v & 7) * 16, odd = wn ? 11 : 14;
        dv[0] = d0 + (hi << odd); dv[2] = d0 + ((hi ^ 1) << odd) + (wn ? 32768 : 4096);
        dv[1] = (dv[0] ^ 64) + (wn ? 16384 : 65536); dv[3] = (dv[2] ^ 64) + (wn ? 16384 : 65536);
    }
    const int rot7 = ((((ROT >> 12) & 15) * (idx & 7) + ((ROT >> 8) & 15) * (idx >> 3) + ((ROT >> 4) & 15) * (xcd >> 1) + (ROT & 15) * (xcd & 1)) & 15) << 7;
    const char* g1src = wn ? ((wm & 1) ? a.bim : a.bre) : a.x;
#define G1_ISSUE(st, i) DMA(g1dst + g1_slot(st) + (i) * 1024, dv[i], g1src + ((((st) << 7) + rot7) & 1920))
    static_for<0, PRE>([&](auto ic) { constexpr int n = decltype(ic)::value; G1_ISSUE(n >> 2, n & 3); });
    TS(14);
    float Lr, Li, cr, ci, L8r, L8i, L16r, L16i, L64r, L64i, Qr, Qi, Wr, Wi, Vr, Vi, Ur, Ui;
    f32x2 L1, C1, L16, Q2r, Q2i;
    unsigned cw;
    {
        WAIT_V((ABL & 1) ? PRE : 4 + PRE);
        cw = take(sm);
        const unsigned c_dt = (__shfl(cw, 16 + (l15 >> 1)) >> ((l15 & 1) * 16)) & 0xffffu, c_re = (__shfl(cw, 32 + (l15 >> 1)) >> ((l15 & 1) * 16)) & 0xffffu,
                       c_im = (__shfl(cw, 48 + (l15 >> 1)) >> ((l15 & 1) * 16)) & 0xffffu;
        const float dt = rbf(expf(bf2f(c_dt)));
        const float lr = bf2f(c_re), li = bf2f(c_im);
        const float e = expf(dt * lr);
        float sn, cs;
        sincosf(dt * li, &sn, &cs);
        Lr = e * cs; Li = e * sn;
        const float nr = Lr - 1.f, ni = Li, den = lr * lr + li * li;
        cr = (nr * lr + ni * li) / den; ci = (ni * lr - nr * li) / den;
        float pr = Lr, pi = Li, tr, ti;
        Qr = cr; Qi = ci; Wr = Vr = 1.f; Wi = Vi = 0.f;
#pragma unroll
        for (int it = 1; it <= 10; ++it) {
            cmul(pr, pi, pr, pi, tr, ti); pr = tr; pi = ti;
            if (it == 3) { L8r = pr; L8i = pi; }
            if (it == 4) { L16r = pr; L16i = pi; }
            if (it == 6) { L64r = pr; L64i = pi; }
            if (it == 7) { Ur = pr; Ui = pi; }
            if (it == 4 || it == 5) { cmul(Vr, Vi, pr, pi, tr, ti); if (!((run >> (it - 4)) & 1)) { Vr = tr; Vi = ti; } }
            if (it >= 4 && it <= 6) { cmul(Qr, Qi, pr, pi, tr, ti); if ((run >> (it - 4)) & 1) { Qr = tr; Qi = ti; } }
            if (it >= 8) { cmul(Wr, Wi, pr, pi, tr, ti); if (!((run >> (it - 8)) & 1)) { Wr = tr; Wi = ti; } }
        }
        cmul(Ur, Ui, Wr, Wi, tr, ti);
        Ur = 2 * run >= 16 - tm ? tr : 0.f; Ui = 2 * run >= 16 - tm ? ti : 0.f;
        if (2 * run + 1 < 16 - tm) Wr = Wi = 0.f;
        cmul(Qr, Qi, L8r, L8i, tr, ti);
        L1 = f32x2{Lr, Li}; C1 = f32x2{cr, ci}; L16 = f32x2{L16r, L16i}; Q2r = f32x2{Qr, tr}; Q2i = f32x2{Qi, ti};
        TIE(Ur); TIE(Ui); TIE(L1); TIE(C1); TIE(L16); TIE(Q2r); TIE(Q2i);
        TIE(L8r); TIE(L8i); TIE(L64r); TIE(L64i); TIE(Vr); TIE(Vi); TIE(Wr); TIE(Wi);
    }
    TAKE_TAG(cw);
    SLICE_TABLE();
    const unsigned tagG = tagA ^ tagB ^ 0x9E3779B9u;
    const int j0 = 4 * run - (32 - 2 * tm);
    const int pneg = 16 * (wm >> 1) + 15 - (32 - 2 * tm);
    unsigned eo[2], pes = lb + L_ES + wid * 256 + l15 * 16;
    unsigned plr = lb + L_AGG + st * 8 + run * 512, est = ((tm * 2 + (wm >> 1)) * 1024 + tn * 64 + st) * 16;
    float nL8i = -L8i, nVi = -Vi;
#pragma unroll
    for (int i = 0; i < 2; ++i) { eo[i] = (j0 + 2 * i < 0 ? 0 : j0 + 2 * i) * 16384 + (tn * 64 + st) * 16; TIE(eo[i]); }
    TIE(plr); TIE(pes); TIE(est); TIE(nL8i); TIE(nVi);
    unsigned hw[2], ta[2];
    {
        const unsigned ht = lb + L_HT + wid * 4096;
#pragma unroll
        for (int j = 0; j < 2; ++j) {
            const int pr = (8 * (g4 & 1) + 4 * j + (l15 >> 2)) ^ ((g4 & 1) * 4), pw = l15 ^ ((l15 >> 3) * 4);
            ta[j] = ht + (g4 >> 1) * 512 + pr * 32 + ((((l15 >> 1) ^ (pr >> 2)) & 1) << 4) + 8 * (lane & 1);
            hw[j] = ht + g4 * 1024 + pw * 32 + (((j ^ (pw >> 2)) & 1) << 4);
            TIE(ta[j]); TIE(hw[j]);
        }
    }
    float nL64i = -L64i, nWi = -Wi, nUi = -Ui;
    unsigned po = pes + ((wm & 2) ? -512 : 512);
    TIE(nL64i); TIE(nWi); TIE(nUi); TIE(po);
    TS(1);
    WAIT_V(PRE);
    BAR();
    static_for<PRE, PRO>([&](auto ic) { constexpr int n = decltype(ic)::value; G1_ISSUE(n >> 2, n & 3); });
    TS(15);

    f32x4 acc[4][2];
    {
#pragma unroll
        for (int i = 0; i < 4; ++i) { acc[i][0] = f32x4{0, 0, 0, 0}; acc[i][1] = f32x4{0, 0, 0, 0}; }
        const int aoff = img_off((wm >> 1) * 64 + l15, g4), boff = img_off(128 + wn * 64 + (wm & 1) * 16 + l15, g4);
        unsigned aad[2][2], bad[2];
#pragma unroll
        for (int ks = 0; ks < 2; ++ks) { aad[ks][0] = lb + (aoff ^ (ks * 64)); aad[ks][1] = lb + (aoff ^ (ks * 64) ^ 128); bad[ks] = lb + (boff ^ (ks * 64)); }
        bf16x8 fa[2][4], fb[2][2];
        WAIT_V(PRO - 4); BAR();
        TS(2);
#define G1_FRAG(g, n) do { constexpr int bo_ = g1_slot((g) >> 1), hi_ = bo_ & ~65535, x_ = (n) < 3 ? (n) >> 1 : (n) - 2; \
        if constexpr ((n) == 0 || (n) == 2) DSR128(fb[(g) & 1][x_], bad[(g) & 1] + hi_, bo_ - hi_ + x_ * 4096); \
        else DSR128(fa[(g) & 1][x_], aad[(g) & 1][x_ & 1] + hi_, bo_ - hi_ + x_ * 2048); } while (0)
        static_for<0, 6>([&](auto ic) { constexpr int n = decltype(ic)::value; G1_FRAG(0, n); });
        static_for<0, 32>([&](auto ic) {
            constexpr int g = decltype(ic)::value, gn = g + 1, n0 = g1_upto(g - 1), nd = g1_upto(g) - n0;
            constexpr bool bar = (gn & 1) == 0 && gn < 32;
            if constexpr (bar) {
                static_assert(g1_cnt(gn >> 1) >= 0, "schedule");
                WAIT_V(g1_cnt(gn >> 1)); WAIT_L(0); BAR();
                if constexpr (((gn >> 1) & 3) == 0 || gn >= 26) TS(gn >= 26 ? 23 + (gn >> 1) - 13 : 16 + (gn >> 3));
#pragma unroll
                for (int i = 0; i < 4; ++i) TIE(fa[g & 1][i]);
                TIE(fb[g & 1][0]); TIE(fb[g & 1][1]);
            }
            static_for<0, 8>([&](auto mc) {
                constexpr int m = decltype(mc)::value, i = m >> 1, j = m & 1;
                if constexpr (!bar && (i == 0 || j == 0)) {
                    WAIT_L(5 - (i == 0 ? j + 1 : i + 2) + (gn < 32 ? (m < 6 ? m : 6) : 0));
                    TIE(fa[g & 1][i]); TIE(fb[g & 1][j]);
                }
                acc[i][j] = __builtin_amdgcn_mfma_f32_16x16x32_bf16(fa[g & 1][i], fb[g & 1][j], acc[i][j], 0, 0, 0);
                SB();
                if constexpr (gn < 32 && m < 6) G1_FRAG(gn, m);
                constexpr int d = nd > 2 ? ((m & 1) ? m >> 1 : -1) : (m == DP0 ? 0 : m == DP1 ? 1 : -1);
                if constexpr (d >= 0 && d < nd) G1_ISSUE((n0 + d) >> 2, (n0 + d) & 3);
                SB();
            });
        });
#undef G1_FRAG
    }
    TS(3);
    __builtin_amdgcn_s_setprio(MPRIO);
    f32x2 ur[8], ui[8], hr, hi;
    const unsigned pmine = lb + L_SY + wid * 4, pall = lb + L_SY + (lane & 7) * 4;
    u32x4 eg[4];
#define E_LOADS "global_load_dwordx4 v[104:107], %[a0], %[b0] sc1\n\tglobal_load_dwordx4 v[108:111], %[a0], %[b1] sc1\n\t" \
                "global_load_dwordx4 v[112:115], %[a1], %[b0] sc1\n\tglobal_load_dwordx4 v[116:119], %[a1], %[b1] sc1\n"
#define E_POLL() asm volatile("s_cmp_lt_i32 %[on], 0\n\ts_cbranch_scc1 1f\n\t" E_LOADS "1:" : "={v[104:107]}"(eg[0]), "={v[108:111]}"(eg[1]), "={v[112:115]}"(eg[2]), "={v[116:119]}"(eg[3]) \
        : [a0] "v"(eo[0]), [a1] "v"(eo[1]), [b0] "s"(ws + WS_E), [b1] "s"(ws + WS_E + 16384), [on] "s"(pneg) : "scc", "memory")
    {
        asm volatile("s_nop 7" : "+v"(acc[0][0]), "+v"(acc[0][1]), "+v"(acc[1][0]), "+v"(acc[1][1]), "+v"(acc[2][0]), "+v"(acc[2][1]), "+v"(acc[3][0]), "+v"(acc[3][1]));
#pragma unroll
        for (int p = 0; p < 8; ++p) { ur[p] = f32x2{acc[p >> 1][0][2 * (p & 1)], acc[p >> 1][0][2 * (p & 1) + 1]}; ui[p] = f32x2{acc[p >> 1][1][2 * (p & 1)], acc[p >> 1][1][2 * (p & 1) + 1]}; }
        f32x2 gr = ur[0], gi = ui[0];
#pragma unroll
        for (int p = 1; p < 8; ++p) { f32x2 nr, ni; pk_cmad(nr, ni, L1, gr, gi, ur[p], ui[p]); gr = nr; gi = ni; }
        float fr, fi;
        cmad(fr, fi, L8r, L8i, nL8i, gr[0], gi[0], gr[1], gi[1]);
        {
            float wr, wi;
            cmul4(wr, wi, Vr, Vi, nVi, fr, fi);
            wr = sum4(wr); wi = sum4(wi);
            const u32x4 gq = {__float_as_uint(wr), tagA, __float_as_uint(wi), tagG};
            if (g4 == 0) GST_B128_SC1(est, gq, ws + WS_E, 0);
        }
        *LDSP(f32x2, plr + 7 * 512) = f32x2{fr, fi};
        *LDSP(unsigned, pes + 8) = 0;
        TS(21);
        if (EP == 2) E_POLL();
        auto coef = [&]() {
#pragma unroll
            for (int p = 0; p < 8; ++p) { f32x2 nr, ni; pk_conj_mul(nr, ni, C1, ur[p], ui[p]); ur[p] = nr; ui[p] = ni; TIE(ur[p]); TIE(ui[p]); }
        };
        if (CFB) coef();
        WAIT_L(0); BAR();
        f32x2 e[7];
#pragma unroll
        for (int j = 0; j < 7; ++j) e[j] = *LDSP(f32x2, plr + j * 512);
        if (EP == 1) E_POLL();
        if (!CFB) coef();
#pragma unroll
        for (int j = 1; j < 7; ++j) cmac2(e[j], L16, e[j - 1]);
        float mr, mi;
        cmad(mr, mi, L8r, L8i, nL8i, e[6][0], e[6][1], gr[0], gi[0]);
        pk_conj_mul(hr, hi, C1, f32x2{e[6][0], mr}, f32x2{e[6][1], mi});
        TIE(hr); TIE(hi);
        TS(4);
    }
    f32x2 G;
    {
        float pr = 0.f, pi = 0.f;
        if (!EP) E_POLL();
        if (pneg >= 0) {
            unsigned t0, t1, t2, left = SPIN_CAP;
            asm volatile("s_branch 2f\n1:\n\ts_sleep %[sl]\n\t" E_LOADS "2:\n\ts_waitcnt vmcnt(0)\n\t"
                         "v_bitop3_b32 %[t0], v105, v109, %[ta] bitop3:0x7e\n\tv_bitop3_b32 %[t1], v113, v117, %[ta] bitop3:0x7e\n\tv_bitop3_b32 %[t2], v107, v111, %[tg] bitop3:0x7e\n\t"
                         "v_or3_b32 %[t0], %[t0], %[t1], %[t2]\n\tv_bitop3_b32 %[t1], v115, v119, %[tg] bitop3:0x7e\n\tv_or_b32 %[t0], %[t0], %[t1]\n\t"
                         "v_cmp_ne_u32 vcc, 0, %[t0]\n\ts_cbranch_vccz 3f\n\ts_sub_u32 %[n], %[n], 1\n\ts_cbranch_scc0 1b\n3:"
                         : "+{v[104:107]}"(eg[0]), "+{v[108:111]}"(eg[1]), "+{v[112:115]}"(eg[2]), "+{v[116:119]}"(eg[3]), [t0] "=&v"(t0), [t1] "=&v"(t1), [t2] "=&v"(t2), [n] "+s"(left)
                         : [a0] "v"(eo[0]), [a1] "v"(eo[1]), [b0] "s"(ws + WS_E), [b1] "s"(ws + WS_E + 16384), [ta] "s"(tagA), [tg] "s"(tagG), [sl] "n"(ESL) : "vcc", "scc", "memory");
            float ar = __uint_as_float(eg[1][0]), ai = __uint_as_float(eg[1][2]), br = __uint_as_float(eg[3][0]), bi = __uint_as_float(eg[3][2]);
            cmac(ar, ai, L64r, L64i, nL64i, __uint_as_float(eg[0][0]), __uint_as_float(eg[0][2]));
            cmac(br, bi, L64r, L64i, nL64i, __uint_as_float(eg[2][0]), __uint_as_float(eg[2][2]));
            cmul4(pr, pi, Ur, Ui, nUi, ar, ai);
            cmac(pr, pi, Wr, Wi, nWi, br, bi);
            pr = sum4(pr); pi = sum4(pi);
        }
        TS(5);
        *LDSP(u32x4, pes) = u32x4{__float_as_uint(pr), __float_as_uint(pi), 1u, 0u};
        if (PQ == 17) lb_post(pmine, 1);
        u32x4 o;
        asm volatile("1:\n\tds_read_b128 %0, %1\n\ts_waitcnt lgkmcnt(0)\n\tv_cmp_eq_u32 vcc, 0, v122\n\ts_cbranch_vccnz 1b" : "=&{v[120:123]}"(o) : "v"(po) : "vcc", "memory");
        G = f32x2{pr + __uint_as_float(o[0]), pi + __uint_as_float(o[1])};
    }
    TS(6);
    {
        pk_conj_mac(hr, hi, G, Q2r, Q2i);
#pragma unroll
        for (int p = 0; p < 8; ++p) { pk_cmac_conj(ur[p], ui[p], L1, hr, hi); hr = ur[p]; hi = ui[p]; }
        const char* hd = ws + WS_H + (size_t)((tm * 4 + (wm & 2)) * 2 + wn) * HP + (tn * 4 + (wm & 1) * 2) * 1024;
#pragma unroll
        for (int e2 = 0; e2 < 2; ++e2)
#pragma unroll
            for (int c = 0; c < 2; ++c) {
                const f32x2* o = c ? ui : ur;
                *LDSP(u32x4, hw[e2] + c * 512) = u32x4{pack2(o[0][e2], o[1][e2]), pack2(o[2][e2], o[3][e2]), pack2(o[4][e2], o[5][e2]), pack2(o[6][e2], o[7][e2])};
            }
        v4i16 t[4][2];
#pragma unroll
        for (int n = 0; n < 4; ++n)
#pragma unroll
            for (int j = 0; j < 2; ++j) t[n][j] = __builtin_amdgcn_ds_read_tr16_b64_v4i16(LDSP(v4i16, ta[j] + n * 1024));
        lb_post(pmine, 2);
        static_for<0, 4>([&](auto ic) {
            constexpr int n = decltype(ic)::value;
            const bf16x8 f = __builtin_shufflevector(t[n][0], t[n][1], 0, 1, 2, 3, 4, 5, 6, 7);
            GST_B128_SC1(l16, f, hd + (n >> 1) * 2 * HP + ((n & 1) ^ wn) * 1024, 0);
        });
    }
    TS(7);
    bf16x8 fa2[AW];
    u32x2 xv[4], dsv;
    const char* Aw = ws + WS_H + (size_t)((tm * 4 + wm) * 2 + wn) * HP;
    const char* g2src = (wn ? a.cim : a.cre) + (size_t)(tn * 64 + wm) * 2048;
    const unsigned g2dst = lb + L_C + wid * 2048;
    unsigned g2v[2];
#pragma unroll
    for (int i = 0; i < 2; ++i) {
        const int pair = i * 4 + (lane >> 4), v = (lane & 15) ^ pair ^ ((wm & 1) * 8);
        g2v[i] = (pair * 2 + (v >> 3)) * 8192 + (v & 7) * 16;
    }
    unsigned cad[2][2];
#pragma unroll
    for (int ks = 0; ks < 2; ++ks)
#pragma unroll
        for (int i = 0; i < 2; ++i) cad[ks][i] = lb + L_C + (img_off((g4 >> 1) * 64 + l15, 4 * wn + (g4 & 1)) ^ (ks * 32) ^ (i * 128));
#define G2_ISSUE(st, i) DMA(g2dst + (((st) + 2) & 3) * G2STAGE + (i) * 1024, g2v[i], g2src + (SLICE(st) << 7))
#define G2_A(g) GLD_B128_SC1(fa2[(g) % AW], l16, Aw + (SLICE((g) >> 2) << 12), ((g) & 3) * 1024)
    {
        const char* xs = a.x + (size_t)(tm * 128 + wm * 32 + wn * 16) * 2048 + tn * 128;
        static_for<0, 4>([&](auto ic) { constexpr int it = decltype(ic)::value; GLD_B64(xv[it], g4 * 8192 + l15 * 8, xs + (it >> 1) * 4096, (it & 1) * 2048); });
        GLD_B64(dsv, l15 * 8, a.dskip + tn * 128, 0);
        {
            lb_wait(lb + L_SY + (wid ^ 1) * 4, 2);
            bf16x8 fo[4];
#pragma unroll
            for (int n = 0; n < 4; ++n) {
                const int o = (n >> 1) * 4096 + ((n & 1) ^ wn) * 1024 - (wm & 1) * 2048;
                fo[n] = __builtin_shufflevector(__builtin_amdgcn_ds_read_tr16_b64_v4i16(LDSP(v4i16, ta[0] + o)), __builtin_amdgcn_ds_read_tr16_b64_v4i16(LDSP(v4i16, ta[1] + o)), 0, 1, 2, 3, 4, 5, 6, 7);
            }
            lb_wait(lb + L_SY + 32 + (lane & 3) * 4, 2);
#pragma unroll
            for (int ks = 0; ks < 2; ++ks)
#pragma unroll
                for (int i = 0; i < 4; ++i) {
                    const bf16x8 c = *LDSP(bf16x8, cad[ks][i & 1] + 2 * G2STAGE + i * 2048);
#pragma unroll
                    for (int j = 0; j < 2; ++j) acc[i][j] = __builtin_amdgcn_mfma_f32_16x16x32_bf16(fo[2 * ks + j], c, ks ? acc[i][j] : f32x4{0, 0, 0, 0}, 0, 0, 0);
                }
#pragma unroll
            for (int i = 0; i < 4; ++i) { TIE(acc[i][0]); TIE(acc[i][1]); }
        }
        TS(16);
        WAIT_V(5);
        lb_post(pmine, 3); lb_wait(pall, 3);
        if (wid == 0 && lane < 16) GST_B64_SC1((tm * 16 + lane) * 128 + tn * 8, tag, flg + FL_H);
        TS(8);
        static_for<4, 10>([&](auto ic) { constexpr int n = decltype(ic)::value; G2_ISSUE(n >> 1, n & 1); });
        TS(13);
        if (wid == 0) {
#pragma unroll 1
            for (unsigned spin = 0; spin < SPIN_CAP; ++spin) {
                GLD_B64_SC1(fl, (tm * 16 + tn) * 128 + (lane & 15) * 8, flg + FL_H, 0);
                WAIT_V(0); TIE(fl);
                if (__all(fl[0] == tagA && fl[1] == tagB)) break;
            }
            lb_post(pmine, 4);
            if (b == 255 && lane == 0) GST_B64_SC1(0u * lane, tag, flg + FL_EPOCH);
        } else lb_wait(lb + L_SY, 4);
        TS(20);
        static_for<4, 4 + AW>([&](auto ic) { constexpr int g = decltype(ic)::value; G2_A(g); });
    }
    TS(9);
    __builtin_amdgcn_s_setprio(0);
    f32x4 y[4];
    {
        bf16x8 fb[2][4];
#define G2_FRAG(g, i) DSR128(fb[(g) & 1][i], cad[(g) & 1][(i) & 1], ((((g) >> 1) + 2) & 3) * G2STAGE + (i) * 2048)
        static_for<0, 4>([&](auto ic) { constexpr int i = decltype(ic)::value; G2_FRAG(2, i); });
        static_for<2, 32>([&](auto ic) {
            constexpr int g = decltype(ic)::value, gn = g + 1;
            constexpr bool bar = (gn & 1) == 0 && gn < 32;
            if constexpr (bar) {
                constexpr int ca = g2_cnt(2 * g, 2 * g), cs = gn >= 10 ? g2_cnt(100 + gn - 6, 2 * g) : 63;
                static_assert(cs >= 0, "schedule");
                WAIT_V(AWB && ca < cs ? ca : cs); WAIT_L(0); BAR();
                if constexpr (!AWB) WAIT_V(ca);
                if constexpr (g == 3) TS(10);
                if constexpr (g == 17 || g == 25) TS(26 + (g >> 3));
#pragma unroll
                for (int i = 0; i < 4; ++i) TIE(fb[g & 1][i]);
                TIE(fa2[(2 * g) % AW]);
            }
            static_for<0, 8>([&](auto mc) {
                constexpr int m = decltype(mc)::value, i = m & 3, j = m >> 2, f = 2 * g + j;
                static_assert(g2_cnt(f, f) >= 0, "schedule");
                if constexpr (i == 0 && (j || !bar)) { WAIT_V(g2_cnt(f, f)); TIE(fa2[f % AW]); }
                if constexpr (!bar && j == 0) { WAIT_L(gn < 32 ? 3 : 3 - i); TIE(fb[g & 1][i]); }
                acc[i][j] = __builtin_amdgcn_mfma_f32_16x16x32_bf16(fa2[f % AW], fb[g & 1][i], acc[i][j], 0, 0, 0);
                SB();
                if constexpr (gn < 32 && m < 4) G2_FRAG(gn, m);
                if constexpr (m == 5 && g2_dma(g)) G2_ISSUE(gn / 2 + 3, gn & 1);
                if constexpr (i == 3 && f + AW < 64) G2_A(f + AW);
                if constexpr (g >= YS && g < YS + 4 && m == 6) {
                    constexpr int it = g - YS;
                    TIE(xv[it]); TIE(dsv);
                    y[it] = f32x4{rb1(bflo(xv[it][0]) * bflo(dsv[0])), rb1(bfhi(xv[it][0]) * bfhi(dsv[0])), rb1(bflo(xv[it][1]) * bflo(dsv[1])), rb1(bfhi(xv[it][1]) * bfhi(dsv[1]))};
                    TIE(y[it]);
                }
                SB();
            });
        });
#undef G2_FRAG
    }
    TS(11);
    {
#pragma unroll
        for (int i = 0; i < 4; ++i) *LDSP(f32x4, lb + L_Y2 + wid * 4096 + l16 + i * 1024) = acc[i][1];
        lb_post(pmine, 5);
#pragma unroll
        for (int r = 0; r < 4; ++r) {
            y[r] = f32x4{2.f * acc[0][0][r] + y[r][0], 2.f * acc[1][0][r] + y[r][1], 2.f * acc[2][0][r] + y[r][2], 2.f * acc[3][0][r] + y[r][3]};
            TIE(y[r]);
        }
        lb_wait(lb + L_SY + (wid ^ 4) * 4, 5);
        char* const ob = a.out + (size_t)(tm * 128 + wm * 32 + wn * 16) * 8192 + 4096 + tn * 256;
        f32x4 w[4];
#pragma unroll
        for (int i = 0; i < 4; ++i) w[i] = *LDSP(f32x4, lb + L_Y2 + (wid ^ 4) * 4096 + l16 + i * 1024);
#pragma unroll
        for (int r = 0; r < 4; ++r) {
            const f32x4 o = {2.f * w[0][r] + y[r][0], 2.f * w[1][r] + y[r][1], 2.f * w[2][r] + y[r][2], 2.f * w[3][r] + y[r][3]};
            GST_B128_NT(g4 * 32768 + l15 * 16, o, ob + r * 8192, 0);
        }
    }
    asm volatile(".Lfk_code_b:");
    TSE(12);
    TS_FLUSH();
}

}

extern "C" void kernel_launch(void* const* d_in, const int* in_sizes, int n_in, void* d_out, int out_size, void* d_ws, size_t ws_size,
                              hipStream_t stream) {
    (void)in_sizes; (void)n_in; (void)out_size; (void)ws_size;
    fk::Args a;
    a.q = (const char*)d_in[0]; a.k = (const char*)d_in[1]; a.v = (const char*)d_in[2]; a.sk = (const char*)d_in[3];
    a.ssmk = (const char*)d_in[4]; a.ssmD = (const char*)d_in[5]; a.x = (const char*)d_in[6]; a.lre = (const char*)d_in[7];
    a.lim = (const char*)d_in[8]; a.bre = (const char*)d_in[9]; a.bim = (const char*)d_in[10]; a.cre = (const char*)d_in[11];
    a.cim = (const char*)d_in[12]; a.logdt = (const char*)d_in[13]; a.dskip = (const char*)d_in[14];
    a.out = (char*)d_out; a.ws = (char*)d_ws;
    hipLaunchKernelGGL(fk::fused, dim3(256), dim3(768), 0, stream, a);
}
```

```cpp
#define TOUCH 0
#include <hip/hip_runtime.h>
#include <stdint.h>
#include <type_traits>

namespace fk {

constexpr int S = 2048, D = 1024, P = 1024;

typedef __attribute__((ext_vector_type(8))) short bf16x8;
typedef __attribute__((ext_vector_type(4))) short s16x4;
typedef __attribute__((ext_vector_type(16))) float f32x16;
typedef __attribute__((ext_vector_type(4))) float f32x4;
typedef __attribute__((ext_vector_type(2))) float f32x2;
typedef __attribute__((ext_vector_type(4))) unsigned u32x4;
typedef __attribute__((ext_vector_type(2))) unsigned u32x2;
#define LDSP(T, a) ((__attribute__((address_space(3))) T*)(uintptr_t)(unsigned)(a))

__device__ __forceinline__ float bf2f(unsigned v) { return __uint_as_float(v << 16); }
__device__ __forceinline__ float bflo(unsigned w) { return __uint_as_float(w << 16); }
__device__ __forceinline__ float bfhi(unsigned w) { return __uint_as_float(w & 0xffff0000u); }
__device__ __forceinline__ uint16_t f2bf(float f) { __bf16 b = (__bf16)f; return __builtin_bit_cast(uint16_t, b); }
__device__ __forceinline__ float rbf(float f) { return bf2f(f2bf(f)); }
typedef __attribute__((ext_vector_type(2))) __bf16 bf16x2;
__device__ __forceinline__ unsigned pack2(float lo, float hi) { return __builtin_bit_cast(unsigned, __builtin_convertvector(f32x2{lo, hi}, bf16x2)); }
__device__ __forceinline__ float rb1(float x) { return __uint_as_float(pack2(0.f, x)); }
__device__ __forceinline__ void cmac(float& xr, float& xi, float ar, float ai, float hr, float hi) {
    asm("v_fmac_f32 %0, %2, %4\n\tv_fmac_f32 %1, %2, %5\n\tv_fma_f32 %0, -%3, %5, %0\n\tv_fmac_f32 %1, %3, %4" : "+v"(xr), "+v"(xi) : "v"(ar), "v"(ai), "v"(hr), "v"(hi));
}
__device__ __forceinline__ void cmad(float& o_r, float& o_i, float ar, float ai, float hr, float hi, float er, float ei) {
    asm("v_fma_f32 %0, %2, %4, %6\n\tv_fma_f32 %1, %2, %5, %7\n\tv_fma_f32 %0, -%3, %5, %0\n\tv_fmac_f32 %1, %3, %4" : "=&v"(o_r), "=&v"(o_i) : "v"(ar), "v"(ai), "v"(hr), "v"(hi), "v"(er), "v"(ei));
}
__device__ __forceinline__ f32x2 cmul4(f32x2 a, f32x2 h) {
    float o_r, o_i;
    asm("v_mul_f32 %0, %2, %4\n\tv_mul_f32 %1, %2, %5\n\tv_fma_f32 %0, -%3, %5, %0\n\tv_fmac_f32 %1, %3, %4" : "=&v"(o_r), "=&v"(o_i) : "v"(a[0]), "v"(a[1]), "v"(h[0]), "v"(h[1]));
    return f32x2{o_r, o_i};
}

#define PK_LO " op_sel:[0,0,0] op_sel_hi:[0,1,1]"
#define PK_HI " op_sel:[1,0,0] op_sel_hi:[1,1,1]"
#define PK_NEG " neg_lo:[1,0,0] neg_hi:[1,0,0]"
__device__ __forceinline__ void pk_cmad(f32x2& o_r, f32x2& o_i, f32x2 a, f32x2 hr, f32x2 hi, f32x2 er, f32x2 ei) {
    asm("v_pk_fma_f32 %0, %2, %3, %5" PK_LO "\n\tv_pk_fma_f32 %1, %2, %4, %6" PK_LO "\n\tv_pk_fma_f32 %0, %2, %4, %0" PK_HI PK_NEG "\n\tv_pk_fma_f32 %1, %2, %3, %1" PK_HI
        : "=&v"(o_r), "=&v"(o_i) : "v"(a), "v"(hr), "v"(hi), "v"(er), "v"(ei));
}
__device__ __forceinline__ void pk_cmac_conj(f32x2& xr, f32x2& xi, f32x2 a, f32x2 hr, f32x2 hi) {
    asm("v_pk_fma_f32 %0, %2, %3, %0" PK_LO "\n\tv_pk_fma_f32 %1, %2, %4, %1" PK_LO "\n\tv_pk_fma_f32 %0, %2, %4, %0" PK_HI "\n\tv_pk_fma_f32 %1, %2, %3, %1" PK_HI PK_NEG
        : "+v"(xr), "+v"(xi) : "v"(a), "v"(hr), "v"(hi));
}
__device__ __forceinline__ void pk_conj_mul(f32x2& o_r, f32x2& o_i, f32x2 a, f32x2 hr, f32x2 hi) {
    asm("v_pk_mul_f32 %0, %2, %3 op_sel:[0,0] op_sel_hi:[0,1]\n\tv_pk_mul_f32 %1, %2, %4 op_sel:[0,0] op_sel_hi:[0,1] neg_lo:[1,0] neg_hi:[1,0]\n\t"
        "v_pk_fma_f32 %0, %2, %4, %0" PK_HI PK_NEG "\n\tv_pk_fma_f32 %1, %2, %3, %1" PK_HI PK_NEG : "=&v"(o_r), "=&v"(o_i) : "v"(a), "v"(hr), "v"(hi));
}
__device__ __forceinline__ void pk_conj_mac(f32x2& xr, f32x2& xi, f32x2 a, f32x2 hr, f32x2 hi) {
    asm("v_pk_fma_f32 %0, %2, %3, %0" PK_LO "\n\tv_pk_fma_f32 %1, %2, %4, %1" PK_LO PK_NEG "\n\tv_pk_fma_f32 %0, %2, %4, %0" PK_HI PK_NEG "\n\tv_pk_fma_f32 %1, %2, %3, %1" PK_HI PK_NEG
        : "+v"(xr), "+v"(xi) : "v"(a), "v"(hr), "v"(hi));
}
__device__ __forceinline__ void cmac2(f32x2& x, f32x2 a, f32x2 h) {
    asm("v_pk_fma_f32 %0, %1, %2, %0" PK_LO "\n\tv_pk_fma_f32 %0, %1, %2, %0 op_sel:[1,1,0] op_sel_hi:[1,0,1] neg_lo:[1,0,0]" : "+v"(x) : "v"(a), "v"(h));
}
__device__ __forceinline__ float sum4(float x) {
    const auto p = __builtin_amdgcn_permlane32_swap(__float_as_uint(x), __float_as_uint(x), false, false);
    x = __uint_as_float(p[0]) + __uint_as_float(p[1]);
    const auto q = __builtin_amdgcn_permlane16_swap(__float_as_uint(x), __float_as_uint(x), false, false);
    return __uint_as_float(q[0]) + __uint_as_float(q[1]);
}

template <int I, int N, class F>
__device__ __forceinline__ void static_for(F&& f) { if constexpr (I < N) { f(std::integral_constant<int, I>{}); static_for<I + 1, N>(f); } }

#define TS_DECL
#define TS0a()
#define TS0b()
#define TSE(i)
#define TS(i)
#define TS_FLUSH()

#define WAIT_V(n) asm volatile("s_waitcnt vmcnt(%0)" ::"n"((n) > 63 ? 63 : (n)) : "memory")
#define WAIT_L(n) asm volatile("s_waitcnt lgkmcnt(%0)" ::"n"((n) > 15 ? 15 : (n)) : "memory")
#define BAR() asm volatile("s_barrier" ::: "memory")
#define SB() __builtin_amdgcn_sched_barrier(0)
template <class T> __device__ __forceinline__ void tie_(T& x) { asm volatile("" : "+v"(x)); }
#define TIE(x) tie_(x)
__device__ __forceinline__ unsigned take(unsigned x) { unsigned y; asm volatile("v_mov_b32 %0, %1" : "=v"(y) : "v"(x)); return y; }
#define DEF_LD(name, ins) template <int OFF, class T> __device__ __forceinline__ void name(T& dst, unsigned voff, const char* sb) { \
    asm volatile(ins : "=v"(dst) : "v"(voff), "s"(sb), "n"(OFF) : "memory"); }
DEF_LD(gld_u16_, "global_load_ushort %0, %1, %2 offset:%3")
DEF_LD(gld_b32_, "global_load_dword %0, %1, %2 offset:%3")
DEF_LD(gld_b64_, "global_load_dwordx2 %0, %1, %2 offset:%3")
DEF_LD(gld_b128_, "global_load_dwordx4 %0, %1, %2 offset:%3")
DEF_LD(gld_b64_nt_, "global_load_dwordx2 %0, %1, %2 offset:%3 nt")
DEF_LD(gld_b128_nt_, "global_load_dwordx4 %0, %1, %2 offset:%3 nt")
DEF_LD(gld_b64_sc1_, "global_load_dwordx2 %0, %1, %2 offset:%3 sc1")
DEF_LD(gld_b128_sc1_, "global_load_dwordx4 %0, %1, %2 offset:%3 sc1")
#define DEF_ST(name, ins) template <int OFF, class T> __device__ __forceinline__ void name(unsigned voff, const T& v, const char* sb) { \
    asm volatile(ins :: "v"(voff), "v"(v), "s"(sb), "n"(OFF) : "memory"); }
DEF_ST(gst_b128_, "global_store_dwordx4 %0, %1, %2 offset:%3\n\ts_nop 1")
DEF_ST(gst_b128_nt_, "global_store_dwordx4 %0, %1, %2 offset:%3 nt\n\ts_nop 1")
DEF_ST(gst_b128_sc1_, "global_store_dwordx4 %0, %1, %2 offset:%3 sc1\n\ts_nop 1")
DEF_ST(gst_b64_sc1_, "global_store_dwordx2 %0, %1, %2 offset:%3 sc1")
#define GLD_U16(dst, voff, sb) gld_u16_<0>(dst, voff, sb)
#define GLD_B32(dst, voff, sb, off) gld_b32_<(off)>(dst, voff, sb)
#define GLD_B64(dst, voff, sb, off) gld_b64_<(off)>(dst, voff, sb)
#define GLD_B128(dst, voff, sb, off) gld_b128_<(off)>(dst, voff, sb)
#define GLD_B64_NT(dst, voff, sb, off) gld_b64_nt_<(off)>(dst, voff, sb)
#define GLD_B128_NT(dst, voff, sb, off) gld_b128_nt_<(off)>(dst, voff, sb)
#define GLD_B64_SC1(dst, voff, sb, off) gld_b64_sc1_<(off)>(dst, voff, sb)
#define GLD_B128_SC1(dst, voff, sb, off) gld_b128_sc1_<(off)>(dst, voff, sb)
#define GST_B128(voff, v, sb, off) gst_b128_<(off)>(voff, v, sb)
#define GST_B128_NT(voff, v, sb, off) gst_b128_nt_<(off)>(voff, v, sb)
#define GST_B128_SC1(voff, v, sb, off) gst_b128_sc1_<(off)>(voff, v, sb)
#define GST_B64_SC1(voff, v, sb) gst_b64_sc1_<0>(voff, v, sb)
__device__ __forceinline__ void dma_(unsigned ldsaddr, unsigned voff, const char* sb) {
    asm volatile("s_mov_b32 m0, %0\n\ts_nop 0\n\tglobal_load_lds_dwordx4 %1, %2" :: "s"(ldsaddr), "v"(voff), "s"(sb) : "memory"); }
__device__ __forceinline__ void dma_nt_(unsigned ldsaddr, unsigned voff, const char* sb) {
    asm volatile("s_mov_b32 m0, %0\n\ts_nop 0\n\tglobal_load_lds_dwordx4 %1, %2 nt" :: "s"(ldsaddr), "v"(voff), "s"(sb) : "memory"); }
#define DMA(ldsaddr, voff, sb) dma_(ldsaddr, voff, sb)
template <int OFF, class T> __device__ __forceinline__ void dsr128_(T& dst, unsigned addr) { asm volatile("ds_read_b128 %0, %1 offset:%2" : "=v"(dst) : "v"(addr), "n"(OFF)); }
template <int OFF, class T> __device__ __forceinline__ void dsr64_(T& dst, unsigned addr) { asm volatile("ds_read_b64 %0, %1 offset:%2" : "=v"(dst) : "v"(addr), "n"(OFF)); }
#define DSR128(dst, addr, off) dsr128_<(off)>(dst, addr)
#define DSR64(dst, addr, off) dsr64_<(off)>(dst, addr)

__device__ __forceinline__ void kv4(unsigned& o01, unsigned& o23, unsigned ka, unsigned kb, unsigned kc, unsigned kd, const unsigned* w, unsigned v0, unsigned v1) {
    unsigned t1, t3, p0, p1, p2, p3;
    asm(
        "v_dot2_f32_bf16 %0, %8, %19, 0\n\tv_dot2_f32_bf16 %1, %9, %18, 0\n\tv_dot2_f32_bf16 %2, %9, %19, 0\n\tv_dot2_f32_bf16 %3, %10, %18, 0\n\t"
        "v_dot2_f32_bf16 %4, %9, %16, 0\n\tv_dot2_f32_bf16 %5, %9, %17, 0\n\tv_dot2_f32_bf16 %6, %10, %16, 0\n\tv_dot2_f32_bf16 %7, %10, %17, 0\n\t"
        "v_cvt_pk_bf16_f32 %0, %0, %4\n\tv_cvt_pk_bf16_f32 %1, %1, %5\n\tv_cvt_pk_bf16_f32 %2, %2, %6\n\tv_cvt_pk_bf16_f32 %3, %3, %7\n\t"
        "v_dot2_f32_bf16 %4, %9, %15, 0\n\tv_dot2_f32_bf16 %5, %10, %14, 0\n\tv_dot2_f32_bf16 %6, %10, %15, 0\n\tv_dot2_f32_bf16 %7, %11, %14, 0\n\t"
        "v_dot2_f32_bf16 %0, %0, %22, 0\n\tv_dot2_f32_bf16 %1, %1, %22, 0\n\tv_dot2_f32_bf16 %2, %2, %22, 0\n\tv_dot2_f32_bf16 %3, %3, %22, 0\n\t"
        "v_cvt_pk_bf16_f32 %0, %0, %4\n\tv_cvt_pk_bf16_f32 %1, %1, %5\n\tv_cvt_pk_bf16_f32 %2, %2, %6\n\tv_cvt_pk_bf16_f32 %3, %3, %7\n\t"
        "v_dot2_f32_bf16 %4, %10, %12, 0\n\tv_dot2_f32_bf16 %5, %10, %13, 0\n\tv_dot2_f32_bf16 %6, %11, %12, 0\n\tv_dot2_f32_bf16 %7, %11, %13, 0\n\t"
        "v_dot2_f32_bf16 %0, %0, %22, 0\n\tv_dot2_f32_bf16 %1, %1, %22, 0\n\tv_dot2_f32_bf16 %2, %2, %22, 0\n\tv_dot2_f32_bf16 %3, %3, %22, 0\n\t"
        "v_cvt_pk_bf16_f32 %0, %0, %4\n\tv_cvt_pk_bf16_f32 %1, %1, %5\n\tv_cvt_pk_bf16_f32 %2, %2, %6\n\tv_cvt_pk_bf16_f32 %3, %3, %7\n\t"
        "v_dot2_f32_bf16 %0, %0, %22, 0\n\tv_dot2_f32_bf16 %1, %1, %22, 0\n\tv_dot2_f32_bf16 %2, %2, %22, 0\n\tv_dot2_f32_bf16 %3, %3, %22, 0\n\t"
        "v_cvt_pk_bf16_f32 %0, %0, 0\n\tv_cvt_pk_bf16_f32 %1, 0, %1\n\tv_cvt_pk_bf16_f32 %2, %2, 0\n\tv_cvt_pk_bf16_f32 %3, 0, %3\n\t"
        "v_dot2_f32_bf16 %0, %0, %20, 0\n\tv_dot2_f32_bf16 %1, %1, %20, 0\n\tv_dot2_f32_bf16 %2, %2, %21, 0\n\tv_dot2_f32_bf16 %3, %3, %21, 0\n\t"
        "s_nop 1\n\tv_cvt_pk_bf16_f32 %0, %1, %0\n\tv_cvt_pk_bf16_f32 %2, %3, %2\n\t"
        : "=&v"(o01), "=&v"(t1), "=&v"(o23), "=&v"(t3), "=&v"(p0), "=&v"(p1), "=&v"(p2), "=&v"(p3)
        : "v"(ka), "v"(kb), "v"(kc), "v"(kd), "v"(w[0]), "v"(w[1]), "v"(w[2]), "v"(w[3]), "v"(w[4]), "v"(w[5]), "v"(w[6]), "v"(w[7]), "v"(v0), "v"(v1), "s"(0x3f803f80u));
}
template <int E> __device__ __forceinline__ void sy_post(unsigned myaddr) { asm volatile("ds_write_b32 %0, %0 offset:%1" :: "v"(myaddr), "n"(E * 64) : "memory"); }
template <int E, int SL = 1> __device__ __forceinline__ unsigned sy_wait(unsigned addr) {
    unsigned v;
    asm volatile("s_branch 2f\n1:\n\ts_sleep %3\n2:\n\tds_read_b32 %0, %1 offset:%2\n\ts_waitcnt lgkmcnt(0)\n\tv_cmp_eq_u32 vcc, 0, %0\n\ts_cbranch_vccnz 1b"
                 : "=&v"(v) : "v"(addr), "n"(E * 64), "n"(SL) : "vcc", "memory");
    return v;
}
template <int OFF, class T> __device__ __forceinline__ void dsrtr_(T& dst, unsigned addr) { asm volatile("ds_read_b64_tr_b16 %0, %1 offset:%2" : "=v"(dst) : "v"(addr), "n"(OFF)); }

constexpr size_t WS_H = 0, WS_KVT = (size_t)10 << 20, WS_ZT = (size_t)15 << 20, WS_E = (size_t)20 << 20, WS_FLAG = WS_E + (2048 << 10);
#define HP 65536
#define KP 8192
#define ZP 4096
constexpr int FL_KV = 0, FL_Z = 65536, FL_H = 131072, FL_EPOCH = 196608;
constexpr unsigned SPIN_CAP = 1u << 17;
#define ABL 0
#define CSPLIT 1
#define MERGE 3
#define CB 13
#define PAUSE 0
#define QLATE 0
#define CVPRIO 0
#define STG_SEL (idx & 3)
#define SP0 3
#define SDP 1
#define LP0 3
#define LDP 2
#define KVS 5
#define KVU 3
#define QIT 1
#define ZB 3
#define OS 2
#define MPRIO 2

#define ROT 0x80A5
#define R2 0x41
#define CPF 0
#define PRE 4
#define PRO 12
#define G1NT 0
#define G1NTS 16
#define TMIX 0
#define TLBT 0
#define CDEF 0
#define CVLATE 1
#define FPRIO 3
#define YS 12
#define DP0 1
#define DP1 5
#define AWB 1
#define PK 0
#define PU 13
#define PC0 13
#define PC1 14
#define PQ 0
#define SPF 0
#define SPH 0
#define NB1 4
#define EP 1
#define ESL 0
#define CFB 0
#define P1 1
#define P0 1
#define BKE 1
#define EALL 0
#define EREP 0
#define FILLB 0
#define CPOST 1
#define CEARLY 0
#define NOOWN 0
#define PFIRST 0
#define ZB0 0
constexpr int it_cnt(int p, int k, int last) {
    const int pos[6] = {PK, PU, PC0, PC1, PQ, PU - 1}, n[6] = {4, 4, 4, 4, 8, 1};
    int c = 0;
    for (int i = 0; i < 6; ++i) if (pos[i] <= last && (pos[i] > p || (pos[i] == p && i > k))) c += n[i];
    return c;
}
constexpr int G1STAGE = 32768;
constexpr int g1_slot(int s) { return (NB1 == 5 ? (s + 3) % 5 : s & 3) * G1STAGE; }
constexpr int L_KS = NB1 == 5 ? 65536 : 131072, L_VS = L_KS + 16384;
constexpr int L_TS = L_KS, L_TR = L_KS + 3072;
constexpr int CST = 4288;
constexpr int UOFF = 4 * CST, UPL = 2304;
constexpr int CHSZ = UOFF + 2 * UPL + 64;
constexpr int L_AGG = 4 * CHSZ, L_ES = L_AGG + 15 * 512, L_SY = L_ES + 2048;
constexpr int L_HT = 98304;
constexpr int L_C = 98304, G2STAGE = 16384;
constexpr int L_ZI = 0;
constexpr int L_Z2 = 20480;
constexpr int L_Y = 40960, L_Y2 = 131072;
static_assert(L_SY + 384 <= L_HT, "lds");
static_assert(L_Y + 32768 <= L_AGG && L_Z2 + 64 * 264 <= L_Y, "lds");

__device__ __forceinline__ int img_off(int r, int c) { return (r >> 1) * 256 + ((((r & 1) * 8 + c) ^ ((r >> 1) & 15)) << 4); }

constexpr int g1_upto(int g) {
    int n = PRO;
    for (int t = 0; t <= g; ++t) {
        int sb = (t + 1) / 2, sub = (t + 1) % 2, allowed = sb >= 1 ? 4 * (sb + NB1 - 1) + 2 * (sub + 1) : 4 * NB1;
        if (allowed > 64) allowed = 64;
        n = n + 4 < allowed ? n + 4 : allowed;
    }
    return n;
}
constexpr int g1_cnt(int s) { return g1_upto(2 * s - 2) - 4 * (s + 1); }
static_assert(g1_upto(32 - 2 * NB1) == 64, "schedule");
constexpr int AW = 8;
constexpr bool g2_dma(int g) { return (g + 1) / 2 >= 2 && (g + 1) / 2 + 3 < 16; }
constexpr int g2_cnt(int id, int at) {
    int n = -1000;
    for (int f = 4; f < 4 + AW; ++f) { ++n; if (f == id) n = 0; }
    for (int f = 4; f < at; ++f) {
        if ((f & 1) && g2_dma(f >> 1)) { ++n; if (100 + (f >> 1) == id) n = 0; }
        if (f + AW < 64) { ++n; if (f + AW == id) n = 0; }
    }
    return n;
}

constexpr int cv_m(int o) { return (o & 1) ? 7 - (o >> 1) : o >> 1; }
constexpr int cv_first(int o) { int p = 0; for (int i = 0; i < o; ++i) p += 8 - cv_m(i); return p; }
constexpr int cv_grp(int p) { int o = 0; while (cv_first(o + 1) <= p) ++o; return o; }
constexpr int cv_read(int p) {
    int slot[40] = {};
    for (int i = 0; i < 40; ++i) slot[i] = -1;
    slot[0] = 2 * 3 + 1;
    for (int o = 0; o < 8; ++o) {
        int q = cv_first(o + 1) - 1;
        for (int h = 0; h < 2; ++h) { while (slot[q] >= 0) ++q; slot[q] = 2 * (o + 4) + h; }
    }
    const int bs[8] = {1, 3, 5, 11, 12, 13, 19, 21};
    for (int jb = 0; jb < 8; ++jb) slot[bs[jb]] = 32 + jb;
    return slot[p];
}
static_assert(cv_read(35) == 22 && cv_read(36) == 23 && cv_read(37) == -1 && cv_read(10) == 11 && cv_read(15) == 12 && cv_read(18) == 15 && cv_read(24) == 17, "conv schedule");
constexpr int cv_wait(int o) {
    int n = 0;
    for (int q = cv_first(o) - 1;; --q) {
        const int rd = cv_read(q < 0 ? q + 36 : q);
        if (rd == 2 * (o + (q < 0 ? 8 : 0)) + 1) return n;
        if (rd >= 0) ++n;
    }
}

struct Args {
    const char *q, *k, *v, *sk, *ssmk, *ssmD, *x, *lre, *lim, *bre, *bim, *cre, *cim, *logdt, *dskip;
    char *out, *ws;
};

typedef __attribute__((ext_vector_type(8))) unsigned long long u64x8;
__global__ __launch_bounds__(768, 3) void fused(Args) {
    __shared__ __attribute__((aligned(1024))) char shm[163840];
    TS0a();
    u64x8 ka0, ka1; unsigned long long ka2;
    const char* pc0;
    int code_n;
    asm volatile("s_load_dwordx16 %0, %5, 0x0\n\ts_load_dwordx16 %1, %5, 0x40\n\ts_load_dwordx2 %2, %5, 0x80\n\t"
                 "s_getpc_b64 %3\n.Lfk_code_0:\n\ts_mov_b32 %4, .Lfk_code_a-.Lfk_code_0-4\n\ts_max_i32 %4, %4, .Lfk_code_b-.Lfk_code_0-4"
                 : "=&s"(ka0), "=&s"(ka1), "=&s"(ka2), "=&s"(pc0), "=&s"(code_n) : "s"(__builtin_amdgcn_kernarg_segment_ptr()) : "scc");
    const int tid = threadIdx.x, lane = tid & 63, wid = __builtin_amdgcn_readfirstlane(tid >> 6);
    const int b = blockIdx.x, xcd = b & 7, idx = b >> 3;
    unsigned cpf;
    asm volatile("s_cmp_lt_u32 %1, 8\n\ts_cbranch_scc1 1f\n\ts_setprio 3\n\ts_cmp_lg_u32 %1, 8\n\ts_cbranch_scc1 1f\n\t"
                 "v_lshl_add_u32 %0, %2, 7, %3\n\tv_min_i32 %0, %4, %0\n\ts_mov_b64 exec, 0xffff\n\tglobal_load_dword %0, %0, %5\n\ts_mov_b64 exec, -1\n1:"
                 : "=&v"(cpf) : "s"(wid), "v"(lane), "s"(idx * 2048), "s"(code_n), "s"(pc0) : "scc", "memory");
    TS_DECL
    const int tm = (xcd >> 1) * 4 + (idx >> 3), tn = (xcd & 1) * 8 + (idx & 7);
    const int wm = wid & 3, wn = (wid >> 2) & 1, l31 = lane & 31, lh = lane >> 5;
    const unsigned lb = (unsigned)(uintptr_t)shm;
    const unsigned l16 = lane * 16;
    const int l15 = lane & 15, g4 = lane >> 4, sq = 2 * wn + (wm & 1), st = sq * 16 + l15, run = (wm >> 1) * 4 + g4;
    unsigned kvv[4];
    const int trow = idx * 64 * 2048 + xcd * 256;
    if (wid < 8) {
        kvv[0] = trow + wm * 32768 + g4 * 2048 + ((l15 ^ (2 * g4)) << 4);
        kvv[1] = kvv[0] + 8192; kvv[2] = (kvv[0] ^ 128) + 16384; kvv[3] = kvv[2] + 8192;
    } else {
        kvv[0] = trow + (idx ? -4 * 2048 : 0) + g4 * 2048 + wm * 64 + l15 * 4;
        asm volatile("v_writelane_b32 %0, 0, 0\n\tv_writelane_b32 %0, 4, 1\n\tv_writelane_b32 %0, %1, 2" : "+v"(kvv[0]) : "s"((xcd * 64 + idx * 2 + (wm >> 1)) * 4));
        kvv[1] = (xcd * 128 + wm * 32 + l31) * 8;
        kvv[2] = kvv[3] = (tn * 64 + lane) * 2;
    }
    unsigned g1dst = lb + wid * 4096;
    asm volatile("s_waitcnt lgkmcnt(0)" : "+s"(ka0), "+s"(ka1), "+s"(ka2), "+s"(g1dst), "+v"(kvv[0]), "+v"(kvv[1]), "+v"(kvv[2]), "+v"(kvv[3]));
    Args a;
    a.q = (const char*)ka0[0]; a.k = (const char*)ka0[1]; a.v = (const char*)ka0[2]; a.sk = (const char*)ka0[3]; a.ssmk = (const char*)ka0[4]; a.ssmD = (const char*)ka0[5];
    a.x = (const char*)ka0[6]; a.lre = (const char*)ka0[7]; a.lim = (const char*)ka1[0]; a.bre = (const char*)ka1[1]; a.bim = (const char*)ka1[2]; a.cre = (const char*)ka1[3];
    a.cim = (const char*)ka1[4]; a.logdt = (const char*)ka1[5]; a.dskip = (const char*)ka1[6]; a.out = (char*)ka1[7]; a.ws = (char*)ka2;
    char* const ws = a.ws;
    const char* const flg = ws + WS_FLAG;
    TS(26);
    unsigned sm, cl[3]; u32x2 kw0;
    asm volatile("s_cmp_gt_u32 %[w], 7\n\ts_cbranch_scc1 1f\n\t"
                 "s_add_u32 m0, %[m], %[n0]\n\ts_nop 0\n\tglobal_load_lds_dwordx4 %[k0], %[kv] nt\n\ts_add_u32 m0, %[m], %[n0]+1024\n\ts_nop 0\n\tglobal_load_lds_dwordx4 %[k1], %[kv] nt\n\t"
                 "s_add_u32 m0, %[m], %[n0]+2048\n\ts_nop 0\n\tglobal_load_lds_dwordx4 %[k2], %[kv] nt\n\ts_add_u32 m0, %[m], %[n0]+3072\n\ts_nop 0\n\tglobal_load_lds_dwordx4 %[k3], %[kv] nt\n\t"
                 "s_branch 2f\n1:\n\tglobal_load_ushort %2, %[k2], %[c1]\n\tglobal_load_ushort %3, %[k2], %[c2]\n\tglobal_load_ushort %4, %[k2], %[c3]\n\t"
                 "v_mov_b64 %1, %[kv]\n\ts_mov_b64 exec, 7\n\tv_mov_b64 %1, %[d]\n\ts_mov_b64 exec, 3\n\tv_mov_b64 %1, %[e]\n\ts_mov_b64 exec, -1\n\t"
                 "v_mad_u64_u32 %1, vcc, %[k0], 1, %1\n\tglobal_load_dword %0, %1, off sc1\n\tglobal_load_dwordx2 %1, %[k1], %[sk]\n2:"
                 : "=&v"(sm), "=&v"(kw0), "=&v"(cl[0]), "=&v"(cl[1]), "=&v"(cl[2])
                 : [w] "s"(wid), [c1] "s"(a.logdt), [c2] "s"(a.lre), [c3] "s"(a.lim), [d] "s"(a.ssmD), [e] "s"(flg + FL_EPOCH), [sk] "s"(a.sk),
                   [k0] "v"(kvv[0]), [k1] "v"(kvv[1]), [k2] "v"(kvv[2]), [k3] "v"(kvv[3]), [kv] "s"(wn ? a.v : a.k), [m] "s"(g1dst), [n0] "n"(L_KS)
                 : "scc", "vcc", "memory");
    TS0b();
    TS(27);
    if (P1) BAR();
#define SLICE_TABLE() unsigned slo, shi; { int ix = idx; asm volatile("" : "+s"(ix)); \
        const int rot2 = (((R2 >> 4) & 15) * (ix >> 3) + (R2 & 15) * (xcd >> 1)) & 15, es = (tn - rot2) & 15; \
        const unsigned long long id = 0xFEDCBA9876543210ull, seq = rot2 ? (id >> (4 * rot2)) | (id << (64 - 4 * rot2)) : id; \
        const unsigned long long lo = (1ull << (4 * es)) - 1, tab = ((seq & lo) << 4) | (seq & ~((lo << 4) | 15)) | tn; \
        slo = (unsigned)tab; shi = (unsigned)(tab >> 32); asm volatile("" : "+s"(slo), "+s"(shi)); }
#define SLICE(st) ((((st) < 8 ? slo : shi) >> (4 * ((st) & 7))) & 15)
    typedef __attribute__((ext_vector_type(4))) short v4i16;
    int role = wid;
    asm volatile("" : "+s"(role));

    if (role >= 8) {
        const int chn = xcd * 128 + idx * 4 + wm;
        const unsigned cb = lb + wm * CHSZ;
        const int r = l15;
        const unsigned fpub = xcd * 256 + idx * 8, fpoll = xcd * 256 + l31 * 8;
        u32x2 fl; u32x4 sw[4], uu[4];
        unsigned tch = 0;
        if (TOUCH) {
            const int ln = wm * 4 + (lane & 3);
            const unsigned fo = (unsigned)(WS_FLAG - WS_E) + (wm == 0 ? FL_H + (tm * 16 + tn) * 128 : (wm == 1 ? FL_KV : FL_Z) + xcd * 256 + (lane & 1) * 128);
            const unsigned o = (lane & 4) && (TOUCH & 2) ? fo : ((tm * 2 + (ln >> 3)) * 1024 + tn * 64) * 16 + (ln & 7) * 128;
            asm volatile("s_mov_b64 exec, 0xff\n\tglobal_load_dword %0, %1, %2\n\ts_mov_b64 exec, -1" : "=&v"(tch) : "v"(o), "s"(ws + WS_E) : "memory");
        }
        SLICE_TABLE();
        TS(1);
        WAIT_V(TOUCH ? 1 : 0);
        asm volatile("" :: "v"(cpf));
        const unsigned hal = take(sm);
        const unsigned tagA = __builtin_amdgcn_readlane(hal, 0) + 1, tagB = __builtin_amdgcn_readlane(hal, 1) + (tagA == 0 ? 1u : 0u);
        const u32x2 tag = {tagA, tagB};
        f32x2 L1, C1;
        {
            const float dt = rbf(expf(bf2f(take(cl[0]))));
            const float lr = bf2f(take(cl[1])), li = bf2f(take(cl[2]));
            const float e = expf(dt * lr);
            float sn, cs;
            sincosf(dt * li, &sn, &cs);
            L1 = f32x2{e * cs, e * sn};
            const float nr = L1[0] - 1.f, ni = L1[1], den = lr * lr + li * li;
            C1 = f32x2{(nr * lr + ni * li) / den, (ni * lr - nr * li) / den};
            TIE(L1); TIE(C1);
        }
        const unsigned dskl = (__builtin_amdgcn_readlane(hal, 2) >> ((wm & 1) * 16)) & 0xffffu;
        u32x4 kvo[4];
        {
            unsigned kw[2][8], ta[2], th[2];
            u32x2 hm[2];
#pragma unroll
            for (int j = 0; j < 2; ++j) {
                const unsigned x = take(kw0[j]);
                const auto y = __builtin_amdgcn_permlane16_swap(x, x, false, false);
#pragma unroll
                for (int p = 0; p < 2; ++p) { kw[p][4 * j] = y[p] & 0xffffu; kw[p][4 * j + 1] = y[p] << 16; kw[p][4 * j + 2] = y[p] >> 16; kw[p][4 * j + 3] = y[p] & 0xffff0000u; }
            }
#pragma unroll
            for (int p = 0; p < 2; ++p) {
                unsigned h[3];
#pragma unroll
                for (int j = 0; j < 3; ++j) h[j] = (__shfl(hal, p * 8 + (r >> 1) + 16 * (j + 1)) >> ((r & 1) * 16)) & 0xffffu;
                hm[p] = idx ? u32x2{h[0] << 16, h[1] | (h[2] << 16)} : u32x2{0u, 0u};
                const int q = r >> 2, pp = r & 3;
                ta[p] = lb + L_KS + (8 * g4 + q) * 256 + (((wm * 4 + p * 2 + (pp >> 1)) ^ (2 * (q | ((g4 & 1) << 2)))) << 4) + (pp & 1) * 8;
                th[p] = (ta[p] ^ 128) - 1024;
                TIE(hm[p]); TIE(ta[p]); TIE(th[p]);
#pragma unroll
                for (int j = 0; j < 8; ++j) TIE(kw[p][j]);
            }
            int sl = ((lane & 3) * 16 + (lane >> 2)) * 4;
            TIE(sl);
            BAR();
            TS(2);
            u32x2 kt[4][3], vt[4][2];
#pragma unroll
            for (int u = 0; u < 4; ++u) {
                const int p = u >> 1, o = (u & 1) * 8192;
                auto tr = [&](unsigned ad) -> u32x2 { return __builtin_bit_cast(u32x2, __builtin_amdgcn_ds_read_tr16_b64_v4i16(LDSP(v4i16, ad))); };
                kt[u][0] = tr(th[p] + o); kt[u][1] = tr(ta[p] + o); vt[u][0] = tr(ta[p] + o + 16384); kt[u][2] = tr(ta[p] + o + 1024); vt[u][1] = tr(ta[p] + o + 17408);
                SB();
            }
            TS(14);
#pragma unroll
            for (int u = 0; u < 4; ++u) {
                const int p = u >> 1, oh = u & 1;
                const u32x2 k0 = oh == 0 && g4 == 0 ? hm[p] : kt[u][0];
                unsigned o[4];
                kv4(o[3], o[2], k0[0], k0[1], kt[u][1][0], kt[u][1][1], kw[p], vt[u][0][0], vt[u][0][1]);
                kv4(o[1], o[0], kt[u][1][0], kt[u][1][1], kt[u][2][0], kt[u][2][1], kw[p], vt[u][1][0], vt[u][1][1]);
                if (u == 0) { TIE(o[0]); TIE(o[2]); WAIT_L(0); }
#pragma unroll
                for (int j = 0; j < 4; ++j) kvo[u][j] = __builtin_amdgcn_ds_bpermute(sl, o[j]);
                SB();
                if (u == 0) TS(15); if (u == 1) TS(16); if (u == 2) TS(17); if (u == 3) TS(18);
                if (u == KVU) BAR();
            }
#pragma unroll
            for (int u = 0; u < 4; ++u) TIE(kvo[u]);
        }
        if (KVU < 3) BAR();
        {
            f32x2 p[11];
            p[0] = L1;
#pragma unroll
            for (int i = 1; i <= 10; ++i) p[i] = cmul4(p[i - 1], p[i - 1]);
            const f32x2 one = {1.f, 0.f}, zero = {0.f, 0.f};
            const unsigned ts = lb + L_TS + l16, tr = lb + L_TR + wm * 2048 + l16;
            if (wm == 0) {
                *LDSP(f32x4, ts) = f32x4{L1[0], L1[1], C1[0], C1[1]};
                *LDSP(f32x4, ts + 1024) = f32x4{p[3][0], p[3][1], p[4][0], p[4][1]};
                *LDSP(f32x4, ts + 2048) = f32x4{p[6][0], p[6][1], __uint_as_float(tagA), __uint_as_float(tagB)};
            }
            f32x2 Q[2], V[2], W[2];
            Q[0] = cmul4(cmul4(C1, (wm & 1) ? p[5] : one), (wm & 2) ? p[6] : one); Q[1] = cmul4(Q[0], p[4]);
            V[1] = (wm & 1) ? one : p[5]; V[0] = cmul4(V[1], p[4]);
            W[1] = cmul4((wm & 1) ? one : p[9], (wm & 2) ? one : p[10]); W[0] = cmul4(W[1], p[8]);
            if (KVU == 3) { TIE(W[0]); TIE(Q[1]); BAR(); }
#pragma unroll
            for (int i = 0; i < 2; ++i) {
                const f32x2 Q8 = cmul4(Q[i], p[3]), U = 4 * wm + 2 * i >= 16 - tm ? cmul4(W[i], p[7]) : zero, Wz = 4 * wm + 2 * i + 1 >= 16 - tm ? W[i] : zero;
                *LDSP(f32x4, tr + i * 1024) = f32x4{Q[i][0], Q8[0], Q[i][1], Q8[1]};
                *LDSP(f32x4, tr + i * 1024 + 8192) = f32x4{Wz[0], Wz[1], U[0], U[1]};
                *LDSP(f32x2, tr + i * 1024 + 16384) = V[i];
            }
            WAIT_L(0);
        }
        TS(3);
        u32x2 qv[8];
        const char* krow = a.ssmk + (size_t)chn * 4096;
        const char* qs = a.q + (size_t)(idx * 64 + wm * 16) * 2048 + xcd * 256;
        const char* usrc = ws + WS_KVT + (size_t)(xcd * 64 + idx * 2 + (wm >> 1)) * KP + (wm & 1) * 4096;
        const char* cs = ((wm & 2) ? a.cim : a.cre) + (size_t)(tn * 64 + (wm & 1) * 2) * 2048;
        unsigned co[4];
#pragma unroll
        for (int i = 0; i < 4; ++i) {
            const int pair = i * 4 + g4, v = r ^ pair, row = pair * 2 + (v >> 3);
            co[i] = ((row & 15) * 4 + (row >> 4)) * 2048 + (v & 7) * 16;
        }
        bool kv_ok = false;
#pragma unroll
        for (int it = 0; it < 4; ++it) asm volatile("" : "=v"(uu[it]));
        auto items = [&](auto pc) {
            constexpr int p = decltype(pc)::value;
            if constexpr (PK == p) { GLD_B128_NT(sw[0], l16, krow, 0); GLD_B128_NT(sw[1], l16, krow, 1024); GLD_B128_NT(sw[2], l16, krow, 2048); GLD_B128_NT(sw[3], l16, krow, 3072); }
            if constexpr (PU == p) {
                WAIT_V(PK == p ? 4 : 0); TIE(fl);
                kv_ok = __all(fl[0] == tagA && fl[1] == tagB);
                const char* us = kv_ok ? usrc : krow;
                GLD_B128_SC1(uu[0], l16, us, 0); GLD_B128_SC1(uu[1], l16, us, 1024); GLD_B128_SC1(uu[2], l16, us, 2048); GLD_B128_SC1(uu[3], l16, us, 3072);
            }
            if constexpr (PC0 == p) { for (int i = 0; i < 4; ++i) DMA(lb + L_C + 2 * G2STAGE + wm * 4096 + i * 1024, co[i], cs + (SLICE(0) << 7)); }
            if constexpr (PC1 == p) { for (int i = 0; i < 4; ++i) DMA(lb + L_C + 3 * G2STAGE + wm * 4096 + i * 1024, co[i], cs + (SLICE(1) << 7)); }
            if constexpr (PQ == p) static_for<0, 8>([&](auto jc) { constexpr int j = decltype(jc)::value; GLD_B64_NT(qv[j], lh * 2048 + l31 * 8, qs + j * 4096, 0); });
            if constexpr (PU == p + 1) GLD_B64_SC1(fl, fpoll, flg + FL_KV, 0);
        };
#define ITEMS(p) items(std::integral_constant<int, p>{})
        {
            const int ph = STG_SEL, ps = SP0 + ph * SDP, pl = LP0 + ph * LDP;
            const char* kvd = ws + WS_KVT + (size_t)(xcd * 64 + wm * 16) * KP + idx * 256;
            int nb1 = 2;
#pragma unroll 1
            for (; nb1 <= ps; ++nb1) BAR();
            static_for<0, 4>([&](auto uc) {
                constexpr int u = decltype(uc)::value;
                GST_B128_SC1((lane >> 3) * KP + (lane & 7) * 16, kvo[u], kvd + (u >> 1) * 8 * KP, (u & 1) * 128);
            });
#pragma unroll 1
            for (; nb1 <= pl; ++nb1) {
                if (nb1 == ps + 4) WAIT_V(0);
                BAR();
                if (nb1 == ps + 4 && wm == 0 && lane == 0) GST_B64_SC1(fpub, tag, flg + FL_KV);
            }
            ITEMS(0);
#pragma unroll 1
            for (; nb1 <= 11; ++nb1) {
                if (nb1 == ps + 4) WAIT_V(it_cnt(0, -1, 0));
                BAR();
                if (nb1 == ps + 4 && wm == 0 && lane == 0) GST_B64_SC1(fpub, tag, flg + FL_KV);
            }
        }
        asm volatile("" :: "v"(tch));
        ITEMS(11);
        BAR();
        ITEMS(12);
        BAR();
        ITEMS(13);
        BAR();
        ITEMS(14);
        BAR();
        ITEMS(15);
        TS(4);
        const unsigned ud = lb + ((wm & 2) + ((lane >> 2) & 1)) * CHSZ + UOFF + (lane & 1) * UPL + (16 + (wm & 1) * 64 + g4 * 4 + ((lane >> 3) & 1) * 2 + ((lane >> 1) & 1)) * 16;
        {
            const u32x4 z4 = {0, 0, 0, 0};
            if (lane < 24) *LDSP(u32x4, lb + L_SY + l16) = z4;
            if (lane < 56) *LDSP(u32x4, lb + L_AGG + wm * 896 + l16) = z4;
            *LDSP(u32x4, lane < 32 ? cb + (lane >> 3) * CST + (lane & 7) * 16 : cb + UOFF + (g4 & 1) * UPL + r * 16) = z4;
        }
        auto fill = [&]() {
#pragma unroll
            for (int it = 0; it < 4; ++it) TIE(sw[it]);
#pragma unroll
            for (int it = 0; it < 4; ++it) {
                const unsigned q = cb + 128 + it * 1024 + l16;
                const unsigned d0 = sw[it][0], d1 = sw[it][1], d2 = sw[it][2], d3 = sw[it][3];
                const unsigned o0 = it ? __builtin_amdgcn_readlane(sw[it ? it - 1 : 0][2], 63) : 0u, o1 = it ? __builtin_amdgcn_readlane(sw[it ? it - 1 : 0][3], 63) : 0u;
                const unsigned x0 = __builtin_amdgcn_update_dpp(o0, d2, 0x138, 0xf, 0xf, false), x1 = __builtin_amdgcn_update_dpp(o1, d3, 0x138, 0xf, 0xf, false);
                const unsigned a0 = __builtin_amdgcn_alignbyte(d0, x1, 2), a1 = __builtin_amdgcn_alignbyte(d1, d0, 2), a2 = __builtin_amdgcn_alignbyte(d2, d1, 2),
                               a3 = __builtin_amdgcn_alignbyte(d3, d2, 2);
                *LDSP(u32x4, q) = sw[it];
                *LDSP(u32x4, q + CST) = u32x4{a0, a1, a2, a3};
                *LDSP(u32x4, q + 2 * CST) = u32x4{x1, d0, d1, d2};
                *LDSP(u32x4, q + 3 * CST) = u32x4{__builtin_amdgcn_alignbyte(x1, x0, 2), a0, a1, a2};
                if (it == 3 && lane == 63) {
                    *LDSP(u32x2, q + 16 + CST) = u32x2{d3 >> 16, 0u};
                    *LDSP(u32x2, q + 16 + 2 * CST) = u32x2{d3, 0u};
                    *LDSP(u32x2, q + 16 + 3 * CST) = u32x2{a3, d3 >> 16};
                }
            }
        };
        if (FILLB) {
            static_assert(!FILLB || PK < PU, "ssm_k has landed");
            if (FILLB > 1) __builtin_amdgcn_s_sleep(FILLB);
            fill();
        }
        WAIT_L(0);
        TS(5);
        BAR();
        __builtin_amdgcn_s_setprio(FPRIO);
        ITEMS(16);
        TS(11);
        WAIT_V(it_cnt(PK, 0, 16));
        TS(12);
        if (!FILLB) fill();
        TS(13);
        WAIT_V(it_cnt(PU, 1, 16));
#pragma unroll
        for (int it = 0; it < 4; ++it) TIE(uu[it]);
        if (!kv_ok) {
            for (unsigned spin = 0; spin < SPIN_CAP; ++spin) {
                GLD_B64_SC1(fl, fpoll, flg + FL_KV, 0);
                WAIT_V(0); TIE(fl);
                if (__all(fl[0] == tagA && fl[1] == tagB)) break;
                __builtin_amdgcn_s_sleep(8);
            }
            GLD_B128_SC1(uu[0], l16, usrc, 0); GLD_B128_SC1(uu[1], l16, usrc, 1024); GLD_B128_SC1(uu[2], l16, usrc, 2048); GLD_B128_SC1(uu[3], l16, usrc, 3072);
            WAIT_V(0);
#pragma unroll
            for (int it = 0; it < 4; ++it) TIE(uu[it]);
        }
#pragma unroll
        for (int it = 0; it < 4; ++it) *LDSP(u32x4, ud + it * 256) = uu[it];
        const unsigned pmine = lb + L_SY + 32 + wm * 4, pall = lb + L_SY + 32 + (lane & 3) * 4;
        sy_post<0>(pmine); sy_wait<0>(lb + L_SY + 32 + (wm ^ 1) * 4);
        if (!CPOST) { WAIT_V(0); sy_post<1>(pmine); }
        __builtin_amdgcn_s_setprio(CVPRIO);
        TS(6);
        f32x4 ca[8];
        {
            unsigned kb = cb + ((3 - r) & 3) * CST + 2 * (64 + 16 + r - 8 * g4 - 7 + ((3 - r) & 3));
            unsigned ub = cb + UOFF + (g4 & 1) * UPL + (15 + r + (g4 >> 1)) * 16 - 64;
#pragma unroll
            for (int i = 0; i < 8; ++i) ca[i] = f32x4{0, 0, 0, 0};
            s16x4 FA[4][2]; bf16x8 FB[2][8];
#define CV_A(s, o, e) DSR64(FA[(o) & 3][e], kb, 64 * ((s) + ((o) >> 3)) + 512 * cv_m((o) & 7) + 8 * (e))
#define CV_B(s, jb) DSR128(FB[(s) & 1][jb], ub, 64 + 256 * (jb) - 32 * (s))
            static_for<0, 8>([&](auto ic) { constexpr int n = decltype(ic)::value; CV_B(0, n); CV_A(0, n >> 1, n & 1); });
            WAIT_L(0);
            auto body = [&]() {
                static_for<0, 72>([&](auto ic) {
                    constexpr int s = decltype(ic)::value / 36, p = decltype(ic)::value % 36, o = cv_grp(p), m = cv_m(o), jb = p - cv_first(o);
                    if constexpr (jb == 0) { WAIT_L(cv_wait(o)); TIE(FA[o & 3][0]); TIE(FA[o & 3][1]); }
                    if constexpr (p == 0) { for (int i = 0; i < 8; ++i) TIE(FB[s][i]); }
                    const bf16x8 fa = __builtin_shufflevector(FA[o & 3][0], FA[o & 3][1], 0, 1, 2, 3, 4, 5, 6, 7);
                    ca[jb + m] = __builtin_amdgcn_mfma_f32_16x16x32_bf16(fa, FB[s][jb], ca[jb + m], 0, 0, 0);
                    SB();
                    constexpr int rd = cv_read(p);
                    if constexpr (rd >= 32) CV_B(s + 1, rd - 32);
                    else if constexpr (rd >= 0) CV_A(s, rd >> 1, rd & 1);
                    SB();
                });
                kb += 128; ub -= 64;
            };
#pragma unroll 1
            for (int it = 0; it < QIT; ++it) body();
            {
                TS(19);
                if (CPOST) { WAIT_V(0); sy_post<1>(pmine); WAIT_L(0); }
                if (CVLATE) sy_wait<1>(lb + L_SY + (CVLATE == 2 ? wm + (lane & 1) * 4 : lane & 7) * 4);
                TS(20);
            }
#pragma unroll 1
            for (int it = QIT; it < 4; ++it) body();
            WAIT_L(0);
#pragma unroll
            for (int i = 0; i < 4; ++i) { TIE(FA[i][0]); TIE(FA[i][1]); }
#pragma unroll
            for (int i = 0; i < 8; ++i) TIE(FB[0][i]);
#undef CV_A
#undef CV_B
        }
        TS(7);
        __builtin_amdgcn_s_setprio(3);
        int nb = 1;
#define HB() do { if (nb < 15) BAR(); ++nb; } while (0)
        if (ZB0) HB();
        {
            const float Dd = bf2f(dskl);
            const char* zd = ws + WS_ZT + (size_t)chn * ZP;
            u32x2 kvr[8];
#pragma unroll
            for (int jt = 0; jt < 8; ++jt) kvr[jt] = *LDSP(u32x2, cb + UOFF + (g4 >> 1) * UPL + (16 + 16 * jt + r) * 16 + (1 - (g4 & 1)) * 8);
#pragma unroll
            for (int jt = 0; jt < 8; ++jt) {
                const f32x4 y = ca[jt];
                const u32x2 kq = kvr[jt];
                *LDSP(u32x2, cb + (jt * 4 + (r >> 2)) * 144 + (r & 3) * 32 + g4 * 8) =
                    u32x2{pack2(rb1(y[0]) + rb1(bfhi(kq[1]) * Dd), rb1(y[1]) + rb1(bflo(kq[1]) * Dd)), pack2(rb1(y[2]) + rb1(bfhi(kq[0]) * Dd), rb1(y[3]) + rb1(bflo(kq[0]) * Dd))};
            }
            if (ZB >= 3) HB();
            u32x4 t[4];
#pragma unroll
            for (int it = 0; it < 4; ++it) t[it] = *LDSP(u32x4, cb + (it * 8 + (lane >> 3)) * 144 + (lane & 7) * 16);
            if (!(ABL & 128)) { GST_B128_SC1(l16, t[0], zd, 0); GST_B128_SC1(l16, t[1], zd, 1024); GST_B128_SC1(l16, t[2], zd, 2048); GST_B128_SC1(l16, t[3], zd, 3072); }
            else { TIE(t[0]); TIE(t[1]); TIE(t[2]); TIE(t[3]); }
        }
        if (ZB >= 2) HB();
        if (ZB >= 1) HB();
        WAIT_V(0);
        sy_post<2>(pmine);
        if (wm == 0) {
            sy_wait<2>(pall);
            if (!(ABL & 1024) && lane == 0) GST_B64_SC1(fpub, tag, flg + FL_Z);
        }
        TS(8);
        HB(); HB(); HB();
        if (!(ABL & 1024) && wm == 0) GLD_B64_SC1(fl, fpoll, flg + FL_Z, 0);
        HB(); HB();
        if (wm == 0) { WAIT_V(0); TIE(fl); *LDSP(unsigned, lb + L_SY + 320) = __all(fl[0] == tagA && fl[1] == tagB) ? 2 : 1; }
        if (__builtin_amdgcn_readfirstlane(sy_wait<5>(lb + L_SY)) != 2) {
            while (nb < 15) { BAR(); ++nb; }
            for (unsigned spin = 0; spin < SPIN_CAP; ++spin) {
                GLD_B64_SC1(fl, fpoll, flg + FL_Z, 0);
                WAIT_V(0); TIE(fl);
                if (__all(fl[0] == tagA && fl[1] == tagB)) break;
                __builtin_amdgcn_s_sleep(8);
            }
        }
        u32x4 zv[4];
        {
            ITEMS(18);
            const char* zsrc = ws + WS_ZT + (size_t)(xcd * 128 + wm * 32) * ZP + idx * 128;
#pragma unroll
            for (int it = 0; it < 4; ++it) if (!(ABL & 256)) GLD_B128_SC1(zv[it], (lane >> 3) * ZP + (lane & 7) * 16, zsrc + it * 8 * ZP, 0);
        }
        HB(); HB();
        WAIT_V(0);
        TS(9);
        {
            const unsigned zi = lb + L_ZI + wm * 5120;
#pragma unroll
            for (int it = 0; it < 4; ++it) { TIE(zv[it]); *LDSP(u32x4, zi + ((lane >> 3) + 8 * it) * 160 + (lane & 7) * 16) = zv[it]; }
            v4i16 z[8];
#pragma unroll
            for (int n = 0; n < 8; ++n) z[n] = __builtin_amdgcn_ds_read_tr16_b64_v4i16(LDSP(v4i16, zi + ((n & 1) * 16 + (lane >> 2)) * 160 + (n >> 1) * 32 + (lane & 3) * 8));
            HB();
#pragma unroll
            for (int n = 0; n < 8; ++n) *LDSP(v4i16, lb + L_Z2 + ((n >> 1) * 16 + r) * 264 + wm * 64 + (n & 1) * 32 + g4 * 8) = z[n];
            sy_post<3>(pmine);
        }
        HB();
        sy_wait<3>(pall);
        char* const oa = a.out + (size_t)(idx * 64 + wm * 16) * 8192 + xcd * 512;
        u32x2 zz[8];
#pragma unroll
        for (int n = 0; n < 8; ++n) zz[n] = *LDSP(u32x2, lb + L_Z2 + (wm * 16 + n * 2 + lh) * 264 + l31 * 8);
#pragma unroll
        for (int n = 0; n < 8; ++n) {
            TIE(qv[n]);
            const u32x2 z = zz[n];
            const f32x4 o = {rb1(bflo(qv[n][0]) * bflo(z[0])), rb1(bfhi(qv[n][0]) * bfhi(z[0])), rb1(bflo(qv[n][1]) * bflo(z[1])), rb1(bfhi(qv[n][1]) * bfhi(z[1]))};
            if (!(ABL & 512)) GST_B128_NT(lh * 8192 + l31 * 16, o, oa + n * 16384, 0); else { f32x4 o2 = o; TIE(o2); }
            if (n < 7 && (n + 1) % OS == 0) HB();
        }
#undef HB
        asm volatile(".Lfk_code_a:");
        TSE(10);
        TS_FLUSH();
        return;
    }

    u32x2 fl;
    unsigned dv[4];
    {
        const int v = l15 ^ g4, hi = v >> 3;
        const int r0 = wn ? tn * 64 + (wm >> 1) * 32 : tm * 128 + (wm >> 1) * 64 + (wm & 1) * 4;
        const unsigned d0 = (r0 + ((g4 * (wn ? 2 : 9)) & (wn ? 6 : 17))) * 2048 + (v & 7) * 16, odd = wn ? 11 : 14;
        dv[0] = d0 + (hi << odd); dv[2] = d0 + ((hi ^ 1) << odd) + (wn ? 32768 : 4096);
        dv[1] = (dv[0] ^ 64) + (wn ? 16384 : 65536); dv[3] = (dv[2] ^ 64) + (wn ? 16384 : 65536);
    }
    const int rot7 = ((((ROT >> 12) & 15) * (idx & 7) + ((ROT >> 8) & 15) * (idx >> 3) + ((ROT >> 4) & 15) * (xcd >> 1) + (ROT & 15) * (xcd & 1)) & 15) << 7;
    const char* g1src = wn ? ((wm & 1) ? a.bim : a.bre) : a.x;
#define G1_ISSUE(st, i) DMA(g1dst + g1_slot(st) + (i) * 1024, dv[i], g1src + ((((st) << 7) + rot7) & 1920))
    static_for<0, PRE>([&](auto ic) { constexpr int n = decltype(ic)::value; G1_ISSUE(n >> 2, n & 3); });
    TS(14);
    if (BKE) {
        WAIT_V(PRE);
        BAR();
        static_for<PRE, PRO>([&](auto ic) { constexpr int n = decltype(ic)::value; G1_ISSUE(n >> 2, n & 3); });
        TS(15);
    }
    SLICE_TABLE();
    const int j0 = 4 * run - (32 - 2 * tm);
    const int pneg = EALL ? 0 : 16 * (wm >> 1) + 15 - (32 - 2 * tm);
    unsigned eo[2], pes = lb + L_ES + wid * 256 + l15 * 16;
    unsigned plr = lb + L_AGG + st * 8 + run * 512, est = ((tm * 2 + (wm >> 1)) * 1024 + tn * 64 + st) * 16 + (EREP ? g4 * 524288 : 0);
#pragma unroll
    for (int i = 0; i < 2; ++i) { eo[i] = (EALL ? (j0 + 2 * i) & 31 : j0 + 2 * i < 0 ? 0 : j0 + 2 * i) * 16384 + (EREP ? (tm >> 2) * 524288 : 0) + (tn * 64 + st) * 16; TIE(eo[i]); }
    TIE(plr); TIE(pes); TIE(est);
    unsigned hw[2], ta[2];
    {
        const unsigned ht = lb + L_HT + wid * 4096;
#pragma unroll
        for (int j = 0; j < 2; ++j) {
            const int pr = (8 * (g4 & 1) + 4 * j + (l15 >> 2)) ^ ((g4 & 1) * 4), pw = l15 ^ ((l15 >> 3) * 4);
            ta[j] = ht + (g4 >> 1) * 512 + pr * 32 + ((((l15 >> 1) ^ (pr >> 2)) & 1) << 4) + 8 * (lane & 1);
            hw[j] = ht + g4 * 1024 + pw * 32 + (((j ^ (pw >> 2)) & 1) << 4);
            TIE(ta[j]); TIE(hw[j]);
        }
    }
    unsigned po = pes + ((wm & 2) ? -512 : 512);
    unsigned cta = lb + L_TS + st * 16, ctb = lb + L_TR + run * 1024 + st * 16;
    TIE(po); TIE(cta); TIE(ctb);
    f32x4 ct[5]; f32x2 V1;
    unsigned pmine = lb + L_SY + wid * 4, pall = lb + L_SY + (lane & 7) * 4, pown = lb + L_SY + (lane < 4 ? 32 + lane * 4 : (wid ^ 1) * 4), pzero = lb + L_SY;
    unsigned g2v[2], cad[2][2], to[2][2];
    unsigned xo = (tm * 128 + wm * 32 + wn * 16 + g4 * 4) * 2048 + tn * 128 + l15 * 8 + 4096, dso = xo & 2047;
    unsigned fst = (tm * 16 + l15) * 128 + tn * 8, fld = (tm * 16 + tn) * 128 + (NOOWN && l15 == tn ? tn ^ 1 : l15) * 8;
#pragma unroll
    for (int i = 0; i < 2; ++i) {
        const int pair = i * 4 + g4, v = l15 ^ pair ^ ((wm & 1) * 8);
        g2v[i] = (pair * 2 + (v >> 3)) * 8192 + (v & 7) * 16 + (tn * 64 + wm) * 2048;
        TIE(g2v[i]);
#pragma unroll
        for (int j = 0; j < 2; ++j) {
            cad[i][j] = lb + L_C + (img_off((g4 >> 1) * 64 + l15, 4 * wn + (g4 & 1)) ^ (i * 32) ^ (j * 128));
            to[i][j] = ta[j] + (i ^ wn) * 1024 - (wm & 1) * 2048;
            TIE(cad[i][j]); TIE(to[i][j]);
        }
    }
    TIE(pmine); TIE(pall); TIE(pown); TIE(pzero); TIE(xo); TIE(dso); TIE(fst); TIE(fld);
    TS(1);
    if (!BKE) {
        WAIT_V(PRE);
        BAR();
        static_for<PRE, PRO>([&](auto ic) { constexpr int n = decltype(ic)::value; G1_ISSUE(n >> 2, n & 3); });
        TS(15);
    }

    f32x4 acc[4][2];
    unsigned tagA, tagB;
    {
#pragma unroll
        for (int i = 0; i < 4; ++i) { acc[i][0] = f32x4{0, 0, 0, 0}; acc[i][1] = f32x4{0, 0, 0, 0}; }
        const int aoff = img_off((wm >> 1) * 64 + l15, g4), boff = img_off(128 + wn * 64 + (wm & 1) * 16 + l15, g4);
        unsigned aad[2][2], bad[2];
#pragma unroll
        for (int ks = 0; ks < 2; ++ks) { aad[ks][0] = lb + (aoff ^ (ks * 64)); aad[ks][1] = lb + (aoff ^ (ks * 64) ^ 128); bad[ks] = lb + (boff ^ (ks * 64)); }
        bf16x8 fa[2][4], fb[2][2];
        WAIT_V(PRO - 4); BAR();
        TS(2);
#define G1_FRAG(g, n) do { constexpr int bo_ = g1_slot((g) >> 1), hi_ = bo_ & ~65535, x_ = (n) < 3 ? (n) >> 1 : (n) - 2; \
        if constexpr ((n) == 0 || (n) == 2) DSR128(fb[(g) & 1][x_], bad[(g) & 1] + hi_, bo_ - hi_ + x_ * 4096); \
        else DSR128(fa[(g) & 1][x_], aad[(g) & 1][x_ & 1] + hi_, bo_ - hi_ + x_ * 2048); } while (0)
        static_for<0, 6>([&](auto ic) { constexpr int n = decltype(ic)::value; G1_FRAG(0, n); });
        static_for<0, 32>([&](auto ic) {
            constexpr int g = decltype(ic)::value, gn = g + 1, n0 = g1_upto(g - 1), nd = g1_upto(g) - n0;
            constexpr bool bar = (gn & 1) == 0 && gn < 32;
            if constexpr (bar) {
                static_assert(g1_cnt(gn >> 1) >= 0, "schedule");
                WAIT_V(g1_cnt(gn >> 1)); WAIT_L(0); BAR();
                if constexpr (((gn >> 1) & 3) == 0 || gn >= 26) TS(gn >= 26 ? 23 + (gn >> 1) - 13 : 16 + (gn >> 3));
#pragma unroll
                for (int i = 0; i < 4; ++i) TIE(fa[g & 1][i]);
                TIE(fb[g & 1][0]); TIE(fb[g & 1][1]);
                if constexpr (g == 5) {
#pragma unroll
                    for (int i = 0; i < 5; ++i) TIE(ct[i]);
                    TIE(V1);
                    tagA = __builtin_amdgcn_readfirstlane(__float_as_uint(ct[2][2])); tagB = __builtin_amdgcn_readfirstlane(__float_as_uint(ct[2][3]));
                    asm volatile("" : "+s"(tagA), "+s"(tagB));
                }
            }
            static_for<0, 8>([&](auto mc) {
                constexpr int m = decltype(mc)::value, i = m >> 1, j = m & 1;
                if constexpr (!bar && (i == 0 || j == 0)) {
                    WAIT_L(5 - (i == 0 ? j + 1 : i + 2) + (gn < 32 ? (m < 6 ? m : 6) : 0));
                    TIE(fa[g & 1][i]); TIE(fb[g & 1][j]);
                }
                acc[i][j] = __builtin_amdgcn_mfma_f32_16x16x32_bf16(fa[g & 1][i], fb[g & 1][j], acc[i][j], 0, 0, 0);
                SB();
                if constexpr (gn < 32 && m < 6) G1_FRAG(gn, m);
                constexpr int d = nd > 2 ? ((m & 1) ? m >> 1 : -1) : (m == DP0 ? 0 : m == DP1 ? 1 : -1);
                if constexpr (d >= 0 && d < nd) G1_ISSUE((n0 + d) >> 2, (n0 + d) & 3);
                if constexpr (g == 4 && m == 6) { DSR128(ct[0], cta, 0); DSR128(ct[1], cta, 1024); DSR128(ct[2], cta, 2048); }
                if constexpr (g == 4 && m == 7) { DSR128(ct[3], ctb, 0); DSR128(ct[4], ctb, 8192); DSR64(V1, ctb, 16384); }
                SB();
            });
        });
#undef G1_FRAG
    }
    TS(3);
    const unsigned tagG = tagA ^ tagB ^ 0x9E3779B9u;
    __builtin_amdgcn_s_setprio(MPRIO);
    f32x2 ur[8], ui[8], hr, hi;
    const f32x2 L1 = {ct[0][0], ct[0][1]}, C1 = {ct[0][2], ct[0][3]}, L16 = {ct[1][2], ct[1][3]}, Q2r = {ct[3][0], ct[3][1]}, Q2i = {ct[3][2], ct[3][3]};
    const float L8r = ct[1][0], L8i = ct[1][1], L64r = ct[2][0], L64i = ct[2][1];
    u32x4 eg[4];
#define E_LOADS "global_load_dwordx4 v[104:107], %[a0], %[b0] sc1\n\tglobal_load_dwordx4 v[108:111], %[a0], %[b1] sc1\n\t" \
                "global_load_dwordx4 v[112:115], %[a1], %[b0] sc1\n\tglobal_load_dwordx4 v[116:119], %[a1], %[b1] sc1\n"
#define E_POLL() asm volatile("s_cmp_lt_i32 %[on], 0\n\ts_cbranch_scc1 1f\n\t" E_LOADS "1:" : "={v[104:107]}"(eg[0]), "={v[108:111]}"(eg[1]), "={v[112:115]}"(eg[2]), "={v[116:119]}"(eg[3]) \
        : [a0] "v"(eo[0]), [a1] "v"(eo[1]), [b0] "s"(ws + WS_E), [b1] "s"(ws + WS_E + 16384), [on] "s"(pneg) : "scc", "memory")
    {
        asm volatile("s_nop 7" : "+v"(acc[0][0]), "+v"(acc[0][1]), "+v"(acc[1][0]), "+v"(acc[1][1]), "+v"(acc[2][0]), "+v"(acc[2][1]), "+v"(acc[3][0]), "+v"(acc[3][1]));
#pragma unroll
        for (int p = 0; p < 8; ++p) { ur[p] = f32x2{acc[p >> 1][0][2 * (p & 1)], acc[p >> 1][0][2 * (p & 1) + 1]}; ui[p] = f32x2{acc[p >> 1][1][2 * (p & 1)], acc[p >> 1][1][2 * (p & 1) + 1]}; }
        f32x2 gr = ur[0], gi = ui[0];
#pragma unroll
        for (int p = 1; p < 8; ++p) { f32x2 nr, ni; pk_cmad(nr, ni, L1, gr, gi, ur[p], ui[p]); gr = nr; gi = ni; }
        float fr, fi;
        cmad(fr, fi, L8r, L8i, gr[0], gi[0], gr[1], gi[1]);
        {
            const f32x2 w = cmul4(V1, f32x2{fr, fi});
            const float wr = sum4(w[0]), wi = sum4(w[1]);
            const u32x4 gq = {__float_as_uint(wr), tagA, __float_as_uint(wi), tagG};
            if (EREP ? (EALL || g4 >= (tm >> 2)) : g4 == 0) GST_B128_SC1(est, gq, ws + WS_E, 0);
        }
        *LDSP(f32x2, plr + 7 * 512) = f32x2{fr, fi};
        *LDSP(unsigned, pes + 8) = 0;
        TS(21);
        if (EP == 2) E_POLL();
        auto coef = [&]() {
#pragma unroll
            for (int p = 0; p < 8; ++p) { f32x2 nr, ni; pk_conj_mul(nr, ni, C1, ur[p], ui[p]); ur[p] = nr; ui[p] = ni; TIE(ur[p]); TIE(ui[p]); }
        };
        if (CFB) coef();
        WAIT_L(0); BAR();
        f32x2 e[7];
#pragma unroll
        for (int j = 0; j < 7; ++j) e[j] = *LDSP(f32x2, plr + j * 512);
        if (EP == 1) E_POLL();
        if (!CFB) coef();
#pragma unroll
        for (int j = 1; j < 7; ++j) cmac2(e[j], L16, e[j - 1]);
        float mr, mi;
        cmad(mr, mi, L8r, L8i, e[6][0], e[6][1], gr[0], gi[0]);
        pk_conj_mul(hr, hi, C1, f32x2{e[6][0], mr}, f32x2{e[6][1], mi});
        TIE(hr); TIE(hi);
        TS(4);
    }
    f32x2 G;
    {
        float pr = 0.f, pi = 0.f;
        if (!EP) E_POLL();
        if (pneg >= 0) {
            unsigned t0, t1, t2, left = SPIN_CAP;
            asm volatile("s_branch 2f\n1:\n\ts_sleep %[sl]\n\t" E_LOADS "2:\n\ts_waitcnt vmcnt(0)\n\t"
                         "v_bitop3_b32 %[t0], v105, v109, %[ta] bitop3:0x7e\n\tv_bitop3_b32 %[t1], v113, v117, %[ta] bitop3:0x7e\n\tv_bitop3_b32 %[t2], v107, v111, %[tg] bitop3:0x7e\n\t"
                         "v_or3_b32 %[t0], %[t0], %[t1], %[t2]\n\tv_bitop3_b32 %[t1], v115, v119, %[tg] bitop3:0x7e\n\tv_or_b32 %[t0], %[t0], %[t1]\n\t"
                         "v_cmp_ne_u32 vcc, 0, %[t0]\n\ts_cbranch_vccz 3f\n\ts_sub_u32 %[n], %[n], 1\n\ts_cbranch_scc0 1b\n3:"
                         : "+{v[104:107]}"(eg[0]), "+{v[108:111]}"(eg[1]), "+{v[112:115]}"(eg[2]), "+{v[116:119]}"(eg[3]), [t0] "=&v"(t0), [t1] "=&v"(t1), [t2] "=&v"(t2), [n] "+s"(left)
                         : [a0] "v"(eo[0]), [a1] "v"(eo[1]), [b0] "s"(ws + WS_E), [b1] "s"(ws + WS_E + 16384), [ta] "s"(tagA), [tg] "s"(tagG), [sl] "n"(ESL) : "vcc", "scc", "memory");
            float ar = __uint_as_float(eg[1][0]), ai = __uint_as_float(eg[1][2]), br = __uint_as_float(eg[3][0]), bi = __uint_as_float(eg[3][2]);
            cmac(ar, ai, L64r, L64i, __uint_as_float(eg[0][0]), __uint_as_float(eg[0][2]));
            cmac(br, bi, L64r, L64i, __uint_as_float(eg[2][0]), __uint_as_float(eg[2][2]));
            if (EALL) { if (j0 < 0) ar = ai = 0.f; if (j0 + 2 < 0) br = bi = 0.f; }
            const f32x2 pu = cmul4(f32x2{ct[4][2], ct[4][3]}, f32x2{ar, ai});
            pr = pu[0]; pi = pu[1];
            cmac(pr, pi, ct[4][0], ct[4][1], br, bi);
            pr = sum4(pr); pi = sum4(pi);
        }
        TS(5);
        *LDSP(u32x4, pes) = u32x4{__float_as_uint(pr), __float_as_uint(pi), 1u, 0u};
        u32x4 o;
        asm volatile("1:\n\tds_read_b128 %0, %1\n\ts_waitcnt lgkmcnt(0)\n\tv_cmp_eq_u32 vcc, 0, v122\n\ts_cbranch_vccnz 1b" : "=&{v[120:123]}"(o) : "v"(po) : "vcc", "memory");
        G = f32x2{pr + __uint_as_float(o[0]), pi + __uint_as_float(o[1])};
    }
    TS(6);
    {
        pk_conj_mac(hr, hi, G, Q2r, Q2i);
#pragma unroll
        for (int p = 0; p < 8; ++p) { pk_cmac_conj(ur[p], ui[p], L1, hr, hi); hr = ur[p]; hi = ui[p]; }
        const char* hd = ws + WS_H + (size_t)((tm * 4 + (wm & 2)) * 2 + wn) * HP + (tn * 4 + (wm & 1) * 2) * 1024;
#pragma unroll
        for (int e2 = 0; e2 < 2; ++e2)
#pragma unroll
            for (int c = 0; c < 2; ++c) {
                const f32x2* o = c ? ui : ur;
                *LDSP(u32x4, hw[e2] + c * 512) = u32x4{pack2(o[0][e2], o[1][e2]), pack2(o[2][e2], o[3][e2]), pack2(o[4][e2], o[5][e2]), pack2(o[6][e2], o[7][e2])};
            }
        v4i16 t[4][2];
#pragma unroll
        for (int n = 0; n < 4; ++n)
#pragma unroll
            for (int j = 0; j < 2; ++j) t[n][j] = __builtin_amdgcn_ds_read_tr16_b64_v4i16(LDSP(v4i16, ta[j] + n * 1024));
        sy_post<1>(pmine);
        static_for<0, 4>([&](auto ic) {
            constexpr int n = decltype(ic)::value;
            const bf16x8 f = __builtin_shufflevector(t[n][0], t[n][1], 0, 1, 2, 3, 4, 5, 6, 7);
            GST_B128_SC1(l16, f, hd + (n >> 1) * 2 * HP + ((n & 1) ^ wn) * 1024, 0);
        });
    }
    TS(7);
    bf16x8 fa2[AW];
    u32x2 xv[4], dsv;
    const char* Aw = ws + WS_H + (size_t)((tm * 4 + wm) * 2 + wn) * HP;
    const char* g2src = wn ? a.cim : a.cre;
    const unsigned g2dst = lb + L_C + wid * 2048;
#define G2_ISSUE(st, i) DMA(g2dst + (((st) + 2) & 3) * G2STAGE + (i) * 1024, g2v[i], g2src + (SLICE(st) << 7))
#define G2_A(g) GLD_B128_SC1(fa2[(g) % AW], l16, Aw + (SLICE((g) >> 2) << 12), ((g) & 3) * 1024)
    {
        {
            sy_wait<1>(pown);
            s16x4 fo[4][2]; bf16x8 c[8];
            static_for<0, 2>([&](auto kc) {
                constexpr int ks = decltype(kc)::value;
                static_for<0, 4>([&](auto ic) { constexpr int i = decltype(ic)::value; dsrtr_<ks * 4096>(fo[2 * ks + (i >> 1)][i & 1], to[i >> 1][i & 1]); });
                static_for<0, 4>([&](auto ic) { constexpr int i = decltype(ic)::value; DSR128(c[4 * ks + i], cad[ks][i & 1], 2 * G2STAGE + i * 2048); });
            });
            static_for<0, 8>([&](auto ic) {
                constexpr int n = decltype(ic)::value, ks = n >> 2, i = n & 3;
                WAIT_L(ks ? 3 - i : 11 - i);
                TIE(c[n]);
                if constexpr (i == 0) { TIE(fo[2 * ks][0]); TIE(fo[2 * ks][1]); TIE(fo[2 * ks + 1][0]); TIE(fo[2 * ks + 1][1]); }
#pragma unroll
                for (int j = 0; j < 2; ++j)
                    acc[i][j] = __builtin_amdgcn_mfma_f32_16x16x32_bf16(__builtin_shufflevector(fo[2 * ks + j][0], fo[2 * ks + j][1], 0, 1, 2, 3, 4, 5, 6, 7), c[n], ks ? acc[i][j] : f32x4{0, 0, 0, 0}, 0, 0, 0);
                SB();
            });
#pragma unroll
            for (int i = 0; i < 4; ++i) { TIE(acc[i][0]); TIE(acc[i][1]); }
        }
        if (CEARLY) sy_post<0>(pmine);
        static_for<0, 4>([&](auto ic) { constexpr int it = decltype(ic)::value; GLD_B64(xv[it], xo, a.x, it * 2048 - 4096); });
        GLD_B64(dsv, dso, a.dskip, 0);
        if (CEARLY) {
            sy_wait<0>(pall);
            static_for<4, 4 + CEARLY>([&](auto ic) { constexpr int n = decltype(ic)::value; G2_ISSUE(n >> 1, n & 1); });
        }
        TS(16);
        WAIT_V(5 + CEARLY);
        sy_post<2>(pmine);
        if (!CEARLY || wid == (NOOWN ? 1 : 0) || (NOOWN == 1 && wid == 0)) sy_wait<2, 0>(pall);
        const u32x2 tagv = {__float_as_uint(ct[2][2]), __float_as_uint(ct[2][3])};
        if (wid == (NOOWN ? 1 : 0)) asm volatile("s_mov_b64 exec, 0xffff\n\tglobal_store_dwordx2 %0, %1, %2 sc1\n\ts_mov_b64 exec, -1" :: "v"(fst), "v"(tagv), "s"(flg + FL_H) : "memory");
        if (PFIRST) asm volatile("s_cmp_lg_u32 %3, 0\n\ts_cbranch_scc1 1f\n\tglobal_load_dwordx2 %0, %1, %2 sc1\n1:" : "=&v"(fl) : "v"(fld), "s"(flg + FL_H), "s"(wid) : "scc", "memory");
        TS(8);
        static_for<4 + CEARLY, 10>([&](auto ic) { constexpr int n = decltype(ic)::value; G2_ISSUE(n >> 1, n & 1); });
        TS(13);
        if (wid == 0) {
            unsigned left = SPIN_CAP;
            if (PFIRST)
            asm volatile("s_waitcnt vmcnt(6)\n\ts_branch 3f\n1:\n\tglobal_load_dwordx2 %0, %2, %3 sc1\n\ts_waitcnt vmcnt(0)\n3:\n\tv_cmp_ne_u64 vcc, %0, %4\n\ts_cbranch_vccz 2f\n\ts_sub_u32 %1, %1, 1\n\ts_cbranch_scc0 1b\n2:"
                         : "+v"(fl), "+s"(left) : "v"(fld), "s"(flg + FL_H), "v"(tagv) : "vcc", "scc", "memory");
            else
            asm volatile("1:\n\tglobal_load_dwordx2 %0, %2, %3 sc1\n\ts_waitcnt vmcnt(0)\n\tv_cmp_ne_u64 vcc, %0, %4\n\ts_cbranch_vccz 2f\n\ts_sub_u32 %1, %1, 1\n\ts_cbranch_scc0 1b\n2:"
                         : "=&v"(fl), "+s"(left) : "v"(fld), "s"(flg + FL_H), "v"(tagv) : "vcc", "scc", "memory");
            sy_post<3>(pmine);
            if (b == 255 && lane == 0) GST_B64_SC1(0u * lane, tagv, flg + FL_EPOCH);
        } else sy_wait<3>(pzero);
        TS(20);
        static_for<4, 4 + AW>([&](auto ic) { constexpr int g = decltype(ic)::value; G2_A(g); });
    }
    TS(9);
    __builtin_amdgcn_s_setprio(0);
    f32x4 y[4];
    {
        bf16x8 fb[2][4];
#define G2_FRAG(g, i) DSR128(fb[(g) & 1][i], cad[(g) & 1][(i) & 1], ((((g) >> 1) + 2) & 3) * G2STAGE + (i) * 2048)
        static_for<0, 4>([&](auto ic) { constexpr int i = decltype(ic)::value; G2_FRAG(2, i); });
        static_for<2, 32>([&](auto ic) {
            constexpr int g = decltype(ic)::value, gn = g + 1;
            constexpr bool bar = (gn & 1) == 0 && gn < 32;
            if constexpr (bar) {
                constexpr int ca = g2_cnt(2 * g, 2 * g), cs = gn >= 10 ? g2_cnt(100 + gn - 6, 2 * g) : 63;
                static_assert(cs >= 0, "schedule");
                WAIT_V(AWB && ca < cs ? ca : cs); WAIT_L(0); BAR();
                if constexpr (!AWB) WAIT_V(ca);
                if constexpr (g == 3) TS(10);
                if constexpr (g == 17 || g == 25) TS(26 + (g >> 3));
#pragma unroll
                for (int i = 0; i < 4; ++i) TIE(fb[g & 1][i]);
                TIE(fa2[(2 * g) % AW]);
            }
            static_for<0, 8>([&](auto mc) {
                constexpr int m = decltype(mc)::value, i = m & 3, j = m >> 2, f = 2 * g + j;
                static_assert(g2_cnt(f, f) >= 0, "schedule");
                if constexpr (i == 0 && (j || !bar)) { WAIT_V(g2_cnt(f, f)); TIE(fa2[f % AW]); }
                if constexpr (!bar && j == 0) { WAIT_L(gn < 32 ? 3 : 3 - i); TIE(fb[g & 1][i]); }
                acc[i][j] = __builtin_amdgcn_mfma_f32_16x16x32_bf16(fa2[f % AW], fb[g & 1][i], acc[i][j], 0, 0, 0);
                SB();
                if constexpr (gn < 32 && m < 4) G2_FRAG(gn, m);
                if constexpr (m == 5 && g2_dma(g)) G2_ISSUE(gn / 2 + 3, gn & 1);
                if constexpr (i == 3 && f + AW < 64) G2_A(f + AW);
                if constexpr (g >= YS && g < YS + 4 && m == 6) {
                    constexpr int it = g - YS;
                    TIE(xv[it]); TIE(dsv);
                    y[it] = f32x4{rb1(bflo(xv[it][0]) * bflo(dsv[0])), rb1(bfhi(xv[it][0]) * bfhi(dsv[0])), rb1(bflo(xv[it][1]) * bflo(dsv[1])), rb1(bfhi(xv[it][1]) * bfhi(dsv[1]))};
                    TIE(y[it]);
                }
                SB();
            });
        });
#undef G2_FRAG
    }
    TS(11);
    {
#pragma unroll
        for (int i = 0; i < 4; ++i) *LDSP(f32x4, lb + L_Y2 + wid * 4096 + l16 + i * 1024) = acc[i][1];
        sy_post<4>(pmine);
#pragma unroll
        for (int r = 0; r < 4; ++r) {
            y[r] = f32x4{2.f * acc[0][0][r] + y[r][0], 2.f * acc[1][0][r] + y[r][1], 2.f * acc[2][0][r] + y[r][2], 2.f * acc[3][0][r] + y[r][3]};
            TIE(y[r]);
        }
        sy_wait<4, 0>(pmine ^ 16);
        char* const ob = a.out + (size_t)(tm * 128 + wm * 32 + wn * 16) * 8192 + 4096 + tn * 256;
        f32x4 w[4];
#pragma unroll
        for (int i = 0; i < 4; ++i) w[i] = *LDSP(f32x4, lb + L_Y2 + (wid ^ 4) * 4096 + l16 + i * 1024);
#pragma unroll
        for (int r = 0; r < 4; ++r) {
            const f32x4 o = {2.f * w[0][r] + y[r][0], 2.f * w[1][r] + y[r][1], 2.f * w[2][r] + y[r][2], 2.f * w[3][r] + y[r][3]};
            GST_B128_NT(g4 * 32768 + l15 * 16, o, ob + r * 8192, 0);
        }
    }
    asm volatile(".Lfk_code_b:");
    TSE(12);
    TS_FLUSH();
}

}

extern "C" void kernel_launch(void* const* d_in, const int* in_sizes, int n_in, void* d_out, int out_size, void* d_ws, size_t ws_size,
                              hipStream_t stream) {
    (void)in_sizes; (void)n_in; (void)out_size; (void)ws_size;
    fk::Args a;
    a.q = (const char*)d_in[0]; a.k = (const char*)d_in[1]; a.v = (const char*)d_in[2]; a.sk = (const char*)d_in[3];
    a.ssmk = (const char*)d_in[4]; a.ssmD = (const char*)d_in[5]; a.x = (const char*)d_in[6]; a.lre = (const char*)d_in[7];
    a.lim = (const char*)d_in[8]; a.bre = (const char*)d_in[9]; a.bim = (const char*)d_in[10]; a.cre = (const char*)d_in[11];
    a.cim = (const char*)d_in[12]; a.logdt = (const char*)d_in[13]; a.dskip = (const char*)d_in[14];
    a.out = (char*)d_out; a.ws = (char*)d_ws;
    hipLaunchKernelGGL(fk::fused, dim3(256), dim3(768), 0, stream, a);
}
```

```cpp
#define FK_TIMING
#define SL1 0
#define ST1 0
#define BS1 0
#define MX1 1
#define SL2 2
#define ST2 2
#define BS2 100
#define MX2 0
#include <hip/hip_runtime.h>
#include <stdint.h>
#include <type_traits>

namespace fk {

constexpr int S = 2048, D = 1024, P = 1024;

typedef __attribute__((ext_vector_type(8))) short bf16x8;
typedef __attribute__((ext_vector_type(4))) short s16x4;
typedef __attribute__((ext_vector_type(16))) float f32x16;
typedef __attribute__((ext_vector_type(4))) float f32x4;
typedef __attribute__((ext_vector_type(2))) float f32x2;
typedef __attribute__((ext_vector_type(4))) unsigned u32x4;
typedef __attribute__((ext_vector_type(2))) unsigned u32x2;
#define LDSP(T, a) ((__attribute__((address_space(3))) T*)(uintptr_t)(unsigned)(a))

__device__ __forceinline__ float bf2f(unsigned v) { return __uint_as_float(v << 16); }
__device__ __forceinline__ float bflo(unsigned w) { return __uint_as_float(w << 16); }
__device__ __forceinline__ float bfhi(unsigned w) { return __uint_as_float(w & 0xffff0000u); }
__device__ __forceinline__ uint16_t f2bf(float f) { __bf16 b = (__bf16)f; return __builtin_bit_cast(uint16_t, b); }
__device__ __forceinline__ float rbf(float f) { return bf2f(f2bf(f)); }
typedef __attribute__((ext_vector_type(2))) __bf16 bf16x2;
__device__ __forceinline__ unsigned pack2(float lo, float hi) { return __builtin_bit_cast(unsigned, __builtin_convertvector(f32x2{lo, hi}, bf16x2)); }
__device__ __forceinline__ float rb1(float x) { return __uint_as_float(pack2(0.f, x)); }
__device__ __forceinline__ void cmac(float& xr, float& xi, float ar, float ai, float nai, float hr, float hi) {
    asm("v_fmac_f32 %0, %2, %5\n\tv_fmac_f32 %1, %2, %6\n\tv_fmac_f32 %0, %4, %6\n\tv_fmac_f32 %1, %3, %5" : "+v"(xr), "+v"(xi) : "v"(ar), "v"(ai), "v"(nai), "v"(hr), "v"(hi));
}
__device__ __forceinline__ void cmad(float& o_r, float& o_i, float ar, float ai, float nai, float hr, float hi, float er, float ei) {
    asm("v_fma_f32 %0, %2, %5, %7\n\tv_fma_f32 %1, %2, %6, %8\n\tv_fmac_f32 %0, %4, %6\n\tv_fmac_f32 %1, %3, %5" : "=&v"(o_r), "=&v"(o_i) : "v"(ar), "v"(ai), "v"(nai), "v"(hr), "v"(hi), "v"(er), "v"(ei));
}
__device__ __forceinline__ void mac4(float& x, float& y, float a0, float a1, float b0, float b1, float h0, float h1) {
    asm("v_fmac_f32 %0, %2, %6\n\tv_fmac_f32 %1, %4, %7\n\tv_fmac_f32 %0, %3, %7\n\tv_fmac_f32 %1, %5, %6" : "+v"(x), "+v"(y) : "v"(a0), "v"(a1), "v"(b0), "v"(b1), "v"(h0), "v"(h1));
}
__device__ __forceinline__ void mul4(float& x, float& y, float a0, float a1, float b0, float b1, float h0, float h1) {
    asm("v_mul_f32 %0, %2, %6\n\tv_mul_f32 %1, %4, %7\n\tv_fmac_f32 %0, %3, %7\n\tv_fmac_f32 %1, %5, %6" : "=&v"(x), "=&v"(y) : "v"(a0), "v"(a1), "v"(b0), "v"(b1), "v"(h0), "v"(h1));
}
__device__ __forceinline__ void cmul4(float& o_r, float& o_i, float ar, float ai, float nai, float hr, float hi) {
    asm("v_mul_f32 %0, %2, %5\n\tv_mul_f32 %1, %2, %6\n\tv_fmac_f32 %0, %4, %6\n\tv_fmac_f32 %1, %3, %5" : "=&v"(o_r), "=&v"(o_i) : "v"(ar), "v"(ai), "v"(nai), "v"(hr), "v"(hi));
}
__device__ __forceinline__ void cmul(float ar, float ai, float br, float bi, float& cr, float& ci) { cr = ar * br - ai * bi; ci = ar * bi + ai * br; }

#define PK_LO " op_sel:[0,0,0] op_sel_hi:[0,1,1]"
#define PK_HI " op_sel:[1,0,0] op_sel_hi:[1,1,1]"
#define PK_NEG " neg_lo:[1,0,0] neg_hi:[1,0,0]"
__device__ __forceinline__ void pk_cmad(f32x2& o_r, f32x2& o_i, f32x2 a, f32x2 hr, f32x2 hi, f32x2 er, f32x2 ei) {
    asm("v_pk_fma_f32 %0, %2, %3, %5" PK_LO "\n\tv_pk_fma_f32 %1, %2, %4, %6" PK_LO "\n\tv_pk_fma_f32 %0, %2, %4, %0" PK_HI PK_NEG "\n\tv_pk_fma_f32 %1, %2, %3, %1" PK_HI
        : "=&v"(o_r), "=&v"(o_i) : "v"(a), "v"(hr), "v"(hi), "v"(er), "v"(ei));
}
__device__ __forceinline__ void pk_cmac_conj(f32x2& xr, f32x2& xi, f32x2 a, f32x2 hr, f32x2 hi) {
    asm("v_pk_fma_f32 %0, %2, %3, %0" PK_LO "\n\tv_pk_fma_f32 %1, %2, %4, %1" PK_LO "\n\tv_pk_fma_f32 %0, %2, %4, %0" PK_HI "\n\tv_pk_fma_f32 %1, %2, %3, %1" PK_HI PK_NEG
        : "+v"(xr), "+v"(xi) : "v"(a), "v"(hr), "v"(hi));
}
__device__ __forceinline__ void pk_conj_mul(f32x2& o_r, f32x2& o_i, f32x2 a, f32x2 hr, f32x2 hi) {
    asm("v_pk_mul_f32 %0, %2, %3 op_sel:[0,0] op_sel_hi:[0,1]\n\tv_pk_mul_f32 %1, %2, %4 op_sel:[0,0] op_sel_hi:[0,1] neg_lo:[1,0] neg_hi:[1,0]\n\t"
        "v_pk_fma_f32 %0, %2, %4, %0" PK_HI PK_NEG "\n\tv_pk_fma_f32 %1, %2, %3, %1" PK_HI PK_NEG : "=&v"(o_r), "=&v"(o_i) : "v"(a), "v"(hr), "v"(hi));
}
__device__ __forceinline__ void pk_conj_mac(f32x2& xr, f32x2& xi, f32x2 a, f32x2 hr, f32x2 hi) {
    asm("v_pk_fma_f32 %0, %2, %3, %0" PK_LO "\n\tv_pk_fma_f32 %1, %2, %4, %1" PK_LO PK_NEG "\n\tv_pk_fma_f32 %0, %2, %4, %0" PK_HI PK_NEG "\n\tv_pk_fma_f32 %1, %2, %3, %1" PK_HI PK_NEG
        : "+v"(xr), "+v"(xi) : "v"(a), "v"(hr), "v"(hi));
}
__device__ __forceinline__ void cmac2(f32x2& x, f32x2 a, f32x2 h) {
    asm("v_pk_fma_f32 %0, %1, %2, %0" PK_LO "\n\tv_pk_fma_f32 %0, %1, %2, %0 op_sel:[1,1,0] op_sel_hi:[1,0,1] neg_lo:[1,0,0]" : "+v"(x) : "v"(a), "v"(h));
}
__device__ __forceinline__ float sum4(float x) {
    const auto p = __builtin_amdgcn_permlane32_swap(__float_as_uint(x), __float_as_uint(x), false, false);
    x = __uint_as_float(p[0]) + __uint_as_float(p[1]);
    const auto q = __builtin_amdgcn_permlane16_swap(__float_as_uint(x), __float_as_uint(x), false, false);
    return __uint_as_float(q[0]) + __uint_as_float(q[1]);
}

template <int I, int N, class F>
__device__ __forceinline__ void static_for(F&& f) { if constexpr (I < N) { f(std::integral_constant<int, I>{}); static_for<I + 1, N>(f); } }

#define TS_DECL unsigned tsa_ = 0, tsb_ = 0;
#define TS0a() unsigned long long ts0a_, ts0b_; asm volatile("s_memrealtime %0\n\ts_memtime %1" : "=s"(ts0a_), "=s"(ts0b_))
#define TS0b() do { asm volatile("" : "+s"(ts0a_), "+s"(ts0b_)); asm volatile("v_writelane_b32 %0, %2, 0\n\tv_writelane_b32 %1, %3, 0" : "+v"(tsa_), "+v"(tsb_) : "s"((unsigned)ts0a_), "s"((unsigned)ts0b_)); } while (0)
#define TS(i) TSE(i)
#define TSE(i) do { unsigned long long a_, b_; asm volatile("s_memrealtime %0\n\ts_memtime %1\n\ts_waitcnt lgkmcnt(0)" : "=s"(a_), "=s"(b_) :: "memory"); \
    asm volatile("v_writelane_b32 %0, %2, %4\n\tv_writelane_b32 %1, %3, %4" : "+v"(tsa_), "+v"(tsb_) : "s"((unsigned)a_), "s"((unsigned)b_), "n"(i)); } while (0)
#define TS_FLUSH() do { const int w_ = threadIdx.x >> 6; if ((w_ == 0 || w_ == 7 || w_ == 8) && (threadIdx.x & 63) < 32) { \
    unsigned long long* tsp_ = (unsigned long long*)(ws + ((size_t)200 << 20)) + ((size_t)blockIdx.x * 3 + (threadIdx.x >> 8)) * 64 + (threadIdx.x & 63) * 2; \
    tsp_[0] = (threadIdx.x & 63) == 31 ? 0x5453545354535453ull : tsa_; tsp_[1] = tsb_; } } while (0)

#define WAIT_V(n) asm volatile("s_waitcnt vmcnt(%0)" ::"n"((n) > 63 ? 63 : (n)) : "memory")
#define WAIT_L(n) asm volatile("s_waitcnt lgkmcnt(%0)" ::"n"((n) > 15 ? 15 : (n)) : "memory")
#define BAR() asm volatile("s_barrier" ::: "memory")
#define SB() __builtin_amdgcn_sched_barrier(0)
template <class T> __device__ __forceinline__ void tie_(T& x) { asm volatile("" : "+v"(x)); }
#define TIE(x) tie_(x)
__device__ __forceinline__ unsigned take(unsigned x) { unsigned y; asm volatile("v_mov_b32 %0, %1" : "=v"(y) : "v"(x)); return y; }
#define DEF_LD(name, ins) template <int OFF, class T> __device__ __forceinline__ void name(T& dst, unsigned voff, const char* sb) { \
    asm volatile(ins : "=v"(dst) : "v"(voff), "s"(sb), "n"(OFF) : "memory"); }
DEF_LD(gld_u16_, "global_load_ushort %0, %1, %2 offset:%3")
DEF_LD(gld_b32_, "global_load_dword %0, %1, %2 offset:%3")
DEF_LD(gld_b64_, "global_load_dwordx2 %0, %1, %2 offset:%3")
DEF_LD(gld_b128_, "global_load_dwordx4 %0, %1, %2 offset:%3")
DEF_LD(gld_b64_nt_, "global_load_dwordx2 %0, %1, %2 offset:%3 nt")
DEF_LD(gld_b128_nt_, "global_load_dwordx4 %0, %1, %2 offset:%3 nt")
DEF_LD(gld_b64_sc1_, "global_load_dwordx2 %0, %1, %2 offset:%3 sc1")
DEF_LD(gld_b128_sc1_, "global_load_dwordx4 %0, %1, %2 offset:%3 sc1")
#define DEF_ST(name, ins) template <int OFF, class T> __device__ __forceinline__ void name(unsigned voff, const T& v, const char* sb) { \
    asm volatile(ins :: "v"(voff), "v"(v), "s"(sb), "n"(OFF) : "memory"); }
DEF_ST(gst_b128_, "global_store_dwordx4 %0, %1, %2 offset:%3\n\ts_nop 1")
DEF_ST(gst_b128_nt_, "global_store_dwordx4 %0, %1, %2 offset:%3 nt\n\ts_nop 1")
DEF_ST(gst_b128_sc1_, "global_store_dwordx4 %0, %1, %2 offset:%3 sc1\n\ts_nop 1")
DEF_ST(gst_b64_sc1_, "global_store_dwordx2 %0, %1, %2 offset:%3 sc1")
#define GLD_U16(dst, voff, sb) gld_u16_<0>(dst, voff, sb)
#define GLD_B32(dst, voff, sb, off) gld_b32_<(off)>(dst, voff, sb)
#define GLD_B64(dst, voff, sb, off) gld_b64_<(off)>(dst, voff, sb)
#define GLD_B128(dst, voff, sb, off) gld_b128_<(off)>(dst, voff, sb)
#define GLD_B64_NT(dst, voff, sb, off) gld_b64_nt_<(off)>(dst, voff, sb)
#define GLD_B128_NT(dst, voff, sb, off) gld_b128_nt_<(off)>(dst, voff, sb)
#define GLD_B64_SC1(dst, voff, sb, off) gld_b64_sc1_<(off)>(dst, voff, sb)
#define GLD_B128_SC1(dst, voff, sb, off) gld_b128_sc1_<(off)>(dst, voff, sb)
#define GST_B128(voff, v, sb, off) gst_b128_<(off)>(voff, v, sb)
#define GST_B128_NT(voff, v, sb, off) gst_b128_nt_<(off)>(voff, v, sb)
#define GST_B128_SC1(voff, v, sb, off) gst_b128_sc1_<(off)>(voff, v, sb)
#define GST_B64_SC1(voff, v, sb) gst_b64_sc1_<0>(voff, v, sb)
__device__ __forceinline__ void dma_(unsigned ldsaddr, unsigned voff, const char* sb) {
    asm volatile("s_mov_b32 m0, %0\n\ts_nop 0\n\tglobal_load_lds_dwordx4 %1, %2" :: "s"(ldsaddr), "v"(voff), "s"(sb) : "memory"); }
__device__ __forceinline__ void dma_nt_(unsigned ldsaddr, unsigned voff, const char* sb) {
    asm volatile("s_mov_b32 m0, %0\n\ts_nop 0\n\tglobal_load_lds_dwordx4 %1, %2 nt" :: "s"(ldsaddr), "v"(voff), "s"(sb) : "memory"); }
#define DMA(ldsaddr, voff, sb) dma_(ldsaddr, voff, sb)
template <int OFF, class T> __device__ __forceinline__ void dsr128_(T& dst, unsigned addr) { asm volatile("ds_read_b128 %0, %1 offset:%2" : "=v"(dst) : "v"(addr), "n"(OFF)); }
template <int OFF, class T> __device__ __forceinline__ void dsr64_(T& dst, unsigned addr) { asm volatile("ds_read_b64 %0, %1 offset:%2" : "=v"(dst) : "v"(addr), "n"(OFF)); }
#define DSR128(dst, addr, off) dsr128_<(off)>(dst, addr)
#define DSR64(dst, addr, off) dsr64_<(off)>(dst, addr)

__device__ __forceinline__ void kv4(unsigned& o01, unsigned& o23, unsigned ka, unsigned kb, unsigned kc, unsigned kd, const unsigned* w, unsigned v0, unsigned v1) {
    unsigned t1, t3, p0, p1, p2, p3;
    asm(
        "v_dot2_f32_bf16 %0, %8, %19, 0\n\tv_dot2_f32_bf16 %1, %9, %18, 0\n\tv_dot2_f32_bf16 %2, %9, %19, 0\n\tv_dot2_f32_bf16 %3, %10, %18, 0\n\t"
        "v_dot2_f32_bf16 %4, %9, %16, 0\n\tv_dot2_f32_bf16 %5, %9, %17, 0\n\tv_dot2_f32_bf16 %6, %10, %16, 0\n\tv_dot2_f32_bf16 %7, %10, %17, 0\n\t"
        "v_cvt_pk_bf16_f32 %0, %0, %4\n\tv_cvt_pk_bf16_f32 %1, %1, %5\n\tv_cvt_pk_bf16_f32 %2, %2, %6\n\tv_cvt_pk_bf16_f32 %3, %3, %7\n\t"
        "v_dot2_f32_bf16 %4, %9, %15, 0\n\tv_dot2_f32_bf16 %5, %10, %14, 0\n\tv_dot2_f32_bf16 %6, %10, %15, 0\n\tv_dot2_f32_bf16 %7, %11, %14, 0\n\t"
        "v_dot2_f32_bf16 %0, %0, %22, 0\n\tv_dot2_f32_bf16 %1, %1, %22, 0\n\tv_dot2_f32_bf16 %2, %2, %22, 0\n\tv_dot2_f32_bf16 %3, %3, %22, 0\n\t"
        "v_cvt_pk_bf16_f32 %0, %0, %4\n\tv_cvt_pk_bf16_f32 %1, %1, %5\n\tv_cvt_pk_bf16_f32 %2, %2, %6\n\tv_cvt_pk_bf16_f32 %3, %3, %7\n\t"
        "v_dot2_f32_bf16 %4, %10, %12, 0\n\tv_dot2_f32_bf16 %5, %10, %13, 0\n\tv_dot2_f32_bf16 %6, %11, %12, 0\n\tv_dot2_f32_bf16 %7, %11, %13, 0\n\t"
        "v_dot2_f32_bf16 %0, %0, %22, 0\n\tv_dot2_f32_bf16 %1, %1, %22, 0\n\tv_dot2_f32_bf16 %2, %2, %22, 0\n\tv_dot2_f32_bf16 %3, %3, %22, 0\n\t"
        "v_cvt_pk_bf16_f32 %0, %0, %4\n\tv_cvt_pk_bf16_f32 %1, %1, %5\n\tv_cvt_pk_bf16_f32 %2, %2, %6\n\tv_cvt_pk_bf16_f32 %3, %3, %7\n\t"
        "v_dot2_f32_bf16 %0, %0, %22, 0\n\tv_dot2_f32_bf16 %1, %1, %22, 0\n\tv_dot2_f32_bf16 %2, %2, %22, 0\n\tv_dot2_f32_bf16 %3, %3, %22, 0\n\t"
        "v_cvt_pk_bf16_f32 %0, %0, 0\n\tv_cvt_pk_bf16_f32 %1, 0, %1\n\tv_cvt_pk_bf16_f32 %2, %2, 0\n\tv_cvt_pk_bf16_f32 %3, 0, %3\n\t"
        "v_dot2_f32_bf16 %0, %0, %20, 0\n\tv_dot2_f32_bf16 %1, %1, %20, 0\n\tv_dot2_f32_bf16 %2, %2, %21, 0\n\tv_dot2_f32_bf16 %3, %3, %21, 0\n\t"
        "s_nop 1\n\tv_cvt_pk_bf16_f32 %0, %1, %0\n\tv_cvt_pk_bf16_f32 %2, %3, %2\n\t"
        : "=&v"(o01), "=&v"(t1), "=&v"(o23), "=&v"(t3), "=&v"(p0), "=&v"(p1), "=&v"(p2), "=&v"(p3)
        : "v"(ka), "v"(kb), "v"(kc), "v"(kd), "v"(w[0]), "v"(w[1]), "v"(w[2]), "v"(w[3]), "v"(w[4]), "v"(w[5]), "v"(w[6]), "v"(w[7]), "v"(v0), "v"(v1), "s"(0x3f803f80u));
}
__device__ __forceinline__ void lb_post(unsigned myaddr, unsigned seq) { asm volatile("ds_write_b32 %0, %1" :: "v"(myaddr), "v"(seq) : "memory"); }
__device__ __forceinline__ void lb_wait(unsigned addr, unsigned seq) {
    unsigned v;
    asm volatile("s_branch 2f\n1:\n\ts_sleep 1\n2:\n\tds_read_b32 %0, %1\n\ts_waitcnt lgkmcnt(0)\n\tv_cmp_gt_u32 vcc, %2, %0\n\ts_cbranch_vccnz 1b" : "=&v"(v) : "v"(addr), "v"(seq) : "vcc", "memory");
}

constexpr size_t WS_H = 0, WS_KVT = (size_t)10 << 20, WS_ZT = (size_t)15 << 20, WS_E = (size_t)20 << 20, WS_FLAG = WS_E + (512 << 10);
#define HP 65536
#define KP 8192
#define ZP 4096
constexpr int FL_KV = 0, FL_Z = 65536, FL_H = 131072, FL_EPOCH = 196608;
constexpr unsigned SPIN_CAP = 1u << 17;
#define ABL 0
#define CSPLIT 1
#define MERGE 3
#define CB 13
#define PAUSE 0
#define QLATE 0
#define CVPRIO 0
#define STG_SEL (idx & 3)
#define SP0 3
#define SDP 1
#define LP0 3
#define LDP 2
#define KVS 5
#define KVU 2
#define QIT 1
#define ZB 3
#define OS 2
#define MPRIO 2

#define ROT 0x80A5
#define R2 0x41
#define CPF 0
#define PRE 4
#define PRO 12
#define G1NT 0
#define G1NTS 16
#define TMIX 0
#define TLBT 0
#define CDEF 0
#define CVLATE 1
#define FPRIO 3
#define YS 12
#define DP0 1
#define DP1 5
#define AWB 1
#define PK 0
#define PU 13
#define PC0 13
#define PC1 14
#define PQ 0
#define SPF 0
#define SPH 0
#define NB1 4
#define EP 1
#define ESL 0
#define CFB 0
#define P1 1
#define P0 1
constexpr int it_cnt(int p, int k, int last) {
    const int pos[6] = {PK, PU, PC0, PC1, PQ, PU - 1}, n[6] = {4, 4, 4, 4, 8, 1};
    int c = 0;
    for (int i = 0; i < 6; ++i) if (pos[i] <= last && (pos[i] > p || (pos[i] == p && i > k))) c += n[i];
    return c;
}
constexpr int G1STAGE = 32768;
constexpr int g1_slot(int s) { return (NB1 == 5 ? (s + 3) % 5 : s & 3) * G1STAGE; }
constexpr int L_KS = NB1 == 5 ? 65536 : 131072, L_VS = L_KS + 16384;
constexpr int CST = 4288;
constexpr int UOFF = 4 * CST, UPL = 2304;
constexpr int CHSZ = UOFF + 2 * UPL + 64;
constexpr int L_AGG = 4 * CHSZ, L_ES = L_AGG + 15 * 512, L_SY = L_ES + 2048;
constexpr int L_HT = 98304;
constexpr int L_C = 98304, G2STAGE = 16384;
constexpr int L_ZI = 0;
constexpr int L_Z2 = 20480;
constexpr int L_Y = 40960, L_Y2 = 131072;
static_assert(L_SY + 64 <= L_HT, "lds");
static_assert(L_Y + 32768 <= L_AGG && L_Z2 + 64 * 264 <= L_Y, "lds");

__device__ __forceinline__ int img_off(int r, int c) { return (r >> 1) * 256 + ((((r & 1) * 8 + c) ^ ((r >> 1) & 15)) << 4); }

constexpr int g1_upto(int g) {
    int n = PRO;
    for (int t = 0; t <= g; ++t) {
        int sb = (t + 1) / 2, sub = (t + 1) % 2, allowed = sb >= 1 ? 4 * (sb + NB1 - 1) + 2 * (sub + 1) : 4 * NB1;
        if (allowed > 64) allowed = 64;
        n = n + 4 < allowed ? n + 4 : allowed;
    }
    return n;
}
constexpr int g1_cnt(int s) { return g1_upto(2 * s - 2) - 4 * (s + 1); }
static_assert(g1_upto(32 - 2 * NB1) == 64, "schedule");
constexpr int AW = 8;
constexpr bool g2_dma(int g) { return (g + 1) / 2 >= 2 && (g + 1) / 2 + 3 < 16; }
constexpr int g2_cnt(int id, int at) {
    int n = -1000;
    for (int f = 4; f < 4 + AW; ++f) { ++n; if (f == id) n = 0; }
    for (int f = 4; f < at; ++f) {
        if ((f & 1) && g2_dma(f >> 1)) { ++n; if (100 + (f >> 1) == id) n = 0; }
        if (f + AW < 64) { ++n; if (f + AW == id) n = 0; }
    }
    return n;
}

constexpr int cv_m(int o) { return (o & 1) ? 7 - (o >> 1) : o >> 1; }
constexpr int cv_first(int o) { int p = 0; for (int i = 0; i < o; ++i) p += 8 - cv_m(i); return p; }
constexpr int cv_grp(int p) { int o = 0; while (cv_first(o + 1) <= p) ++o; return o; }
constexpr int cv_read(int p) {
    int slot[40] = {};
    for (int i = 0; i < 40; ++i) slot[i] = -1;
    slot[0] = 2 * 3 + 1;
    for (int o = 0; o < 8; ++o) {
        int q = cv_first(o + 1) - 1;
        for (int h = 0; h < 2; ++h) { while (slot[q] >= 0) ++q; slot[q] = 2 * (o + 4) + h; }
    }
    const int bs[8] = {1, 3, 5, 11, 12, 13, 19, 21};
    for (int jb = 0; jb < 8; ++jb) slot[bs[jb]] = 32 + jb;
    return slot[p];
}
static_assert(cv_read(35) == 22 && cv_read(36) == 23 && cv_read(37) == -1 && cv_read(10) == 11 && cv_read(15) == 12 && cv_read(18) == 15 && cv_read(24) == 17, "conv schedule");
constexpr int cv_wait(int o) {
    int n = 0;
    for (int q = cv_first(o) - 1;; --q) {
        const int rd = cv_read(q < 0 ? q + 36 : q);
        if (rd == 2 * (o + (q < 0 ? 8 : 0)) + 1) return n;
        if (rd >= 0) ++n;
    }
}

struct Args {
    const char *q, *k, *v, *sk, *ssmk, *ssmD, *x, *lre, *lim, *bre, *bim, *cre, *cim, *logdt, *dskip;
    char *out, *ws;
};

typedef __attribute__((ext_vector_type(8))) unsigned long long u64x8;
__global__ __launch_bounds__(768, 3) void fused(Args) {
    __shared__ __attribute__((aligned(1024))) char shm[163840];
    TS0a();
    u64x8 ka0, ka1; unsigned long long ka2;
    const char* pc0;
    int code_n;
    asm volatile("s_load_dwordx16 %0, %5, 0x0\n\ts_load_dwordx16 %1, %5, 0x40\n\ts_load_dwordx2 %2, %5, 0x80\n\t"
                 "s_getpc_b64 %3\n.Lfk_code_0:\n\ts_mov_b32 %4, .Lfk_code_a-.Lfk_code_0-4\n\ts_max_i32 %4, %4, .Lfk_code_b-.Lfk_code_0-4"
                 : "=&s"(ka0), "=&s"(ka1), "=&s"(ka2), "=&s"(pc0), "=&s"(code_n) : "s"(__builtin_amdgcn_kernarg_segment_ptr()) : "scc");
    const int tid = threadIdx.x, lane = tid & 63, wid = __builtin_amdgcn_readfirstlane(tid >> 6);
    const int b = blockIdx.x, xcd = b & 7, idx = b >> 3;
    unsigned cpf;
    asm volatile("s_cmp_lt_u32 %1, 8\n\ts_cbranch_scc1 1f\n\ts_setprio 3\n\ts_cmp_lg_u32 %1, 8\n\ts_cbranch_scc1 1f\n\t"
                 "v_lshl_add_u32 %0, %2, 7, %3\n\tv_min_i32 %0, %4, %0\n\ts_mov_b64 exec, 0xffff\n\tglobal_load_dword %0, %0, %5\n\ts_mov_b64 exec, -1\n1:"
                 : "=&v"(cpf) : "s"(wid), "v"(lane), "s"(idx * 2048), "s"(code_n), "s"(pc0) : "scc", "memory");
    TS_DECL
    const int tm = (xcd >> 1) * 4 + (idx >> 3), tn = (xcd & 1) * 8 + (idx & 7);
    const int wm = wid & 3, wn = (wid >> 2) & 1, l31 = lane & 31, lh = lane >> 5;
    const unsigned lb = (unsigned)(uintptr_t)shm;
    const unsigned l16 = lane * 16;
    const int l15 = lane & 15, g4 = lane >> 4, sq = 2 * wn + (wm & 1), st = sq * 16 + l15, run = (wm >> 1) * 4 + g4;
    unsigned so, kvv[4];
    const int trow = idx * 64 * 2048 + xcd * 256;
    if (wid < 8) {
        so = (tn * 64 + sq * 16) * 2 + (l15 & 7) * 4;
        kvv[0] = trow + wm * 32768 + g4 * 2048 + ((l15 ^ (2 * g4)) << 4);
        kvv[1] = kvv[0] + 8192; kvv[2] = (kvv[0] ^ 128) + 16384; kvv[3] = kvv[2] + 8192;
#pragma unroll
        for (int i = 0; i < 4; ++i) TIE(kvv[i]);
    } else {
        so = trow + (idx ? -4 * 2048 : 0) + g4 * 2048 + wm * 64 + l15 * 4;
        asm volatile("v_writelane_b32 %0, %1, 2" : "+v"(so) : "s"((xcd * 64 + idx * 2 + (wm >> 1)) * 4));
        kvv[0] = (xcd * 128 + wm * 32 + l31) * 8;
        TIE(kvv[0]);
    }
    unsigned g1dst = lb + wid * 4096;
    asm volatile("v_writelane_b32 %4, 0, 0\n\tv_writelane_b32 %4, 4, 1\n\ts_waitcnt lgkmcnt(0)" : "+s"(ka0), "+s"(ka1), "+s"(ka2), "+s"(g1dst), "+v"(so));
    Args a;
    a.q = (const char*)ka0[0]; a.k = (const char*)ka0[1]; a.v = (const char*)ka0[2]; a.sk = (const char*)ka0[3]; a.ssmk = (const char*)ka0[4]; a.ssmD = (const char*)ka0[5];
    a.x = (const char*)ka0[6]; a.lre = (const char*)ka0[7]; a.lim = (const char*)ka1[0]; a.bre = (const char*)ka1[1]; a.bim = (const char*)ka1[2]; a.cre = (const char*)ka1[3];
    a.cim = (const char*)ka1[4]; a.logdt = (const char*)ka1[5]; a.dskip = (const char*)ka1[6]; a.out = (char*)ka1[7]; a.ws = (char*)ka2;
    char* const ws = a.ws;
    const char* const flg = ws + WS_FLAG;
    TS(26);
    unsigned sm; u32x2 kw0;
    {
        unsigned long long p;
        asm volatile("s_cmp_gt_u32 %[w], 7\n\ts_cbranch_scc0 1f\n\tv_mov_b64 %2, %[k]\n\ts_mov_b64 exec, 7\n\tv_mov_b64 %2, %[d]\n\ts_branch 4f\n1:\n\t"
                     "v_mov_b64 %2, %[c3]\n\ts_mov_b32 exec_hi, 0xffff\n\tv_mov_b64 %2, %[c2]\n\ts_mov_b32 exec_hi, 0\n\tv_mov_b64 %2, %[c1]\n4:\n\t"
                     "s_mov_b64 exec, 3\n\tv_mov_b64 %2, %[e]\n\ts_mov_b64 exec, -1\n\t"
                     "v_mad_u64_u32 %2, vcc, %[o], 1, %2\n\tglobal_load_dword %0, %2, off sc1\n\ts_cbranch_scc0 2f\n\tglobal_load_dwordx2 %1, %[ko], %[sk]\n\ts_branch 3f\n2:\n\t"
                     "s_add_u32 m0, %[m], %[n0]\n\ts_nop 0\n\tglobal_load_lds_dwordx4 %[ko], %[kv] nt\n\ts_add_u32 m0, %[m], %[n0]+1024\n\ts_nop 0\n\tglobal_load_lds_dwordx4 %[k1], %[kv] nt\n\t"
                     "s_add_u32 m0, %[m], %[n0]+2048\n\ts_nop 0\n\tglobal_load_lds_dwordx4 %[k2], %[kv] nt\n\ts_add_u32 m0, %[m], %[n0]+3072\n\ts_nop 0\n\tglobal_load_lds_dwordx4 %[k3], %[kv] nt\n3:"
                     : "=&v"(sm), "=&v"(kw0), "=&v"(p)
                     : [k] "s"(a.k), [w] "s"(wid), [c1] "s"(a.logdt), [c2] "s"(a.lre), [c3] "s"(a.lim), [d] "s"(a.ssmD), [e] "s"(flg + FL_EPOCH), [o] "v"(so), [sk] "s"(a.sk),
                       [ko] "v"(kvv[0]), [k1] "v"(kvv[1]), [k2] "v"(kvv[2]), [k3] "v"(kvv[3]), [kv] "s"(wn ? a.v : a.k), [m] "s"(g1dst), [n0] "n"(L_KS)
                     : "scc", "vcc", "memory");
    }
    TS0b();
    TS(27);
    if (P1) BAR();
#define TAKE_TAG(w) const unsigned tagA = __builtin_amdgcn_readlane(w, 0) + 1, tagB = __builtin_amdgcn_readlane(w, 1) + (tagA == 0 ? 1u : 0u); \
    const u32x2 tag = {tagA, tagB}
#define SLICE_TABLE() unsigned slo, shi; { int ix = idx; asm volatile("" : "+s"(ix)); \
        const int rot2 = (((R2 >> 4) & 15) * (ix >> 3) + (R2 & 15) * (xcd >> 1)) & 15, es = (tn - rot2) & 15; \
        const unsigned long long id = 0xFEDCBA9876543210ull, seq = rot2 ? (id >> (4 * rot2)) | (id << (64 - 4 * rot2)) : id; \
        const unsigned long long lo = (1ull << (4 * es)) - 1, tab = ((seq & lo) << 4) | (seq & ~((lo << 4) | 15)) | tn; \
        slo = (unsigned)tab; shi = (unsigned)(tab >> 32); asm volatile("" : "+s"(slo), "+s"(shi)); }
#define SLICE(st) ((((st) < 8 ? slo : shi) >> (4 * ((st) & 7))) & 15)
    typedef __attribute__((ext_vector_type(4))) short v4i16;
    int role = wid;
    asm volatile("" : "+s"(role));

    if (role >= 8) {
        const int chn = xcd * 128 + idx * 4 + wm;
        const unsigned cb = lb + wm * CHSZ;
        const int r = l15;
        const unsigned fpub = xcd * 256 + idx * 8, fpoll = xcd * 256 + l31 * 8;
        u32x2 fl; u32x4 sw[4], uu[4];
        SLICE_TABLE();
        TS(1);
        WAIT_V(0);
        asm volatile("" :: "v"(cpf));
        const unsigned hal = take(sm);
        TAKE_TAG(hal);
        const unsigned dskl = (__builtin_amdgcn_readlane(hal, 2) >> ((wm & 1) * 16)) & 0xffffu;
        u32x4 kvo[4];
        {
            unsigned kw[2][8], ta[2], th[2];
            u32x2 hm[2];
#pragma unroll
            for (int j = 0; j < 2; ++j) {
                const unsigned x = take(kw0[j]);
                const auto y = __builtin_amdgcn_permlane16_swap(x, x, false, false);
#pragma unroll
                for (int p = 0; p < 2; ++p) { kw[p][4 * j] = y[p] & 0xffffu; kw[p][4 * j + 1] = y[p] << 16; kw[p][4 * j + 2] = y[p] >> 16; kw[p][4 * j + 3] = y[p] & 0xffff0000u; }
            }
#pragma unroll
            for (int p = 0; p < 2; ++p) {
                unsigned h[3];
#pragma unroll
                for (int j = 0; j < 3; ++j) h[j] = (__shfl(hal, p * 8 + (r >> 1) + 16 * (j + 1)) >> ((r & 1) * 16)) & 0xffffu;
                hm[p] = idx ? u32x2{h[0] << 16, h[1] | (h[2] << 16)} : u32x2{0u, 0u};
                const int q = r >> 2, pp = r & 3;
                ta[p] = lb + L_KS + (8 * g4 + q) * 256 + (((wm * 4 + p * 2 + (pp >> 1)) ^ (2 * (q | ((g4 & 1) << 2)))) << 4) + (pp & 1) * 8;
                th[p] = (ta[p] ^ 128) - 1024;
                TIE(hm[p]); TIE(ta[p]); TIE(th[p]);
#pragma unroll
                for (int j = 0; j < 8; ++j) TIE(kw[p][j]);
            }
            int sl = ((lane & 3) * 16 + (lane >> 2)) * 4;
            TIE(sl);
            BAR();
            TS(2);
            u32x2 kt[4][3], vt[4][2];
#pragma unroll
            for (int u = 0; u < 4; ++u) {
                const int p = u >> 1, o = (u & 1) * 8192;
                auto tr = [&](unsigned ad) -> u32x2 { return __builtin_bit_cast(u32x2, __builtin_amdgcn_ds_read_tr16_b64_v4i16(LDSP(v4i16, ad))); };
                kt[u][0] = tr(th[p] + o); kt[u][1] = tr(ta[p] + o); vt[u][0] = tr(ta[p] + o + 16384); kt[u][2] = tr(ta[p] + o + 1024); vt[u][1] = tr(ta[p] + o + 17408);
                SB();
            }
            TS(14);
#pragma unroll
            for (int u = 0; u < 4; ++u) {
                const int p = u >> 1, oh = u & 1;
                const u32x2 k0 = oh == 0 && g4 == 0 ? hm[p] : kt[u][0];
                unsigned o[4];
                kv4(o[3], o[2], k0[0], k0[1], kt[u][1][0], kt[u][1][1], kw[p], vt[u][0][0], vt[u][0][1]);
                if (u > KVU) { TIE(o[3]); BAR(); }
                kv4(o[1], o[0], kt[u][1][0], kt[u][1][1], kt[u][2][0], kt[u][2][1], kw[p], vt[u][1][0], vt[u][1][1]);
                if (u == 0) { TIE(o[0]); TIE(o[2]); WAIT_L(0); }
#pragma unroll
                for (int j = 0; j < 4; ++j) kvo[u][j] = __builtin_amdgcn_ds_bpermute(sl, o[j]);
                SB();
                if (u == 0) TS(15); if (u == 1) TS(16); if (u == 2) TS(17); if (u == 3) TS(18);
                if (u >= KVU) BAR();
            }
#pragma unroll
            for (int u = 0; u < 4; ++u) TIE(kvo[u]);
        }
        TS(3);
        u32x2 qv[8];
        const char* krow = a.ssmk + (size_t)chn * 4096;
        const char* qs = a.q + (size_t)(idx * 64 + wm * 16) * 2048 + xcd * 256;
        const char* usrc = ws + WS_KVT + (size_t)(xcd * 64 + idx * 2 + (wm >> 1)) * KP + (wm & 1) * 4096;
        const char* cs = ((wm & 2) ? a.cim : a.cre) + (size_t)(tn * 64 + (wm & 1) * 2) * 2048;
        unsigned co[4];
#pragma unroll
        for (int i = 0; i < 4; ++i) {
            const int pair = i * 4 + g4, v = r ^ pair, row = pair * 2 + (v >> 3);
            co[i] = ((row & 15) * 4 + (row >> 4)) * 2048 + (v & 7) * 16;
        }
        bool kv_ok = false;
#pragma unroll
        for (int it = 0; it < 4; ++it) asm volatile("" : "=v"(uu[it]));
        auto items = [&](auto pc) {
            constexpr int p = decltype(pc)::value;
            if constexpr (PK == p) { GLD_B128_NT(sw[0], l16, krow, 0); GLD_B128_NT(sw[1], l16, krow, 1024); GLD_B128_NT(sw[2], l16, krow, 2048); GLD_B128_NT(sw[3], l16, krow, 3072); }
            if constexpr (PU == p) {
                WAIT_V(PK == p ? 4 : 0); TIE(fl);
                kv_ok = __all(fl[0] == tagA && fl[1] == tagB);
                const char* us = kv_ok ? usrc : krow;
                GLD_B128_SC1(uu[0], l16, us, 0); GLD_B128_SC1(uu[1], l16, us, 1024); GLD_B128_SC1(uu[2], l16, us, 2048); GLD_B128_SC1(uu[3], l16, us, 3072);
            }
            if constexpr (PC0 == p) { for (int i = 0; i < 4; ++i) DMA(lb + L_C + 2 * G2STAGE + wm * 4096 + i * 1024, co[i], cs + (SLICE(0) << 7)); }
            if constexpr (PC1 == p) { for (int i = 0; i < 4; ++i) DMA(lb + L_C + 3 * G2STAGE + wm * 4096 + i * 1024, co[i], cs + (SLICE(1) << 7)); }
            if constexpr (PQ == p) static_for<0, 8>([&](auto jc) { constexpr int j = decltype(jc)::value; GLD_B64_NT(qv[j], lh * 2048 + l31 * 8, qs + j * 4096, 0); });
            if constexpr (PU == p + 1) GLD_B64_SC1(fl, fpoll, flg + FL_KV, 0);
        };
#define ITEMS(p) items(std::integral_constant<int, p>{})
        {
            const int ph = STG_SEL, ps = SP0 + ph * SDP, pl = LP0 + ph * LDP;
            const char* kvd = ws + WS_KVT + (size_t)(xcd * 64 + wm * 16) * KP + idx * 256;
            int nb1 = 1 + 2 * (3 - KVU);
#pragma unroll 1
            for (; nb1 <= ps; ++nb1) BAR();
            static_for<0, 4>([&](auto uc) {
                constexpr int u = decltype(uc)::value;
                GST_B128_SC1((lane >> 3) * KP + (lane & 7) * 16, kvo[u], kvd + (u >> 1) * 8 * KP, (u & 1) * 128);
            });
#pragma unroll 1
            for (; nb1 <= pl; ++nb1) {
                if (nb1 == ps + 4) WAIT_V(0);
                BAR();
                if (nb1 == ps + 4 && wm == 0 && lane == 0) GST_B64_SC1(fpub, tag, flg + FL_KV);
            }
            ITEMS(0);
#pragma unroll 1
            for (; nb1 <= 11; ++nb1) {
                if (nb1 == ps + 4) WAIT_V(it_cnt(0, -1, 0));
                BAR();
                if (nb1 == ps + 4 && wm == 0 && lane == 0) GST_B64_SC1(fpub, tag, flg + FL_KV);
            }
        }
        ITEMS(11);
        BAR();
        ITEMS(12);
        BAR();
        ITEMS(13);
        BAR();
        ITEMS(14);
        BAR();
        ITEMS(15);
        TS(4);
        const unsigned ud = lb + ((wm & 2) + ((lane >> 2) & 1)) * CHSZ + UOFF + (lane & 1) * UPL + (16 + (wm & 1) * 64 + g4 * 4 + ((lane >> 3) & 1) * 2 + ((lane >> 1) & 1)) * 16;
        {
            const u32x4 z4 = {0, 0, 0, 0};
            if (lane < 16) *LDSP(unsigned, lb + L_SY + lane * 4) = 0;
            if (lane < 56) *LDSP(u32x4, lb + L_AGG + wm * 896 + l16) = z4;
            *LDSP(u32x4, lane < 32 ? cb + (lane >> 3) * CST + (lane & 7) * 16 : cb + UOFF + (g4 & 1) * UPL + r * 16) = z4;
        }
        WAIT_L(0);
        TS(5);
        BAR();
        __builtin_amdgcn_s_setprio(FPRIO);
        ITEMS(16);
        TS(11);
        WAIT_V(it_cnt(PK, 0, 16));
        TS(12);
        if (!(ABL & 32)) {
#pragma unroll
            for (int it = 0; it < 4; ++it) TIE(sw[it]);
#pragma unroll
            for (int it = 0; it < 4; ++it) {
                const unsigned q = cb + 128 + it * 1024 + l16;
                const unsigned d0 = sw[it][0], d1 = sw[it][1], d2 = sw[it][2], d3 = sw[it][3];
                const unsigned o0 = it ? __builtin_amdgcn_readlane(sw[it ? it - 1 : 0][2], 63) : 0u, o1 = it ? __builtin_amdgcn_readlane(sw[it ? it - 1 : 0][3], 63) : 0u;
                const unsigned x0 = __builtin_amdgcn_update_dpp(o0, d2, 0x138, 0xf, 0xf, false), x1 = __builtin_amdgcn_update_dpp(o1, d3, 0x138, 0xf, 0xf, false);
                const unsigned a0 = __builtin_amdgcn_alignbyte(d0, x1, 2), a1 = __builtin_amdgcn_alignbyte(d1, d0, 2), a2 = __builtin_amdgcn_alignbyte(d2, d1, 2),
                               a3 = __builtin_amdgcn_alignbyte(d3, d2, 2);
                *LDSP(u32x4, q) = sw[it];
                *LDSP(u32x4, q + CST) = u32x4{a0, a1, a2, a3};
                *LDSP(u32x4, q + 2 * CST) = u32x4{x1, d0, d1, d2};
                *LDSP(u32x4, q + 3 * CST) = u32x4{__builtin_amdgcn_alignbyte(x1, x0, 2), a0, a1, a2};
                if (it == 3 && lane == 63) {
                    *LDSP(u32x2, q + 16 + CST) = u32x2{d3 >> 16, 0u};
                    *LDSP(u32x2, q + 16 + 2 * CST) = u32x2{d3, 0u};
                    *LDSP(u32x2, q + 16 + 3 * CST) = u32x2{a3, d3 >> 16};
                }
            }
        }
        TS(13);
        WAIT_V(it_cnt(PU, 1, 16));
#pragma unroll
        for (int it = 0; it < 4; ++it) TIE(uu[it]);
        if (!kv_ok) {
            for (unsigned spin = 0; spin < SPIN_CAP; ++spin) {
                GLD_B64_SC1(fl, fpoll, flg + FL_KV, 0);
                WAIT_V(0); TIE(fl);
                if (__all(fl[0] == tagA && fl[1] == tagB)) break;
                __builtin_amdgcn_s_sleep(8);
            }
            GLD_B128_SC1(uu[0], l16, usrc, 0); GLD_B128_SC1(uu[1], l16, usrc, 1024); GLD_B128_SC1(uu[2], l16, usrc, 2048); GLD_B128_SC1(uu[3], l16, usrc, 3072);
            WAIT_V(0);
#pragma unroll
            for (int it = 0; it < 4; ++it) TIE(uu[it]);
        }
#pragma unroll
        for (int it = 0; it < 4; ++it) *LDSP(u32x4, ud + it * 256) = uu[it];
        lb_post(lb + L_SY + 32 + wm * 4, 1); lb_wait(lb + L_SY + 32 + (wm ^ 1) * 4, 1);
        __builtin_amdgcn_s_setprio(CVPRIO);
        TS(6);
        f32x4 ca[8];
        {
            unsigned kb = cb + ((3 - r) & 3) * CST + 2 * (64 + 16 + r - 8 * g4 - 7 + ((3 - r) & 3));
            unsigned ub = cb + UOFF + (g4 & 1) * UPL + (15 + r + (g4 >> 1)) * 16 - 64;
#pragma unroll
            for (int i = 0; i < 8; ++i) ca[i] = f32x4{0, 0, 0, 0};
            s16x4 FA[4][2]; bf16x8 FB[2][8];
#define CV_A(s, o, e) DSR64(FA[(o) & 3][e], kb, 64 * ((s) + ((o) >> 3)) + 512 * cv_m((o) & 7) + 8 * (e))
#define CV_B(s, jb) DSR128(FB[(s) & 1][jb], ub, 64 + 256 * (jb) - 32 * (s))
            static_for<0, 8>([&](auto ic) { constexpr int n = decltype(ic)::value; CV_B(0, n); CV_A(0, n >> 1, n & 1); });
            WAIT_L(0);
            auto body = [&]() {
                static_for<0, 72>([&](auto ic) {
                    constexpr int s = decltype(ic)::value / 36, p = decltype(ic)::value % 36, o = cv_grp(p), m = cv_m(o), jb = p - cv_first(o);
                    if constexpr (jb == 0) { WAIT_L(cv_wait(o)); TIE(FA[o & 3][0]); TIE(FA[o & 3][1]); }
                    if constexpr (p == 0) { for (int i = 0; i < 8; ++i) TIE(FB[s][i]); }
                    const bf16x8 fa = __builtin_shufflevector(FA[o & 3][0], FA[o & 3][1], 0, 1, 2, 3, 4, 5, 6, 7);
                    ca[jb + m] = __builtin_amdgcn_mfma_f32_16x16x32_bf16(fa, FB[s][jb], ca[jb + m], 0, 0, 0);
                    SB();
                    constexpr int rd = cv_read(p);
                    if constexpr (rd >= 32) CV_B(s + 1, rd - 32);
                    else if constexpr (rd >= 0) CV_A(s, rd >> 1, rd & 1);
                    SB();
                });
                kb += 128; ub -= 64;
            };
#pragma unroll 1
            for (int it = 0; it < QIT; ++it) body();
            {
                WAIT_V(0);
                lb_post(lb + L_SY + 32 + wm * 4, 2);
                WAIT_L(0);
                if (PQ == 17) { lb_wait(lb + L_SY + (lane & 7) * 4, 1); ITEMS(17); }
                if (CVLATE) lb_wait(lb + L_SY + (lane & 7) * 4, 2);
            }
#pragma unroll 1
            for (int it = QIT; it < 4; ++it) body();
            WAIT_L(0);
#pragma unroll
            for (int i = 0; i < 4; ++i) { TIE(FA[i][0]); TIE(FA[i][1]); }
#pragma unroll
            for (int i = 0; i < 8; ++i) TIE(FB[0][i]);
#undef CV_A
#undef CV_B
        }
        TS(7);
        __builtin_amdgcn_s_setprio(3);
        int nb = 1;
#define HB() do { if (nb < 15) BAR(); ++nb; } while (0)
        {
            const float Dd = bf2f(dskl);
            const char* zd = ws + WS_ZT + (size_t)chn * ZP;
            u32x2 kvr[8];
#pragma unroll
            for (int jt = 0; jt < 8; ++jt) kvr[jt] = *LDSP(u32x2, cb + UOFF + (g4 >> 1) * UPL + (16 + 16 * jt + r) * 16 + (1 - (g4 & 1)) * 8);
#pragma unroll
            for (int jt = 0; jt < 8; ++jt) {
                const f32x4 y = ca[jt];
                const u32x2 kq = kvr[jt];
                *LDSP(u32x2, cb + (jt * 4 + (r >> 2)) * 144 + (r & 3) * 32 + g4 * 8) =
                    u32x2{pack2(rb1(y[0]) + rb1(bfhi(kq[1]) * Dd), rb1(y[1]) + rb1(bflo(kq[1]) * Dd)), pack2(rb1(y[2]) + rb1(bfhi(kq[0]) * Dd), rb1(y[3]) + rb1(bflo(kq[0]) * Dd))};
            }
            if (ZB >= 3) HB();
            u32x4 t[4];
#pragma unroll
            for (int it = 0; it < 4; ++it) t[it] = *LDSP(u32x4, cb + (it * 8 + (lane >> 3)) * 144 + (lane & 7) * 16);
            if (!(ABL & 128)) { GST_B128_SC1(l16, t[0], zd, 0); GST_B128_SC1(l16, t[1], zd, 1024); GST_B128_SC1(l16, t[2], zd, 2048); GST_B128_SC1(l16, t[3], zd, 3072); }
            else { TIE(t[0]); TIE(t[1]); TIE(t[2]); TIE(t[3]); }
        }
        if (ZB >= 2) HB();
        if (ZB >= 1) HB();
        WAIT_V(0);
        lb_post(lb + L_SY + 32 + wm * 4, 3);
        if (wm == 0) {
            lb_wait(lb + L_SY + 32 + (lane & 3) * 4, 3);
            if (!(ABL & 1024) && lane == 0) GST_B64_SC1(fpub, tag, flg + FL_Z);
        }
        TS(8);
        HB(); HB(); HB();
        if (!(ABL & 1024) && wm == 0) GLD_B64_SC1(fl, fpoll, flg + FL_Z, 0);
        HB(); HB();
        if (wm == 0) { WAIT_V(0); TIE(fl); lb_post(lb + L_SY + 48, __all(fl[0] == tagA && fl[1] == tagB) ? 2 : 1); }
        lb_wait(lb + L_SY + 48, 1);
        if (__builtin_amdgcn_readfirstlane(*LDSP(unsigned, lb + L_SY + 48)) != 2) {
            while (nb < 15) { BAR(); ++nb; }
            for (unsigned spin = 0; spin < SPIN_CAP; ++spin) {
                GLD_B64_SC1(fl, fpoll, flg + FL_Z, 0);
                WAIT_V(0); TIE(fl);
                if (__all(fl[0] == tagA && fl[1] == tagB)) break;
                __builtin_amdgcn_s_sleep(8);
            }
        }
        u32x4 zv[4];
        {
            ITEMS(18);
            const char* zsrc = ws + WS_ZT + (size_t)(xcd * 128 + wm * 32) * ZP + idx * 128;
#pragma unroll
            for (int it = 0; it < 4; ++it) if (!(ABL & 256)) GLD_B128_SC1(zv[it], (lane >> 3) * ZP + (lane & 7) * 16, zsrc + it * 8 * ZP, 0);
        }
        HB(); HB();
        WAIT_V(0);
        TS(9);
        {
            const unsigned zi = lb + L_ZI + wm * 5120;
#pragma unroll
            for (int it = 0; it < 4; ++it) { TIE(zv[it]); *LDSP(u32x4, zi + ((lane >> 3) + 8 * it) * 160 + (lane & 7) * 16) = zv[it]; }
            v4i16 z[8];
#pragma unroll
            for (int n = 0; n < 8; ++n) z[n] = __builtin_amdgcn_ds_read_tr16_b64_v4i16(LDSP(v4i16, zi + ((n & 1) * 16 + (lane >> 2)) * 160 + (n >> 1) * 32 + (lane & 3) * 8));
            HB();
#pragma unroll
            for (int n = 0; n < 8; ++n) *LDSP(v4i16, lb + L_Z2 + ((n >> 1) * 16 + r) * 264 + wm * 64 + (n & 1) * 32 + g4 * 8) = z[n];
            lb_post(lb + L_SY + 32 + wm * 4, 4);
        }
        HB();
        lb_wait(lb + L_SY + 32 + (lane & 3) * 4, 4);
        char* const oa = a.out + (size_t)(idx * 64 + wm * 16) * 8192 + xcd * 512;
        u32x2 zz[8];
#pragma unroll
        for (int n = 0; n < 8; ++n) zz[n] = *LDSP(u32x2, lb + L_Z2 + (wm * 16 + n * 2 + lh) * 264 + l31 * 8);
#pragma unroll
        for (int n = 0; n < 8; ++n) {
            TIE(qv[n]);
            const u32x2 z = zz[n];
            const f32x4 o = {rb1(bflo(qv[n][0]) * bflo(z[0])), rb1(bfhi(qv[n][0]) * bfhi(z[0])), rb1(bflo(qv[n][1]) * bflo(z[1])), rb1(bfhi(qv[n][1]) * bfhi(z[1]))};
            if (!(ABL & 512)) GST_B128_NT(lh * 8192 + l31 * 16, o, oa + n * 16384, 0); else { f32x4 o2 = o; TIE(o2); }
            if (n < 7 && (n + 1) % OS == 0) HB();
        }
#undef HB
        asm volatile(".Lfk_code_a:");
        TSE(10);
        TS_FLUSH();
        return;
    }

    u32x2 fl;
    unsigned dv[4];
    {
        const int v = l15 ^ g4, hi = v >> 3;
        const int r0 = wn ? tn * 64 + (wm >> 1) * 32 : tm * 128 + (wm >> 1) * 64 + (wm & 1) * 4;
        const unsigned d0 = (r0 + ((g4 * (wn ? 2 : 9)) & (wn ? 6 : 17))) * 2048 + (v & 7) * 16, odd = wn ? 11 : 14;
        dv[0] = d0 + (hi << odd); dv[2] = d0 + ((hi ^ 1) << odd) + (wn ? 32768 : 4096);
        dv[1] = (dv[0] ^ 64) + (wn ? 16384 : 65536); dv[3] = (dv[2] ^ 64) + (wn ? 16384 : 65536);
    }
    const int rot7 = ((((ROT >> 12) & 15) * (idx & 7) + ((ROT >> 8) & 15) * (idx >> 3) + ((ROT >> 4) & 15) * (xcd >> 1) + (ROT & 15) * (xcd & 1)) & 15) << 7;
    const char* g1src = wn ? ((wm & 1) ? a.bim : a.bre) : a.x;
#define G1_ISSUE(st, i) DMA(g1dst + g1_slot(st) + (i) * 1024, dv[i], g1src + ((((st) << 7) + rot7) & 1920))
    static_for<0, PRE>([&](auto ic) { constexpr int n = decltype(ic)::value; G1_ISSUE(n >> 2, n & 3); });
    TS(14);
    float Lr, Li, cr, ci, L8r, L8i, L16r, L16i, L64r, L64i, Qr, Qi, Wr, Wi, Vr, Vi, Ur, Ui;
    f32x2 L1, C1, L16, Q2r, Q2i;
    unsigned cw;
    {
        WAIT_V((ABL & 1) ? PRE : 4 + PRE);
        cw = take(sm);
        const unsigned c_dt = (__shfl(cw, 16 + (l15 >> 1)) >> ((l15 & 1) * 16)) & 0xffffu, c_re = (__shfl(cw, 32 + (l15 >> 1)) >> ((l15 & 1) * 16)) & 0xffffu,
                       c_im = (__shfl(cw, 48 + (l15 >> 1)) >> ((l15 & 1) * 16)) & 0xffffu;
        const float dt = rbf(expf(bf2f(c_dt)));
        const float lr = bf2f(c_re), li = bf2f(c_im);
        const float e = expf(dt * lr);
        float sn, cs;
        sincosf(dt * li, &sn, &cs);
        Lr = e * cs; Li = e * sn;
        const float nr = Lr - 1.f, ni = Li, den = lr * lr + li * li;
        cr = (nr * lr + ni * li) / den; ci = (ni * lr - nr * li) / den;
        float pr = Lr, pi = Li, tr, ti;
        Qr = cr; Qi = ci; Wr = Vr = 1.f; Wi = Vi = 0.f;
#pragma unroll
        for (int it = 1; it <= 10; ++it) {
            cmul(pr, pi, pr, pi, tr, ti); pr = tr; pi = ti;
            if (it == 3) { L8r = pr; L8i = pi; }
            if (it == 4) { L16r = pr; L16i = pi; }
            if (it == 6) { L64r = pr; L64i = pi; }
            if (it == 7) { Ur = pr; Ui = pi; }
            if (it == 4 || it == 5) { cmul(Vr, Vi, pr, pi, tr, ti); if (!((run >> (it - 4)) & 1)) { Vr = tr; Vi = ti; } }
            if (it >= 4 && it <= 6) { cmul(Qr, Qi, pr, pi, tr, ti); if ((run >> (it - 4)) & 1) { Qr = tr; Qi = ti; } }
            if (it >= 8) { cmul(Wr, Wi, pr, pi, tr, ti); if (!((run >> (it - 8)) & 1)) { Wr = tr; Wi = ti; } }
        }
        cmul(Ur, Ui, Wr, Wi, tr, ti);
        Ur = 2 * run >= 16 - tm ? tr : 0.f; Ui = 2 * run >= 16 - tm ? ti : 0.f;
        if (2 * run + 1 < 16 - tm) Wr = Wi = 0.f;
        cmul(Qr, Qi, L8r, L8i, tr, ti);
        L1 = f32x2{Lr, Li}; C1 = f32x2{cr, ci}; L16 = f32x2{L16r, L16i}; Q2r = f32x2{Qr, tr}; Q2i = f32x2{Qi, ti};
        TIE(Ur); TIE(Ui); TIE(L1); TIE(C1); TIE(L16); TIE(Q2r); TIE(Q2i);
        TIE(L8r); TIE(L8i); TIE(L64r); TIE(L64i); TIE(Vr); TIE(Vi); TIE(Wr); TIE(Wi);
    }
    TAKE_TAG(cw);
    SLICE_TABLE();
    const unsigned tagG = tagA ^ tagB ^ 0x9E3779B9u;
    const int j0 = 4 * run - (32 - 2 * tm);
    const int pneg = 16 * (wm >> 1) + 15 - (32 - 2 * tm);
    unsigned eo[2], pes = lb + L_ES + wid * 256 + l15 * 16;
    unsigned plr = lb + L_AGG + st * 8 + run * 512, est = ((tm * 2 + (wm >> 1)) * 1024 + tn * 64 + st) * 16;
    float nL8i = -L8i, nVi = -Vi;
#pragma unroll
    for (int i = 0; i < 2; ++i) { eo[i] = (j0 + 2 * i < 0 ? 0 : j0 + 2 * i) * 16384 + (tn * 64 + st) * 16; TIE(eo[i]); }
    TIE(plr); TIE(pes); TIE(est); TIE(nL8i); TIE(nVi);
    unsigned hw[2], ta[2];
    {
        const unsigned ht = lb + L_HT + wid * 4096;
#pragma unroll
        for (int j = 0; j < 2; ++j) {
            const int pr = (8 * (g4 & 1) + 4 * j + (l15 >> 2)) ^ ((g4 & 1) * 4), pw = l15 ^ ((l15 >> 3) * 4);
            ta[j] = ht + (g4 >> 1) * 512 + pr * 32 + ((((l15 >> 1) ^ (pr >> 2)) & 1) << 4) + 8 * (lane & 1);
            hw[j] = ht + g4 * 1024 + pw * 32 + (((j ^ (pw >> 2)) & 1) << 4);
            TIE(ta[j]); TIE(hw[j]);
        }
    }
    float nL64i = -L64i, nWi = -Wi, nUi = -Ui;
    unsigned po = pes + ((wm & 2) ? -512 : 512);
    TIE(nL64i); TIE(nWi); TIE(nUi); TIE(po);
    TS(1);
    WAIT_V(PRE);
    BAR();
    static_for<PRE, PRO>([&](auto ic) { constexpr int n = decltype(ic)::value; G1_ISSUE(n >> 2, n & 3); });
    TS(15);

    f32x4 acc[4][2];
    {
#pragma unroll
        for (int i = 0; i < 4; ++i) { acc[i][0] = f32x4{0, 0, 0, 0}; acc[i][1] = f32x4{0, 0, 0, 0}; }
        const int aoff = img_off((wm >> 1) * 64 + l15, g4), boff = img_off(128 + wn * 64 + (wm & 1) * 16 + l15, g4);
        unsigned aad[2][2], bad[2];
#pragma unroll
        for (int ks = 0; ks < 2; ++ks) { aad[ks][0] = lb + (aoff ^ (ks * 64)); aad[ks][1] = lb + (aoff ^ (ks * 64) ^ 128); bad[ks] = lb + (boff ^ (ks * 64)); }
        bf16x8 fa[2][4], fb[2][2];
        WAIT_V(PRO - 4); BAR();
        TS(2);
#define G1_FRAG(g, n) do { constexpr int bo_ = g1_slot((g) >> 1), hi_ = bo_ & ~65535, x_ = (n) < 3 ? (n) >> 1 : (n) - 2; \
        if constexpr ((n) == 0 || (n) == 2) DSR128(fb[(g) & 1][x_], bad[(g) & 1] + hi_, bo_ - hi_ + x_ * 4096); \
        else DSR128(fa[(g) & 1][x_], aad[(g) & 1][x_ & 1] + hi_, bo_ - hi_ + x_ * 2048); } while (0)
        static_for<0, 6>([&](auto ic) { constexpr int n = decltype(ic)::value; G1_FRAG(0, n); });
        static_for<0, 32>([&](auto ic) {
            constexpr int g = decltype(ic)::value, gn = g + 1, n0 = g1_upto(g - 1), nd = g1_upto(g) - n0;
            constexpr bool bar = (gn & 1) == 0 && gn < 32;
            if constexpr (bar) {
                static_assert(g1_cnt(gn >> 1) >= 0, "schedule");
                WAIT_V(g1_cnt(gn >> 1)); WAIT_L(0); BAR();
                if constexpr (((gn >> 1) & 3) == 0 || gn >= 26) TS(gn >= 26 ? 23 + (gn >> 1) - 13 : 16 + (gn >> 3));
#pragma unroll
                for (int i = 0; i < 4; ++i) TIE(fa[g & 1][i]);
                TIE(fb[g & 1][0]); TIE(fb[g & 1][1]);
            }
            static_for<0, 8>([&](auto mc) {
                constexpr int m = decltype(mc)::value, i = m >> 1, j = m & 1;
                if constexpr (!bar && (i == 0 || j == 0)) {
                    WAIT_L(5 - (i == 0 ? j + 1 : i + 2) + (gn < 32 ? (m < 6 ? m : 6) : 0));
                    TIE(fa[g & 1][i]); TIE(fb[g & 1][j]);
                }
                acc[i][j] = __builtin_amdgcn_mfma_f32_16x16x32_bf16(fa[g & 1][i], fb[g & 1][j], acc[i][j], 0, 0, 0);
                SB();
                if constexpr (gn < 32 && m < 6) G1_FRAG(gn, m);
                constexpr int d = nd > 2 ? ((m & 1) ? m >> 1 : -1) : (m == DP0 ? 0 : m == DP1 ? 1 : -1);
                if constexpr (d >= 0 && d < nd) G1_ISSUE((n0 + d) >> 2, (n0 + d) & 3);
                SB();
            });
        });
#undef G1_FRAG
    }
    TS(3);
    __builtin_amdgcn_s_setprio(MPRIO);
    f32x2 ur[8], ui[8], hr, hi;
    const unsigned pmine = lb + L_SY + wid * 4, pall = lb + L_SY + (lane & 7) * 4;
    u32x4 eg[4];
#define E_LOADS "global_load_dwordx4 v[104:107], %[a0], %[b0] sc1\n\tglobal_load_dwordx4 v[108:111], %[a0], %[b1] sc1\n\t" \
                "global_load_dwordx4 v[112:115], %[a1], %[b0] sc1\n\tglobal_load_dwordx4 v[116:119], %[a1], %[b1] sc1\n"
#define E_POLL() asm volatile("s_cmp_lt_i32 %[on], 0\n\ts_cbranch_scc1 1f\n\t" E_LOADS "1:" : "={v[104:107]}"(eg[0]), "={v[108:111]}"(eg[1]), "={v[112:115]}"(eg[2]), "={v[116:119]}"(eg[3]) \
        : [a0] "v"(eo[0]), [a1] "v"(eo[1]), [b0] "s"(ws + WS_E), [b1] "s"(ws + WS_E + 16384), [on] "s"(pneg) : "scc", "memory")
    {
        asm volatile("s_nop 7" : "+v"(acc[0][0]), "+v"(acc[0][1]), "+v"(acc[1][0]), "+v"(acc[1][1]), "+v"(acc[2][0]), "+v"(acc[2][1]), "+v"(acc[3][0]), "+v"(acc[3][1]));
#pragma unroll
        for (int p = 0; p < 8; ++p) { ur[p] = f32x2{acc[p >> 1][0][2 * (p & 1)], acc[p >> 1][0][2 * (p & 1) + 1]}; ui[p] = f32x2{acc[p >> 1][1][2 * (p & 1)], acc[p >> 1][1][2 * (p & 1) + 1]}; }
        f32x2 gr = ur[0], gi = ui[0];
#pragma unroll
        for (int p = 1; p < 8; ++p) { f32x2 nr, ni; pk_cmad(nr, ni, L1, gr, gi, ur[p], ui[p]); gr = nr; gi = ni; }
        float fr, fi;
        cmad(fr, fi, L8r, L8i, nL8i, gr[0], gi[0], gr[1], gi[1]);
        {
            float wr, wi;
            cmul4(wr, wi, Vr, Vi, nVi, fr, fi);
            wr = sum4(wr); wi = sum4(wi);
            const u32x4 gq = {__float_as_uint(wr), tagA, __float_as_uint(wi), tagG};
            if (g4 == 0) GST_B128_SC1(est, gq, ws + WS_E, 0);
        }
        *LDSP(f32x2, plr + 7 * 512) = f32x2{fr, fi};
        *LDSP(unsigned, pes + 8) = 0;
        TS(21);
        if (EP == 2) E_POLL();
        auto coef = [&]() {
#pragma unroll
            for (int p = 0; p < 8; ++p) { f32x2 nr, ni; pk_conj_mul(nr, ni, C1, ur[p], ui[p]); ur[p] = nr; ui[p] = ni; TIE(ur[p]); TIE(ui[p]); }
        };
        if (CFB) coef();
        WAIT_L(0); BAR();
        f32x2 e[7];
#pragma unroll
        for (int j = 0; j < 7; ++j) e[j] = *LDSP(f32x2, plr + j * 512);
        if (EP == 1) E_POLL();
        if (!CFB) coef();
#pragma unroll
        for (int j = 1; j < 7; ++j) cmac2(e[j], L16, e[j - 1]);
        float mr, mi;
        cmad(mr, mi, L8r, L8i, nL8i, e[6][0], e[6][1], gr[0], gi[0]);
        pk_conj_mul(hr, hi, C1, f32x2{e[6][0], mr}, f32x2{e[6][1], mi});
        TIE(hr); TIE(hi);
        TS(4);
    }
    f32x2 G;
    {
        float pr = 0.f, pi = 0.f;
        if (!EP) E_POLL();
        if (pneg >= 0) {
            unsigned t0, t1, t2, left = SPIN_CAP;
            asm volatile("s_branch 2f\n1:\n\ts_sleep %[sl]\n\t" E_LOADS "2:\n\ts_waitcnt vmcnt(0)\n\t"
                         "v_bitop3_b32 %[t0], v105, v109, %[ta] bitop3:0x7e\n\tv_bitop3_b32 %[t1], v113, v117, %[ta] bitop3:0x7e\n\tv_bitop3_b32 %[t2], v107, v111, %[tg] bitop3:0x7e\n\t"
                         "v_or3_b32 %[t0], %[t0], %[t1], %[t2]\n\tv_bitop3_b32 %[t1], v115, v119, %[tg] bitop3:0x7e\n\tv_or_b32 %[t0], %[t0], %[t1]\n\t"
                         "v_cmp_ne_u32 vcc, 0, %[t0]\n\ts_cbranch_vccz 3f\n\ts_sub_u32 %[n], %[n], 1\n\ts_cbranch_scc0 1b\n3:"
                         : "+{v[104:107]}"(eg[0]), "+{v[108:111]}"(eg[1]), "+{v[112:115]}"(eg[2]), "+{v[116:119]}"(eg[3]), [t0] "=&v"(t0), [t1] "=&v"(t1), [t2] "=&v"(t2), [n] "+s"(left)
                         : [a0] "v"(eo[0]), [a1] "v"(eo[1]), [b0] "s"(ws + WS_E), [b1] "s"(ws + WS_E + 16384), [ta] "s"(tagA), [tg] "s"(tagG), [sl] "n"(ESL) : "vcc", "scc", "memory");
            float ar = __uint_as_float(eg[1][0]), ai = __uint_as_float(eg[1][2]), br = __uint_as_float(eg[3][0]), bi = __uint_as_float(eg[3][2]);
            cmac(ar, ai, L64r, L64i, nL64i, __uint_as_float(eg[0][0]), __uint_as_float(eg[0][2]));
            cmac(br, bi, L64r, L64i, nL64i, __uint_as_float(eg[2][0]), __uint_as_float(eg[2][2]));
            cmul4(pr, pi, Ur, Ui, nUi, ar, ai);
            cmac(pr, pi, Wr, Wi, nWi, br, bi);
            pr = sum4(pr); pi = sum4(pi);
        }
        TS(5);
        *LDSP(u32x4, pes) = u32x4{__float_as_uint(pr), __float_as_uint(pi), 1u, 0u};
        if (PQ == 17) lb_post(pmine, 1);
        u32x4 o;
        asm volatile("1:\n\tds_read_b128 %0, %1\n\ts_waitcnt lgkmcnt(0)\n\tv_cmp_eq_u32 vcc, 0, v122\n\ts_cbranch_vccnz 1b" : "=&{v[120:123]}"(o) : "v"(po) : "vcc", "memory");
        G = f32x2{pr + __uint_as_float(o[0]), pi + __uint_as_float(o[1])};
    }
    TS(6);
    {
        pk_conj_mac(hr, hi, G, Q2r, Q2i);
#pragma unroll
        for (int p = 0; p < 8; ++p) { pk_cmac_conj(ur[p], ui[p], L1, hr, hi); hr = ur[p]; hi = ui[p]; }
        const char* hd = ws + WS_H + (size_t)((tm * 4 + (wm & 2)) * 2 + wn) * HP + (tn * 4 + (wm & 1) * 2) * 1024;
#pragma unroll
        for (int e2 = 0; e2 < 2; ++e2)
#pragma unroll
            for (int c = 0; c < 2; ++c) {
                const f32x2* o = c ? ui : ur;
                *LDSP(u32x4, hw[e2] + c * 512) = u32x4{pack2(o[0][e2], o[1][e2]), pack2(o[2][e2], o[3][e2]), pack2(o[4][e2], o[5][e2]), pack2(o[6][e2], o[7][e2])};
            }
        v4i16 t[4][2];
#pragma unroll
        for (int n = 0; n < 4; ++n)
#pragma unroll
            for (int j = 0; j < 2; ++j) t[n][j] = __builtin_amdgcn_ds_read_tr16_b64_v4i16(LDSP(v4i16, ta[j] + n * 1024));
        lb_post(pmine, 2);
        static_for<0, 4>([&](auto ic) {
            constexpr int n = decltype(ic)::value;
            const bf16x8 f = __builtin_shufflevector(t[n][0], t[n][1], 0, 1, 2, 3, 4, 5, 6, 7);
            GST_B128_SC1(l16, f, hd + (n >> 1) * 2 * HP + ((n & 1) ^ wn) * 1024, 0);
        });
    }
    TS(7);
    bf16x8 fa2[AW];
    u32x2 xv[4], dsv;
    const char* Aw = ws + WS_H + (size_t)((tm * 4 + wm) * 2 + wn) * HP;
    const char* g2src = (wn ? a.cim : a.cre) + (size_t)(tn * 64 + wm) * 2048;
    const unsigned g2dst = lb + L_C + wid * 2048;
    unsigned g2v[2];
#pragma unroll
    for (int i = 0; i < 2; ++i) {
        const int pair = i * 4 + (lane >> 4), v = (lane & 15) ^ pair ^ ((wm & 1) * 8);
        g2v[i] = (pair * 2 + (v >> 3)) * 8192 + (v & 7) * 16;
    }
    unsigned cad[2][2];
#pragma unroll
    for (int ks = 0; ks < 2; ++ks)
#pragma unroll
        for (int i = 0; i < 2; ++i) cad[ks][i] = lb + L_C + (img_off((g4 >> 1) * 64 + l15, 4 * wn + (g4 & 1)) ^ (ks * 32) ^ (i * 128));
#define G2_ISSUE(st, i) DMA(g2dst + (((st) + 2) & 3) * G2STAGE + (i) * 1024, g2v[i], g2src + (SLICE(st) << 7))
#define G2_A(g) GLD_B128_SC1(fa2[(g) % AW], l16, Aw + (SLICE((g) >> 2) << 12), ((g) & 3) * 1024)
    {
        const char* xs = a.x + (size_t)(tm * 128 + wm * 32 + wn * 16) * 2048 + tn * 128;
        static_for<0, 4>([&](auto ic) { constexpr int it = decltype(ic)::value; GLD_B64(xv[it], g4 * 8192 + l15 * 8, xs + (it >> 1) * 4096, (it & 1) * 2048); });
        GLD_B64(dsv, l15 * 8, a.dskip + tn * 128, 0);
        {
            lb_wait(lb + L_SY + (wid ^ 1) * 4, 2);
            bf16x8 fo[4];
#pragma unroll
            for (int n = 0; n < 4; ++n) {
                const int o = (n >> 1) * 4096 + ((n & 1) ^ wn) * 1024 - (wm & 1) * 2048;
                fo[n] = __builtin_shufflevector(__builtin_amdgcn_ds_read_tr16_b64_v4i16(LDSP(v4i16, ta[0] + o)), __builtin_amdgcn_ds_read_tr16_b64_v4i16(LDSP(v4i16, ta[1] + o)), 0, 1, 2, 3, 4, 5, 6, 7);
            }
            lb_wait(lb + L_SY + 32 + (lane & 3) * 4, 2);
#pragma unroll
            for (int ks = 0; ks < 2; ++ks)
#pragma unroll
                for (int i = 0; i < 4; ++i) {
                    const bf16x8 c = *LDSP(bf16x8, cad[ks][i & 1] + 2 * G2STAGE + i * 2048);
#pragma unroll
                    for (int j = 0; j < 2; ++j) acc[i][j] = __builtin_amdgcn_mfma_f32_16x16x32_bf16(fo[2 * ks + j], c, ks ? acc[i][j] : f32x4{0, 0, 0, 0}, 0, 0, 0);
                }
#pragma unroll
            for (int i = 0; i < 4; ++i) { TIE(acc[i][0]); TIE(acc[i][1]); }
        }
        TS(16);
        WAIT_V(5);
        lb_post(pmine, 3); lb_wait(pall, 3);
        if (wid == 0 && lane < 16) GST_B64_SC1((tm * 16 + lane) * 128 + tn * 8, tag, flg + FL_H);
        TS(8);
        static_for<4, 10>([&](auto ic) { constexpr int n = decltype(ic)::value; G2_ISSUE(n >> 1, n & 1); });
        TS(13);
        if (wid == 0) {
#pragma unroll 1
            for (unsigned spin = 0; spin < SPIN_CAP; ++spin) {
                GLD_B64_SC1(fl, (tm * 16 + tn) * 128 + (lane & 15) * 8, flg + FL_H, 0);
                WAIT_V(0); TIE(fl);
                if (spin == 0) TS(22);
                if (__all(fl[0] == tagA && fl[1] == tagB)) break;
            }
            lb_post(pmine, 4);
            if (b == 255 && lane == 0) GST_B64_SC1(0u * lane, tag, flg + FL_EPOCH);
        } else lb_wait(lb + L_SY, 4);
        TS(20);
        static_for<4, 4 + AW>([&](auto ic) { constexpr int g = decltype(ic)::value; G2_A(g); });
    }
    TS(9);
    __builtin_amdgcn_s_setprio(0);
    f32x4 y[4];
    {
        bf16x8 fb[2][4];
#define G2_FRAG(g, i) DSR128(fb[(g) & 1][i], cad[(g) & 1][(i) & 1], ((((g) >> 1) + 2) & 3) * G2STAGE + (i) * 2048)
        static_for<0, 4>([&](auto ic) { constexpr int i = decltype(ic)::value; G2_FRAG(2, i); });
        static_for<2, 32>([&](auto ic) {
            constexpr int g = decltype(ic)::value, gn = g + 1;
            constexpr bool bar = (gn & 1) == 0 && gn < 32;
            if constexpr (bar) {
                constexpr int ca = g2_cnt(2 * g, 2 * g), cs = gn >= 10 ? g2_cnt(100 + gn - 6, 2 * g) : 63;
                static_assert(cs >= 0, "schedule");
                WAIT_V(AWB && ca < cs ? ca : cs); WAIT_L(0); BAR();
                if constexpr (!AWB) WAIT_V(ca);
                if constexpr (g == 3) TS(10);
                if constexpr (g == 17 || g == 25) TS(26 + (g >> 3));
#pragma unroll
                for (int i = 0; i < 4; ++i) TIE(fb[g & 1][i]);
                TIE(fa2[(2 * g) % AW]);
            }
            static_for<0, 8>([&](auto mc) {
                constexpr int m = decltype(mc)::value, i = m & 3, j = m >> 2, f = 2 * g + j;
                static_assert(g2_cnt(f, f) >= 0, "schedule");
                if constexpr (i == 0 && (j || !bar)) { WAIT_V(g2_cnt(f, f)); TIE(fa2[f % AW]); }
                if constexpr (!bar && j == 0) { WAIT_L(gn < 32 ? 3 : 3 - i); TIE(fb[g & 1][i]); }
                acc[i][j] = __builtin_amdgcn_mfma_f32_16x16x32_bf16(fa2[f % AW], fb[g & 1][i], acc[i][j], 0, 0, 0);
                SB();
                if constexpr (gn < 32 && m < 4) G2_FRAG(gn, m);
                if constexpr (m == 5 && g2_dma(g)) G2_ISSUE(gn / 2 + 3, gn & 1);
                if constexpr (i == 3 && f + AW < 64) G2_A(f + AW);
                if constexpr (g >= YS && g < YS + 4 && m == 6) {
                    constexpr int it = g - YS;
                    TIE(xv[it]); TIE(dsv);
                    y[it] = f32x4{rb1(bflo(xv[it][0]) * bflo(dsv[0])), rb1(bfhi(xv[it][0]) * bfhi(dsv[0])), rb1(bflo(xv[it][1]) * bflo(dsv[1])), rb1(bfhi(xv[it][1]) * bfhi(dsv[1]))};
                    TIE(y[it]);
                }
                SB();
            });
        });
#undef G2_FRAG
    }
    TS(11);
    {
#pragma unroll
        for (int i = 0; i < 4; ++i) *LDSP(f32x4, lb + L_Y2 + wid * 4096 + l16 + i * 1024) = acc[i][1];
        lb_post(pmine, 5);
#pragma unroll
        for (int r = 0; r < 4; ++r) {
            y[r] = f32x4{2.f * acc[0][0][r] + y[r][0], 2.f * acc[1][0][r] + y[r][1], 2.f * acc[2][0][r] + y[r][2], 2.f * acc[3][0][r] + y[r][3]};
            TIE(y[r]);
        }
        lb_wait(lb + L_SY + (wid ^ 4) * 4, 5);
        char* const ob = a.out + (size_t)(tm * 128 + wm * 32 + wn * 16) * 8192 + 4096 + tn * 256;
        f32x4 w[4];
#pragma unroll
        for (int i = 0; i < 4; ++i) w[i] = *LDSP(f32x4, lb + L_Y2 + (wid ^ 4) * 4096 + l16 + i * 1024);
#pragma unroll
        for (int r = 0; r < 4; ++r) {
            const f32x4 o = {2.f * w[0][r] + y[r][0], 2.f * w[1][r] + y[r][1], 2.f * w[2][r] + y[r][2], 2.f * w[3][r] + y[r][3]};
            GST_B128_NT(g4 * 32768 + l15 * 16, o, ob + r * 8192, 0);
        }
    }
    asm volatile(".Lfk_code_b:");
    TSE(12);
    TS_FLUSH();
}

__global__ __launch_bounds__(256) void gate_k(char* ws) {
    __shared__ unsigned sh[8];
    const unsigned now = (unsigned)__builtin_amdgcn_s_memrealtime();
    if (threadIdx.x < 8) sh[threadIdx.x] = 0;
    __syncthreads();
    const unsigned long long* ts = (const unsigned long long*)(ws + ((size_t)200 << 20)) + (size_t)threadIdx.x * 3 * 64;
    atomicMax(&sh[0], now - (unsigned)ts[0]);
    __syncthreads();
    const unsigned g0 = now - sh[0];
    const unsigned a = (unsigned)ts[SL1 * 64 + ST1 * 2] - g0, b = (unsigned)ts[SL2 * 64 + ST2 * 2] - g0;
    atomicAdd(&sh[1], a); atomicAdd(&sh[2], b); atomicMax(&sh[3], a); atomicMax(&sh[4], b);
    __syncthreads();
    const int va = MX1 ? sh[3] : sh[1] / 256, vb = MX2 ? sh[4] : sh[2] / 256;
    int q1 = (va - BS1 + 5) / 10, q2 = (vb - BS2 + 5) / 10;
    q1 = q1 < 0 ? 0 : q1 > 63 ? 63 : q1; q2 = q2 < 0 ? 0 : q2 > 63 ? 63 : q2;
    unsigned len = 10000 + (q1 * 64 + q2) * 100;
    if (sh[0] > 9000) len = 0;
    while ((unsigned)__builtin_amdgcn_s_memrealtime() - g0 < len) __builtin_amdgcn_s_sleep(1);
}
}

extern "C" void kernel_launch(void* const* d_in, const int* in_sizes, int n_in, void* d_out, int out_size, void* d_ws, size_t ws_size,
                              hipStream_t stream) {
    (void)in_sizes; (void)n_in; (void)out_size; (void)ws_size;
    fk::Args a;
    a.q = (const char*)d_in[0]; a.k = (const char*)d_in[1]; a.v = (const char*)d_in[2]; a.sk = (const char*)d_in[3];
    a.ssmk = (const char*)d_in[4]; a.ssmD = (const char*)d_in[5]; a.x = (const char*)d_in[6]; a.lre = (const char*)d_in[7];
    a.lim = (const char*)d_in[8]; a.bre = (const char*)d_in[9]; a.bim = (const char*)d_in[10]; a.cre = (const char*)d_in[11];
    a.cim = (const char*)d_in[12]; a.logdt = (const char*)d_in[13]; a.dskip = (const char*)d_in[14];
    a.out = (char*)d_out; a.ws = (char*)d_ws;
    hipLaunchKernelGGL(fk::fused, dim3(256), dim3(768), 0, stream, a);
    hipLaunchKernelGGL(fk::gate_k, dim3(1), dim3(256), 0, stream, (char*)d_ws);
}
```

```cpp
#include <hip/hip_runtime.h>
#include <stdint.h>
#include <type_traits>

namespace fk {

constexpr int S = 2048, D = 1024, P = 1024;

typedef __attribute__((ext_vector_type(8))) short bf16x8;
typedef __attribute__((ext_vector_type(4))) short s16x4;
typedef __attribute__((ext_vector_type(16))) float f32x16;
typedef __attribute__((ext_vector_type(4))) float f32x4;
typedef __attribute__((ext_vector_type(2))) float f32x2;
typedef __attribute__((ext_vector_type(4))) unsigned u32x4;
typedef __attribute__((ext_vector_type(2))) unsigned u32x2;
#define LDSP(T, a) ((__attribute__((address_space(3))) T*)(uintptr_t)(unsigned)(a))

__device__ __forceinline__ float bf2f(unsigned v) { return __uint_as_float(v << 16); }
__device__ __forceinline__ float bflo(unsigned w) { return __uint_as_float(w << 16); }
__device__ __forceinline__ float bfhi(unsigned w) { return __uint_as_float(w & 0xffff0000u); }
__device__ __forceinline__ uint16_t f2bf(float f) { __bf16 b = (__bf16)f; return __builtin_bit_cast(uint16_t, b); }
__device__ __forceinline__ float rbf(float f) { return bf2f(f2bf(f)); }
typedef __attribute__((ext_vector_type(2))) __bf16 bf16x2;
__device__ __forceinline__ unsigned pack2(float lo, float hi) { return __builtin_bit_cast(unsigned, __builtin_convertvector(f32x2{lo, hi}, bf16x2)); }
__device__ __forceinline__ float rb1(float x) { return __uint_as_float(pack2(0.f, x)); }
__device__ __forceinline__ void cmac(float& xr, float& xi, float ar, float ai, float hr, float hi) {
    asm("v_fmac_f32 %0, %2, %4\n\tv_fmac_f32 %1, %2, %5\n\tv_fma_f32 %0, -%3, %5, %0\n\tv_fmac_f32 %1, %3, %4" : "+v"(xr), "+v"(xi) : "v"(ar), "v"(ai), "v"(hr), "v"(hi));
}
__device__ __forceinline__ void cmad(float& o_r, float& o_i, float ar, float ai, float hr, float hi, float er, float ei) {
    asm("v_fma_f32 %0, %2, %4, %6\n\tv_fma_f32 %1, %2, %5, %7\n\tv_fma_f32 %0, -%3, %5, %0\n\tv_fmac_f32 %1, %3, %4" : "=&v"(o_r), "=&v"(o_i) : "v"(ar), "v"(ai), "v"(hr), "v"(hi), "v"(er), "v"(ei));
}
__device__ __forceinline__ f32x2 cmul4(f32x2 a, f32x2 h) {
    float o_r, o_i;
    asm("v_mul_f32 %0, %2, %4\n\tv_mul_f32 %1, %2, %5\n\tv_fma_f32 %0, -%3, %5, %0\n\tv_fmac_f32 %1, %3, %4" : "=&v"(o_r), "=&v"(o_i) : "v"(a[0]), "v"(a[1]), "v"(h[0]), "v"(h[1]));
    return f32x2{o_r, o_i};
}

#define PK_LO " op_sel:[0,0,0] op_sel_hi:[0,1,1]"
#define PK_HI " op_sel:[1,0,0] op_sel_hi:[1,1,1]"
#define PK_NEG " neg_lo:[1,0,0] neg_hi:[1,0,0]"
__device__ __forceinline__ void pk_cmad(f32x2& o_r, f32x2& o_i, f32x2 a, f32x2 hr, f32x2 hi, f32x2 er, f32x2 ei) {
    asm("v_pk_fma_f32 %0, %2, %3, %5" PK_LO "\n\tv_pk_fma_f32 %1, %2, %4, %6" PK_LO "\n\tv_pk_fma_f32 %0, %2, %4, %0" PK_HI PK_NEG "\n\tv_pk_fma_f32 %1, %2, %3, %1" PK_HI
        : "=&v"(o_r), "=&v"(o_i) : "v"(a), "v"(hr), "v"(hi), "v"(er), "v"(ei));
}
__device__ __forceinline__ void pk_cmac_conj(f32x2& xr, f32x2& xi, f32x2 a, f32x2 hr, f32x2 hi) {
    asm("v_pk_fma_f32 %0, %2, %3, %0" PK_LO "\n\tv_pk_fma_f32 %1, %2, %4, %1" PK_LO "\n\tv_pk_fma_f32 %0, %2, %4, %0" PK_HI "\n\tv_pk_fma_f32 %1, %2, %3, %1" PK_HI PK_NEG
        : "+v"(xr), "+v"(xi) : "v"(a), "v"(hr), "v"(hi));
}
__device__ __forceinline__ void pk_conj_mul(f32x2& o_r, f32x2& o_i, f32x2 a, f32x2 hr, f32x2 hi) {
    asm("v_pk_mul_f32 %0, %2, %3 op_sel:[0,0] op_sel_hi:[0,1]\n\tv_pk_mul_f32 %1, %2, %4 op_sel:[0,0] op_sel_hi:[0,1] neg_lo:[1,0] neg_hi:[1,0]\n\t"
        "v_pk_fma_f32 %0, %2, %4, %0" PK_HI PK_NEG "\n\tv_pk_fma_f32 %1, %2, %3, %1" PK_HI PK_NEG : "=&v"(o_r), "=&v"(o_i) : "v"(a), "v"(hr), "v"(hi));
}
__device__ __forceinline__ void pk_conj_mac(f32x2& xr, f32x2& xi, f32x2 a, f32x2 hr, f32x2 hi) {
    asm("v_pk_fma_f32 %0, %2, %3, %0" PK_LO "\n\tv_pk_fma_f32 %1, %2, %4, %1" PK_LO PK_NEG "\n\tv_pk_fma_f32 %0, %2, %4, %0" PK_HI PK_NEG "\n\tv_pk_fma_f32 %1, %2, %3, %1" PK_HI PK_NEG
        : "+v"(xr), "+v"(xi) : "v"(a), "v"(hr), "v"(hi));
}
__device__ __forceinline__ void cmac2(f32x2& x, f32x2 a, f32x2 h) {
    asm("v_pk_fma_f32 %0, %1, %2, %0" PK_LO "\n\tv_pk_fma_f32 %0, %1, %2, %0 op_sel:[1,1,0] op_sel_hi:[1,0,1] neg_lo:[1,0,0]" : "+v"(x) : "v"(a), "v"(h));
}
__device__ __forceinline__ float sum4(float x) {
    const auto p = __builtin_amdgcn_permlane32_swap(__float_as_uint(x), __float_as_uint(x), false, false);
    x = __uint_as_float(p[0]) + __uint_as_float(p[1]);
    const auto q = __builtin_amdgcn_permlane16_swap(__float_as_uint(x), __float_as_uint(x), false, false);
    return __uint_as_float(q[0]) + __uint_as_float(q[1]);
}

template <int I, int N, class F>
__device__ __forceinline__ void static_for(F&& f) { if constexpr (I < N) { f(std::integral_constant<int, I>{}); static_for<I + 1, N>(f); } }

#define TS_DECL
#define TS0a()
#define TS0b()
#define TSE(i)
#define TS(i)
#define TS_FLUSH()

#define WAIT_V(n) asm volatile("s_waitcnt vmcnt(%0)" ::"n"((n) > 63 ? 63 : (n)) : "memory")
#define WAIT_L(n) asm volatile("s_waitcnt lgkmcnt(%0)" ::"n"((n) > 15 ? 15 : (n)) : "memory")
#define BAR() asm volatile("s_barrier" ::: "memory")
#define SB() __builtin_amdgcn_sched_barrier(0)
template <class T> __device__ __forceinline__ void tie_(T& x) { asm volatile("" : "+v"(x)); }
#define TIE(x) tie_(x)
__device__ __forceinline__ unsigned take(unsigned x) { unsigned y; asm volatile("v_mov_b32 %0, %1" : "=v"(y) : "v"(x)); return y; }
#define DEF_LD(name, ins) template <int OFF, class T> __device__ __forceinline__ void name(T& dst, unsigned voff, const char* sb) { \
    asm volatile(ins : "=v"(dst) : "v"(voff), "s"(sb), "n"(OFF) : "memory"); }
DEF_LD(gld_u16_, "global_load_ushort %0, %1, %2 offset:%3")
DEF_LD(gld_b32_, "global_load_dword %0, %1, %2 offset:%3")
DEF_LD(gld_b64_, "global_load_dwordx2 %0, %1, %2 offset:%3")
DEF_LD(gld_b128_, "global_load_dwordx4 %0, %1, %2 offset:%3")
#define NTQ " nt"
#define NTK " nt"
DEF_LD(gld_b64_nt_, "global_load_dwordx2 %0, %1, %2 offset:%3" NTQ)
DEF_LD(gld_b128_nt_, "global_load_dwordx4 %0, %1, %2 offset:%3" NTQ)
DEF_LD(gld_b64_sc1_, "global_load_dwordx2 %0, %1, %2 offset:%3 sc1")
DEF_LD(gld_b128_sc1_, "global_load_dwordx4 %0, %1, %2 offset:%3 sc1")
#define DEF_ST(name, ins) template <int OFF, class T> __device__ __forceinline__ void name(unsigned voff, const T& v, const char* sb) { \
    asm volatile(ins :: "v"(voff), "v"(v), "s"(sb), "n"(OFF) : "memory"); }
DEF_ST(gst_b128_, "global_store_dwordx4 %0, %1, %2 offset:%3\n\ts_nop 1")
DEF_ST(gst_b128_nt_, "global_store_dwordx4 %0, %1, %2 offset:%3 nt\n\ts_nop 1")
DEF_ST(gst_b128_sc1_, "global_store_dwordx4 %0, %1, %2 offset:%3 sc1\n\ts_nop 1")
DEF_ST(gst_b64_sc1_, "global_store_dwordx2 %0, %1, %2 offset:%3 sc1")
#define GLD_U16(dst, voff, sb) gld_u16_<0>(dst, voff, sb)
#define GLD_B32(dst, voff, sb, off) gld_b32_<(off)>(dst, voff, sb)
#define GLD_B64(dst, voff, sb, off) gld_b64_<(off)>(dst, voff, sb)
#define GLD_B128(dst, voff, sb, off) gld_b128_<(off)>(dst, voff, sb)
#define GLD_B64_NT(dst, voff, sb, off) gld_b64_nt_<(off)>(dst, voff, sb)
#define GLD_B128_NT(dst, voff, sb, off) gld_b128_nt_<(off)>(dst, voff, sb)
#define GLD_B64_SC1(dst, voff, sb, off) gld_b64_sc1_<(off)>(dst, voff, sb)
#define GLD_B128_SC1(dst, voff, sb, off) gld_b128_sc1_<(off)>(dst, voff, sb)
#define GST_B128(voff, v, sb, off) gst_b128_<(off)>(voff, v, sb)
#define GST_B128_NT(voff, v, sb, off) gst_b128_nt_<(off)>(voff, v, sb)
#define GST_B128_SC1(voff, v, sb, off) gst_b128_sc1_<(off)>(voff, v, sb)
#define GST_B64_SC1(voff, v, sb) gst_b64_sc1_<0>(voff, v, sb)
__device__ __forceinline__ void dma_(unsigned ldsaddr, unsigned voff, const char* sb) {
    asm volatile("s_mov_b32 m0, %0\n\ts_nop 0\n\tglobal_load_lds_dwordx4 %1, %2" :: "s"(ldsaddr), "v"(voff), "s"(sb) : "memory"); }
__device__ __forceinline__ void dma_nt_(unsigned ldsaddr, unsigned voff, const char* sb) {
    asm volatile("s_mov_b32 m0, %0\n\ts_nop 0\n\tglobal_load_lds_dwordx4 %1, %2 nt" :: "s"(ldsaddr), "v"(voff), "s"(sb) : "memory"); }
#define DMA(ldsaddr, voff, sb) dma_(ldsaddr, voff, sb)
template <int OFF, class T> __device__ __forceinline__ void dsr128_(T& dst, unsigned addr) { asm volatile("ds_read_b128 %0, %1 offset:%2" : "=v"(dst) : "v"(addr), "n"(OFF)); }
template <int OFF, class T> __device__ __forceinline__ void dsr64_(T& dst, unsigned addr) { asm volatile("ds_read_b64 %0, %1 offset:%2" : "=v"(dst) : "v"(addr), "n"(OFF)); }
#define DSR128(dst, addr, off) dsr128_<(off)>(dst, addr)
#define DSR64(dst, addr, off) dsr64_<(off)>(dst, addr)

__device__ __forceinline__ void kv4(unsigned& o01, unsigned& o23, unsigned ka, unsigned kb, unsigned kc, unsigned kd, const unsigned* w, unsigned v0, unsigned v1) {
    unsigned t1, t3, p0, p1, p2, p3;
    asm(
        "v_dot2_f32_bf16 %0, %8, %19, 0\n\tv_dot2_f32_bf16 %1, %9, %18, 0\n\tv_dot2_f32_bf16 %2, %9, %19, 0\n\tv_dot2_f32_bf16 %3, %10, %18, 0\n\t"
        "v_dot2_f32_bf16 %4, %9, %16, 0\n\tv_dot2_f32_bf16 %5, %9, %17, 0\n\tv_dot2_f32_bf16 %6, %10, %16, 0\n\tv_dot2_f32_bf16 %7, %10, %17, 0\n\t"
        "v_cvt_pk_bf16_f32 %0, %0, %4\n\tv_cvt_pk_bf16_f32 %1, %1, %5\n\tv_cvt_pk_bf16_f32 %2, %2, %6\n\tv_cvt_pk_bf16_f32 %3, %3, %7\n\t"
        "v_dot2_f32_bf16 %4, %9, %15, 0\n\tv_dot2_f32_bf16 %5, %10, %14, 0\n\tv_dot2_f32_bf16 %6, %10, %15, 0\n\tv_dot2_f32_bf16 %7, %11, %14, 0\n\t"
        "v_dot2_f32_bf16 %0, %0, %22, 0\n\tv_dot2_f32_bf16 %1, %1, %22, 0\n\tv_dot2_f32_bf16 %2, %2, %22, 0\n\tv_dot2_f32_bf16 %3, %3, %22, 0\n\t"
        "v_cvt_pk_bf16_f32 %0, %0, %4\n\tv_cvt_pk_bf16_f32 %1, %1, %5\n\tv_cvt_pk_bf16_f32 %2, %2, %6\n\tv_cvt_pk_bf16_f32 %3, %3, %7\n\t"
        "v_dot2_f32_bf16 %4, %10, %12, 0\n\tv_dot2_f32_bf16 %5, %10, %13, 0\n\tv_dot2_f32_bf16 %6, %11, %12, 0\n\tv_dot2_f32_bf16 %7, %11, %13, 0\n\t"
        "v_dot2_f32_bf16 %0, %0, %22, 0\n\tv_dot2_f32_bf16 %1, %1, %22, 0\n\tv_dot2_f32_bf16 %2, %2, %22, 0\n\tv_dot2_f32_bf16 %3, %3, %22, 0\n\t"
        "v_cvt_pk_bf16_f32 %0, %0, %4\n\tv_cvt_pk_bf16_f32 %1, %1, %5\n\tv_cvt_pk_bf16_f32 %2, %2, %6\n\tv_cvt_pk_bf16_f32 %3, %3, %7\n\t"
        "v_dot2_f32_bf16 %0, %0, %22, 0\n\tv_dot2_f32_bf16 %1, %1, %22, 0\n\tv_dot2_f32_bf16 %2, %2, %22, 0\n\tv_dot2_f32_bf16 %3, %3, %22, 0\n\t"
        "v_cvt_pk_bf16_f32 %0, %0, 0\n\tv_cvt_pk_bf16_f32 %1, 0, %1\n\tv_cvt_pk_bf16_f32 %2, %2, 0\n\tv_cvt_pk_bf16_f32 %3, 0, %3\n\t"
        "v_dot2_f32_bf16 %0, %0, %20, 0\n\tv_dot2_f32_bf16 %1, %1, %20, 0\n\tv_dot2_f32_bf16 %2, %2, %21, 0\n\tv_dot2_f32_bf16 %3, %3, %21, 0\n\t"
        "s_nop 1\n\tv_cvt_pk_bf16_f32 %0, %1, %0\n\tv_cvt_pk_bf16_f32 %2, %3, %2\n\t"
        : "=&v"(o01), "=&v"(t1), "=&v"(o23), "=&v"(t3), "=&v"(p0), "=&v"(p1), "=&v"(p2), "=&v"(p3)
        : "v"(ka), "v"(kb), "v"(kc), "v"(kd), "v"(w[0]), "v"(w[1]), "v"(w[2]), "v"(w[3]), "v"(w[4]), "v"(w[5]), "v"(w[6]), "v"(w[7]), "v"(v0), "v"(v1), "s"(0x3f803f80u));
}
template <int E> __device__ __forceinline__ void sy_post(unsigned myaddr) { asm volatile("ds_write_b32 %0, %0 offset:%1" :: "v"(myaddr), "n"(E * 64) : "memory"); }
template <int E, int SL = 1> __device__ __forceinline__ unsigned sy_wait(unsigned addr) {
    unsigned v;
    asm volatile("s_branch 2f\n1:\n\ts_sleep %3\n2:\n\tds_read_b32 %0, %1 offset:%2\n\ts_waitcnt lgkmcnt(0)\n\tv_cmp_eq_u32 vcc, 0, %0\n\ts_cbranch_vccnz 1b"
                 : "=&v"(v) : "v"(addr), "n"(E * 64), "n"(SL) : "vcc", "memory");
    return v;
}
template <int OFF, class T> __device__ __forceinline__ void dsrtr_(T& dst, unsigned addr) { asm volatile("ds_read_b64_tr_b16 %0, %1 offset:%2" : "=v"(dst) : "v"(addr), "n"(OFF)); }

constexpr size_t WS_H = 0, WS_KVT = (size_t)10 << 20, WS_ZT = (size_t)15 << 20, WS_E = (size_t)20 << 20, WS_FLAG = WS_E + (2048 << 10);
#define HP 65536
#define KP 8192
#define ZP 4096
constexpr int FL_KV = 0, FL_Z = 65536, FL_H = 131072, FL_EPOCH = 196608;
constexpr unsigned SPIN_CAP = 1u << 17;
#define ABL 0
#define CSPLIT 1
#define MERGE 3
#define CB 13
#define PAUSE 0
#define QLATE 0
#define CVPRIO 0
#define STG_SEL (idx & 3)
#define SP0 3
#define SDP 1
#define LP0 3
#define LDP 2
#define KVS 5
#define KVU 3
#define QIT 1
#define ZB 3
#define OS 2
#define MPRIO 2

#define ROT 0x80A5
#define R2 0x41
#define CPF 0
#define PRE 4
#define PRO 12
#define G1NT 0
#define G1NTS 16
#define TMIX 0
#define TLBT 0
#define CDEF 0
#define CVLATE 1
#define FPRIO 3
#define YS 12
#define DP0 1
#define DP1 5
#define AWB 1
#define PK 0
#define PU 13
#define PC0 13
#define PC1 14
#define PQ 0
#define SPF 0
#define SPH 0
#define NB1 4
#define EP 1
#define ESL 0
#define CFB 0
#define P1 1
#define P0 1
#define BKE 0
#define EALL 0
#define EREP 0
#define TOUCH 0
#define FILLB 0
#define CPOST 1
#define CEARLY 0
#define NOOWN 0
#define PFIRST 0
#define ZB0 0
#define CATCH 4
#define PWE 0
#define SWAP 1
#define KVM 0x100
#define KFD 4
constexpr int it_cnt(int p, int k, int last) {
    const int pos[6] = {PK, PU, PC0, PC1, PQ, PU - 1}, n[6] = {4, 4, 4, 4, 8, 1};
    int c = 0;
    for (int i = 0; i < 6; ++i) if (pos[i] <= last && (pos[i] > p || (pos[i] == p && i > k))) c += n[i];
    return c;
}
constexpr int G1STAGE = 32768;
constexpr int g1_slot(int s) { return (NB1 == 5 ? (s + 3) % 5 : s & 3) * G1STAGE; }
constexpr int L_KS = NB1 == 5 ? 65536 : 131072, L_VS = L_KS + 16384;
constexpr int L_TS = 3 * G1STAGE, L_TR = L_TS + 3072;
constexpr int CST = 4288;
constexpr int UOFF = 4 * CST, UPL = 2304;
constexpr int CHSZ = UOFF + 2 * UPL + 32;
constexpr int L_AGG = 4 * CHSZ, L_ES = L_AGG + 15 * 512, L_SY = L_ES + 2048;
constexpr int L_HT = 98304;
constexpr int L_C = 98304, G2STAGE = 16384;
constexpr int L_ZI = 0;
constexpr int L_Z2 = 20480;
constexpr int L_Y = 40960, L_Y2 = 131072;
static_assert(L_SY + 384 <= L_HT, "lds");
static_assert(L_Y + 32768 <= L_AGG && L_Z2 + 64 * 264 <= L_Y, "lds");

__device__ __forceinline__ int img_off(int r, int c) { return (r >> 1) * 256 + ((((r & 1) * 8 + c) ^ ((r >> 1) & 15)) << 4); }

constexpr int g1_upto(int g) {
    int n = PRO;
    for (int t = 0; t <= g; ++t) {
        int sb = (t + 1) / 2, sub = (t + 1) % 2, allowed = sb >= 1 ? 4 * (sb + NB1 - 1) + 2 * (sub + 1) : 4 * NB1;
        if (allowed > 64) allowed = 64;
        if (t >= 2 * LP0 - 1 && t < 2 * LP0 - 1 + PAUSE) continue;
        n = n + CATCH < allowed ? n + CATCH : allowed;
    }
    return n;
}
constexpr int g1_cnt(int s) { return g1_upto(2 * s - 2) - 4 * (s + 1); }
static_assert(g1_upto(28) == 64, "schedule");
constexpr int AW = 8;
constexpr bool g2_dma(int g) { return (g + 1) / 2 >= 2 && (g + 1) / 2 + 3 < 16; }
constexpr int g2_cnt(int id, int at) {
    int n = -1000;
    for (int f = 4; f < 4 + AW; ++f) { ++n; if (f == id) n = 0; }
    for (int f = 4; f < at; ++f) {
        if ((f & 1) && g2_dma(f >> 1)) { ++n; if (100 + (f >> 1) == id) n = 0; }
        if (f + AW < 64) { ++n; if (f + AW == id) n = 0; }
    }
    return n;
}

constexpr int cv_m(int o) { return (o & 1) ? 7 - (o >> 1) : o >> 1; }
constexpr int cv_first(int o) { int p = 0; for (int i = 0; i < o; ++i) p += 8 - cv_m(i); return p; }
constexpr int cv_grp(int p) { int o = 0; while (cv_first(o + 1) <= p) ++o; return o; }
constexpr int cv_read(int p) {
    int slot[40] = {};
    for (int i = 0; i < 40; ++i) slot[i] = -1;
    slot[0] = 2 * 3 + 1;
    for (int o = 0; o < 8; ++o) {
        int q = cv_first(o + 1) - 1;
        for (int h = 0; h < 2; ++h) { while (slot[q] >= 0) ++q; slot[q] = 2 * (o + 4) + h; }
    }
    const int bs[8] = {1, 3, 5, 11, 12, 13, 19, 21};
    for (int jb = 0; jb < 8; ++jb) slot[bs[jb]] = 32 + jb;
    return slot[p];
}
static_assert(cv_read(35) == 22 && cv_read(36) == 23 && cv_read(37) == -1 && cv_read(10) == 11 && cv_read(15) == 12 && cv_read(18) == 15 && cv_read(24) == 17, "conv schedule");
constexpr int cv_wait(int o) {
    int n = 0;
    for (int q = cv_first(o) - 1;; --q) {
        const int rd = cv_read(q < 0 ? q + 36 : q);
        if (rd == 2 * (o + (q < 0 ? 8 : 0)) + 1) return n;
        if (rd >= 0) ++n;
    }
}

struct Args {
    const char *q, *k, *v, *sk, *ssmk, *ssmD, *x, *lre, *lim, *bre, *bim, *cre, *cim, *logdt, *dskip;
    char *out, *ws;
};

typedef __attribute__((ext_vector_type(8))) unsigned long long u64x8;
__global__ __launch_bounds__(768, 3) void fused(Args) {
    __shared__ __attribute__((aligned(1024))) char shm[163840];
    TS0a();
    u64x8 ka0, ka1; unsigned long long ka2;
    const char* pc0;
    int code_n;
    asm volatile("s_load_dwordx16 %0, %5, 0x0\n\ts_load_dwordx16 %1, %5, 0x40\n\ts_load_dwordx2 %2, %5, 0x80\n\t"
                 "s_getpc_b64 %3\n.Lfk_code_0:\n\ts_mov_b32 %4, .Lfk_code_a-.Lfk_code_0-4\n\ts_max_i32 %4, %4, .Lfk_code_b-.Lfk_code_0-4"
                 : "=&s"(ka0), "=&s"(ka1), "=&s"(ka2), "=&s"(pc0), "=&s"(code_n) : "s"(__builtin_amdgcn_kernarg_segment_ptr()) : "scc");
    const int tid = threadIdx.x, lane = tid & 63, wraw = __builtin_amdgcn_readfirstlane(tid >> 6);
    const int wid = !SWAP ? wraw : wraw < 4 ? wraw + 8 : wraw - 4;
    const int b = blockIdx.x, xcd = b & 7, idx = b >> 3;
    unsigned cpf;
    asm volatile("s_cmp_lt_u32 %1, 8\n\ts_cbranch_scc1 1f\n\ts_setprio 3\n\ts_cmp_lg_u32 %1, 8\n\ts_cbranch_scc1 1f\n\t"
                 "v_lshl_add_u32 %0, %2, 7, %3\n\tv_min_i32 %0, %4, %0\n\ts_mov_b64 exec, 0xffff\n\tglobal_load_dword %0, %0, %5\n\ts_mov_b64 exec, -1\n1:"
                 : "=&v"(cpf) : "s"(wid), "v"(lane), "s"(idx * 2048), "s"(code_n), "s"(pc0) : "scc", "memory");
    TS_DECL
    const int tm = (xcd >> 1) * 4 + (idx >> 3), tn = (xcd & 1) * 8 + (idx & 7);
    const int wm = wid & 3, wn = (wid >> 2) & 1, l31 = lane & 31, lh = lane >> 5;
    const unsigned lb = (unsigned)(uintptr_t)shm;
    const unsigned l16 = lane * 16;
    const int l15 = lane & 15, g4 = lane >> 4, sq = 2 * wn + (wm & 1), st = sq * 16 + l15, run = (wm >> 1) * 4 + g4;
    unsigned kvv[4];
    const int trow = idx * 64 * 2048 + xcd * 256;
    if (wid < 8) {
        kvv[0] = trow + wm * 32768 + g4 * 2048 + ((l15 ^ (2 * g4)) << 4);
        kvv[1] = kvv[0] + 8192; kvv[2] = (kvv[0] ^ 128) + 16384; kvv[3] = kvv[2] + 8192;
    } else {
        kvv[0] = trow + (idx ? -4 * 2048 : 0) + g4 * 2048 + wm * 64 + l15 * 4;
        asm volatile("v_writelane_b32 %0, 0, 0\n\tv_writelane_b32 %0, 4, 1\n\tv_writelane_b32 %0, %1, 2" : "+v"(kvv[0]) : "s"((xcd * 64 + idx * 2 + (wm >> 1)) * 4));
        kvv[1] = (xcd * 128 + wm * 32 + l31) * 8;
        kvv[2] = kvv[3] = (tn * 64 + lane) * 2;
    }
    unsigned g1dst = lb + wid * 4096;
    asm volatile("s_waitcnt lgkmcnt(0)" : "+s"(ka0), "+s"(ka1), "+s"(ka2), "+s"(g1dst), "+v"(kvv[0]), "+v"(kvv[1]), "+v"(kvv[2]), "+v"(kvv[3]));
    Args a;
    a.q = (const char*)ka0[0]; a.k = (const char*)ka0[1]; a.v = (const char*)ka0[2]; a.sk = (const char*)ka0[3]; a.ssmk = (const char*)ka0[4]; a.ssmD = (const char*)ka0[5];
    a.x = (const char*)ka0[6]; a.lre = (const char*)ka0[7]; a.lim = (const char*)ka1[0]; a.bre = (const char*)ka1[1]; a.bim = (const char*)ka1[2]; a.cre = (const char*)ka1[3];
    a.cim = (const char*)ka1[4]; a.logdt = (const char*)ka1[5]; a.dskip = (const char*)ka1[6]; a.out = (char*)ka1[7]; a.ws = (char*)ka2;
    char* const ws = a.ws;
    const char* const flg = ws + WS_FLAG;
    TS(26);
    unsigned sm, cl[3]; u32x2 kw0;
    asm volatile("s_cmp_gt_u32 %[w], 7\n\ts_cbranch_scc1 1f\n\t"
                 "s_add_u32 m0, %[m], %[n0]\n\ts_nop 0\n\tglobal_load_lds_dwordx4 %[k0], %[kv]" NTK "\n\ts_add_u32 m0, %[m], %[n0]+1024\n\ts_nop 0\n\tglobal_load_lds_dwordx4 %[k1], %[kv]" NTK "\n\t"
                 "s_add_u32 m0, %[m], %[n0]+2048\n\ts_nop 0\n\tglobal_load_lds_dwordx4 %[k2], %[kv]" NTK "\n\ts_add_u32 m0, %[m], %[n0]+3072\n\ts_nop 0\n\tglobal_load_lds_dwordx4 %[k3], %[kv]" NTK "\n\t"
                 "s_branch 2f\n1:\n\tglobal_load_ushort %2, %[k2], %[c1]\n\tglobal_load_ushort %3, %[k2], %[c2]\n\tglobal_load_ushort %4, %[k2], %[c3]\n\t"
                 "v_mov_b64 %1, %[kv]\n\ts_mov_b64 exec, 7\n\tv_mov_b64 %1, %[d]\n\ts_mov_b64 exec, 3\n\tv_mov_b64 %1, %[e]\n\ts_mov_b64 exec, -1\n\t"
                 "v_mad_u64_u32 %1, vcc, %[k0], 1, %1\n\tglobal_load_dword %0, %1, off sc1\n\tglobal_load_dwordx2 %1, %[k1], %[sk]\n2:"
                 : "=&v"(sm), "=&v"(kw0), "=&v"(cl[0]), "=&v"(cl[1]), "=&v"(cl[2])
                 : [w] "s"(wid), [c1] "s"(a.logdt), [c2] "s"(a.lre), [c3] "s"(a.lim), [d] "s"(a.ssmD), [e] "s"(flg + FL_EPOCH), [sk] "s"(a.sk),
                   [k0] "v"(kvv[0]), [k1] "v"(kvv[1]), [k2] "v"(kvv[2]), [k3] "v"(kvv[3]), [kv] "s"(wn ? a.v : a.k), [m] "s"(g1dst), [n0] "n"(L_KS)
                 : "scc", "vcc", "memory");
    TS0b();
    TS(27);
    if (P1) BAR();
#define SLICE_TABLE() unsigned slo, shi; { int ix = idx; asm volatile("" : "+s"(ix)); \
        const int rot2 = (((R2 >> 4) & 15) * (ix >> 3) + (R2 & 15) * (xcd >> 1)) & 15, es = (tn - rot2) & 15; \
        const unsigned long long id = 0xFEDCBA9876543210ull, seq = rot2 ? (id >> (4 * rot2)) | (id << (64 - 4 * rot2)) : id; \
        const unsigned long long lo = (1ull << (4 * es)) - 1, tab = ((seq & lo) << 4) | (seq & ~((lo << 4) | 15)) | tn; \
        slo = (unsigned)tab; shi = (unsigned)(tab >> 32); asm volatile("" : "+s"(slo), "+s"(shi)); }
#define SLICE(st) ((((st) < 8 ? slo : shi) >> (4 * ((st) & 7))) & 15)
    typedef __attribute__((ext_vector_type(4))) short v4i16;
    int role = wid;
    asm volatile("" : "+s"(role));

    if (role >= 8) {
        const int chn = xcd * 128 + idx * 4 + wm;
        const unsigned cb = lb + wm * CHSZ;
        const int r = l15;
        const unsigned fpub = xcd * 256 + idx * 8, fpoll = xcd * 256 + l31 * 8;
        u32x2 fl; u32x4 sw[4], uu[4];
        unsigned tch = 0;
        if (TOUCH) {
            const int ln = wm * 4 + (lane & 3);
            const unsigned fo = (unsigned)(WS_FLAG - WS_E) + (wm == 0 ? FL_H + (tm * 16 + tn) * 128 : (wm == 1 ? FL_KV : FL_Z) + xcd * 256 + (lane & 1) * 128);
            const unsigned o = (lane & 4) && (TOUCH & 2) ? fo : ((tm * 2 + (ln >> 3)) * 1024 + tn * 64) * 16 + (ln & 7) * 128;
            asm volatile("s_mov_b64 exec, 0xff\n\tglobal_load_dword %0, %1, %2\n\ts_mov_b64 exec, -1" : "=&v"(tch) : "v"(o), "s"(ws + WS_E) : "memory");
        }
        SLICE_TABLE();
        TS(1);
        WAIT_V(TOUCH ? 1 : 0);
        TS(22);
        asm volatile("" :: "v"(cpf));
        const unsigned hal = take(sm);
        const unsigned tagA = __builtin_amdgcn_readlane(hal, 0) + 1, tagB = __builtin_amdgcn_readlane(hal, 1) + (tagA == 0 ? 1u : 0u);
        const u32x2 tag = {tagA, tagB};
        f32x2 L1, C1;
        {
            const float dt = rbf(expf(bf2f(take(cl[0]))));
            const float lr = bf2f(take(cl[1])), li = bf2f(take(cl[2]));
            const float e = expf(dt * lr);
            float sn, cs;
            sincosf(dt * li, &sn, &cs);
            L1 = f32x2{e * cs, e * sn};
            const float nr = L1[0] - 1.f, ni = L1[1], den = lr * lr + li * li;
            C1 = f32x2{(nr * lr + ni * li) / den, (ni * lr - nr * li) / den};
            TIE(L1); TIE(C1);
        }
        f32x2 p[11];
        p[0] = L1;
        if (PWE) {
#pragma unroll
            for (int i = 1; i <= 10; ++i) { p[i] = cmul4(p[i - 1], p[i - 1]); TIE(p[i]); }
        }
        {
            if (!PWE) {
#pragma unroll
                for (int i = 1; i <= 10; ++i) p[i] = cmul4(p[i - 1], p[i - 1]);
            }
            const f32x2 one = {1.f, 0.f}, zero = {0.f, 0.f};
            const unsigned ts = lb + L_TS + l16, tr = lb + L_TR + wm * 2048 + l16;
            if (wm == 0) {
                *LDSP(f32x4, ts) = f32x4{L1[0], L1[1], C1[0], C1[1]};
                *LDSP(f32x4, ts + 1024) = f32x4{p[3][0], p[3][1], p[4][0], p[4][1]};
                *LDSP(f32x4, ts + 2048) = f32x4{p[6][0], p[6][1], __uint_as_float(tagA), __uint_as_float(tagB)};
            }
            f32x2 Q[2], V[2], W[2];
            Q[0] = cmul4(cmul4(C1, (wm & 1) ? p[5] : one), (wm & 2) ? p[6] : one); Q[1] = cmul4(Q[0], p[4]);
            V[1] = (wm & 1) ? one : p[5]; V[0] = cmul4(V[1], p[4]);
            W[1] = cmul4((wm & 1) ? one : p[9], (wm & 2) ? one : p[10]); W[0] = cmul4(W[1], p[8]);
#pragma unroll
            for (int i = 0; i < 2; ++i) {
                const f32x2 Q8 = cmul4(Q[i], p[3]), U = 4 * wm + 2 * i >= 16 - tm ? cmul4(W[i], p[7]) : zero, Wz = 4 * wm + 2 * i + 1 >= 16 - tm ? W[i] : zero;
                *LDSP(f32x4, tr + i * 1024) = f32x4{Q[i][0], Q8[0], Q[i][1], Q8[1]};
                *LDSP(f32x4, tr + i * 1024 + 8192) = f32x4{Wz[0], Wz[1], U[0], U[1]};
                *LDSP(f32x2, tr + i * 1024 + 16384) = V[i];
            }
        }
        const unsigned dskl = (__builtin_amdgcn_readlane(hal, 2) >> ((wm & 1) * 16)) & 0xffffu;
        u32x4 kvo[4];
        {
            unsigned kw[2][8], ta[2], th[2];
            u32x2 hm[2];
#pragma unroll
            for (int j = 0; j < 2; ++j) {
                const unsigned x = take(kw0[j]);
                const auto y = __builtin_amdgcn_permlane16_swap(x, x, false, false);
#pragma unroll
                for (int p = 0; p < 2; ++p) { kw[p][4 * j] = y[p] & 0xffffu; kw[p][4 * j + 1] = y[p] << 16; kw[p][4 * j + 2] = y[p] >> 16; kw[p][4 * j + 3] = y[p] & 0xffff0000u; }
            }
#pragma unroll
            for (int p = 0; p < 2; ++p) {
                unsigned h[3];
#pragma unroll
                for (int j = 0; j < 3; ++j) h[j] = (__shfl(hal, p * 8 + (r >> 1) + 16 * (j + 1)) >> ((r & 1) * 16)) & 0xffffu;
                hm[p] = idx ? u32x2{h[0] << 16, h[1] | (h[2] << 16)} : u32x2{0u, 0u};
                const int q = r >> 2, pp = r & 3;
                ta[p] = lb + L_KS + (8 * g4 + q) * 256 + (((wm * 4 + p * 2 + (pp >> 1)) ^ (2 * (q | ((g4 & 1) << 2)))) << 4) + (pp & 1) * 8;
                th[p] = (ta[p] ^ 128) - 1024;
                TIE(hm[p]); TIE(ta[p]); TIE(th[p]);
#pragma unroll
                for (int j = 0; j < 8; ++j) TIE(kw[p][j]);
            }
            WAIT_L(0);
            TS(21);
            BAR();
            TS(2);
            u32x2 kt[4][3], vt[4][2];
#pragma unroll
            for (int u = 0; u < 4; ++u) {
                const int p = u >> 1, o = (u & 1) * 8192;
                auto tr = [&](unsigned ad) -> u32x2 { return __builtin_bit_cast(u32x2, __builtin_amdgcn_ds_read_tr16_b64_v4i16(LDSP(v4i16, ad))); };
                kt[u][0] = tr(th[p] + o); kt[u][1] = tr(ta[p] + o); vt[u][0] = tr(ta[p] + o + 16384); kt[u][2] = tr(ta[p] + o + 1024); vt[u][1] = tr(ta[p] + o + 17408);
                SB();
            }
            TS(14);
            if (KVM & 1) BAR();
#pragma unroll
            for (int u = 0; u < 4; ++u) {
                const int p = u >> 1, oh = u & 1;
                const u32x2 k0 = oh == 0 && g4 == 0 ? hm[p] : kt[u][0];
                unsigned o[4];
                kv4(o[3], o[2], k0[0], k0[1], kt[u][1][0], kt[u][1][1], kw[p], vt[u][0][0], vt[u][0][1]);
                if ((KVM >> (2 * u + 1)) & 1) { TIE(o[3]); BAR(); TIE(vt[u][1]); }
                kv4(o[1], o[0], kt[u][1][0], kt[u][1][1], kt[u][2][0], kt[u][2][1], kw[p], vt[u][1][0], vt[u][1][1]);
                if (u == 0) { TIE(o[0]); TIE(o[2]); WAIT_L(0); }
                kvo[u] = u32x4{o[0], o[1], o[2], o[3]};
                TIE(kvo[u]);
                SB();
                if (u == 0) TS(15); if (u == 1) TS(16); if (u == 2) TS(17); if (u == 3) TS(18);
                if ((KVM >> (2 * u + 2)) & 1) { BAR(); if (u < 3) TIE(vt[u + 1][0]); }
            }
#pragma unroll
            for (int u = 0; u < 4; ++u) TIE(kvo[u]);
        }
        TS(3);
        u32x2 qv[8];
        const char* krow = a.ssmk + (size_t)chn * 4096;
        const char* qs = a.q + (size_t)(idx * 64 + wm * 16) * 2048 + xcd * 256;
        const char* usrc = ws + WS_KVT + (size_t)(xcd * 8 + (idx >> 2)) * 65536 + wm * 16384 + (idx & 3) * 64;
        const unsigned uo = (lane >> 2) * 256 + (lane & 3) * 16;
        const char* cs = ((wm & 2) ? a.cim : a.cre) + (size_t)(tn * 64 + (wm & 1) * 2) * 2048;
        unsigned co[4];
#pragma unroll
        for (int i = 0; i < 4; ++i) {
            const int pair = i * 4 + g4, v = r ^ pair, row = pair * 2 + (v >> 3);
            co[i] = ((row & 15) * 4 + (row >> 4)) * 2048 + (v & 7) * 16;
        }
        bool kv_ok = false;
#pragma unroll
        for (int it = 0; it < 4; ++it) asm volatile("" : "=v"(uu[it]));
        auto items = [&](auto pc) {
            constexpr int p = decltype(pc)::value;
            if constexpr (PK == p) { GLD_B128_NT(sw[0], l16, krow, 0); GLD_B128_NT(sw[1], l16, krow, 1024); GLD_B128_NT(sw[2], l16, krow, 2048); GLD_B128_NT(sw[3], l16, krow, 3072); }
            if constexpr (PU == p) {
                WAIT_V(PK == p ? 4 : 0); TIE(fl);
                kv_ok = __all(fl[0] == tagA && fl[1] == tagB);
                const char* us = kv_ok ? usrc : a.x;
                for (int it = 0; it < 4; ++it) GLD_B128_SC1(uu[it], uo, us + it * 4096, 0);
            }
            if constexpr (PC0 == p) { for (int i = 0; i < 4; ++i) DMA(lb + L_C + 2 * G2STAGE + wm * 4096 + i * 1024, co[i], cs + (SLICE(0) << 7)); }
            if constexpr (PC1 == p) { for (int i = 0; i < 4; ++i) DMA(lb + L_C + 3 * G2STAGE + wm * 4096 + i * 1024, co[i], cs + (SLICE(1) << 7)); }
            if constexpr (PQ == p) static_for<0, 8>([&](auto jc) { constexpr int j = decltype(jc)::value; GLD_B64_NT(qv[j], lh * 2048 + l31 * 8, qs + j * 4096, 0); });
            if constexpr (PU == p + 1) GLD_B64_SC1(fl, fpoll, flg + FL_KV, 0);
        };
#define ITEMS(p) items(std::integral_constant<int, p>{})
        {
            const int ph = STG_SEL, ps = SP0 + ph * SDP, pl = LP0 + ph * LDP;
            const char* kvd = ws + WS_KVT + (size_t)(xcd * 8 + wm * 2) * 65536 + idx * 2048;
            int nb1 = __builtin_popcount(KVM);
            static_assert(__builtin_popcount(KVM) <= SP0 + 1 && SP0 + 3 * SDP + KFD <= 11, "schedule");
#pragma unroll 1
            for (; nb1 <= ps; ++nb1) BAR();
            static_for<0, 4>([&](auto uc) {
                constexpr int u = decltype(uc)::value;
                GST_B128_SC1(l16, kvo[u], kvd + (u >> 1) * 65536, (u & 1) * 1024);
            });
#pragma unroll 1
            for (; nb1 <= pl; ++nb1) {
                if (nb1 == ps + KFD) WAIT_V(0);
                BAR();
                if (nb1 == ps + KFD && wm == 0 && lane == 0) GST_B64_SC1(fpub, tag, flg + FL_KV);
            }
            ITEMS(0);
#pragma unroll 1
            for (; nb1 <= 11; ++nb1) {
                if (nb1 == ps + KFD) WAIT_V(it_cnt(0, -1, 0));
                BAR();
                if (nb1 == ps + KFD && wm == 0 && lane == 0) GST_B64_SC1(fpub, tag, flg + FL_KV);
            }
        }
        asm volatile("" :: "v"(tch));
        ITEMS(11);
        BAR();
        ITEMS(12);
        BAR();
        ITEMS(13);
        BAR();
        ITEMS(14);
        BAR();
        ITEMS(15);
        TS(4);
        const unsigned ud = lb + (lane & 3) * CHSZ + UOFF + ((lane >> 2) & 1) * UPL + (16 + wm * 32 + (lane >> 3)) * 16;
        {
            const u32x4 z4 = {0, 0, 0, 0};
            if (lane < 24) *LDSP(u32x4, lb + L_SY + l16) = z4;
            if (lane < 56) *LDSP(u32x4, lb + L_AGG + wm * 896 + l16) = z4;
            *LDSP(u32x4, lane < 32 ? cb + (lane >> 3) * CST + (lane & 7) * 16 : cb + UOFF + (g4 & 1) * UPL + r * 16) = z4;
        }
        auto fill = [&]() {
#pragma unroll
            for (int it = 0; it < 4; ++it) TIE(sw[it]);
#pragma unroll
            for (int it = 0; it < 4; ++it) {
                const unsigned q = cb + 128 + it * 1024 + l16;
                const unsigned d0 = sw[it][0], d1 = sw[it][1], d2 = sw[it][2], d3 = sw[it][3];
                const unsigned o0 = it ? __builtin_amdgcn_readlane(sw[it ? it - 1 : 0][2], 63) : 0u, o1 = it ? __builtin_amdgcn_readlane(sw[it ? it - 1 : 0][3], 63) : 0u;
                const unsigned x0 = __builtin_amdgcn_update_dpp(o0, d2, 0x138, 0xf, 0xf, false), x1 = __builtin_amdgcn_update_dpp(o1, d3, 0x138, 0xf, 0xf, false);
                const unsigned a0 = __builtin_amdgcn_alignbyte(d0, x1, 2), a1 = __builtin_amdgcn_alignbyte(d1, d0, 2), a2 = __builtin_amdgcn_alignbyte(d2, d1, 2),
                               a3 = __builtin_amdgcn_alignbyte(d3, d2, 2);
                *LDSP(u32x4, q) = sw[it];
                *LDSP(u32x4, q + CST) = u32x4{a0, a1, a2, a3};
                *LDSP(u32x4, q + 2 * CST) = u32x4{x1, d0, d1, d2};
                *LDSP(u32x4, q + 3 * CST) = u32x4{__builtin_amdgcn_alignbyte(x1, x0, 2), a0, a1, a2};
                if (it == 3 && lane == 63) {
                    *LDSP(u32x2, q + 16 + CST) = u32x2{d3 >> 16, 0u};
                    *LDSP(u32x2, q + 16 + 2 * CST) = u32x2{d3, 0u};
                    *LDSP(u32x2, q + 16 + 3 * CST) = u32x2{a3, d3 >> 16};
                }
            }
        };
        if (FILLB) {
            static_assert(!FILLB || PK < PU, "ssm_k has landed");
            if (FILLB > 1) __builtin_amdgcn_s_sleep(FILLB);
            fill();
        }
        WAIT_L(0);
        TS(5);
        BAR();
        __builtin_amdgcn_s_setprio(FPRIO);
        ITEMS(16);
        TS(11);
        WAIT_V(it_cnt(PK, 0, 16));
        TS(12);
        if (!FILLB) fill();
        TS(13);
        WAIT_V(it_cnt(PU, 1, 16));
#pragma unroll
        for (int it = 0; it < 4; ++it) TIE(uu[it]);
        if (!kv_ok) {
            for (unsigned spin = 0; spin < SPIN_CAP; ++spin) {
                GLD_B64_SC1(fl, fpoll, flg + FL_KV, 0);
                WAIT_V(0); TIE(fl);
                if (__all(fl[0] == tagA && fl[1] == tagB)) break;
                __builtin_amdgcn_s_sleep(8);
            }
#pragma unroll
            for (int it = 0; it < 4; ++it) GLD_B128_SC1(uu[it], uo, usrc + it * 4096, 0);
            WAIT_V(0);
#pragma unroll
            for (int it = 0; it < 4; ++it) TIE(uu[it]);
        }
#pragma unroll
        for (int it = 0; it < 4; ++it) *LDSP(u32x4, ud + it * 128) = uu[it];
        const unsigned pmine = lb + L_SY + 32 + wm * 4, pall = lb + L_SY + 32 + (lane & 3) * 4;
        sy_post<0>(pmine); sy_wait<0>(pall);
        if (!CPOST) { WAIT_V(0); sy_post<1>(pmine); }
        __builtin_amdgcn_s_setprio(CVPRIO);
        TS(6);
        f32x4 ca[8];
        {
            unsigned kb = cb + ((3 - r) & 3) * CST + 2 * (64 + 16 + r - 8 * g4 - 7 + ((3 - r) & 3));
            unsigned ub = cb + UOFF + (g4 & 1) * UPL + (15 + r + (g4 >> 1)) * 16 - 64;
#pragma unroll
            for (int i = 0; i < 8; ++i) ca[i] = f32x4{0, 0, 0, 0};
            s16x4 FA[4][2]; bf16x8 FB[2][8];
#define CV_A(s, o, e) DSR64(FA[(o) & 3][e], kb, 64 * ((s) + ((o) >> 3)) + 512 * cv_m((o) & 7) + 8 * (e))
#define CV_B(s, jb) DSR128(FB[(s) & 1][jb], ub, 64 + 256 * (jb) - 32 * (s))
            static_for<0, 8>([&](auto ic) { constexpr int n = decltype(ic)::value; CV_B(0, n); CV_A(0, n >> 1, n & 1); });
            WAIT_L(0);
            auto body = [&]() {
                static_for<0, 72>([&](auto ic) {
                    constexpr int s = decltype(ic)::value / 36, p = decltype(ic)::value % 36, o = cv_grp(p), m = cv_m(o), jb = p - cv_first(o);
                    if constexpr (jb == 0) { WAIT_L(cv_wait(o)); TIE(FA[o & 3][0]); TIE(FA[o & 3][1]); }
                    if constexpr (p == 0) { for (int i = 0; i < 8; ++i) TIE(FB[s][i]); }
                    const bf16x8 fa = __builtin_shufflevector(FA[o & 3][0], FA[o & 3][1], 0, 1, 2, 3, 4, 5, 6, 7);
                    ca[jb + m] = __builtin_amdgcn_mfma_f32_16x16x32_bf16(fa, FB[s][jb], ca[jb + m], 0, 0, 0);
                    SB();
                    constexpr int rd = cv_read(p);
                    if constexpr (rd >= 32) CV_B(s + 1, rd - 32);
                    else if constexpr (rd >= 0) CV_A(s, rd >> 1, rd & 1);
                    SB();
                });
                kb += 128; ub -= 64;
            };
#pragma unroll 1
            for (int it = 0; it < QIT; ++it) body();
            {
                TS(19);
                if (CPOST) { WAIT_V(0); sy_post<1>(pmine); WAIT_L(0); }
                if (CVLATE) sy_wait<1>(lb + L_SY + (CVLATE == 2 ? wm + (lane & 1) * 4 : lane & 7) * 4);
                TS(20);
            }
#pragma unroll 1
            for (int it = QIT; it < 4; ++it) body();
            WAIT_L(0);
#pragma unroll
            for (int i = 0; i < 4; ++i) { TIE(FA[i][0]); TIE(FA[i][1]); }
#pragma unroll
            for (int i = 0; i < 8; ++i) TIE(FB[0][i]);
#undef CV_A
#undef CV_B
        }
        TS(7);
        __builtin_amdgcn_s_setprio(3);
        int nb = 1;
#define HB() do { if (nb < 15) BAR(); ++nb; } while (0)
        if (ZB0) HB();
        {
            const float Dd = bf2f(dskl);
            const char* zd = ws + WS_ZT + (size_t)chn * ZP;
            u32x2 kvr[8];
#pragma unroll
            for (int jt = 0; jt < 8; ++jt) kvr[jt] = *LDSP(u32x2, cb + UOFF + (g4 >> 1) * UPL + (16 + 16 * jt + r) * 16 + (1 - (g4 & 1)) * 8);
#pragma unroll
            for (int jt = 0; jt < 8; ++jt) {
                const f32x4 y = ca[jt];
                const u32x2 kq = kvr[jt];
                *LDSP(u32x2, cb + (jt * 4 + (r >> 2)) * 144 + (r & 3) * 32 + g4 * 8) =
                    u32x2{pack2(rb1(y[0]) + rb1(bfhi(kq[1]) * Dd), rb1(y[1]) + rb1(bflo(kq[1]) * Dd)), pack2(rb1(y[2]) + rb1(bfhi(kq[0]) * Dd), rb1(y[3]) + rb1(bflo(kq[0]) * Dd))};
            }
            if (ZB >= 3) HB();
            u32x4 t[4];
#pragma unroll
            for (int it = 0; it < 4; ++it) t[it] = *LDSP(u32x4, cb + (it * 8 + (lane >> 3)) * 144 + (lane & 7) * 16);
            if (!(ABL & 128)) { GST_B128_SC1(l16, t[0], zd, 0); GST_B128_SC1(l16, t[1], zd, 1024); GST_B128_SC1(l16, t[2], zd, 2048); GST_B128_SC1(l16, t[3], zd, 3072); }
            else { TIE(t[0]); TIE(t[1]); TIE(t[2]); TIE(t[3]); }
        }
        if (ZB >= 2) HB();
        if (ZB >= 1) HB();
        WAIT_V(0);
        sy_post<2>(pmine);
        if (wm == 0) {
            sy_wait<2>(pall);
            if (!(ABL & 1024) && lane == 0) GST_B64_SC1(fpub, tag, flg + FL_Z);
        }
        TS(8);
        HB(); HB(); HB();
        if (!(ABL & 1024) && wm == 0) GLD_B64_SC1(fl, fpoll, flg + FL_Z, 0);
        HB(); HB();
        if (wm == 0) { WAIT_V(0); TIE(fl); *LDSP(unsigned, lb + L_SY + 320) = __all(fl[0] == tagA && fl[1] == tagB) ? 2 : 1; }
        if (__builtin_amdgcn_readfirstlane(sy_wait<5>(lb + L_SY)) != 2) {
            while (nb < 15) { BAR(); ++nb; }
            for (unsigned spin = 0; spin < SPIN_CAP; ++spin) {
                GLD_B64_SC1(fl, fpoll, flg + FL_Z, 0);
                WAIT_V(0); TIE(fl);
                if (__all(fl[0] == tagA && fl[1] == tagB)) break;
                __builtin_amdgcn_s_sleep(8);
            }
        }
        u32x4 zv[4];
        {
            ITEMS(18);
            const char* zsrc = ws + WS_ZT + (size_t)(xcd * 128 + wm * 32) * ZP + idx * 128;
#pragma unroll
            for (int it = 0; it < 4; ++it) if (!(ABL & 256)) GLD_B128_SC1(zv[it], (lane >> 3) * ZP + (lane & 7) * 16, zsrc + it * 8 * ZP, 0);
        }
        HB(); HB();
        WAIT_V(0);
        TS(9);
        {
            const unsigned zi = lb + L_ZI + wm * 5120;
#pragma unroll
            for (int it = 0; it < 4; ++it) { TIE(zv[it]); *LDSP(u32x4, zi + ((lane >> 3) + 8 * it) * 160 + (lane & 7) * 16) = zv[it]; }
            v4i16 z[8];
#pragma unroll
            for (int n = 0; n < 8; ++n) z[n] = __builtin_amdgcn_ds_read_tr16_b64_v4i16(LDSP(v4i16, zi + ((n & 1) * 16 + (lane >> 2)) * 160 + (n >> 1) * 32 + (lane & 3) * 8));
            HB();
#pragma unroll
            for (int n = 0; n < 8; ++n) *LDSP(v4i16, lb + L_Z2 + ((n >> 1) * 16 + r) * 264 + wm * 64 + (n & 1) * 32 + g4 * 8) = z[n];
            sy_post<3>(pmine);
        }
        HB();
        sy_wait<3>(pall);
        char* const oa = a.out + (size_t)(idx * 64 + wm * 16) * 8192 + xcd * 512;
        u32x2 zz[8];
#pragma unroll
        for (int n = 0; n < 8; ++n) zz[n] = *LDSP(u32x2, lb + L_Z2 + (wm * 16 + n * 2 + lh) * 264 + l31 * 8);
#pragma unroll
        for (int n = 0; n < 8; ++n) {
            TIE(qv[n]);
            const u32x2 z = zz[n];
            const f32x4 o = {rb1(bflo(qv[n][0]) * bflo(z[0])), rb1(bfhi(qv[n][0]) * bfhi(z[0])), rb1(bflo(qv[n][1]) * bflo(z[1])), rb1(bfhi(qv[n][1]) * bfhi(z[1]))};
            if (!(ABL & 512)) GST_B128_NT(lh * 8192 + l31 * 16, o, oa + n * 16384, 0); else { f32x4 o2 = o; TIE(o2); }
            if (n < 7 && (n + 1) % OS == 0) HB();
        }
#undef HB
        asm volatile(".Lfk_code_a:");
        TSE(10);
        TS_FLUSH();
        return;
    }

    u32x2 fl;
    unsigned dv[4];
    {
        const int v = l15 ^ g4, hi = v >> 3;
        const int r0 = wn ? tn * 64 + (wm >> 1) * 32 : tm * 128 + (wm >> 1) * 64 + (wm & 1) * 4;
        const unsigned d0 = (r0 + ((g4 * (wn ? 2 : 9)) & (wn ? 6 : 17))) * 2048 + (v & 7) * 16, odd = wn ? 11 : 14;
        dv[0] = d0 + (hi << odd); dv[2] = d0 + ((hi ^ 1) << odd) + (wn ? 32768 : 4096);
        dv[1] = (dv[0] ^ 64) + (wn ? 16384 : 65536); dv[3] = (dv[2] ^ 64) + (wn ? 16384 : 65536);
    }
    const int rot7 = ((((ROT >> 12) & 15) * (idx & 7) + ((ROT >> 8) & 15) * (idx >> 3) + ((ROT >> 4) & 15) * (xcd >> 1) + (ROT & 15) * (xcd & 1)) & 15) << 7;
    const char* g1src = wn ? ((wm & 1) ? a.bim : a.bre) : a.x;
#define G1_ISSUE(st, i) DMA(g1dst + g1_slot(st) + (i) * 1024, dv[i], g1src + ((((st) << 7) + rot7) & 1920))
    static_for<0, PRE>([&](auto ic) { constexpr int n = decltype(ic)::value; G1_ISSUE(n >> 2, n & 3); });
    TS(14);
    SLICE_TABLE();
    const int j0 = 4 * run - (32 - 2 * tm);
    const int pneg = EALL ? 0 : 16 * (wm >> 1) + 15 - (32 - 2 * tm);
    unsigned eo[2], pes = lb + L_ES + wid * 256 + l15 * 16;
    unsigned plr = lb + L_AGG + st * 8 + run * 512, est = ((tm * 2 + (wm >> 1)) * 1024 + tn * 64 + st) * 16 + (EREP ? g4 * 524288 : 0);
#pragma unroll
    for (int i = 0; i < 2; ++i) { eo[i] = (EALL ? (j0 + 2 * i) & 31 : j0 + 2 * i < 0 ? 0 : j0 + 2 * i) * 16384 + (EREP ? (tm >> 2) * 524288 : 0) + (tn * 64 + st) * 16; TIE(eo[i]); }
    TIE(plr); TIE(pes); TIE(est);
    unsigned hw[2], ta[2];
    {
        const unsigned ht = lb + L_HT + wid * 4096;
#pragma unroll
        for (int j = 0; j < 2; ++j) {
            const int pr = (8 * (g4 & 1) + 4 * j + (l15 >> 2)) ^ ((g4 & 1) * 4), pw = l15 ^ ((l15 >> 3) * 4);
            ta[j] = ht + (g4 >> 1) * 512 + pr * 32 + ((((l15 >> 1) ^ (pr >> 2)) & 1) << 4) + 8 * (lane & 1);
            hw[j] = ht + g4 * 1024 + pw * 32 + (((j ^ (pw >> 2)) & 1) << 4);
            TIE(ta[j]); TIE(hw[j]);
        }
    }
    unsigned po = pes + ((wm & 2) ? -512 : 512);
    unsigned cta = lb + L_TS + st * 16, ctb = lb + L_TR + run * 1024 + st * 16;
    TIE(po); TIE(cta); TIE(ctb);
    f32x4 ct[5]; f32x2 V1;
    unsigned pmine = lb + L_SY + wid * 4, pall = lb + L_SY + (lane & 7) * 4, pown = lb + L_SY + (lane < 4 ? 32 + lane * 4 : (wid ^ 1) * 4), pzero = lb + L_SY;
    unsigned g2v[2], cad[2][2], to[2][2];
    unsigned xo = (tm * 128 + wm * 32 + wn * 16 + g4 * 4) * 2048 + tn * 128 + l15 * 8 + 4096, dso = xo & 2047;
    unsigned fst = (tm * 16 + l15) * 128 + tn * 8, fld = (tm * 16 + tn) * 128 + (NOOWN && l15 == tn ? tn ^ 1 : l15) * 8;
#pragma unroll
    for (int i = 0; i < 2; ++i) {
        const int pair = i * 4 + g4, v = l15 ^ pair ^ ((wm & 1) * 8);
        g2v[i] = (pair * 2 + (v >> 3)) * 8192 + (v & 7) * 16 + (tn * 64 + wm) * 2048;
        TIE(g2v[i]);
#pragma unroll
        for (int j = 0; j < 2; ++j) {
            cad[i][j] = lb + L_C + (img_off((g4 >> 1) * 64 + l15, 4 * wn + (g4 & 1)) ^ (i * 32) ^ (j * 128));
            to[i][j] = ta[j] + (i ^ wn) * 1024 - (wm & 1) * 2048;
            TIE(cad[i][j]); TIE(to[i][j]);
        }
    }
    TIE(pmine); TIE(pall); TIE(pown); TIE(pzero); TIE(xo); TIE(dso); TIE(fst); TIE(fld);
    TS(1);
    {
        WAIT_V(PRE);
        BAR();
        static_assert(PRO <= 12 && NB1 == 4, "the constants sit in slot 3");
        DSR128(ct[0], cta, 0); DSR128(ct[1], cta, 1024); DSR128(ct[2], cta, 2048); DSR128(ct[3], ctb, 0); DSR128(ct[4], ctb, 8192); DSR64(V1, ctb, 16384);
        static_for<PRE, PRO>([&](auto ic) { constexpr int n = decltype(ic)::value; G1_ISSUE(n >> 2, n & 3); });
        TS(15);
    }
    WAIT_L(0);
#pragma unroll
    for (int i = 0; i < 5; ++i) TIE(ct[i]);
    TIE(V1);
    unsigned tagA = __builtin_amdgcn_readfirstlane(__float_as_uint(ct[2][2])), tagB = __builtin_amdgcn_readfirstlane(__float_as_uint(ct[2][3]));
    asm volatile("" : "+s"(tagA), "+s"(tagB));

    f32x4 acc[4][2];
    {
#pragma unroll
        for (int i = 0; i < 4; ++i) { acc[i][0] = f32x4{0, 0, 0, 0}; acc[i][1] = f32x4{0, 0, 0, 0}; }
        const int aoff = img_off((wm >> 1) * 64 + l15, g4), boff = img_off(128 + wn * 64 + (wm & 1) * 16 + l15, g4);
        unsigned aad[2][2], bad[2];
#pragma unroll
        for (int ks = 0; ks < 2; ++ks) { aad[ks][0] = lb + (aoff ^ (ks * 64)); aad[ks][1] = lb + (aoff ^ (ks * 64) ^ 128); bad[ks] = lb + (boff ^ (ks * 64)); }
        bf16x8 fa[2][4], fb[2][2];
        WAIT_V(PRO - 4); BAR();
        TS(2);
#define G1_FRAG(g, n) do { constexpr int bo_ = g1_slot((g) >> 1), hi_ = bo_ & ~65535, x_ = (n) < 3 ? (n) >> 1 : (n) - 2; \
        if constexpr ((n) == 0 || (n) == 2) DSR128(fb[(g) & 1][x_], bad[(g) & 1] + hi_, bo_ - hi_ + x_ * 4096); \
        else DSR128(fa[(g) & 1][x_], aad[(g) & 1][x_ & 1] + hi_, bo_ - hi_ + x_ * 2048); } while (0)
        static_for<0, 6>([&](auto ic) { constexpr int n = decltype(ic)::value; G1_FRAG(0, n); });
        static_for<0, 32>([&](auto ic) {
            constexpr int g = decltype(ic)::value, gn = g + 1, n0 = g1_upto(g - 1), nd = g1_upto(g) - n0;
            constexpr bool bar = (gn & 1) == 0 && gn < 32;
            if constexpr (bar) {
                static_assert(g1_cnt(gn >> 1) >= 0, "schedule");
                WAIT_V(g1_cnt(gn >> 1)); WAIT_L(0); BAR();
                if constexpr (((gn >> 1) & 3) == 0 || gn >= 26) TS(gn >= 26 ? 23 + (gn >> 1) - 13 : 16 + (gn >> 3));
#pragma unroll
                for (int i = 0; i < 4; ++i) TIE(fa[g & 1][i]);
                TIE(fb[g & 1][0]); TIE(fb[g & 1][1]);
            }
            static_for<0, 8>([&](auto mc) {
                constexpr int m = decltype(mc)::value, i = m >> 1, j = m & 1;
                if constexpr (!bar && (i == 0 || j == 0)) {
                    WAIT_L(5 - (i == 0 ? j + 1 : i + 2) + (gn < 32 ? (m < 6 ? m : 6) : 0));
                    TIE(fa[g & 1][i]); TIE(fb[g & 1][j]);
                }
                acc[i][j] = __builtin_amdgcn_mfma_f32_16x16x32_bf16(fa[g & 1][i], fb[g & 1][j], acc[i][j], 0, 0, 0);
                SB();
                if constexpr (gn < 32 && m < 6) G1_FRAG(gn, m);
                constexpr int d = nd > 2 ? ((m & 1) ? m >> 1 : -1) : (m == DP0 ? 0 : m == DP1 ? 1 : -1);
                if constexpr (d >= 0 && d < nd) G1_ISSUE((n0 + d) >> 2, (n0 + d) & 3);
                SB();
            });
        });
#undef G1_FRAG
    }
    TS(3);
    const unsigned tagG = tagA ^ tagB ^ 0x9E3779B9u;
    __builtin_amdgcn_s_setprio(MPRIO);
    f32x2 ur[8], ui[8], hr, hi;
    const f32x2 L1 = {ct[0][0], ct[0][1]}, C1 = {ct[0][2], ct[0][3]}, L16 = {ct[1][2], ct[1][3]}, Q2r = {ct[3][0], ct[3][1]}, Q2i = {ct[3][2], ct[3][3]};
    const float L8r = ct[1][0], L8i = ct[1][1], L64r = ct[2][0], L64i = ct[2][1];
    u32x4 eg[4];
#define E_LOADS "global_load_dwordx4 v[104:107], %[a0], %[b0] sc1\n\tglobal_load_dwordx4 v[108:111], %[a0], %[b1] sc1\n\t" \
                "global_load_dwordx4 v[112:115], %[a1], %[b0] sc1\n\tglobal_load_dwordx4 v[116:119], %[a1], %[b1] sc1\n"
#define E_POLL() asm volatile("s_cmp_lt_i32 %[on], 0\n\ts_cbranch_scc1 1f\n\t" E_LOADS "1:" : "={v[104:107]}"(eg[0]), "={v[108:111]}"(eg[1]), "={v[112:115]}"(eg[2]), "={v[116:119]}"(eg[3]) \
        : [a0] "v"(eo[0]), [a1] "v"(eo[1]), [b0] "s"(ws + WS_E), [b1] "s"(ws + WS_E + 16384), [on] "s"(pneg) : "scc", "memory")
    {
        asm volatile("s_nop 7" : "+v"(acc[0][0]), "+v"(acc[0][1]), "+v"(acc[1][0]), "+v"(acc[1][1]), "+v"(acc[2][0]), "+v"(acc[2][1]), "+v"(acc[3][0]), "+v"(acc[3][1]));
#pragma unroll
        for (int p = 0; p < 8; ++p) { ur[p] = f32x2{acc[p >> 1][0][2 * (p & 1)], acc[p >> 1][0][2 * (p & 1) + 1]}; ui[p] = f32x2{acc[p >> 1][1][2 * (p & 1)], acc[p >> 1][1][2 * (p & 1) + 1]}; }
        f32x2 gr = ur[0], gi = ui[0];
#pragma unroll
        for (int p = 1; p < 8; ++p) { f32x2 nr, ni; pk_cmad(nr, ni, L1, gr, gi, ur[p], ui[p]); gr = nr; gi = ni; }
        float fr, fi;
        cmad(fr, fi, L8r, L8i, gr[0], gi[0], gr[1], gi[1]);
        {
            const f32x2 w = cmul4(V1, f32x2{fr, fi});
            const float wr = sum4(w[0]), wi = sum4(w[1]);
            const u32x4 gq = {__float_as_uint(wr), tagA, __float_as_uint(wi), tagG};
            if (EREP ? (EALL || g4 >= (tm >> 2)) : g4 == 0) GST_B128_SC1(est, gq, ws + WS_E, 0);
        }
        *LDSP(f32x2, plr + 7 * 512) = f32x2{fr, fi};
        *LDSP(unsigned, pes + 8) = 0;
        TS(21);
        if (EP == 2) E_POLL();
        auto coef = [&]() {
#pragma unroll
            for (int p = 0; p < 8; ++p) { f32x2 nr, ni; pk_conj_mul(nr, ni, C1, ur[p], ui[p]); ur[p] = nr; ui[p] = ni; TIE(ur[p]); TIE(ui[p]); }
        };
        if (CFB) coef();
        WAIT_L(0); BAR();
        f32x2 e[7];
#pragma unroll
        for (int j = 0; j < 7; ++j) e[j] = *LDSP(f32x2, plr + j * 512);
        if (EP == 1) E_POLL();
        if (!CFB) coef();
#pragma unroll
        for (int j = 1; j < 7; ++j) cmac2(e[j], L16, e[j - 1]);
        float mr, mi;
        cmad(mr, mi, L8r, L8i, e[6][0], e[6][1], gr[0], gi[0]);
        pk_conj_mul(hr, hi, C1, f32x2{e[6][0], mr}, f32x2{e[6][1], mi});
        TIE(hr); TIE(hi);
        TS(4);
    }
    f32x2 G;
    {
        float pr = 0.f, pi = 0.f;
        if (!EP) E_POLL();
        if (pneg >= 0) {
            unsigned t0, t1, t2, left = SPIN_CAP;
            asm volatile("s_branch 2f\n1:\n\ts_sleep %[sl]\n\t" E_LOADS "2:\n\ts_waitcnt vmcnt(0)\n\t"
                         "v_bitop3_b32 %[t0], v105, v109, %[ta] bitop3:0x7e\n\tv_bitop3_b32 %[t1], v113, v117, %[ta] bitop3:0x7e\n\tv_bitop3_b32 %[t2], v107, v111, %[tg] bitop3:0x7e\n\t"
                         "v_or3_b32 %[t0], %[t0], %[t1], %[t2]\n\tv_bitop3_b32 %[t1], v115, v119, %[tg] bitop3:0x7e\n\tv_or_b32 %[t0], %[t0], %[t1]\n\t"
                         "v_cmp_ne_u32 vcc, 0, %[t0]\n\ts_cbranch_vccz 3f\n\ts_sub_u32 %[n], %[n], 1\n\ts_cbranch_scc0 1b\n3:"
                         : "+{v[104:107]}"(eg[0]), "+{v[108:111]}"(eg[1]), "+{v[112:115]}"(eg[2]), "+{v[116:119]}"(eg[3]), [t0] "=&v"(t0), [t1] "=&v"(t1), [t2] "=&v"(t2), [n] "+s"(left)
                         : [a0] "v"(eo[0]), [a1] "v"(eo[1]), [b0] "s"(ws + WS_E), [b1] "s"(ws + WS_E + 16384), [ta] "s"(tagA), [tg] "s"(tagG), [sl] "n"(ESL) : "vcc", "scc", "memory");
            float ar = __uint_as_float(eg[1][0]), ai = __uint_as_float(eg[1][2]), br = __uint_as_float(eg[3][0]), bi = __uint_as_float(eg[3][2]);
            cmac(ar, ai, L64r, L64i, __uint_as_float(eg[0][0]), __uint_as_float(eg[0][2]));
            cmac(br, bi, L64r, L64i, __uint_as_float(eg[2][0]), __uint_as_float(eg[2][2]));
            if (EALL) { if (j0 < 0) ar = ai = 0.f; if (j0 + 2 < 0) br = bi = 0.f; }
            const f32x2 pu = cmul4(f32x2{ct[4][2], ct[4][3]}, f32x2{ar, ai});
            pr = pu[0]; pi = pu[1];
            cmac(pr, pi, ct[4][0], ct[4][1], br, bi);
            pr = sum4(pr); pi = sum4(pi);
        }
        TS(5);
        *LDSP(u32x4, pes) = u32x4{__float_as_uint(pr), __float_as_uint(pi), 1u, 0u};
        u32x4 o;
        asm volatile("1:\n\tds_read_b128 %0, %1\n\ts_waitcnt lgkmcnt(0)\n\tv_cmp_eq_u32 vcc, 0, v122\n\ts_cbranch_vccnz 1b" : "=&{v[120:123]}"(o) : "v"(po) : "vcc", "memory");
        G = f32x2{pr + __uint_as_float(o[0]), pi + __uint_as_float(o[1])};
    }
    TS(6);
    {
        pk_conj_mac(hr, hi, G, Q2r, Q2i);
#pragma unroll
        for (int p = 0; p < 8; ++p) { pk_cmac_conj(ur[p], ui[p], L1, hr, hi); hr = ur[p]; hi = ui[p]; }
        const char* hd = ws + WS_H + (size_t)((tm * 4 + (wm & 2)) * 2 + wn) * HP + (tn * 4 + (wm & 1) * 2) * 1024;
#pragma unroll
        for (int e2 = 0; e2 < 2; ++e2)
#pragma unroll
            for (int c = 0; c < 2; ++c) {
                const f32x2* o = c ? ui : ur;
                *LDSP(u32x4, hw[e2] + c * 512) = u32x4{pack2(o[0][e2], o[1][e2]), pack2(o[2][e2], o[3][e2]), pack2(o[4][e2], o[5][e2]), pack2(o[6][e2], o[7][e2])};
            }
        v4i16 t[4][2];
#pragma unroll
        for (int n = 0; n < 4; ++n)
#pragma unroll
            for (int j = 0; j < 2; ++j) t[n][j] = __builtin_amdgcn_ds_read_tr16_b64_v4i16(LDSP(v4i16, ta[j] + n * 1024));
        sy_post<1>(pmine);
        static_for<0, 4>([&](auto ic) {
            constexpr int n = decltype(ic)::value;
            const bf16x8 f = __builtin_shufflevector(t[n][0], t[n][1], 0, 1, 2, 3, 4, 5, 6, 7);
            GST_B128_SC1(l16, f, hd + (n >> 1) * 2 * HP + ((n & 1) ^ wn) * 1024, 0);
        });
    }
    TS(7);
    bf16x8 fa2[AW];
    u32x2 xv[4], dsv;
    const char* Aw = ws + WS_H + (size_t)((tm * 4 + wm) * 2 + wn) * HP;
    const char* g2src = wn ? a.cim : a.cre;
    const unsigned g2dst = lb + L_C + wid * 2048;
#define G2_ISSUE(st, i) DMA(g2dst + (((st) + 2) & 3) * G2STAGE + (i) * 1024, g2v[i], g2src + (SLICE(st) << 7))
#define G2_A(g) GLD_B128_SC1(fa2[(g) % AW], l16, Aw + (SLICE((g) >> 2) << 12), ((g) & 3) * 1024)
    {
        {
            sy_wait<1>(pown);
            s16x4 fo[4][2]; bf16x8 c[8];
            static_for<0, 2>([&](auto kc) {
                constexpr int ks = decltype(kc)::value;
                static_for<0, 4>([&](auto ic) { constexpr int i = decltype(ic)::value; dsrtr_<ks * 4096>(fo[2 * ks + (i >> 1)][i & 1], to[i >> 1][i & 1]); });
                static_for<0, 4>([&](auto ic) { constexpr int i = decltype(ic)::value; DSR128(c[4 * ks + i], cad[ks][i & 1], 2 * G2STAGE + i * 2048); });
            });
            static_for<0, 8>([&](auto ic) {
                constexpr int n = decltype(ic)::value, ks = n >> 2, i = n & 3;
                WAIT_L(ks ? 3 - i : 11 - i);
                TIE(c[n]);
                if constexpr (i == 0) { TIE(fo[2 * ks][0]); TIE(fo[2 * ks][1]); TIE(fo[2 * ks + 1][0]); TIE(fo[2 * ks + 1][1]); }
#pragma unroll
                for (int j = 0; j < 2; ++j)
                    acc[i][j] = __builtin_amdgcn_mfma_f32_16x16x32_bf16(__builtin_shufflevector(fo[2 * ks + j][0], fo[2 * ks + j][1], 0, 1, 2, 3, 4, 5, 6, 7), c[n], ks ? acc[i][j] : f32x4{0, 0, 0, 0}, 0, 0, 0);
                SB();
            });
#pragma unroll
            for (int i = 0; i < 4; ++i) { TIE(acc[i][0]); TIE(acc[i][1]); }
        }
        if (CEARLY) sy_post<0>(pmine);
        static_for<0, 4>([&](auto ic) { constexpr int it = decltype(ic)::value; GLD_B64(xv[it], xo, a.x, it * 2048 - 4096); });
        GLD_B64(dsv, dso, a.dskip, 0);
        if (CEARLY) {
            sy_wait<0>(pall);
            static_for<4, 4 + CEARLY>([&](auto ic) { constexpr int n = decltype(ic)::value; G2_ISSUE(n >> 1, n & 1); });
        }
        TS(16);
        WAIT_V(5 + CEARLY);
        sy_post<2>(pmine);
        if (!CEARLY || wid == (NOOWN ? 1 : 0) || (NOOWN == 1 && wid == 0)) sy_wait<2, 0>(pall);
        const u32x2 tagv = {__float_as_uint(ct[2][2]), __float_as_uint(ct[2][3])};
        if (wid == (NOOWN ? 1 : 0)) asm volatile("s_mov_b64 exec, 0xffff\n\tglobal_store_dwordx2 %0, %1, %2 sc1\n\ts_mov_b64 exec, -1" :: "v"(fst), "v"(tagv), "s"(flg + FL_H) : "memory");
        if (PFIRST) asm volatile("s_cmp_lg_u32 %3, 0\n\ts_cbranch_scc1 1f\n\tglobal_load_dwordx2 %0, %1, %2 sc1\n1:" : "=&v"(fl) : "v"(fld), "s"(flg + FL_H), "s"(wid) : "scc", "memory");
        TS(8);
        static_for<4 + CEARLY, 10>([&](auto ic) { constexpr int n = decltype(ic)::value; G2_ISSUE(n >> 1, n & 1); });
        TS(13);
        if (wid == 0) {
            unsigned left = SPIN_CAP;
            if (PFIRST)
            asm volatile("s_waitcnt vmcnt(6)\n\ts_branch 3f\n1:\n\tglobal_load_dwordx2 %0, %2, %3 sc1\n\ts_waitcnt vmcnt(0)\n3:\n\tv_cmp_ne_u64 vcc, %0, %4\n\ts_cbranch_vccz 2f\n\ts_sub_u32 %1, %1, 1\n\ts_cbranch_scc0 1b\n2:"
                         : "+v"(fl), "+s"(left) : "v"(fld), "s"(flg + FL_H), "v"(tagv) : "vcc", "scc", "memory");
            else
            asm volatile("1:\n\tglobal_load_dwordx2 %0, %2, %3 sc1\n\ts_waitcnt vmcnt(0)\n\tv_cmp_ne_u64 vcc, %0, %4\n\ts_cbranch_vccz 2f\n\ts_sub_u32 %1, %1, 1\n\ts_cbranch_scc0 1b\n2:"
                         : "=&v"(fl), "+s"(left) : "v"(fld), "s"(flg + FL_H), "v"(tagv) : "vcc", "scc", "memory");
            sy_post<3>(pmine);
            if (b == 255 && lane == 0) GST_B64_SC1(0u * lane, tagv, flg + FL_EPOCH);
        } else sy_wait<3>(pzero);
        TS(20);
        static_for<4, 4 + AW>([&](auto ic) { constexpr int g = decltype(ic)::value; G2_A(g); });
    }
    TS(9);
    __builtin_amdgcn_s_setprio(0);
    f32x4 y[4];
    {
        bf16x8 fb[2][4];
#define G2_FRAG(g, i) DSR128(fb[(g) & 1][i], cad[(g) & 1][(i) & 1], ((((g) >> 1) + 2) & 3) * G2STAGE + (i) * 2048)
        static_for<0, 4>([&](auto ic) { constexpr int i = decltype(ic)::value; G2_FRAG(2, i); });
        static_for<2, 32>([&](auto ic) {
            constexpr int g = decltype(ic)::value, gn = g + 1;
            constexpr bool bar = (gn & 1) == 0 && gn < 32;
            if constexpr (bar) {
                constexpr int ca = g2_cnt(2 * g, 2 * g), cs = gn >= 10 ? g2_cnt(100 + gn - 6, 2 * g) : 63;
                static_assert(cs >= 0, "schedule");
                WAIT_V(AWB && ca < cs ? ca : cs); WAIT_L(0); BAR();
                if constexpr (!AWB) WAIT_V(ca);
                if constexpr (g == 3) TS(10);
                if constexpr (g == 17 || g == 25) TS(26 + (g >> 3));
#pragma unroll
                for (int i = 0; i < 4; ++i) TIE(fb[g & 1][i]);
                TIE(fa2[(2 * g) % AW]);
            }
            static_for<0, 8>([&](auto mc) {
                constexpr int m = decltype(mc)::value, i = m & 3, j = m >> 2, f = 2 * g + j;
                static_assert(g2_cnt(f, f) >= 0, "schedule");
                if constexpr (i == 0 && (j || !bar)) { WAIT_V(g2_cnt(f, f)); TIE(fa2[f % AW]); }
                if constexpr (!bar && j == 0) { WAIT_L(gn < 32 ? 3 : 3 - i); TIE(fb[g & 1][i]); }
                acc[i][j] = __builtin_amdgcn_mfma_f32_16x16x32_bf16(fa2[f % AW], fb[g & 1][i], acc[i][j], 0, 0, 0);
                SB();
                if constexpr (gn < 32 && m < 4) G2_FRAG(gn, m);
                if constexpr (m == 5 && g2_dma(g)) G2_ISSUE(gn / 2 + 3, gn & 1);
                if constexpr (i == 3 && f + AW < 64) G2_A(f + AW);
                if constexpr (g >= YS && g < YS + 4 && m == 6) {
                    constexpr int it = g - YS;
                    TIE(xv[it]); TIE(dsv);
                    y[it] = f32x4{rb1(bflo(xv[it][0]) * bflo(dsv[0])), rb1(bfhi(xv[it][0]) * bfhi(dsv[0])), rb1(bflo(xv[it][1]) * bflo(dsv[1])), rb1(bfhi(xv[it][1]) * bfhi(dsv[1]))};
                    TIE(y[it]);
                }
                SB();
            });
        });
#undef G2_FRAG
    }
    TS(11);
    {
#pragma unroll
        for (int i = 0; i < 4; ++i) *LDSP(f32x4, lb + L_Y2 + wid * 4096 + l16 + i * 1024) = acc[i][1];
        sy_post<4>(pmine);
#pragma unroll
        for (int r = 0; r < 4; ++r) {
            y[r] = f32x4{2.f * acc[0][0][r] + y[r][0], 2.f * acc[1][0][r] + y[r][1], 2.f * acc[2][0][r] + y[r][2], 2.f * acc[3][0][r] + y[r][3]};
            TIE(y[r]);
        }
        sy_wait<4, 0>(pmine ^ 16);
        char* const ob = a.out + (size_t)(tm * 128 + wm * 32 + wn * 16) * 8192 + 4096 + tn * 256;
        f32x4 w[4];
#pragma unroll
        for (int i = 0; i < 4; ++i) w[i] = *LDSP(f32x4, lb + L_Y2 + (wid ^ 4) * 4096 + l16 + i * 1024);
#pragma unroll
        for (int r = 0; r < 4; ++r) {
            const f32x4 o = {2.f * w[0][r] + y[r][0], 2.f * w[1][r] + y[r][1], 2.f * w[2][r] + y[r][2], 2.f * w[3][r] + y[r][3]};
            GST_B128_NT(g4 * 32768 + l15 * 16, o, ob + r * 8192, 0);
        }
    }
    asm volatile(".Lfk_code_b:");
    TSE(12);
    TS_FLUSH();
}

}

extern "C" void kernel_launch(void* const* d_in, const int* in_sizes, int n_in, void* d_out, int out_size, void* d_ws, size_t ws_size,
                              hipStream_t stream) {
    (void)in_sizes; (void)n_in; (void)out_size; (void)ws_size;
    fk::Args a;
    a.q = (const char*)d_in[0]; a.k = (const char*)d_in[1]; a.v = (const char*)d_in[2]; a.sk = (const char*)d_in[3];
    a.ssmk = (const char*)d_in[4]; a.ssmD = (const char*)d_in[5]; a.x = (const char*)d_in[6]; a.lre = (const char*)d_in[7];
    a.lim = (const char*)d_in[8]; a.bre = (const char*)d_in[9]; a.bim = (const char*)d_in[10]; a.cre = (const char*)d_in[11];
    a.cim = (const char*)d_in[12]; a.logdt = (const char*)d_in[13]; a.dskip = (const char*)d_in[14];
    a.out = (char*)d_out; a.ws = (char*)d_ws;
    hipLaunchKernelGGL(fk::fused, dim3(256), dim3(768), 0, stream, a);
}
```

```cpp
#include <hip/hip_runtime.h>
#include <stdint.h>
#include <type_traits>

namespace fk {

constexpr int S = 2048, D = 1024, P = 1024;

typedef __attribute__((ext_vector_type(8))) short bf16x8;
typedef __attribute__((ext_vector_type(4))) short s16x4;
typedef __attribute__((ext_vector_type(16))) float f32x16;
typedef __attribute__((ext_vector_type(4))) float f32x4;
typedef __attribute__((ext_vector_type(2))) float f32x2;
typedef __attribute__((ext_vector_type(4))) unsigned u32x4;
typedef __attribute__((ext_vector_type(2))) unsigned u32x2;
#define LDSP(T, a) ((__attribute__((address_space(3))) T*)(uintptr_t)(unsigned)(a))

__device__ __forceinline__ float bf2f(unsigned v) { return __uint_as_float(v << 16); }
__device__ __forceinline__ float bflo(unsigned w) { return __uint_as_float(w << 16); }
__device__ __forceinline__ float bfhi(unsigned w) { return __uint_as_float(w & 0xffff0000u); }
__device__ __forceinline__ uint16_t f2bf(float f) { __bf16 b = (__bf16)f; return __builtin_bit_cast(uint16_t, b); }
__device__ __forceinline__ float rbf(float f) { return bf2f(f2bf(f)); }
typedef __attribute__((ext_vector_type(2))) __bf16 bf16x2;
__device__ __forceinline__ unsigned pack2(float lo, float hi) { return __builtin_bit_cast(unsigned, __builtin_convertvector(f32x2{lo, hi}, bf16x2)); }
__device__ __forceinline__ float rb1(float x) { return __uint_as_float(pack2(0.f, x)); }
__device__ __forceinline__ void cmac(float& xr, float& xi, float ar, float ai, float hr, float hi) {
    asm("v_fmac_f32 %0, %2, %4\n\tv_fmac_f32 %1, %2, %5\n\tv_fma_f32 %0, -%3, %5, %0\n\tv_fmac_f32 %1, %3, %4" : "+v"(xr), "+v"(xi) : "v"(ar), "v"(ai), "v"(hr), "v"(hi));
}
__device__ __forceinline__ void cmad(float& o_r, float& o_i, float ar, float ai, float hr, float hi, float er, float ei) {
    asm("v_fma_f32 %0, %2, %4, %6\n\tv_fma_f32 %1, %2, %5, %7\n\tv_fma_f32 %0, -%3, %5, %0\n\tv_fmac_f32 %1, %3, %4" : "=&v"(o_r), "=&v"(o_i) : "v"(ar), "v"(ai), "v"(hr), "v"(hi), "v"(er), "v"(ei));
}
__device__ __forceinline__ f32x2 cmul4(f32x2 a, f32x2 h) {
    float o_r, o_i;
    asm("v_mul_f32 %0, %2, %4\n\tv_mul_f32 %1, %2, %5\n\tv_fma_f32 %0, -%3, %5, %0\n\tv_fmac_f32 %1, %3, %4" : "=&v"(o_r), "=&v"(o_i) : "v"(a[0]), "v"(a[1]), "v"(h[0]), "v"(h[1]));
    return f32x2{o_r, o_i};
}

#define PK_LO " op_sel:[0,0,0] op_sel_hi:[0,1,1]"
#define PK_HI " op_sel:[1,0,0] op_sel_hi:[1,1,1]"
#define PK_NEG " neg_lo:[1,0,0] neg_hi:[1,0,0]"
__device__ __forceinline__ void pk_cmad(f32x2& o_r, f32x2& o_i, f32x2 a, f32x2 hr, f32x2 hi, f32x2 er, f32x2 ei) {
    asm("v_pk_fma_f32 %0, %2, %3, %5" PK_LO "\n\tv_pk_fma_f32 %1, %2, %4, %6" PK_LO "\n\tv_pk_fma_f32 %0, %2, %4, %0" PK_HI PK_NEG "\n\tv_pk_fma_f32 %1, %2, %3, %1" PK_HI
        : "=&v"(o_r), "=&v"(o_i) : "v"(a), "v"(hr), "v"(hi), "v"(er), "v"(ei));
}
__device__ __forceinline__ void pk_cmac_conj(f32x2& xr, f32x2& xi, f32x2 a, f32x2 hr, f32x2 hi) {
    asm("v_pk_fma_f32 %0, %2, %3, %0" PK_LO "\n\tv_pk_fma_f32 %1, %2, %4, %1" PK_LO "\n\tv_pk_fma_f32 %0, %2, %4, %0" PK_HI "\n\tv_pk_fma_f32 %1, %2, %3, %1" PK_HI PK_NEG
        : "+v"(xr), "+v"(xi) : "v"(a), "v"(hr), "v"(hi));
}
__device__ __forceinline__ void pk_conj_mul(f32x2& o_r, f32x2& o_i, f32x2 a, f32x2 hr, f32x2 hi) {
    asm("v_pk_mul_f32 %0, %2, %3 op_sel:[0,0] op_sel_hi:[0,1]\n\tv_pk_mul_f32 %1, %2, %4 op_sel:[0,0] op_sel_hi:[0,1] neg_lo:[1,0] neg_hi:[1,0]\n\t"
        "v_pk_fma_f32 %0, %2, %4, %0" PK_HI PK_NEG "\n\tv_pk_fma_f32 %1, %2, %3, %1" PK_HI PK_NEG : "=&v"(o_r), "=&v"(o_i) : "v"(a), "v"(hr), "v"(hi));
}
__device__ __forceinline__ void pk_conj_mac(f32x2& xr, f32x2& xi, f32x2 a, f32x2 hr, f32x2 hi) {
    asm("v_pk_fma_f32 %0, %2, %3, %0" PK_LO "\n\tv_pk_fma_f32 %1, %2, %4, %1" PK_LO PK_NEG "\n\tv_pk_fma_f32 %0, %2, %4, %0" PK_HI PK_NEG "\n\tv_pk_fma_f32 %1, %2, %3, %1" PK_HI PK_NEG
        : "+v"(xr), "+v"(xi) : "v"(a), "v"(hr), "v"(hi));
}
__device__ __forceinline__ void cmac2(f32x2& x, f32x2 a, f32x2 h) {
    asm("v_pk_fma_f32 %0, %1, %2, %0" PK_LO "\n\tv_pk_fma_f32 %0, %1, %2, %0 op_sel:[1,1,0] op_sel_hi:[1,0,1] neg_lo:[1,0,0]" : "+v"(x) : "v"(a), "v"(h));
}
__device__ __forceinline__ float sum4(float x) {
    const auto p = __builtin_amdgcn_permlane32_swap(__float_as_uint(x), __float_as_uint(x), false, false);
    x = __uint_as_float(p[0]) + __uint_as_float(p[1]);
    const auto q = __builtin_amdgcn_permlane16_swap(__float_as_uint(x), __float_as_uint(x), false, false);
    return __uint_as_float(q[0]) + __uint_as_float(q[1]);
}

template <int I, int N, class F>
__device__ __forceinline__ void static_for(F&& f) { if constexpr (I < N) { f(std::integral_constant<int, I>{}); static_for<I + 1, N>(f); } }

#define TS_DECL
#define TS0a()
#define TS0b()
#define TSE(i)
#define TS(i)
#define TS_FLUSH()

#define WAIT_V(n) asm volatile("s_waitcnt vmcnt(%0)" ::"n"((n) > 63 ? 63 : (n)) : "memory")
#define WAIT_L(n) asm volatile("s_waitcnt lgkmcnt(%0)" ::"n"((n) > 15 ? 15 : (n)) : "memory")
#define BAR() asm volatile("s_barrier" ::: "memory")
#define SB() __builtin_amdgcn_sched_barrier(0)
template <class T> __device__ __forceinline__ void tie_(T& x) { asm volatile("" : "+v"(x)); }
#define TIE(x) tie_(x)
__device__ __forceinline__ unsigned take(unsigned x) { unsigned y; asm volatile("v_mov_b32 %0, %1" : "=v"(y) : "v"(x)); return y; }
#define DEF_LD(name, ins) template <int OFF, class T> __device__ __forceinline__ void name(T& dst, unsigned voff, const char* sb) { \
    asm volatile(ins : "=v"(dst) : "v"(voff), "s"(sb), "n"(OFF) : "memory"); }
DEF_LD(gld_u16_, "global_load_ushort %0, %1, %2 offset:%3")
DEF_LD(gld_b32_, "global_load_dword %0, %1, %2 offset:%3")
DEF_LD(gld_b64_, "global_load_dwordx2 %0, %1, %2 offset:%3")
DEF_LD(gld_b128_, "global_load_dwordx4 %0, %1, %2 offset:%3")
#define NTQ " nt"
#define NTK " nt"
DEF_LD(gld_b64_nt_, "global_load_dwordx2 %0, %1, %2 offset:%3" NTQ)
DEF_LD(gld_b128_nt_, "global_load_dwordx4 %0, %1, %2 offset:%3" NTQ)
DEF_LD(gld_b64_sc1_, "global_load_dwordx2 %0, %1, %2 offset:%3 sc1")
DEF_LD(gld_b128_sc1_, "global_load_dwordx4 %0, %1, %2 offset:%3 sc1")
#define DEF_ST(name, ins) template <int OFF, class T> __device__ __forceinline__ void name(unsigned voff, const T& v, const char* sb) { \
    asm volatile(ins :: "v"(voff), "v"(v), "s"(sb), "n"(OFF) : "memory"); }
DEF_ST(gst_b128_, "global_store_dwordx4 %0, %1, %2 offset:%3\n\ts_nop 1")
DEF_ST(gst_b128_nt_, "global_store_dwordx4 %0, %1, %2 offset:%3 nt\n\ts_nop 1")
DEF_ST(gst_b128_sc1_, "global_store_dwordx4 %0, %1, %2 offset:%3 sc1\n\ts_nop 1")
DEF_ST(gst_b64_sc1_, "global_store_dwordx2 %0, %1, %2 offset:%3 sc1")
#define GLD_U16(dst, voff, sb) gld_u16_<0>(dst, voff, sb)
#define GLD_B32(dst, voff, sb, off) gld_b32_<(off)>(dst, voff, sb)
#define GLD_B64(dst, voff, sb, off) gld_b64_<(off)>(dst, voff, sb)
#define GLD_B128(dst, voff, sb, off) gld_b128_<(off)>(dst, voff, sb)
#define GLD_B64_NT(dst, voff, sb, off) gld_b64_nt_<(off)>(dst, voff, sb)
#define GLD_B128_NT(dst, voff, sb, off) gld_b128_nt_<(off)>(dst, voff, sb)
#define GLD_B64_SC1(dst, voff, sb, off) gld_b64_sc1_<(off)>(dst, voff, sb)
#define GLD_B128_SC1(dst, voff, sb, off) gld_b128_sc1_<(off)>(dst, voff, sb)
#define GST_B128(voff, v, sb, off) gst_b128_<(off)>(voff, v, sb)
#define GST_B128_NT(voff, v, sb, off) gst_b128_nt_<(off)>(voff, v, sb)
#define GST_B128_SC1(voff, v, sb, off) gst_b128_sc1_<(off)>(voff, v, sb)
#define GST_B64_SC1(voff, v, sb) gst_b64_sc1_<0>(voff, v, sb)
__device__ __forceinline__ void dma_(unsigned ldsaddr, unsigned voff, const char* sb) {
    asm volatile("s_mov_b32 m0, %0\n\ts_nop 0\n\tglobal_load_lds_dwordx4 %1, %2" :: "s"(ldsaddr), "v"(voff), "s"(sb) : "memory"); }
__device__ __forceinline__ void dma_nt_(unsigned ldsaddr, unsigned voff, const char* sb) {
    asm volatile("s_mov_b32 m0, %0\n\ts_nop 0\n\tglobal_load_lds_dwordx4 %1, %2 nt" :: "s"(ldsaddr), "v"(voff), "s"(sb) : "memory"); }
#define DMA(ldsaddr, voff, sb) dma_(ldsaddr, voff, sb)
template <int OFF, class T> __device__ __forceinline__ void dsr128_(T& dst, unsigned addr) { asm volatile("ds_read_b128 %0, %1 offset:%2" : "=v"(dst) : "v"(addr), "n"(OFF)); }
template <int OFF, class T> __device__ __forceinline__ void dsr64_(T& dst, unsigned addr) { asm volatile("ds_read_b64 %0, %1 offset:%2" : "=v"(dst) : "v"(addr), "n"(OFF)); }
template <int OFF, class T> __device__ __forceinline__ void dsr2_(T& dst, unsigned addr) { asm volatile("ds_read2_b32 %0, %1 offset0:%2 offset1:%3" : "=v"(dst) : "v"(addr), "n"(OFF), "n"(OFF + 1)); }
#define DSR128(dst, addr, off) dsr128_<(off)>(dst, addr)
#define DSR64(dst, addr, off) dsr64_<(off)>(dst, addr)

__device__ __forceinline__ void kv4(unsigned& o01, unsigned& o23, unsigned ka, unsigned kb, unsigned kc, unsigned kd, const unsigned* w, unsigned v0, unsigned v1) {
    unsigned t1, t3, p0, p1, p2, p3;
    asm(
        "v_dot2_f32_bf16 %0, %8, %19, 0\n\tv_dot2_f32_bf16 %1, %9, %18, 0\n\tv_dot2_f32_bf16 %2, %9, %19, 0\n\tv_dot2_f32_bf16 %3, %10, %18, 0\n\t"
        "v_dot2_f32_bf16 %4, %9, %16, 0\n\tv_dot2_f32_bf16 %5, %9, %17, 0\n\tv_dot2_f32_bf16 %6, %10, %16, 0\n\tv_dot2_f32_bf16 %7, %10, %17, 0\n\t"
        "v_cvt_pk_bf16_f32 %0, %0, %4\n\tv_cvt_pk_bf16_f32 %1, %1, %5\n\tv_cvt_pk_bf16_f32 %2, %2, %6\n\tv_cvt_pk_bf16_f32 %3, %3, %7\n\t"
        "v_dot2_f32_bf16 %4, %9, %15, 0\n\tv_dot2_f32_bf16 %5, %10, %14, 0\n\tv_dot2_f32_bf16 %6, %10, %15, 0\n\tv_dot2_f32_bf16 %7, %11, %14, 0\n\t"
        "v_dot2_f32_bf16 %0, %0, %22, 0\n\tv_dot2_f32_bf16 %1, %1, %22, 0\n\tv_dot2_f32_bf16 %2, %2, %22, 0\n\tv_dot2_f32_bf16 %3, %3, %22, 0\n\t"
        "v_cvt_pk_bf16_f32 %0, %0, %4\n\tv_cvt_pk_bf16_f32 %1, %1, %5\n\tv_cvt_pk_bf16_f32 %2, %2, %6\n\tv_cvt_pk_bf16_f32 %3, %3, %7\n\t"
        "v_dot2_f32_bf16 %4, %10, %12, 0\n\tv_dot2_f32_bf16 %5, %10, %13, 0\n\tv_dot2_f32_bf16 %6, %11, %12, 0\n\tv_dot2_f32_bf16 %7, %11, %13, 0\n\t"
        "v_dot2_f32_bf16 %0, %0, %22, 0\n\tv_dot2_f32_bf16 %1, %1, %22, 0\n\tv_dot2_f32_bf16 %2, %2, %22, 0\n\tv_dot2_f32_bf16 %3, %3, %22, 0\n\t"
        "v_cvt_pk_bf16_f32 %0, %0, %4\n\tv_cvt_pk_bf16_f32 %1, %1, %5\n\tv_cvt_pk_bf16_f32 %2, %2, %6\n\tv_cvt_pk_bf16_f32 %3, %3, %7\n\t"
        "v_dot2_f32_bf16 %0, %0, %22, 0\n\tv_dot2_f32_bf16 %1, %1, %22, 0\n\tv_dot2_f32_bf16 %2, %2, %22, 0\n\tv_dot2_f32_bf16 %3, %3, %22, 0\n\t"
        "v_cvt_pk_bf16_f32 %0, %0, 0\n\tv_cvt_pk_bf16_f32 %1, 0, %1\n\tv_cvt_pk_bf16_f32 %2, %2, 0\n\tv_cvt_pk_bf16_f32 %3, 0, %3\n\t"
        "v_dot2_f32_bf16 %0, %0, %20, 0\n\tv_dot2_f32_bf16 %1, %1, %20, 0\n\tv_dot2_f32_bf16 %2, %2, %21, 0\n\tv_dot2_f32_bf16 %3, %3, %21, 0\n\t"
        "s_nop 1\n\tv_cvt_pk_bf16_f32 %0, %1, %0\n\tv_cvt_pk_bf16_f32 %2, %3, %2\n\t"
        : "=&v"(o01), "=&v"(t1), "=&v"(o23), "=&v"(t3), "=&v"(p0), "=&v"(p1), "=&v"(p2), "=&v"(p3)
        : "v"(ka), "v"(kb), "v"(kc), "v"(kd), "v"(w[0]), "v"(w[1]), "v"(w[2]), "v"(w[3]), "v"(w[4]), "v"(w[5]), "v"(w[6]), "v"(w[7]), "v"(v0), "v"(v1), "s"(0x3f803f80u));
}
__device__ __forceinline__ f32x4 mul4bf(u32x2 x, const unsigned* d) {
    float a, b, c, e;
    asm("v_dot2_f32_bf16 %0, %4, %6, 0\n\tv_dot2_f32_bf16 %1, %4, %7, 0\n\tv_dot2_f32_bf16 %2, %5, %8, 0\n\tv_dot2_f32_bf16 %3, %5, %9, 0\n\t"
        "v_cvt_pk_bf16_f32 %0, 0, %0\n\tv_cvt_pk_bf16_f32 %1, 0, %1\n\tv_cvt_pk_bf16_f32 %2, 0, %2\n\tv_cvt_pk_bf16_f32 %3, 0, %3"
        : "=&v"(a), "=&v"(b), "=&v"(c), "=&v"(e) : "v"(x[0]), "v"(x[1]), "v"(d[0]), "v"(d[1]), "v"(d[2]), "v"(d[3]));
    return f32x4{a, b, c, e};
}
template <int BIT> __device__ __forceinline__ void quad_swap(u32x4& a, u32x4& b) {
    unsigned n0, n1, n2, n3, b0 = b[0], b1 = b[1], b2 = b[2], b3 = b[3];
#define QS_(d, s, k) "v_cndmask_b32_dpp " d ", " s ", " k ", vcc quad_perm:[%14,%15,%16,%17] row_mask:0xf bank_mask:0xf\n\t"
    asm("s_mov_b64 vcc, %12\n\ts_nop 0\n\t" QS_("%0", "%4", "%8") QS_("%1", "%5", "%9") QS_("%2", "%6", "%10") QS_("%3", "%7", "%11")
        "s_mov_b64 vcc, %13\n\t" QS_("%4", "%8", "%4") QS_("%5", "%9", "%5") QS_("%6", "%10", "%6") QS_("%7", "%11", "%7")
        : "=&v"(n0), "=&v"(n1), "=&v"(n2), "=&v"(n3), "+v"(b0), "+v"(b1), "+v"(b2), "+v"(b3)
        : "v"(a[0]), "v"(a[1]), "v"(a[2]), "v"(a[3]), "s"(BIT ? 0x3333333333333333ull : 0x5555555555555555ull), "s"(BIT ? 0xccccccccccccccccull : 0xaaaaaaaaaaaaaaaaull),
          "n"(BIT ? 2 : 1), "n"(BIT ? 3 : 0), "n"(BIT ? 0 : 3), "n"(BIT ? 1 : 2) : "vcc");
#undef QS_
    a = u32x4{n0, n1, n2, n3}; b = u32x4{b0, b1, b2, b3};
}
template <int E> __device__ __forceinline__ void sy_post(unsigned myaddr) { asm volatile("ds_write_b32 %0, %0 offset:%1" :: "v"(myaddr), "n"(E * 64) : "memory"); }
template <int E, int SL = 1> __device__ __forceinline__ unsigned sy_wait(unsigned addr) {
    unsigned v;
    asm volatile("s_branch 2f\n1:\n\ts_sleep %3\n2:\n\tds_read_b32 %0, %1 offset:%2\n\ts_waitcnt lgkmcnt(0)\n\tv_cmp_eq_u32 vcc, 0, %0\n\ts_cbranch_vccnz 1b"
                 : "=&v"(v) : "v"(addr), "n"(E * 64), "n"(SL) : "vcc", "memory");
    return v;
}
template <int OFF, class T> __device__ __forceinline__ void dsrtr_(T& dst, unsigned addr) { asm volatile("ds_read_b64_tr_b16 %0, %1 offset:%2" : "=v"(dst) : "v"(addr), "n"(OFF)); }

constexpr size_t WS_H = 0, WS_KVT = (size_t)10 << 20, WS_ZT = (size_t)15 << 20, WS_E = (size_t)20 << 20, WS_FLAG = WS_E + (2048 << 10);
#define HP 65536
#define KP 8192
#define ZP 4096
constexpr int FL_KV = 0, FL_Z = 65536, FL_H = 131072, FL_EPOCH = 196608;
constexpr unsigned SPIN_CAP = 1u << 17;
#define ABL 0
#define CSPLIT 1
#define MERGE 3
#define CB 13
#define PAUSE 0
#define QLATE 0
#define CVPRIO 0
#define STG_SEL (idx & 3)
#define SP0 3
#define SDP 1
#define LP0 3
#define LDP 2
#define KVS 5
#define KVU 3
#define QIT 1
#define ZB 3
#define OS 2
#define MPRIO 2

#define ROT 0x80A5
#define R2 0x41
#define CPF 0
#define PRE 4
#define PRO 12
#define G1NT 0
#define G1NTS 16
#define TMIX 0
#define TLBT 0
#define CDEF 0
#define CVLATE 1
#define FPRIO 3
#define YS 0
#define DP0 1
#define DP1 5
#define AWB 1
#define PK 0
#define PU 13
#define PC0 13
#define PC1 14
#define PC2 15
#define PC3 16
#define PQ 0
#define SPF 0
#define SPH 0
#define NB1 4
#define EP 1
#define ESL 0
#define CFB 0
#define P1 1
#define P0 1
#define BKE 0
#define EALL 0
#define EREP 0
#define TOUCH 0
#define FILLB 0
#define CPOST 1
#define CEARLY 0
#define NOOWN 0
#define PFIRST 0
#define ZB0 0
#define CATCH 4
#define PWE 0
#define SWAP 1
#define KVM 0x100
#define KFD 4
#define ZW 2
#define ZX 1
#define ZP0 3
#define BARM 0x22220
#define NR 6
#define WPS 2
#define CD0 3
#define AWX 8
#define AWN 8
#define CRATE 2
#define ZSB 0
#define ZFB 1
#define ZSL 0
#define OSL 0
constexpr int it_cnt(int p, int k, int last) {
    const int pos[8] = {PK, PU, PC0, PC1, PQ, PU - 1, PC2, PC3}, n[8] = {4, 4, 4, 4, 8, 1, 4, 4};
    int c = 0;
    for (int i = 0; i < 8; ++i) if (pos[i] <= last && (pos[i] > p || (pos[i] == p && i > k))) c += n[i];
    return c;
}
constexpr int G1STAGE = 32768;
constexpr int g1_slot(int s) { return (NB1 == 5 ? (s + 3) % 5 : s & 3) * G1STAGE; }
constexpr int L_KS = NB1 == 5 ? 65536 : 131072, L_VS = L_KS + 16384;
constexpr int L_TS = 3 * G1STAGE, L_TR = L_TS + 3072;
constexpr int CST = 4288;
constexpr int UOFF = 2 * CST + 128, UPL = 2304;
constexpr int CHSZ = UOFF + 2 * UPL + 32;
constexpr int L_AGG = 4 * CHSZ, L_ES = L_AGG + 15 * 512, L_SY = L_ES + 2048;
constexpr int L_HT = 98304;
constexpr int L_C = 98304, G2STAGE = 16384;
constexpr int L_Y = 16384;
static_assert(L_SY + 384 <= 65536 && L_AGG >= 49152 && UOFF % 16 == 0 && CHSZ % 256 == 32, "lds");

__device__ __forceinline__ int img_off(int r, int c) { return (r >> 1) * 256 + ((((r & 1) * 8 + c) ^ ((r >> 1) & 15)) << 4); }

constexpr int g1_upto(int g) {
    int n = PRO;
    for (int t = 0; t <= g; ++t) {
        int sb = (t + 1) / 2, sub = (t + 1) % 2, allowed = sb >= 1 ? 4 * (sb + NB1 - 1) + 2 * (sub + 1) : 4 * NB1;
        if (allowed > 64) allowed = 64;
        if (t >= 2 * LP0 - 1 && t < 2 * LP0 - 1 + PAUSE) continue;
        n = n + CATCH < allowed ? n + CATCH : allowed;
    }
    return n;
}
constexpr int g1_cnt(int s) { return g1_upto(2 * s - 2) - 4 * (s + 1); }
static_assert(g1_upto(28) == 64, "schedule");
constexpr int AW = AWN;
constexpr int g2_place(int s) { return s < 5 ? L_C + ((s + 2) & 3) * G2STAGE : s < 7 ? (s - 1) * G2STAGE : s < 10 ? (9 - s) * G2STAGE : g2_place(s - 9); }
constexpr bool g2_bar(int g) { return (BARM >> g) & 1; }
struct G2Plan { int upto[33], wr[33], ald[64][2], seq[164], use[64]; };
constexpr G2Plan g2_plan() {
    G2Plan p = {};
    int nc = 14 + 2 * NR, nw = 0, nx = 4, lastb = 0, n = 0;
    for (int i = 0; i < 164; ++i) p.seq[i] = 1000;
    for (; nx < 4 + AW; ++nx) p.seq[nx] = ++n;
    for (int t = 0; t < 32; ++t) {
        p.upto[t] = nc; p.wr[t] = nw;
        if (t < 2) continue;
        if (g2_bar(t)) lastb = t;
        const int w = AW + nw < AWX ? AW + nw : AWX;
        for (int f = 2 * t; f < 2 * t + 2; ++f) {
            p.use[f] = n;
            if (nc < 32 && nc == p.upto[t] + (f & 1) && (f & 1) < CRATE && t >= CD0 && lastb >= (nc < 20 ? 1 : (nc | 1) - 18)) p.seq[100 + nc++] = ++n;
            for (int k = 0; k < 2; ++k) {
                p.ald[f][k] = -1;
                if (nx < 64 && nx - f - k < w) { p.ald[f][k] = nx; p.seq[nx++] = ++n; }
            }
        }
        for (int k = 0; k < WPS; ++k) if (nw < 2 * NR && lastb >= (nw < 6 ? 1 : (nw | 1) - 4)) ++nw;
    }
    p.upto[32] = nc; p.wr[32] = nw;
    return p;
}
constexpr G2Plan G2P = g2_plan();
constexpr int g2_upto(int g) { return G2P.upto[g + 1]; }
constexpr int g2_wr(int g) { return G2P.wr[g + 1]; }
constexpr int g2_ald(int f, int k) { return G2P.ald[f][k]; }
constexpr int g2_cnt(int id, int at) { return G2P.use[at] - G2P.seq[id]; }
constexpr int g2_cover(int g) { int s = (g + 1) / 2; while (s < 15 && !g2_bar(2 * s + 1)) ++s; return s; }
static_assert(BARM >> 17 && g2_cover(3) < 7 && CRATE <= 2, "the epilogue needs stage 8 behind a barrier");

constexpr int cv_m(int o) { return (o & 1) ? 7 - (o >> 1) : o >> 1; }
constexpr int cv_first(int o) { int p = 0; for (int i = 0; i < o; ++i) p += 8 - cv_m(i); return p; }
constexpr int cv_grp(int p) { int o = 0; while (cv_first(o + 1) <= p) ++o; return o; }
constexpr int cv_read(int p) {
    int slot[40] = {};
    for (int i = 0; i < 40; ++i) slot[i] = -1;
    slot[0] = 2 * 3 + 1;
    for (int o = 0; o < 8; ++o) {
        int q = cv_first(o + 1) - 1;
        for (int h = 0; h < 2; ++h) { while (slot[q] >= 0) ++q; slot[q] = 2 * (o + 4) + h; }
    }
    const int bs[8] = {1, 3, 5, 11, 12, 13, 19, 21};
    for (int jb = 0; jb < 8; ++jb) slot[bs[jb]] = 32 + jb;
    return slot[p];
}
static_assert(cv_read(35) == 22 && cv_read(36) == 23 && cv_read(37) == -1 && cv_read(10) == 11 && cv_read(15) == 12 && cv_read(18) == 15 && cv_read(24) == 17, "conv schedule");
constexpr int cv_wait(int o) {
    int n = 0;
    for (int q = cv_first(o) - 1;; --q) {
        const int rd = cv_read(q < 0 ? q + 36 : q);
        if (rd == 2 * (o + (q < 0 ? 8 : 0)) + 1) return n;
        if (rd >= 0) ++n;
    }
}

struct Args {
    const char *q, *k, *v, *sk, *ssmk, *ssmD, *x, *lre, *lim, *bre, *bim, *cre, *cim, *logdt, *dskip;
    char *out, *ws;
};

typedef __attribute__((ext_vector_type(8))) unsigned long long u64x8;
__global__ __launch_bounds__(768, 3) void fused(Args) {
    __shared__ __attribute__((aligned(1024))) char shm[163840];
    TS0a();
    u64x8 ka0, ka1; unsigned long long ka2;
    const char* pc0;
    int code_n;
    asm volatile("s_load_dwordx16 %0, %5, 0x0\n\ts_load_dwordx16 %1, %5, 0x40\n\ts_load_dwordx2 %2, %5, 0x80\n\t"
                 "s_getpc_b64 %3\n.Lfk_code_0:\n\ts_mov_b32 %4, .Lfk_code_a-.Lfk_code_0-4\n\ts_max_i32 %4, %4, .Lfk_code_b-.Lfk_code_0-4"
                 : "=&s"(ka0), "=&s"(ka1), "=&s"(ka2), "=&s"(pc0), "=&s"(code_n) : "s"(__builtin_amdgcn_kernarg_segment_ptr()) : "scc");
    const int tid = threadIdx.x, lane = tid & 63, wraw = __builtin_amdgcn_readfirstlane(tid >> 6);
    const int wid = !SWAP ? wraw : wraw < 4 ? wraw + 8 : wraw - 4;
    const int b = blockIdx.x, xcd = b & 7, idx = b >> 3;
    asm volatile("s_cmp_lt_u32 %0, 8\n\ts_cbranch_scc1 1f\n\ts_setprio 3\n\ts_cmp_lg_u32 %0, 8\n\ts_cbranch_scc1 1f\n\t"
                 "v_lshl_add_u32 v161, %1, 7, %2\n\tv_min_i32 v161, %3, v161\n\ts_mov_b64 exec, 0xffff\n\tglobal_load_dword v161, v161, %4\n\ts_mov_b64 exec, -1\n1:"
                 :: "s"(wid), "v"(lane), "s"(idx * 2048), "s"(code_n), "s"(pc0) : "scc", "memory", "v161");
    TS_DECL
    const int tm = (xcd >> 1) * 4 + (idx >> 3), tn = (xcd & 1) * 8 + (idx & 7);
    const int wm = wid & 3, wn = (wid >> 2) & 1, l31 = lane & 31, lh = lane >> 5;
    const unsigned lb = (unsigned)(uintptr_t)shm;
    const unsigned l16 = lane * 16;
    const int l15 = lane & 15, g4 = lane >> 4, sq = 2 * wn + (wm & 1), st = sq * 16 + l15, run = (wm >> 1) * 4 + g4;
    unsigned kvv[4];
    const int trow = idx * 64 * 2048 + xcd * 256;
    if (wid < 8) {
        kvv[0] = trow + wm * 32768 + g4 * 2048 + ((l15 ^ (2 * g4)) << 4);
        kvv[1] = kvv[0] + 8192; kvv[2] = (kvv[0] ^ 128) + 16384; kvv[3] = kvv[2] + 8192;
    } else {
        kvv[0] = trow + (idx ? -4 * 2048 : 0) + g4 * 2048 + wm * 64 + l15 * 4;
        asm volatile("v_writelane_b32 %0, 0, 0\n\tv_writelane_b32 %0, 4, 1\n\tv_writelane_b32 %0, %1, 2" : "+v"(kvv[0]) : "s"((xcd * 64 + idx * 2 + (wm >> 1)) * 4));
        kvv[1] = (xcd * 128 + wm * 32 + l31) * 8;
        kvv[2] = kvv[3] = (tn * 64 + lane) * 2;
    }
    unsigned g1dst = lb + wid * 4096;
    asm volatile("s_waitcnt lgkmcnt(0)" : "+s"(ka0), "+s"(ka1), "+s"(ka2), "+s"(g1dst), "+v"(kvv[0]), "+v"(kvv[1]), "+v"(kvv[2]), "+v"(kvv[3]));
    Args a;
    a.q = (const char*)ka0[0]; a.k = (const char*)ka0[1]; a.v = (const char*)ka0[2]; a.sk = (const char*)ka0[3]; a.ssmk = (const char*)ka0[4]; a.ssmD = (const char*)ka0[5];
    a.x = (const char*)ka0[6]; a.lre = (const char*)ka0[7]; a.lim = (const char*)ka1[0]; a.bre = (const char*)ka1[1]; a.bim = (const char*)ka1[2]; a.cre = (const char*)ka1[3];
    a.cim = (const char*)ka1[4]; a.logdt = (const char*)ka1[5]; a.dskip = (const char*)ka1[6]; a.out = (char*)ka1[7]; a.ws = (char*)ka2;
    char* const ws = a.ws;
    const char* const flg = ws + WS_FLAG;
    TS(26);
    asm volatile("s_cmp_gt_u32 %[w], 7\n\ts_cbranch_scc1 1f\n\t"
                 "s_add_u32 m0, %[m], %[n0]\n\ts_nop 0\n\tglobal_load_lds_dwordx4 %[k0], %[kv]" NTK "\n\ts_add_u32 m0, %[m], %[n0]+1024\n\ts_nop 0\n\tglobal_load_lds_dwordx4 %[k1], %[kv]" NTK "\n\t"
                 "s_add_u32 m0, %[m], %[n0]+2048\n\ts_nop 0\n\tglobal_load_lds_dwordx4 %[k2], %[kv]" NTK "\n\ts_add_u32 m0, %[m], %[n0]+3072\n\ts_nop 0\n\tglobal_load_lds_dwordx4 %[k3], %[kv]" NTK "\n\t"
                 "s_branch 2f\n1:\n\tglobal_load_ushort v164, %[k2], %[c1]\n\tglobal_load_ushort v165, %[k2], %[c2]\n\tglobal_load_ushort v166, %[k2], %[c3]\n\t"
                 "v_mov_b64 v[162:163], %[kv]\n\ts_mov_b64 exec, 7\n\tv_mov_b64 v[162:163], %[d]\n\ts_mov_b64 exec, 3\n\tv_mov_b64 v[162:163], %[e]\n\ts_mov_b64 exec, -1\n\t"
                 "v_mad_u64_u32 v[162:163], vcc, %[k0], 1, v[162:163]\n\tglobal_load_dword v160, v[162:163], off sc1\n\tglobal_load_dwordx2 v[162:163], %[k1], %[sk]\n2:"
                 :
                 : [w] "s"(wid), [c1] "s"(a.logdt), [c2] "s"(a.lre), [c3] "s"(a.lim), [d] "s"(a.ssmD), [e] "s"(flg + FL_EPOCH), [sk] "s"(a.sk),
                   [k0] "v"(kvv[0]), [k1] "v"(kvv[1]), [k2] "v"(kvv[2]), [k3] "v"(kvv[3]), [kv] "s"(wn ? a.v : a.k), [m] "s"(g1dst), [n0] "n"(L_KS)
                 : "scc", "vcc", "memory", "v160", "v161", "v162", "v163", "v164", "v165", "v166");
    TS0b();
    TS(27);
    if (P1) BAR();
#define SLICE_TABLE() unsigned slo, shi; { int ix = idx; asm volatile("" : "+s"(ix)); \
        const int rot2 = (((R2 >> 4) & 15) * (ix >> 3) + (R2 & 15) * (xcd >> 1)) & 15, es = (tn - rot2) & 15; \
        const unsigned long long id = 0xFEDCBA9876543210ull, seq = rot2 ? (id >> (4 * rot2)) | (id << (64 - 4 * rot2)) : id; \
        const unsigned long long lo = (1ull << (4 * es)) - 1, tab = ((seq & lo) << 4) | (seq & ~((lo << 4) | 15)) | tn; \
        slo = (unsigned)tab; shi = (unsigned)(tab >> 32); asm volatile("" : "+s"(slo), "+s"(shi)); }
#define SLICE(st) ((((st) < 8 ? slo : shi) >> (4 * ((st) & 7))) & 15)
    typedef __attribute__((ext_vector_type(4))) short v4i16;
    int role = wid;
    asm volatile("" : "+s"(role));

    if (role >= 8) {
        const int chn = xcd * 128 + idx * 4 + wm;
        const unsigned cb = lb + wm * CHSZ;
        const int r = l15;
        const unsigned fpub = xcd * 256 + idx * 8, fpoll = xcd * 256 + l31 * 8;
        u32x2 fl; u32x4 sw[4], uu[4];
        unsigned tch = 0;
        if (TOUCH) {
            const int ln = wm * 4 + (lane & 3);
            const unsigned fo = (unsigned)(WS_FLAG - WS_E) + (wm == 0 ? FL_H + (tm * 16 + tn) * 128 : (wm == 1 ? FL_KV : FL_Z) + xcd * 256 + (lane & 1) * 128);
            const unsigned o = (lane & 4) && (TOUCH & 2) ? fo : ((tm * 2 + (ln >> 3)) * 1024 + tn * 64) * 16 + (ln & 7) * 128;
            asm volatile("s_mov_b64 exec, 0xff\n\tglobal_load_dword %0, %1, %2\n\ts_mov_b64 exec, -1" : "=&v"(tch) : "v"(o), "s"(ws + WS_E) : "memory");
        }
        SLICE_TABLE();
        TS(1);
        unsigned sm, cl[3]; u32x2 kw0;
        asm volatile("s_waitcnt vmcnt(0)" : "={v160}"(sm), "={v[162:163]}"(kw0), "={v164}"(cl[0]), "={v165}"(cl[1]), "={v166}"(cl[2]) :: "memory", "v161");
        TS(22);
        const unsigned hal = take(sm);
        const unsigned tagA = __builtin_amdgcn_readlane(hal, 0) + 1, tagB = __builtin_amdgcn_readlane(hal, 1) + (tagA == 0 ? 1u : 0u);
        const u32x2 tag = {tagA, tagB};
        f32x2 L1, C1;
        {
            const float dt = rbf(expf(bf2f(take(cl[0]))));
            const float lr = bf2f(take(cl[1])), li = bf2f(take(cl[2]));
            const float e = expf(dt * lr);
            float sn, cs;
            sincosf(dt * li, &sn, &cs);
            L1 = f32x2{e * cs, e * sn};
            const float nr = L1[0] - 1.f, ni = L1[1], den = lr * lr + li * li;
            C1 = f32x2{(nr * lr + ni * li) / den, (ni * lr - nr * li) / den};
            TIE(L1); TIE(C1);
        }
        f32x2 p[11];
        p[0] = L1;
        if (PWE) {
#pragma unroll
            for (int i = 1; i <= 10; ++i) { p[i] = cmul4(p[i - 1], p[i - 1]); TIE(p[i]); }
        }
        {
            if (!PWE) {
#pragma unroll
                for (int i = 1; i <= 10; ++i) p[i] = cmul4(p[i - 1], p[i - 1]);
            }
            const f32x2 one = {1.f, 0.f}, zero = {0.f, 0.f};
            const unsigned ts = lb + L_TS + l16, tr = lb + L_TR + wm * 2048 + l16;
            if (wm == 0) {
                *LDSP(f32x4, ts) = f32x4{L1[0], L1[1], C1[0], C1[1]};
                *LDSP(f32x4, ts + 1024) = f32x4{p[3][0], p[3][1], p[4][0], p[4][1]};
                *LDSP(f32x4, ts + 2048) = f32x4{p[6][0], p[6][1], __uint_as_float(tagA), __uint_as_float(tagB)};
            }
            f32x2 Q[2], V[2], W[2];
            Q[0] = cmul4(cmul4(C1, (wm & 1) ? p[5] : one), (wm & 2) ? p[6] : one); Q[1] = cmul4(Q[0], p[4]);
            V[1] = (wm & 1) ? one : p[5]; V[0] = cmul4(V[1], p[4]);
            W[1] = cmul4((wm & 1) ? one : p[9], (wm & 2) ? one : p[10]); W[0] = cmul4(W[1], p[8]);
#pragma unroll
            for (int i = 0; i < 2; ++i) {
                const f32x2 Q8 = cmul4(Q[i], p[3]), U = 4 * wm + 2 * i >= 16 - tm ? cmul4(W[i], p[7]) : zero, Wz = 4 * wm + 2 * i + 1 >= 16 - tm ? W[i] : zero;
                *LDSP(f32x4, tr + i * 1024) = f32x4{Q[i][0], Q8[0], Q[i][1], Q8[1]};
                *LDSP(f32x4, tr + i * 1024 + 8192) = f32x4{Wz[0], Wz[1], U[0], U[1]};
                *LDSP(f32x2, tr + i * 1024 + 16384) = V[i];
            }
        }
        const unsigned dskl = (__builtin_amdgcn_readlane(hal, 2) >> ((wm & 1) * 16)) & 0xffffu;
        u32x4 kvo[4];
        {
            unsigned kw[2][8], ta[2], th[2];
            u32x2 hm[2];
#pragma unroll
            for (int j = 0; j < 2; ++j) {
                const unsigned x = take(kw0[j]);
                const auto y = __builtin_amdgcn_permlane16_swap(x, x, false, false);
#pragma unroll
                for (int p = 0; p < 2; ++p) { kw[p][4 * j] = y[p] & 0xffffu; kw[p][4 * j + 1] = y[p] << 16; kw[p][4 * j + 2] = y[p] >> 16; kw[p][4 * j + 3] = y[p] & 0xffff0000u; }
            }
#pragma unroll
            for (int p = 0; p < 2; ++p) {
                unsigned h[3];
#pragma unroll
                for (int j = 0; j < 3; ++j) h[j] = (__shfl(hal, p * 8 + (r >> 1) + 16 * (j + 1)) >> ((r & 1) * 16)) & 0xffffu;
                hm[p] = idx ? u32x2{h[0] << 16, h[1] | (h[2] << 16)} : u32x2{0u, 0u};
                const int q = r >> 2, pp = r & 3;
                ta[p] = lb + L_KS + (8 * g4 + q) * 256 + (((wm * 4 + p * 2 + (pp >> 1)) ^ (2 * (q | ((g4 & 1) << 2)))) << 4) + (pp & 1) * 8;
                th[p] = (ta[p] ^ 128) - 1024;
                TIE(hm[p]); TIE(ta[p]); TIE(th[p]);
#pragma unroll
                for (int j = 0; j < 8; ++j) TIE(kw[p][j]);
            }
            WAIT_L(0);
            TS(21);
            BAR();
            TS(2);
            u32x2 kt[4][3], vt[4][2];
#pragma unroll
            for (int u = 0; u < 4; ++u) {
                const int p = u >> 1, o = (u & 1) * 8192;
                auto tr = [&](unsigned ad) -> u32x2 { return __builtin_bit_cast(u32x2, __builtin_amdgcn_ds_read_tr16_b64_v4i16(LDSP(v4i16, ad))); };
                kt[u][0] = tr(th[p] + o); kt[u][1] = tr(ta[p] + o); vt[u][0] = tr(ta[p] + o + 16384); kt[u][2] = tr(ta[p] + o + 1024); vt[u][1] = tr(ta[p] + o + 17408);
                SB();
            }
            TS(14);
            if (KVM & 1) BAR();
#pragma unroll
            for (int u = 0; u < 4; ++u) {
                const int p = u >> 1, oh = u & 1;
                const u32x2 k0 = oh == 0 && g4 == 0 ? hm[p] : kt[u][0];
                unsigned o[4];
                kv4(o[3], o[2], k0[0], k0[1], kt[u][1][0], kt[u][1][1], kw[p], vt[u][0][0], vt[u][0][1]);
                if ((KVM >> (2 * u + 1)) & 1) { TIE(o[3]); BAR(); TIE(vt[u][1]); }
                kv4(o[1], o[0], kt[u][1][0], kt[u][1][1], kt[u][2][0], kt[u][2][1], kw[p], vt[u][1][0], vt[u][1][1]);
                if (u == 0) { TIE(o[0]); TIE(o[2]); WAIT_L(0); }
                kvo[u] = u32x4{o[0], o[1], o[2], o[3]};
                TIE(kvo[u]);
                SB();
                if (u == 0) TS(15); if (u == 1) TS(16); if (u == 2) TS(17); if (u == 3) TS(18);
                if ((KVM >> (2 * u + 2)) & 1) { BAR(); if (u < 3) TIE(vt[u + 1][0]); }
            }
#pragma unroll
            for (int u = 0; u < 4; ++u) TIE(kvo[u]);
        }
        TS(3);
        u32x2 qv[8];
        const char* krow = a.ssmk + (size_t)chn * 4096;
        const char* qs = a.q + (size_t)(idx * 64 + wm * 16) * 2048 + xcd * 256;
        const char* usrc = ws + WS_KVT + (size_t)(xcd * 8 + (idx >> 2)) * 65536 + wm * 16384 + (idx & 3) * 64;
        const unsigned uo = (lane >> 2) * 256 + (lane & 3) * 16;
        const char* cs = ((wm & 2) ? a.cim : a.cre) + (size_t)(tn * 64 + (wm & 1) * 2) * 2048;
        unsigned co[4];
#pragma unroll
        for (int i = 0; i < 4; ++i) {
            const int pair = i * 4 + g4, v = r ^ pair, row = pair * 2 + (v >> 3);
            co[i] = ((row & 15) * 4 + (row >> 4)) * 2048 + (v & 7) * 16;
        }
        bool kv_ok = false;
#pragma unroll
        for (int it = 0; it < 4; ++it) asm volatile("" : "=v"(uu[it]));
        auto items = [&](auto pc) {
            constexpr int p = decltype(pc)::value;
            if constexpr (PK == p) { GLD_B128_NT(sw[0], l16, krow, 0); GLD_B128_NT(sw[1], l16, krow, 1024); GLD_B128_NT(sw[2], l16, krow, 2048); GLD_B128_NT(sw[3], l16, krow, 3072); }
            if constexpr (PU == p) {
                WAIT_V(PK == p ? 4 : 0); TIE(fl);
                kv_ok = __all(fl[0] == tagA && fl[1] == tagB);
                const char* us = kv_ok ? usrc : a.x;
                for (int it = 0; it < 4; ++it) GLD_B128_SC1(uu[it], uo, us + it * 4096, 0);
            }
            if constexpr (PC0 == p) { for (int i = 0; i < 4; ++i) DMA(lb + L_C + 2 * G2STAGE + wm * 4096 + i * 1024, co[i], cs + (SLICE(0) << 7)); }
            if constexpr (PC1 == p) { for (int i = 0; i < 4; ++i) DMA(lb + L_C + 3 * G2STAGE + wm * 4096 + i * 1024, co[i], cs + (SLICE(1) << 7)); }
            if constexpr (PQ == p) static_for<0, 8>([&](auto jc) { constexpr int j = decltype(jc)::value; GLD_B64_NT(qv[j], lh * 8192 + l31 * 8, qs + (j >> 2) * 16384 + (j & 2) * 2048, (j & 1) * 2048); });
            if constexpr (PU == p + 1) GLD_B64_SC1(fl, fpoll, flg + FL_KV, 0);
            if constexpr (PC2 == p) { for (int i = 0; i < 4; ++i) DMA(lb + g2_place(5) + wm * 4096 + i * 1024, co[i], cs + (SLICE(5) << 7)); }
            if constexpr (PC3 == p) { for (int i = 0; i < 4; ++i) DMA(lb + g2_place(6) + wm * 4096 + i * 1024, co[i], cs + (SLICE(6) << 7)); }
        };
#define ITEMS(p) items(std::integral_constant<int, p>{})
        {
            const int ph = STG_SEL, ps = SP0 + ph * SDP, pl = LP0 + ph * LDP;
            const char* kvd = ws + WS_KVT + (size_t)(xcd * 8 + wm * 2) * 65536 + idx * 2048;
            int nb1 = __builtin_popcount(KVM);
            static_assert(__builtin_popcount(KVM) <= SP0 + 1 && SP0 + 3 * SDP + KFD <= 11, "schedule");
#pragma unroll 1
            for (; nb1 <= ps; ++nb1) BAR();
            static_for<0, 4>([&](auto uc) {
                constexpr int u = decltype(uc)::value;
                GST_B128_SC1(l16, kvo[u], kvd + (u >> 1) * 65536, (u & 1) * 1024);
            });
#pragma unroll 1
            for (; nb1 <= pl; ++nb1) {
                if (nb1 == ps + KFD) WAIT_V(0);
                BAR();
                if (nb1 == ps + KFD && wm == 0 && lane == 0) GST_B64_SC1(fpub, tag, flg + FL_KV);
            }
            ITEMS(0);
#pragma unroll 1
            for (; nb1 <= 11; ++nb1) {
                if (nb1 == ps + KFD) WAIT_V(it_cnt(0, -1, 0));
                BAR();
                if (nb1 == ps + KFD && wm == 0 && lane == 0) GST_B64_SC1(fpub, tag, flg + FL_KV);
            }
        }
        asm volatile("" :: "v"(tch));
        ITEMS(11);
        BAR();
        ITEMS(12);
        BAR();
        ITEMS(13);
        BAR();
        ITEMS(14);
        BAR();
        ITEMS(15);
        TS(4);
        const unsigned ud = lb + (lane & 3) * CHSZ + UOFF + ((lane >> 2) & 1) * UPL + (16 + wm * 32 + (lane >> 3)) * 16;
        {
            const u32x4 z4 = {0, 0, 0, 0};
            if (lane < 24) *LDSP(u32x4, lb + L_SY + l16) = z4;
            if (lane < 56) *LDSP(u32x4, lb + L_AGG + wm * 896 + l16) = z4;
            *LDSP(u32x4, lane < 32 ? cb + ((lane >> 3) & 1) * CST + (lane & 7) * 16 : cb + UOFF + (g4 & 1) * UPL + r * 16) = z4;
        }
        auto fill = [&]() {
#pragma unroll
            for (int it = 0; it < 4; ++it) TIE(sw[it]);
#pragma unroll
            for (int it = 0; it < 4; ++it) {
                const unsigned q = cb + 128 + it * 1024 + l16;
                const unsigned d0 = sw[it][0], d1 = sw[it][1], d2 = sw[it][2], d3 = sw[it][3];
                const unsigned o1 = it ? __builtin_amdgcn_readlane(sw[it ? it - 1 : 0][3], 63) : 0u;
                const unsigned x1 = __builtin_amdgcn_update_dpp(o1, d3, 0x138, 0xf, 0xf, false);
                *LDSP(u32x4, q) = sw[it];
                *LDSP(u32x4, q + CST) = u32x4{__builtin_amdgcn_alignbyte(d0, x1, 2), __builtin_amdgcn_alignbyte(d1, d0, 2), __builtin_amdgcn_alignbyte(d2, d1, 2), __builtin_amdgcn_alignbyte(d3, d2, 2)};
            }
        };
        if (FILLB) {
            static_assert(!FILLB || PK < PU, "ssm_k has landed");
            if (FILLB > 1) __builtin_amdgcn_s_sleep(FILLB);
            fill();
        }
        WAIT_L(0);
        TS(5);
        BAR();
        __builtin_amdgcn_s_setprio(FPRIO);
        ITEMS(16);
        TS(11);
        WAIT_V(it_cnt(PK, 0, 16));
        TS(12);
        if (!FILLB) fill();
        TS(13);
        WAIT_V(it_cnt(PU, 1, 16));
#pragma unroll
        for (int it = 0; it < 4; ++it) TIE(uu[it]);
        if (!kv_ok) {
            for (unsigned spin = 0; spin < SPIN_CAP; ++spin) {
                GLD_B64_SC1(fl, fpoll, flg + FL_KV, 0);
                WAIT_V(0); TIE(fl);
                if (__all(fl[0] == tagA && fl[1] == tagB)) break;
                __builtin_amdgcn_s_sleep(8);
            }
#pragma unroll
            for (int it = 0; it < 4; ++it) GLD_B128_SC1(uu[it], uo, usrc + it * 4096, 0);
            WAIT_V(0);
#pragma unroll
            for (int it = 0; it < 4; ++it) TIE(uu[it]);
        }
#pragma unroll
        for (int it = 0; it < 4; ++it) *LDSP(u32x4, ud + it * 128) = uu[it];
        const unsigned pmine = lb + L_SY + 32 + wm * 4, pall = lb + L_SY + 32 + (lane & 3) * 4;
        sy_post<0>(pmine); sy_wait<0>(pall);
        if (!CPOST) { WAIT_V(0); sy_post<1>(pmine); }
        __builtin_amdgcn_s_setprio(CVPRIO);
        TS(6);
        f32x4 ca[8];
        {
            unsigned kb[4];
#pragma unroll
            for (int i = 0; i < 4; ++i) { kb[i] = cb + (~r & 1) * CST + 2 * (64 + 16 + r - 8 * g4 - 7 + (~r & 1)) + i * 1024; TIE(kb[i]); }
            unsigned ub = cb + UOFF + (g4 & 1) * UPL + (15 + r + (g4 >> 1)) * 16 - 64;
#pragma unroll
            for (int i = 0; i < 8; ++i) ca[i] = f32x4{0, 0, 0, 0};
            s16x4 FA[4][2]; bf16x8 FB[2][8];
#define CV_A(s, o, e) dsr2_<16 * ((s) + ((o) >> 3)) + 128 * (cv_m((o) & 7) & 1) + 2 * (e)>(FA[(o) & 3][e], kb[cv_m((o) & 7) >> 1])
#define CV_B(s, jb) DSR128(FB[(s) & 1][jb], ub, 64 + 256 * (jb) - 32 * (s))
            static_for<0, 8>([&](auto ic) { constexpr int n = decltype(ic)::value; CV_B(0, n); CV_A(0, n >> 1, n & 1); });
            WAIT_L(0);
            auto body = [&]() {
                static_for<0, 72>([&](auto ic) {
                    constexpr int s = decltype(ic)::value / 36, p = decltype(ic)::value % 36, o = cv_grp(p), m = cv_m(o), jb = p - cv_first(o);
                    if constexpr (jb == 0) { WAIT_L(cv_wait(o)); TIE(FA[o & 3][0]); TIE(FA[o & 3][1]); }
                    if constexpr (p == 0) { for (int i = 0; i < 8; ++i) TIE(FB[s][i]); }
                    const bf16x8 fa = __builtin_shufflevector(FA[o & 3][0], FA[o & 3][1], 0, 1, 2, 3, 4, 5, 6, 7);
                    ca[jb + m] = __builtin_amdgcn_mfma_f32_16x16x32_bf16(fa, FB[s][jb], ca[jb + m], 0, 0, 0);
                    SB();
                    constexpr int rd = cv_read(p);
                    if constexpr (rd >= 32) CV_B(s + 1, rd - 32);
                    else if constexpr (rd >= 0) CV_A(s, rd >> 1, rd & 1);
                    SB();
                });
                for (int i = 0; i < 4; ++i) { kb[i] += 128; TIE(kb[i]); }
                ub -= 64;
            };
#pragma unroll 1
            for (int it = 0; it < QIT; ++it) body();
            {
                TS(19);
                if (CPOST) { WAIT_V(0); sy_post<1>(pmine); WAIT_L(0); }
                if (CVLATE) sy_wait<1>(lb + L_SY + (CVLATE == 2 ? wm + (lane & 1) * 4 : lane & 7) * 4);
                TS(20);
            }
#pragma unroll 1
            for (int it = QIT; it < 4; ++it) body();
            WAIT_L(0);
#pragma unroll
            for (int i = 0; i < 4; ++i) { TIE(FA[i][0]); TIE(FA[i][1]); }
#pragma unroll
            for (int i = 0; i < 8; ++i) TIE(FB[0][i]);
#undef CV_A
#undef CV_B
        }
        TS(7);
        __builtin_amdgcn_s_setprio(3);
        constexpr int NBAR = __builtin_popcount(BARM);
        {
            const float Dd = bf2f(dskl);
            const char* zd = ws + WS_ZT + (size_t)(xcd * 128 + wm * 32) * 4096 + idx * 128;
            u32x2 kvr[8];
#pragma unroll
            for (int jt = 0; jt < 8; ++jt) kvr[jt] = *LDSP(u32x2, cb + UOFF + (g4 >> 1) * UPL + (16 + 16 * jt + r) * 16 + (1 - (g4 & 1)) * 8);
#pragma unroll
            for (int jt = 0; jt < 8; ++jt) {
                const f32x4 y = ca[jt];
                const u32x2 kq = kvr[jt];
                *LDSP(u32x2, cb + (jt * 4 + (r >> 2)) * 144 + (r & 3) * 32 + g4 * 8) =
                    u32x2{pack2(rb1(y[0]) + rb1(bfhi(kq[1]) * Dd), rb1(y[1]) + rb1(bflo(kq[1]) * Dd)), pack2(rb1(y[2]) + rb1(bfhi(kq[0]) * Dd), rb1(y[3]) + rb1(bflo(kq[0]) * Dd))};
            }
            unsigned za = lb + (r >> 2) * CHSZ + (wm * 8 + (g4 >> 1)) * 144 + ((g4 & 1) * 2 + ((r >> 1) & 1)) * 32 + (r & 1) * 8, zo = (lane >> 3) * 4096 + (lane & 7) * 16;
            TIE(za); TIE(zo);
            sy_post<3>(pmine); sy_wait<3, 0>(pall);
            v4i16 t[4][2];
#pragma unroll
            for (int it = 0; it < 4; ++it)
#pragma unroll
                for (int e = 0; e < 2; ++e) t[it][e] = __builtin_amdgcn_ds_read_tr16_b64_v4i16(LDSP(v4i16, za + it * 288 + e * 16));
#pragma unroll
            for (int it = 0; it < 4; ++it) { TIE(t[it][0]); TIE(t[it][1]); }
            if (ZSB) BAR();
#pragma unroll
            for (int it = 0; it < 4; ++it) { const bf16x8 f = __builtin_shufflevector(t[it][0], t[it][1], 0, 1, 2, 3, 4, 5, 6, 7); GST_B128_SC1(zo, f, zd + it * 32768, 0); }
        }
#pragma unroll
        for (int i = ZSB; i < ZFB; ++i) BAR();
        WAIT_V(0);
        sy_post<2>(pmine);
        if (wm == 0) {
            sy_wait<2>(pall);
            if (!(ABL & 1024) && lane == 0) GST_B64_SC1(fpub, tag, flg + FL_Z);
        }
        TS(8);
#pragma unroll
        for (int i = ZFB; i < NBAR - 1; ++i) BAR();
        if (ZSL) __builtin_amdgcn_s_sleep(ZSL);
        GLD_B64_SC1(fl, fpoll, flg + FL_Z, 0);
        BAR();
        {
            unsigned left = SPIN_CAP;
            asm volatile("s_branch 2f\n1:\n\ts_sleep 4\n\tglobal_load_dwordx2 %0, %2, %3 sc1\n2:\n\ts_waitcnt vmcnt(0)\n\tv_cmp_ne_u64 vcc, %4, %0\n\ts_cbranch_vccz 3f\n\ts_sub_u32 %1, %1, 1\n\ts_cbranch_scc0 1b\n3:"
                         : "+v"(fl), "+s"(left) : "v"(fpoll), "s"(flg + FL_Z), "s"(tag) : "vcc", "scc", "memory");
        }
        u32x4 zv[4];
        {
            ITEMS(18);
            const char* zsrc = ws + WS_ZT + (size_t)(xcd * 128 + idx * 4 + wm) * 4096;
            static_for<0, 4>([&](auto ic) { constexpr int it = decltype(ic)::value; GLD_B128_SC1(zv[it], (l31 >> 2) * 512 + lh * 64 + (lane & 3) * 16, zsrc, it * 128); });
        }
        WAIT_V(0);
        TS(9);
#pragma unroll
        for (int it = 0; it < 4; ++it) TIE(zv[it]);
        quad_swap<0>(zv[0], zv[1]); quad_swap<0>(zv[2], zv[3]);
        quad_swap<1>(zv[0], zv[2]); quad_swap<1>(zv[1], zv[3]);
#pragma unroll
        for (int it = 0; it < 4; ++it) TIE(zv[it]);
        char* const oa = a.out + (size_t)(idx * 64 + wm * 16) * 8192 + xcd * 512;
#pragma unroll
        for (int n = 0; n < 8; ++n) {
            TIE(qv[n]);
            const unsigned z0 = zv[n & 3][2 * (n >> 2)], z1 = zv[n & 3][2 * (n >> 2) + 1];
            const f32x4 o = {rb1(bflo(qv[n][0]) * bflo(z0)), rb1(bfhi(qv[n][0]) * bfhi(z0)), rb1(bflo(qv[n][1]) * bflo(z1)), rb1(bfhi(qv[n][1]) * bfhi(z1))};
            GST_B128_NT(lh * 32768 + l31 * 16, o, oa + ((n >> 2) * 8 + (n & 3)) * 8192, 0);
            if (OSL) __builtin_amdgcn_s_sleep(OSL);
        }
        asm volatile(".Lfk_code_a:");
        TSE(10);
        TS_FLUSH();
        return;
    }

    u32x2 fl;
    unsigned dv[4];
    {
        const int v = l15 ^ g4, hi = v >> 3;
        const int r0 = wn ? tn * 64 + (wm >> 1) * 32 : tm * 128 + (wm >> 1) * 64 + (wm & 1) * 4;
        const unsigned d0 = (r0 + ((g4 * (wn ? 2 : 9)) & (wn ? 6 : 17))) * 2048 + (v & 7) * 16, odd = wn ? 11 : 14;
        dv[0] = d0 + (hi << odd); dv[2] = d0 + ((hi ^ 1) << odd) + (wn ? 32768 : 4096);
        dv[1] = (dv[0] ^ 64) + (wn ? 16384 : 65536); dv[3] = (dv[2] ^ 64) + (wn ? 16384 : 65536);
    }
    const int rot7 = ((((ROT >> 12) & 15) * (idx & 7) + ((ROT >> 8) & 15) * (idx >> 3) + ((ROT >> 4) & 15) * (xcd >> 1) + (ROT & 15) * (xcd & 1)) & 15) << 7;
    const char* g1src = wn ? ((wm & 1) ? a.bim : a.bre) : a.x;
#define G1_ISSUE(st, i) DMA(g1dst + g1_slot(st) + (i) * 1024, dv[i], g1src + ((((st) << 7) + rot7) & 1920))
    static_for<0, PRE>([&](auto ic) { constexpr int n = decltype(ic)::value; G1_ISSUE(n >> 2, n & 3); });
    TS(14);
    SLICE_TABLE();
    const int j0 = 4 * run - (32 - 2 * tm);
    const int pneg = EALL ? 0 : 16 * (wm >> 1) + 15 - (32 - 2 * tm);
    unsigned eo[2], pes = lb + L_ES + wid * 256 + l15 * 16;
    unsigned plr = lb + L_AGG + st * 8 + run * 512, est = ((tm * 2 + (wm >> 1)) * 1024 + tn * 64 + st) * 16 + (EREP ? g4 * 524288 : 0);
#pragma unroll
    for (int i = 0; i < 2; ++i) { eo[i] = (EALL ? (j0 + 2 * i) & 31 : j0 + 2 * i < 0 ? 0 : j0 + 2 * i) * 16384 + (EREP ? (tm >> 2) * 524288 : 0) + (tn * 64 + st) * 16; TIE(eo[i]); }
    TIE(plr); TIE(pes); TIE(est);
    unsigned hw[2], ta[2];
    {
        const unsigned ht = lb + L_HT + wid * 4096;
#pragma unroll
        for (int j = 0; j < 2; ++j) {
            const int pr = (8 * (g4 & 1) + 4 * j + (l15 >> 2)) ^ ((g4 & 1) * 4), pw = l15 ^ ((l15 >> 3) * 4);
            ta[j] = ht + (g4 >> 1) * 512 + pr * 32 + ((((l15 >> 1) ^ (pr >> 2)) & 1) << 4) + 8 * (lane & 1);
            hw[j] = ht + g4 * 1024 + pw * 32 + (((j ^ (pw >> 2)) & 1) << 4);
            TIE(ta[j]); TIE(hw[j]);
        }
    }
    unsigned po = pes + ((wm & 2) ? -512 : 512);
    unsigned cta = lb + L_TS + st * 16, ctb = lb + L_TR + run * 1024 + st * 16;
    TIE(po); TIE(cta); TIE(ctb);
    f32x4 ct[5]; f32x2 V1;
    unsigned pmine = lb + L_SY + wid * 4, pall = lb + L_SY + (lane & 7) * 4, pown = lb + L_SY + (lane < 4 ? 32 + lane * 4 : (wid ^ 1) * 4), pzero = lb + L_SY;
    unsigned g2v[2], cad[2][2], to[2][2];
    unsigned xo = (tm * 128 + wm * 32 + wn * 16 + g4 * 4) * 2048 + tn * 128 + l15 * 8 + 4096, dso = xo & 2047;
    unsigned fst = (tm * 16 + l15) * 128 + tn * 8, fld = (tm * 16 + tn) * 128 + (NOOWN && l15 == tn ? tn ^ 1 : l15) * 8;
#pragma unroll
    for (int i = 0; i < 2; ++i) {
        const int pair = i * 4 + g4, v = l15 ^ pair ^ ((wm & 1) * 8);
        g2v[i] = (pair * 2 + (v >> 3)) * 8192 + (v & 7) * 16 + (tn * 64 + wm) * 2048;
        TIE(g2v[i]);
#pragma unroll
        for (int j = 0; j < 2; ++j) {
            cad[i][j] = lb + L_C + (img_off((g4 >> 1) * 64 + l15, 4 * wn + (g4 & 1)) ^ (i * 32) ^ (j * 128));
            to[i][j] = ta[j] + (i ^ wn) * 1024 - (wm & 1) * 2048;
            TIE(cad[i][j]); TIE(to[i][j]);
        }
    }
    unsigned cwa = lb + wid * 2048 + l16;
    TIE(cwa);
    TIE(pmine); TIE(pall); TIE(pown); TIE(pzero); TIE(xo); TIE(dso); TIE(fst); TIE(fld);
    TS(1);
    {
        WAIT_V(PRE);
        BAR();
        static_assert(PRO <= 12 && NB1 == 4, "the constants sit in slot 3");
        DSR128(ct[0], cta, 0); DSR128(ct[1], cta, 1024); DSR128(ct[2], cta, 2048); DSR128(ct[3], ctb, 0); DSR128(ct[4], ctb, 8192); DSR64(V1, ctb, 16384);
        static_for<PRE, PRO>([&](auto ic) { constexpr int n = decltype(ic)::value; G1_ISSUE(n >> 2, n & 3); });
        TS(15);
    }
    WAIT_L(0);
#pragma unroll
    for (int i = 0; i < 5; ++i) TIE(ct[i]);
    TIE(V1);
    unsigned tagA = __builtin_amdgcn_readfirstlane(__float_as_uint(ct[2][2])), tagB = __builtin_amdgcn_readfirstlane(__float_as_uint(ct[2][3]));
    asm volatile("" : "+s"(tagA), "+s"(tagB));

    f32x4 acc[4][2];
    {
#pragma unroll
        for (int i = 0; i < 4; ++i) { acc[i][0] = f32x4{0, 0, 0, 0}; acc[i][1] = f32x4{0, 0, 0, 0}; }
        const int aoff = img_off((wm >> 1) * 64 + l15, g4), boff = img_off(128 + wn * 64 + (wm & 1) * 16 + l15, g4);
        unsigned aad[2][2], bad[2];
#pragma unroll
        for (int ks = 0; ks < 2; ++ks) { aad[ks][0] = lb + (aoff ^ (ks * 64)); aad[ks][1] = lb + (aoff ^ (ks * 64) ^ 128); bad[ks] = lb + (boff ^ (ks * 64)); }
        bf16x8 fa[2][4], fb[2][2];
        WAIT_V(PRO - 4); BAR();
        TS(2);
#define G1_FRAG(g, n) do { constexpr int bo_ = g1_slot((g) >> 1), hi_ = bo_ & ~65535, x_ = (n) < 3 ? (n) >> 1 : (n) - 2; \
        if constexpr ((n) == 0 || (n) == 2) DSR128(fb[(g) & 1][x_], bad[(g) & 1] + hi_, bo_ - hi_ + x_ * 4096); \
        else DSR128(fa[(g) & 1][x_], aad[(g) & 1][x_ & 1] + hi_, bo_ - hi_ + x_ * 2048); } while (0)
        static_for<0, 6>([&](auto ic) { constexpr int n = decltype(ic)::value; G1_FRAG(0, n); });
        static_for<0, 32>([&](auto ic) {
            constexpr int g = decltype(ic)::value, gn = g + 1, n0 = g1_upto(g - 1), nd = g1_upto(g) - n0;
            constexpr bool bar = (gn & 1) == 0 && gn < 32;
            if constexpr (bar) {
                static_assert(g1_cnt(gn >> 1) >= 0, "schedule");
                WAIT_V(g1_cnt(gn >> 1)); WAIT_L(0); BAR();
                if constexpr (((gn >> 1) & 3) == 0 || gn >= 26) TS(gn >= 26 ? 23 + (gn >> 1) - 13 : 16 + (gn >> 3));
#pragma unroll
                for (int i = 0; i < 4; ++i) TIE(fa[g & 1][i]);
                TIE(fb[g & 1][0]); TIE(fb[g & 1][1]);
            }
            static_for<0, 8>([&](auto mc) {
                constexpr int m = decltype(mc)::value, i = m >> 1, j = m & 1;
                if constexpr (!bar && (i == 0 || j == 0)) {
                    WAIT_L(5 - (i == 0 ? j + 1 : i + 2) + (gn < 32 ? (m < 6 ? m : 6) : 0));
                    TIE(fa[g & 1][i]); TIE(fb[g & 1][j]);
                }
                acc[i][j] = __builtin_amdgcn_mfma_f32_16x16x32_bf16(fa[g & 1][i], fb[g & 1][j], acc[i][j], 0, 0, 0);
                SB();
                if constexpr (gn < 32 && m < 6) G1_FRAG(gn, m);
                constexpr int d = nd > 2 ? ((m & 1) ? m >> 1 : -1) : (m == DP0 ? 0 : m == DP1 ? 1 : -1);
                if constexpr (d >= 0 && d < nd) G1_ISSUE((n0 + d) >> 2, (n0 + d) & 3);
                SB();
            });
        });
#undef G1_FRAG
    }
    TS(3);
    const unsigned tagG = tagA ^ tagB ^ 0x9E3779B9u;
    __builtin_amdgcn_s_setprio(MPRIO);
    f32x2 ur[8], ui[8], hr, hi;
    const f32x2 L1 = {ct[0][0], ct[0][1]}, C1 = {ct[0][2], ct[0][3]}, L16 = {ct[1][2], ct[1][3]}, Q2r = {ct[3][0], ct[3][1]}, Q2i = {ct[3][2], ct[3][3]};
    const float L8r = ct[1][0], L8i = ct[1][1], L64r = ct[2][0], L64i = ct[2][1];
    u32x4 eg[4];
#define E_LOADS "global_load_dwordx4 v[104:107], %[a0], %[b0] sc1\n\tglobal_load_dwordx4 v[108:111], %[a0], %[b1] sc1\n\t" \
                "global_load_dwordx4 v[112:115], %[a1], %[b0] sc1\n\tglobal_load_dwordx4 v[116:119], %[a1], %[b1] sc1\n"
#define E_POLL() asm volatile("s_cmp_lt_i32 %[on], 0\n\ts_cbranch_scc1 1f\n\t" E_LOADS "1:" : "={v[104:107]}"(eg[0]), "={v[108:111]}"(eg[1]), "={v[112:115]}"(eg[2]), "={v[116:119]}"(eg[3]) \
        : [a0] "v"(eo[0]), [a1] "v"(eo[1]), [b0] "s"(ws + WS_E), [b1] "s"(ws + WS_E + 16384), [on] "s"(pneg) : "scc", "memory")
    {
        asm volatile("s_nop 7" : "+v"(acc[0][0]), "+v"(acc[0][1]), "+v"(acc[1][0]), "+v"(acc[1][1]), "+v"(acc[2][0]), "+v"(acc[2][1]), "+v"(acc[3][0]), "+v"(acc[3][1]));
#pragma unroll
        for (int p = 0; p < 8; ++p) { ur[p] = f32x2{acc[p >> 1][0][2 * (p & 1)], acc[p >> 1][0][2 * (p & 1) + 1]}; ui[p] = f32x2{acc[p >> 1][1][2 * (p & 1)], acc[p >> 1][1][2 * (p & 1) + 1]}; }
        f32x2 gr = ur[0], gi = ui[0];
#pragma unroll
        for (int p = 1; p < 8; ++p) { f32x2 nr, ni; pk_cmad(nr, ni, L1, gr, gi, ur[p], ui[p]); gr = nr; gi = ni; }
        float fr, fi;
        cmad(fr, fi, L8r, L8i, gr[0], gi[0], gr[1], gi[1]);
        {
            const f32x2 w = cmul4(V1, f32x2{fr, fi});
            const float wr = sum4(w[0]), wi = sum4(w[1]);
            const u32x4 gq = {__float_as_uint(wr), tagA, __float_as_uint(wi), tagG};
            if (EREP ? (EALL || g4 >= (tm >> 2)) : g4 == 0) GST_B128_SC1(est, gq, ws + WS_E, 0);
        }
        *LDSP(f32x2, plr + 7 * 512) = f32x2{fr, fi};
        *LDSP(unsigned, pes + 8) = 0;
        TS(21);
        if (EP == 2) E_POLL();
        auto coef = [&]() {
#pragma unroll
            for (int p = 0; p < 8; ++p) { f32x2 nr, ni; pk_conj_mul(nr, ni, C1, ur[p], ui[p]); ur[p] = nr; ui[p] = ni; TIE(ur[p]); TIE(ui[p]); }
        };
        if (CFB) coef();
        WAIT_L(0); BAR();
        f32x2 e[7];
#pragma unroll
        for (int j = 0; j < 7; ++j) e[j] = *LDSP(f32x2, plr + j * 512);
        if (EP == 1) E_POLL();
        if (!CFB) coef();
#pragma unroll
        for (int j = 1; j < 7; ++j) cmac2(e[j], L16, e[j - 1]);
        float mr, mi;
        cmad(mr, mi, L8r, L8i, e[6][0], e[6][1], gr[0], gi[0]);
        pk_conj_mul(hr, hi, C1, f32x2{e[6][0], mr}, f32x2{e[6][1], mi});
        TIE(hr); TIE(hi);
        TS(4);
    }
    f32x2 G;
    {
        float pr = 0.f, pi = 0.f;
        if (!EP) E_POLL();
        if (pneg >= 0) {
            unsigned t0, t1, t2, left = SPIN_CAP;
            asm volatile("s_branch 2f\n1:\n\ts_sleep %[sl]\n\t" E_LOADS "2:\n\ts_waitcnt vmcnt(0)\n\t"
                         "v_bitop3_b32 %[t0], v105, v109, %[ta] bitop3:0x7e\n\tv_bitop3_b32 %[t1], v113, v117, %[ta] bitop3:0x7e\n\tv_bitop3_b32 %[t2], v107, v111, %[tg] bitop3:0x7e\n\t"
                         "v_or3_b32 %[t0], %[t0], %[t1], %[t2]\n\tv_bitop3_b32 %[t1], v115, v119, %[tg] bitop3:0x7e\n\tv_or_b32 %[t0], %[t0], %[t1]\n\t"
                         "v_cmp_ne_u32 vcc, 0, %[t0]\n\ts_cbranch_vccz 3f\n\ts_sub_u32 %[n], %[n], 1\n\ts_cbranch_scc0 1b\n3:"
                         : "+{v[104:107]}"(eg[0]), "+{v[108:111]}"(eg[1]), "+{v[112:115]}"(eg[2]), "+{v[116:119]}"(eg[3]), [t0] "=&v"(t0), [t1] "=&v"(t1), [t2] "=&v"(t2), [n] "+s"(left)
                         : [a0] "v"(eo[0]), [a1] "v"(eo[1]), [b0] "s"(ws + WS_E), [b1] "s"(ws + WS_E + 16384), [ta] "s"(tagA), [tg] "s"(tagG), [sl] "n"(ESL) : "vcc", "scc", "memory");
            float ar = __uint_as_float(eg[1][0]), ai = __uint_as_float(eg[1][2]), br = __uint_as_float(eg[3][0]), bi = __uint_as_float(eg[3][2]);
            cmac(ar, ai, L64r, L64i, __uint_as_float(eg[0][0]), __uint_as_float(eg[0][2]));
            cmac(br, bi, L64r, L64i, __uint_as_float(eg[2][0]), __uint_as_float(eg[2][2]));
            if (EALL) { if (j0 < 0) ar = ai = 0.f; if (j0 + 2 < 0) br = bi = 0.f; }
            const f32x2 pu = cmul4(f32x2{ct[4][2], ct[4][3]}, f32x2{ar, ai});
            pr = pu[0]; pi = pu[1];
            cmac(pr, pi, ct[4][0], ct[4][1], br, bi);
            pr = sum4(pr); pi = sum4(pi);
        }
        TS(5);
        *LDSP(u32x4, pes) = u32x4{__float_as_uint(pr), __float_as_uint(pi), 1u, 0u};
        u32x4 o;
        asm volatile("1:\n\tds_read_b128 %0, %1\n\ts_waitcnt lgkmcnt(0)\n\tv_cmp_eq_u32 vcc, 0, v122\n\ts_cbranch_vccnz 1b" : "=&{v[120:123]}"(o) : "v"(po) : "vcc", "memory");
        G = f32x2{pr + __uint_as_float(o[0]), pi + __uint_as_float(o[1])};
    }
    TS(6);
    {
        pk_conj_mac(hr, hi, G, Q2r, Q2i);
#pragma unroll
        for (int p = 0; p < 8; ++p) { pk_cmac_conj(ur[p], ui[p], L1, hr, hi); hr = ur[p]; hi = ui[p]; }
        const char* hd = ws + WS_H + (size_t)((tm * 4 + (wm & 2)) * 2 + wn) * HP + (tn * 4 + (wm & 1) * 2) * 1024;
#pragma unroll
        for (int e2 = 0; e2 < 2; ++e2)
#pragma unroll
            for (int c = 0; c < 2; ++c) {
                const f32x2* o = c ? ui : ur;
                *LDSP(u32x4, hw[e2] + c * 512) = u32x4{pack2(o[0][e2], o[1][e2]), pack2(o[2][e2], o[3][e2]), pack2(o[4][e2], o[5][e2]), pack2(o[6][e2], o[7][e2])};
            }
        v4i16 t[4][2];
#pragma unroll
        for (int n = 0; n < 4; ++n)
#pragma unroll
            for (int j = 0; j < 2; ++j) t[n][j] = __builtin_amdgcn_ds_read_tr16_b64_v4i16(LDSP(v4i16, ta[j] + n * 1024));
        sy_post<1>(pmine);
        static_for<0, 4>([&](auto ic) {
            constexpr int n = decltype(ic)::value;
            const bf16x8 f = __builtin_shufflevector(t[n][0], t[n][1], 0, 1, 2, 3, 4, 5, 6, 7);
            GST_B128_SC1(l16, f, hd + (n >> 1) * 2 * HP + ((n & 1) ^ wn) * 1024, 0);
        });
    }
    TS(7);
    bf16x8 fa2[64];
    u32x2 xv[4], dsv;
    u32x4 cr[2 * NR + 1];
    const char* Aw = ws + WS_H + (size_t)((tm * 4 + wm) * 2 + wn) * HP;
    const char* g2src = wn ? a.cim : a.cre;
    const unsigned g2dst = lb + wid * 2048;
#define G2_ISSUE(st, i) DMA(g2dst + g2_place(st) + (i) * 1024, g2v[i], g2src + (SLICE(st) << 7))
#define G2_A(g) GLD_B128_SC1(fa2[g], l16, Aw + (SLICE((g) >> 2) << 12), ((g) & 3) * 1024)
    {
        {
            sy_wait<1>(pown);
            s16x4 fo[4][2]; bf16x8 c[8];
            static_for<0, 2>([&](auto kc) {
                constexpr int ks = decltype(kc)::value;
                static_for<0, 4>([&](auto ic) { constexpr int i = decltype(ic)::value; dsrtr_<ks * 4096>(fo[2 * ks + (i >> 1)][i & 1], to[i >> 1][i & 1]); });
                static_for<0, 4>([&](auto ic) { constexpr int i = decltype(ic)::value; DSR128(c[4 * ks + i], cad[ks][i & 1], 2 * G2STAGE + i * 2048); });
            });
            static_for<0, 8>([&](auto ic) {
                constexpr int n = decltype(ic)::value, ks = n >> 2, i = n & 3;
                WAIT_L(ks ? 3 - i : 11 - i);
                TIE(c[n]);
                if constexpr (i == 0) { TIE(fo[2 * ks][0]); TIE(fo[2 * ks][1]); TIE(fo[2 * ks + 1][0]); TIE(fo[2 * ks + 1][1]); }
#pragma unroll
                for (int j = 0; j < 2; ++j)
                    acc[i][j] = __builtin_amdgcn_mfma_f32_16x16x32_bf16(__builtin_shufflevector(fo[2 * ks + j][0], fo[2 * ks + j][1], 0, 1, 2, 3, 4, 5, 6, 7), c[n], ks ? acc[i][j] : f32x4{0, 0, 0, 0}, 0, 0, 0);
                SB();
            });
#pragma unroll
            for (int i = 0; i < 4; ++i) { TIE(acc[i][0]); TIE(acc[i][1]); }
        }
        if (CEARLY) sy_post<0>(pmine);
        static_for<0, 4>([&](auto ic) { constexpr int it = decltype(ic)::value; GLD_B64(xv[it], xo, a.x, it * 2048 - 4096); });
        GLD_B64(dsv, dso, a.dskip, 0);
        if (CEARLY) {
            sy_wait<0>(pall);
            static_for<4, 4 + CEARLY>([&](auto ic) { constexpr int n = decltype(ic)::value; G2_ISSUE(n >> 1, n & 1); });
        }
        TS(16);
        WAIT_V(5 + CEARLY);
        sy_post<2>(pmine);
        if (!CEARLY || wid == (NOOWN ? 1 : 0) || (NOOWN == 1 && wid == 0)) sy_wait<2, 0>(pall);
        const u32x2 tagv = {__float_as_uint(ct[2][2]), __float_as_uint(ct[2][3])};
        if (wid == (NOOWN ? 1 : 0)) asm volatile("s_mov_b64 exec, 0xffff\n\tglobal_store_dwordx2 %0, %1, %2 sc1\n\ts_mov_b64 exec, -1" :: "v"(fst), "v"(tagv), "s"(flg + FL_H) : "memory");
        if (PFIRST) asm volatile("s_cmp_lg_u32 %3, 0\n\ts_cbranch_scc1 1f\n\tglobal_load_dwordx2 %0, %1, %2 sc1\n1:" : "=&v"(fl) : "v"(fld), "s"(flg + FL_H), "s"(wid) : "scc", "memory");
        TS(8);
        static_for<4 + CEARLY, 10>([&](auto ic) { constexpr int n = decltype(ic)::value; G2_ISSUE(n >> 1, n & 1); });
        static_for<0, 2 * NR>([&](auto ic) { constexpr int n = decltype(ic)::value; GLD_B128(cr[n], g2v[n & 1], g2src + (SLICE(7 + (n >> 1)) << 7), 0); });
        TS(13);
        if (wid == 0) {
            unsigned left = SPIN_CAP;
            if (PFIRST)
            asm volatile("s_waitcnt vmcnt(6)\n\ts_branch 3f\n1:\n\tglobal_load_dwordx2 %0, %2, %3 sc1\n\ts_waitcnt vmcnt(0)\n3:\n\tv_cmp_ne_u64 vcc, %0, %4\n\ts_cbranch_vccz 2f\n\ts_sub_u32 %1, %1, 1\n\ts_cbranch_scc0 1b\n2:"
                         : "+v"(fl), "+s"(left) : "v"(fld), "s"(flg + FL_H), "v"(tagv) : "vcc", "scc", "memory");
            else
            asm volatile("1:\n\tglobal_load_dwordx2 %0, %2, %3 sc1\n\ts_waitcnt vmcnt(0)\n\tv_cmp_ne_u64 vcc, %0, %4\n\ts_cbranch_vccz 2f\n\ts_sub_u32 %1, %1, 1\n\ts_cbranch_scc0 1b\n2:"
                         : "=&v"(fl), "+s"(left) : "v"(fld), "s"(flg + FL_H), "v"(tagv) : "vcc", "scc", "memory");
            sy_post<3>(pmine);
            if (b == 255 && lane == 0) GST_B64_SC1(0u * lane, tagv, flg + FL_EPOCH);
        } else if (g2_bar(3)) sy_wait<3>(pzero);
        else { WAIT_V(2 * NR); sy_post<3>(pmine); }
        if (!g2_bar(3)) sy_wait<3>(pall);
        TS(20);
        static_for<4, 4 + AW>([&](auto ic) { constexpr int g = decltype(ic)::value; G2_A(g); });
    }
    TS(9);
    __builtin_amdgcn_s_setprio(0);
    unsigned dm[4];
    auto dmask = [&]() { TIE(dsv); dm[0] = dsv[0] & 0xffffu; dm[1] = dsv[0] & 0xffff0000u; dm[2] = dsv[1] & 0xffffu; dm[3] = dsv[1] & 0xffff0000u; TIE(dm[0]); TIE(dm[1]); TIE(dm[2]); TIE(dm[3]); };
    auto skip = [&](int it) { TIE(xv[it]); const f32x4 y = mul4bf(xv[it], dm); for (int i = 0; i < 4; ++i) acc[i][0][it] += 0.5f * y[i]; if (it & 1) { for (int i = 0; i < 4; ++i) TIE(acc[i][0]); } };
    {
        bf16x8 fb[2][4];
#define G2_FRAG(g, i) do { constexpr int pl_ = g2_place((g) >> 1), b_ = pl_ >= L_C ? L_C : pl_ >= 32768 ? 32768 : 0; DSR128(fb[(g) & 1][i], cad[(g) & 1][(i) & 1] + (b_ - L_C), pl_ - b_ + (i) * 2048); } while (0)
        static_for<0, 4>([&](auto ic) { constexpr int i = decltype(ic)::value; G2_FRAG(2, i); });
        if (YS == 0) { WAIT_V(AW + 6 + 2 * NR); dmask(); skip(0); skip(1); skip(2); skip(3); }
        static_for<2, 32>([&](auto ic) {
            constexpr int g = decltype(ic)::value, gn = g + 1;
            constexpr bool bar = g2_bar(g);
            constexpr int n0 = g2_upto(g - 1), n1 = g2_upto(g);
            if constexpr (bar) {
                constexpr int ca = g2_cnt(2 * g, 2 * g), cs = g2_cover(g) >= 7 + NR ? g2_cnt(100 + 2 * g2_cover(g) + 1, 2 * g) : 63;
                static_assert(cs >= 0 && g2_wr(g - 1) >= 2 * (g2_cover(g) < 6 + NR ? g2_cover(g) - 6 : NR), "schedule");
                WAIT_V(AWB && ca < cs ? ca : cs); WAIT_L(0); BAR();
                if constexpr (!AWB) WAIT_V(ca);
#pragma unroll
                for (int i = 0; i < 4; ++i) TIE(fb[g & 1][i]);
                TIE(fa2[2 * g]);
            }
            static_for<0, 8>([&](auto mc) {
                constexpr int m = decltype(mc)::value, i = m & 3, j = m >> 2, f = 2 * g + j;
                static_assert(g2_cnt(f, f) >= 0, "schedule");
                if constexpr (i == 0 && (j || !bar)) { WAIT_V(g2_cnt(f, f)); TIE(fa2[f]); }
                if constexpr (!bar && j == 0) { WAIT_L((gn < 32 ? 3 : 3 - i) + g2_wr(g - 1) - g2_wr(g - 2)); TIE(fb[g & 1][i]); }
                acc[i][j] = __builtin_amdgcn_mfma_f32_16x16x32_bf16(fa2[f], fb[g & 1][i], acc[i][j], 0, 0, 0);
                SB();
                if constexpr (gn < 32 && m < 4) G2_FRAG(gn, m);
                if constexpr (m >= 4 && g2_wr(g - 1) + m - 4 < g2_wr(g)) {
                    constexpr int n = g2_wr(g - 1) + m - 4, pl = g2_place(7 + (n >> 1)), pb = pl >= L_C ? L_C : 0;
                    asm volatile("ds_write_b128 %0, %1 offset:%2" :: "v"(cwa + pb), "v"(cr[n]), "n"(pl - pb + (n & 1) * 1024) : "memory");
                }
                if constexpr (i == 1 && n0 + j < n1) G2_ISSUE((n0 + j) >> 1, (n0 + j) & 1);
                if constexpr (i >= 2 && g2_ald(f, i & 1) >= 0) G2_A(g2_ald(f, i & 1));
                SB();
            });
        });
#undef G2_FRAG
    }
    TS(11);
    {
#pragma unroll
        for (int i = 0; i < 4; ++i) *LDSP(f32x4, lb + L_Y + wid * 4096 + l16 + i * 1024) = acc[i][1];
        sy_post<4>(pmine);
        f32x4 y[4];
#pragma unroll
        for (int r = 0; r < 4; ++r) {
            y[r] = f32x4{2.f * acc[0][0][r], 2.f * acc[1][0][r], 2.f * acc[2][0][r], 2.f * acc[3][0][r]};
            TIE(y[r]);
        }
        sy_wait<4, 0>(pmine ^ 16);
        char* const ob = a.out + (size_t)(tm * 128 + wm * 32 + wn * 16) * 8192 + 4096 + tn * 256;
        f32x4 w[4];
#pragma unroll
        for (int i = 0; i < 4; ++i) w[i] = *LDSP(f32x4, lb + L_Y + (wid ^ 4) * 4096 + l16 + i * 1024);
#pragma unroll
        for (int r = 0; r < 4; ++r) {
            const f32x4 o = {2.f * w[0][r] + y[r][0], 2.f * w[1][r] + y[r][1], 2.f * w[2][r] + y[r][2], 2.f * w[3][r] + y[r][3]};
            GST_B128_NT(g4 * 32768 + l15 * 16, o, ob + r * 8192, 0);
        }
    }
    asm volatile(".Lfk_code_b:");
    TSE(12);
    TS_FLUSH();
}

}

extern "C" void kernel_launch(void* const* d_in, const int* in_sizes, int n_in, void* d_out, int out_size, void* d_ws, size_t ws_size,
                              hipStream_t stream) {
    (void)in_sizes; (void)n_in; (void)out_size; (void)ws_size;
    fk::Args a;
    a.q = (const char*)d_in[0]; a.k = (const char*)d_in[1]; a.v = (const char*)d_in[2]; a.sk = (const char*)d_in[3];
    a.ssmk = (const char*)d_in[4]; a.ssmD = (const char*)d_in[5]; a.x = (const char*)d_in[6]; a.lre = (const char*)d_in[7];
    a.lim = (const char*)d_in[8]; a.bre = (const char*)d_in[9]; a.bim = (const char*)d_in[10]; a.cre = (const char*)d_in[11];
    a.cim = (const char*)d_in[12]; a.logdt = (const char*)d_in[13]; a.dskip = (const char*)d_in[14];
    a.out = (char*)d_out; a.ws = (char*)d_ws;
    hipLaunchKernelGGL(fk::fused, dim3(256), dim3(768), 0, stream, a);
}
```

```cpp
#include <hip/hip_runtime.h>
#include <stdint.h>
#include <type_traits>

namespace fk {

constexpr int S = 2048, D = 1024, P = 1024;

typedef __attribute__((ext_vector_type(8))) short bf16x8;
typedef __attribute__((ext_vector_type(4))) short s16x4;
typedef __attribute__((ext_vector_type(16))) float f32x16;
typedef __attribute__((ext_vector_type(4))) float f32x4;
typedef __attribute__((ext_vector_type(2))) float f32x2;
typedef __attribute__((ext_vector_type(4))) unsigned u32x4;
typedef __attribute__((ext_vector_type(2))) unsigned u32x2;
#define LDSP(T, a) ((__attribute__((address_space(3))) T*)(uintptr_t)(unsigned)(a))

__device__ __forceinline__ float bf2f(unsigned v) { return __uint_as_float(v << 16); }
__device__ __forceinline__ float bflo(unsigned w) { return __uint_as_float(w << 16); }
__device__ __forceinline__ float bfhi(unsigned w) { return __uint_as_float(w & 0xffff0000u); }
__device__ __forceinline__ uint16_t f2bf(float f) { __bf16 b = (__bf16)f; return __builtin_bit_cast(uint16_t, b); }
__device__ __forceinline__ float rbf(float f) { return bf2f(f2bf(f)); }
typedef __attribute__((ext_vector_type(2))) __bf16 bf16x2;
__device__ __forceinline__ unsigned pack2(float lo, float hi) { return __builtin_bit_cast(unsigned, __builtin_convertvector(f32x2{lo, hi}, bf16x2)); }
__device__ __forceinline__ float rb1(float x) { return __uint_as_float(pack2(0.f, x)); }
__device__ __forceinline__ void cmac(float& xr, float& xi, float ar, float ai, float hr, float hi) {
    asm("v_fmac_f32 %0, %2, %4\n\tv_fmac_f32 %1, %2, %5\n\tv_fma_f32 %0, -%3, %5, %0\n\tv_fmac_f32 %1, %3, %4" : "+v"(xr), "+v"(xi) : "v"(ar), "v"(ai), "v"(hr), "v"(hi));
}
__device__ __forceinline__ void cmad(float& o_r, float& o_i, float ar, float ai, float hr, float hi, float er, float ei) {
    asm("v_fma_f32 %0, %2, %4, %6\n\tv_fma_f32 %1, %2, %5, %7\n\tv_fma_f32 %0, -%3, %5, %0\n\tv_fmac_f32 %1, %3, %4" : "=&v"(o_r), "=&v"(o_i) : "v"(ar), "v"(ai), "v"(hr), "v"(hi), "v"(er), "v"(ei));
}
__device__ __forceinline__ f32x2 cmul4(f32x2 a, f32x2 h) {
    float o_r, o_i;
    asm("v_mul_f32 %0, %2, %4\n\tv_mul_f32 %1, %2, %5\n\tv_fma_f32 %0, -%3, %5, %0\n\tv_fmac_f32 %1, %3, %4" : "=&v"(o_r), "=&v"(o_i) : "v"(a[0]), "v"(a[1]), "v"(h[0]), "v"(h[1]));
    return f32x2{o_r, o_i};
}

#define PK_LO " op_sel:[0,0,0] op_sel_hi:[0,1,1]"
#define PK_HI " op_sel:[1,0,0] op_sel_hi:[1,1,1]"
#define PK_NEG " neg_lo:[1,0,0] neg_hi:[1,0,0]"
__device__ __forceinline__ void pk_cmad(f32x2& o_r, f32x2& o_i, f32x2 a, f32x2 hr, f32x2 hi, f32x2 er, f32x2 ei) {
    asm("v_pk_fma_f32 %0, %2, %3, %5" PK_LO "\n\tv_pk_fma_f32 %1, %2, %4, %6" PK_LO "\n\tv_pk_fma_f32 %0, %2, %4, %0" PK_HI PK_NEG "\n\tv_pk_fma_f32 %1, %2, %3, %1" PK_HI
        : "=&v"(o_r), "=&v"(o_i) : "v"(a), "v"(hr), "v"(hi), "v"(er), "v"(ei));
}
__device__ __forceinline__ void pk_cmac_conj(f32x2& xr, f32x2& xi, f32x2 a, f32x2 hr, f32x2 hi) {
    asm("v_pk_fma_f32 %0, %2, %3, %0" PK_LO "\n\tv_pk_fma_f32 %1, %2, %4, %1" PK_LO "\n\tv_pk_fma_f32 %0, %2, %4, %0" PK_HI "\n\tv_pk_fma_f32 %1, %2, %3, %1" PK_HI PK_NEG
        : "+v"(xr), "+v"(xi) : "v"(a), "v"(hr), "v"(hi));
}
__device__ __forceinline__ void pk_conj_mul(f32x2& o_r, f32x2& o_i, f32x2 a, f32x2 hr, f32x2 hi) {
    asm("v_pk_mul_f32 %0, %2, %3 op_sel:[0,0] op_sel_hi:[0,1]\n\tv_pk_mul_f32 %1, %2, %4 op_sel:[0,0] op_sel_hi:[0,1] neg_lo:[1,0] neg_hi:[1,0]\n\t"
        "v_pk_fma_f32 %0, %2, %4, %0" PK_HI PK_NEG "\n\tv_pk_fma_f32 %1, %2, %3, %1" PK_HI PK_NEG : "=&v"(o_r), "=&v"(o_i) : "v"(a), "v"(hr), "v"(hi));
}
__device__ __forceinline__ void pk_conj_mac(f32x2& xr, f32x2& xi, f32x2 a, f32x2 hr, f32x2 hi) {
    asm("v_pk_fma_f32 %0, %2, %3, %0" PK_LO "\n\tv_pk_fma_f32 %1, %2, %4, %1" PK_LO PK_NEG "\n\tv_pk_fma_f32 %0, %2, %4, %0" PK_HI PK_NEG "\n\tv_pk_fma_f32 %1, %2, %3, %1" PK_HI PK_NEG
        : "+v"(xr), "+v"(xi) : "v"(a), "v"(hr), "v"(hi));
}
__device__ __forceinline__ void cmac2(f32x2& x, f32x2 a, f32x2 h) {
    asm("v_pk_fma_f32 %0, %1, %2, %0" PK_LO "\n\tv_pk_fma_f32 %0, %1, %2, %0 op_sel:[1,1,0] op_sel_hi:[1,0,1] neg_lo:[1,0,0]" : "+v"(x) : "v"(a), "v"(h));
}
#define PLN 0
__device__ __forceinline__ void c2_mac(f32x2& xr, f32x2& xi, float p, float q, float r, float s, f32x2 hr, f32x2 hi) {
    float a = xr[0], b = xi[0], c = xr[1], d = xi[1];
    asm("v_fmac_f32 %0, %4, %8\n\tv_fmac_f32 %1, %6, %9\n\tv_fmac_f32 %2, %4, %10\n\tv_fmac_f32 %3, %6, %11\n\t"
        "v_fmac_f32 %0, %5, %9\n\tv_fmac_f32 %1, %7, %8\n\tv_fmac_f32 %2, %5, %11\n\tv_fmac_f32 %3, %7, %10"
        : "+v"(a), "+v"(b), "+v"(c), "+v"(d) : "v"(p), "v"(q), "v"(r), "v"(s), "v"(hr[0]), "v"(hi[0]), "v"(hr[1]), "v"(hi[1]));
    xr = f32x2{a, c}; xi = f32x2{b, d};
}
__device__ __forceinline__ void c2_mad(f32x2& xr, f32x2& xi, float p, float q, float r, float s, f32x2 hr, f32x2 hi, f32x2 er, f32x2 ei) {
    float a, b, c, d;
    asm("v_fma_f32 %0, %4, %8, %12\n\tv_fma_f32 %1, %6, %9, %13\n\tv_fma_f32 %2, %4, %10, %14\n\tv_fma_f32 %3, %6, %11, %15\n\t"
        "v_fmac_f32 %0, %5, %9\n\tv_fmac_f32 %1, %7, %8\n\tv_fmac_f32 %2, %5, %11\n\tv_fmac_f32 %3, %7, %10"
        : "=&v"(a), "=&v"(b), "=&v"(c), "=&v"(d) : "v"(p), "v"(q), "v"(r), "v"(s), "v"(hr[0]), "v"(hi[0]), "v"(hr[1]), "v"(hi[1]), "v"(er[0]), "v"(ei[0]), "v"(er[1]), "v"(ei[1]));
    xr = f32x2{a, c}; xi = f32x2{b, d};
}
__device__ __forceinline__ void c2_mul(f32x2& xr, f32x2& xi, float p, float q, float r, float s, f32x2 hr, f32x2 hi) {
    float a, b, c, d;
    asm("v_mul_f32 %0, %4, %8\n\tv_mul_f32 %1, %6, %9\n\tv_mul_f32 %2, %4, %10\n\tv_mul_f32 %3, %6, %11\n\t"
        "v_fmac_f32 %0, %5, %9\n\tv_fmac_f32 %1, %7, %8\n\tv_fmac_f32 %2, %5, %11\n\tv_fmac_f32 %3, %7, %10"
        : "=&v"(a), "=&v"(b), "=&v"(c), "=&v"(d) : "v"(p), "v"(q), "v"(r), "v"(s), "v"(hr[0]), "v"(hi[0]), "v"(hr[1]), "v"(hi[1]));
    xr = f32x2{a, c}; xi = f32x2{b, d};
}
__device__ __forceinline__ float add1(float a, float b) { float c; asm("v_add_f32 %0, %1, %2" : "=v"(c) : "v"(a), "v"(b)); return c; }
__device__ __forceinline__ float sum4(float x) {
    const auto p = __builtin_amdgcn_permlane32_swap(__float_as_uint(x), __float_as_uint(x), false, false);
    x = __uint_as_float(p[0]) + __uint_as_float(p[1]);
    const auto q = __builtin_amdgcn_permlane16_swap(__float_as_uint(x), __float_as_uint(x), false, false);
    return __uint_as_float(q[0]) + __uint_as_float(q[1]);
}

template <int I, int N, class F>
__device__ __forceinline__ void static_for(F&& f) { if constexpr (I < N) { f(std::integral_constant<int, I>{}); static_for<I + 1, N>(f); } }

#define TS_DECL
#define TS0a()
#define TS0b()
#define TSE(i)
#define TS(i)
#define TS_FLUSH()

#define WAIT_V(n) asm volatile("s_waitcnt vmcnt(%0)" ::"n"((n) > 63 ? 63 : (n)) : "memory")
#define WAIT_L(n) asm volatile("s_waitcnt lgkmcnt(%0)" ::"n"((n) > 15 ? 15 : (n)) : "memory")
#define BAR() asm volatile("s_barrier" ::: "memory")
#define SB() __builtin_amdgcn_sched_barrier(0)
template <class T> __device__ __forceinline__ void tie_(T& x) { asm volatile("" : "+v"(x)); }
#define TIE(x) tie_(x)
__device__ __forceinline__ unsigned take(unsigned x) { unsigned y; asm volatile("v_mov_b32 %0, %1" : "=v"(y) : "v"(x)); return y; }
#define DEF_LD(name, ins) template <int OFF, class T> __device__ __forceinline__ void name(T& dst, unsigned voff, const char* sb) { \
    asm volatile(ins : "=v"(dst) : "v"(voff), "s"(sb), "n"(OFF) : "memory"); }
DEF_LD(gld_u16_, "global_load_ushort %0, %1, %2 offset:%3")
DEF_LD(gld_b32_, "global_load_dword %0, %1, %2 offset:%3")
DEF_LD(gld_b64_, "global_load_dwordx2 %0, %1, %2 offset:%3")
DEF_LD(gld_b128_, "global_load_dwordx4 %0, %1, %2 offset:%3")
#define NTQ " nt"
#define NTK " nt"
DEF_LD(gld_b64_nt_, "global_load_dwordx2 %0, %1, %2 offset:%3" NTQ)
DEF_LD(gld_b128_nt_, "global_load_dwordx4 %0, %1, %2 offset:%3" NTQ)
DEF_LD(gld_b64_sc1_, "global_load_dwordx2 %0, %1, %2 offset:%3 sc1")
DEF_LD(gld_b128_sc1_, "global_load_dwordx4 %0, %1, %2 offset:%3 sc1")
#define DEF_ST(name, ins) template <int OFF, class T> __device__ __forceinline__ void name(unsigned voff, const T& v, const char* sb) { \
    asm volatile(ins :: "v"(voff), "v"(v), "s"(sb), "n"(OFF) : "memory"); }
DEF_ST(gst_b128_, "global_store_dwordx4 %0, %1, %2 offset:%3\n\ts_nop 1")
DEF_ST(gst_b128_nt_, "global_store_dwordx4 %0, %1, %2 offset:%3 nt\n\ts_nop 1")
DEF_ST(gst_b128_sc1_, "global_store_dwordx4 %0, %1, %2 offset:%3 sc1\n\ts_nop 1")
DEF_ST(gst_b64_sc1_, "global_store_dwordx2 %0, %1, %2 offset:%3 sc1")
#define GLD_U16(dst, voff, sb) gld_u16_<0>(dst, voff, sb)
#define GLD_B32(dst, voff, sb, off) gld_b32_<(off)>(dst, voff, sb)
#define GLD_B64(dst, voff, sb, off) gld_b64_<(off)>(dst, voff, sb)
#define GLD_B128(dst, voff, sb, off) gld_b128_<(off)>(dst, voff, sb)
#define GLD_B64_NT(dst, voff, sb, off) gld_b64_nt_<(off)>(dst, voff, sb)
#define GLD_B128_NT(dst, voff, sb, off) gld_b128_nt_<(off)>(dst, voff, sb)
#define GLD_B64_SC1(dst, voff, sb, off) gld_b64_sc1_<(off)>(dst, voff, sb)
#define GLD_B128_SC1(dst, voff, sb, off) gld_b128_sc1_<(off)>(dst, voff, sb)
#define GST_B128(voff, v, sb, off) gst_b128_<(off)>(voff, v, sb)
#define GST_B128_NT(voff, v, sb, off) gst_b128_nt_<(off)>(voff, v, sb)
#define GST_B128_SC1(voff, v, sb, off) gst_b128_sc1_<(off)>(voff, v, sb)
#define GST_B64_SC1(voff, v, sb) gst_b64_sc1_<0>(voff, v, sb)
__device__ __forceinline__ void dma_(unsigned ldsaddr, unsigned voff, const char* sb) {
    asm volatile("s_mov_b32 m0, %0\n\ts_nop 0\n\tglobal_load_lds_dwordx4 %1, %2" :: "s"(ldsaddr), "v"(voff), "s"(sb) : "memory"); }
__device__ __forceinline__ void dma_nt_(unsigned ldsaddr, unsigned voff, const char* sb) {
    asm volatile("s_mov_b32 m0, %0\n\ts_nop 0\n\tglobal_load_lds_dwordx4 %1, %2 nt" :: "s"(ldsaddr), "v"(voff), "s"(sb) : "memory"); }
#define DMA(ldsaddr, voff, sb) dma_(ldsaddr, voff, sb)
template <int OFF, class T> __device__ __forceinline__ void dsr128_(T& dst, unsigned addr) { asm volatile("ds_read_b128 %0, %1 offset:%2" : "=v"(dst) : "v"(addr), "n"(OFF)); }
template <int OFF, class T> __device__ __forceinline__ void dsr64_(T& dst, unsigned addr) { asm volatile("ds_read_b64 %0, %1 offset:%2" : "=v"(dst) : "v"(addr), "n"(OFF)); }
template <int OFF, class T> __device__ __forceinline__ void dsr2_(T& dst, unsigned addr) { asm volatile("ds_read2_b32 %0, %1 offset0:%2 offset1:%3" : "=v"(dst) : "v"(addr), "n"(OFF), "n"(OFF + 1)); }
#define DSR128(dst, addr, off) dsr128_<(off)>(dst, addr)
#define DSR64(dst, addr, off) dsr64_<(off)>(dst, addr)

__device__ __forceinline__ void kv4(unsigned& o01, unsigned& o23, unsigned ka, unsigned kb, unsigned kc, unsigned kd, const unsigned* w, unsigned v0, unsigned v1) {
    unsigned t1, t3, p0, p1, p2, p3;
    asm(
        "v_dot2_f32_bf16 %0, %8, %19, 0\n\tv_dot2_f32_bf16 %1, %9, %18, 0\n\tv_dot2_f32_bf16 %2, %9, %19, 0\n\tv_dot2_f32_bf16 %3, %10, %18, 0\n\t"
        "v_dot2_f32_bf16 %4, %9, %16, 0\n\tv_dot2_f32_bf16 %5, %9, %17, 0\n\tv_dot2_f32_bf16 %6, %10, %16, 0\n\tv_dot2_f32_bf16 %7, %10, %17, 0\n\t"
        "v_cvt_pk_bf16_f32 %0, %0, %4\n\tv_cvt_pk_bf16_f32 %1, %1, %5\n\tv_cvt_pk_bf16_f32 %2, %2, %6\n\tv_cvt_pk_bf16_f32 %3, %3, %7\n\t"
        "v_dot2_f32_bf16 %4, %9, %15, 0\n\tv_dot2_f32_bf16 %5, %10, %14, 0\n\tv_dot2_f32_bf16 %6, %10, %15, 0\n\tv_dot2_f32_bf16 %7, %11, %14, 0\n\t"
        "v_dot2_f32_bf16 %0, %0, %22, 0\n\tv_dot2_f32_bf16 %1, %1, %22, 0\n\tv_dot2_f32_bf16 %2, %2, %22, 0\n\tv_dot2_f32_bf16 %3, %3, %22, 0\n\t"
        "v_cvt_pk_bf16_f32 %0, %0, %4\n\tv_cvt_pk_bf16_f32 %1, %1, %5\n\tv_cvt_pk_bf16_f32 %2, %2, %6\n\tv_cvt_pk_bf16_f32 %3, %3, %7\n\t"
        "v_dot2_f32_bf16 %4, %10, %12, 0\n\tv_dot2_f32_bf16 %5, %10, %13, 0\n\tv_dot2_f32_bf16 %6, %11, %12, 0\n\tv_dot2_f32_bf16 %7, %11, %13, 0\n\t"
        "v_dot2_f32_bf16 %0, %0, %22, 0\n\tv_dot2_f32_bf16 %1, %1, %22, 0\n\tv_dot2_f32_bf16 %2, %2, %22, 0\n\tv_dot2_f32_bf16 %3, %3, %22, 0\n\t"
        "v_cvt_pk_bf16_f32 %0, %0, %4\n\tv_cvt_pk_bf16_f32 %1, %1, %5\n\tv_cvt_pk_bf16_f32 %2, %2, %6\n\tv_cvt_pk_bf16_f32 %3, %3, %7\n\t"
        "v_dot2_f32_bf16 %0, %0, %22, 0\n\tv_dot2_f32_bf16 %1, %1, %22, 0\n\tv_dot2_f32_bf16 %2, %2, %22, 0\n\tv_dot2_f32_bf16 %3, %3, %22, 0\n\t"
        "v_cvt_pk_bf16_f32 %0, %0, 0\n\tv_cvt_pk_bf16_f32 %1, 0, %1\n\tv_cvt_pk_bf16_f32 %2, %2, 0\n\tv_cvt_pk_bf16_f32 %3, 0, %3\n\t"
        "v_dot2_f32_bf16 %0, %0, %20, 0\n\tv_dot2_f32_bf16 %1, %1, %20, 0\n\tv_dot2_f32_bf16 %2, %2, %21, 0\n\tv_dot2_f32_bf16 %3, %3, %21, 0\n\t"
        "s_nop 1\n\tv_cvt_pk_bf16_f32 %0, %1, %0\n\tv_cvt_pk_bf16_f32 %2, %3, %2\n\t"
        : "=&v"(o01), "=&v"(t1), "=&v"(o23), "=&v"(t3), "=&v"(p0), "=&v"(p1), "=&v"(p2), "=&v"(p3)
        : "v"(ka), "v"(kb), "v"(kc), "v"(kd), "v"(w[0]), "v"(w[1]), "v"(w[2]), "v"(w[3]), "v"(w[4]), "v"(w[5]), "v"(w[6]), "v"(w[7]), "v"(v0), "v"(v1), "s"(0x3f803f80u));
}
__device__ __forceinline__ void kv4p(unsigned& o01, unsigned& o23, unsigned ka, unsigned kb, unsigned kc, unsigned kd, const unsigned* w, unsigned v0, unsigned v1) {
    unsigned t1, t3, p0, p1, p2, p3, e0, e1, e2, e3, e4, e5, e6;
    asm(
        "v_and_b32 %8, %25, %15\n\tv_lshlrev_b32 %9, 16, %16\n\tv_and_b32 %10, %25, %16\n\tv_lshlrev_b32 %11, 16, %17\n\t"
        "v_and_b32 %12, %25, %17\n\tv_mul_f32 %0, %22, %8\n\tv_mul_f32 %1, %22, %9\n\tv_mul_f32 %2, %22, %10\n\t"
        "v_mul_f32 %3, %22, %11\n\tv_mul_f32 %4, %21, %9\n\tv_mul_f32 %5, %21, %10\n\tv_mul_f32 %6, %21, %11\n\t"
        "v_mul_f32 %7, %21, %12\n\tv_lshlrev_b32 %13, 16, %18\n\tv_and_b32 %14, %25, %18\n\tv_cvt_pk_bf16_f32 %0, 0, %0\n\t"
        "v_cvt_pk_bf16_f32 %1, 0, %1\n\tv_cvt_pk_bf16_f32 %2, 0, %2\n\tv_cvt_pk_bf16_f32 %3, 0, %3\n\tv_cvt_pk_bf16_f32 %4, 0, %4\n\t"
        "v_cvt_pk_bf16_f32 %5, 0, %5\n\tv_cvt_pk_bf16_f32 %6, 0, %6\n\tv_cvt_pk_bf16_f32 %7, 0, %7\n\tv_add_f32 %0, %0, %4\n\t"
        "v_add_f32 %1, %1, %5\n\tv_add_f32 %2, %2, %6\n\tv_add_f32 %3, %3, %7\n\tv_mul_f32 %4, %20, %10\n\t"
        "v_mul_f32 %5, %20, %11\n\tv_mul_f32 %6, %20, %12\n\tv_mul_f32 %7, %20, %13\n\tv_cvt_pk_bf16_f32 %0, 0, %0\n\t"
        "v_cvt_pk_bf16_f32 %1, 0, %1\n\tv_cvt_pk_bf16_f32 %2, 0, %2\n\tv_cvt_pk_bf16_f32 %3, 0, %3\n\tv_cvt_pk_bf16_f32 %4, 0, %4\n\t"
        "v_cvt_pk_bf16_f32 %5, 0, %5\n\tv_cvt_pk_bf16_f32 %6, 0, %6\n\tv_cvt_pk_bf16_f32 %7, 0, %7\n\tv_add_f32 %0, %0, %4\n\t"
        "v_add_f32 %1, %1, %5\n\tv_add_f32 %2, %2, %6\n\tv_add_f32 %3, %3, %7\n\tv_mul_f32 %4, %19, %11\n\t"
        "v_mul_f32 %5, %19, %12\n\tv_mul_f32 %6, %19, %13\n\tv_mul_f32 %7, %19, %14\n\tv_cvt_pk_bf16_f32 %0, 0, %0\n\t"
        "v_cvt_pk_bf16_f32 %1, 0, %1\n\tv_cvt_pk_bf16_f32 %2, 0, %2\n\tv_cvt_pk_bf16_f32 %3, 0, %3\n\tv_cvt_pk_bf16_f32 %4, 0, %4\n\t"
        "v_cvt_pk_bf16_f32 %5, 0, %5\n\tv_cvt_pk_bf16_f32 %6, 0, %6\n\tv_cvt_pk_bf16_f32 %7, 0, %7\n\tv_add_f32 %0, %0, %4\n\t"
        "v_add_f32 %1, %1, %5\n\tv_add_f32 %2, %2, %6\n\tv_add_f32 %3, %3, %7\n\tv_lshlrev_b32 %4, 16, %23\n\t"
        "v_and_b32 %5, %25, %23\n\tv_lshlrev_b32 %6, 16, %24\n\tv_and_b32 %7, %25, %24\n\tv_cvt_pk_bf16_f32 %0, 0, %0\n\t"
        "v_cvt_pk_bf16_f32 %1, 0, %1\n\tv_cvt_pk_bf16_f32 %2, 0, %2\n\tv_cvt_pk_bf16_f32 %3, 0, %3\n\tv_mul_f32 %0, %0, %4\n\t"
        "v_mul_f32 %1, %1, %5\n\tv_mul_f32 %2, %2, %6\n\tv_mul_f32 %3, %3, %7\n\tv_cvt_pk_bf16_f32 %0, %1, %0\n\t"
        "v_cvt_pk_bf16_f32 %2, %3, %2\n\t"
        : "=&v"(o01), "=&v"(t1), "=&v"(o23), "=&v"(t3), "=&v"(p0), "=&v"(p1), "=&v"(p2), "=&v"(p3), "=&v"(e0), "=&v"(e1), "=&v"(e2), "=&v"(e3), "=&v"(e4), "=&v"(e5), "=&v"(e6)
        : "v"(ka), "v"(kb), "v"(kc), "v"(kd), "v"(w[1]), "v"(w[3]), "v"(w[5]), "v"(w[7]), "v"(v0), "v"(v1), "s"(0xffff0000u));
}
__device__ __forceinline__ f32x4 mul4bf(u32x2 x, const unsigned* d) {
    float a, b, c, e;
    asm("v_dot2_f32_bf16 %0, %4, %6, 0\n\tv_dot2_f32_bf16 %1, %4, %7, 0\n\tv_dot2_f32_bf16 %2, %5, %8, 0\n\tv_dot2_f32_bf16 %3, %5, %9, 0\n\t"
        "v_cvt_pk_bf16_f32 %0, 0, %0\n\tv_cvt_pk_bf16_f32 %1, 0, %1\n\tv_cvt_pk_bf16_f32 %2, 0, %2\n\tv_cvt_pk_bf16_f32 %3, 0, %3"
        : "=&v"(a), "=&v"(b), "=&v"(c), "=&v"(e) : "v"(x[0]), "v"(x[1]), "v"(d[0]), "v"(d[1]), "v"(d[2]), "v"(d[3]));
    return f32x4{a, b, c, e};
}
template <int BIT> __device__ __forceinline__ void quad_swap(u32x4& a, u32x4& b) {
    unsigned n0, n1, n2, n3, b0 = b[0], b1 = b[1], b2 = b[2], b3 = b[3];
#define QS_(d, s, k) "v_cndmask_b32_dpp " d ", " s ", " k ", vcc quad_perm:[%14,%15,%16,%17] row_mask:0xf bank_mask:0xf\n\t"
    asm("s_mov_b64 vcc, %12\n\ts_nop 0\n\t" QS_("%0", "%4", "%8") QS_("%1", "%5", "%9") QS_("%2", "%6", "%10") QS_("%3", "%7", "%11")
        "s_mov_b64 vcc, %13\n\t" QS_("%4", "%8", "%4") QS_("%5", "%9", "%5") QS_("%6", "%10", "%6") QS_("%7", "%11", "%7")
        : "=&v"(n0), "=&v"(n1), "=&v"(n2), "=&v"(n3), "+v"(b0), "+v"(b1), "+v"(b2), "+v"(b3)
        : "v"(a[0]), "v"(a[1]), "v"(a[2]), "v"(a[3]), "s"(BIT ? 0x3333333333333333ull : 0x5555555555555555ull), "s"(BIT ? 0xccccccccccccccccull : 0xaaaaaaaaaaaaaaaaull),
          "n"(BIT ? 2 : 1), "n"(BIT ? 3 : 0), "n"(BIT ? 0 : 3), "n"(BIT ? 1 : 2) : "vcc");
#undef QS_
    a = u32x4{n0, n1, n2, n3}; b = u32x4{b0, b1, b2, b3};
}
#define IF_(c, C) "s_cmp_lg_u32 " c ", " C "\n\ts_cbranch_scc1 1f\n\t"
template <int C, int N> __device__ __forceinline__ void wait_if(int c) { asm volatile(IF_("%0", "%1") "s_waitcnt vmcnt(%2)\n1:" :: "s"(c), "n"(C), "n"(N) : "scc", "memory"); }
template <int C> __device__ __forceinline__ void flag_if(int c, unsigned voff, u32x2 v, const char* sb) {
    asm volatile(IF_("%0", "%1") "s_mov_b64 exec, 1\n\tglobal_store_dwordx2 %2, %3, %4 sc1\n\ts_mov_b64 exec, -1\n1:" :: "s"(c), "n"(C), "v"(voff), "v"(v), "s"(sb) : "scc", "memory"); }
template <int C> __device__ __forceinline__ void st4_if(int c, unsigned voff, const u32x4* v, const char* s0, const char* s1) {
    asm volatile(IF_("%0", "%1") "global_store_dwordx4 %2, %3, %7 sc1\n\tglobal_store_dwordx4 %2, %4, %7 offset:1024 sc1\n\tglobal_store_dwordx4 %2, %5, %8 sc1\n\tglobal_store_dwordx4 %2, %6, %8 offset:1024 sc1\n\ts_nop 1\n1:"
                 :: "s"(c), "n"(C), "v"(voff), "v"(v[0]), "v"(v[1]), "v"(v[2]), "v"(v[3]), "s"(s0), "s"(s1) : "scc", "memory"); }
template <int C> __device__ __forceinline__ void ld4_if(int c, unsigned voff, u32x4* v, const char* s0) {
    asm volatile(IF_("%4", "%5") "global_load_dwordx4 %0, %6, %7 nt\n\tglobal_load_dwordx4 %1, %6, %7 offset:1024 nt\n\tglobal_load_dwordx4 %2, %6, %7 offset:2048 nt\n\tglobal_load_dwordx4 %3, %6, %7 offset:3072 nt\n1:"
                 : "+v"(v[0]), "+v"(v[1]), "+v"(v[2]), "+v"(v[3]) : "s"(c), "n"(C), "v"(voff), "s"(s0) : "scc", "memory"); }
template <int C> __device__ __forceinline__ void ld8_if(int c, unsigned voff, u32x2* v, const char* s0, const char* s1) {
    asm volatile(IF_("%8", "%9") "global_load_dwordx2 %0, %10, %11 offset:-4096 nt\n\tglobal_load_dwordx2 %1, %10, %11 offset:-2048 nt\n\tglobal_load_dwordx2 %2, %10, %11 nt\n\tglobal_load_dwordx2 %3, %10, %11 offset:2048 nt\n\t"
                 "global_load_dwordx2 %4, %10, %12 offset:-4096 nt\n\tglobal_load_dwordx2 %5, %10, %12 offset:-2048 nt\n\tglobal_load_dwordx2 %6, %10, %12 nt\n\tglobal_load_dwordx2 %7, %10, %12 offset:2048 nt\n1:"
                 : "+v"(v[0]), "+v"(v[1]), "+v"(v[2]), "+v"(v[3]), "+v"(v[4]), "+v"(v[5]), "+v"(v[6]), "+v"(v[7]) : "s"(c), "n"(C), "v"(voff), "s"(s0), "s"(s1) : "scc", "memory"); }
template <int E> __device__ __forceinline__ void sy_post(unsigned myaddr) { asm volatile("ds_write_b32 %0, %0 offset:%1" :: "v"(myaddr), "n"(E * 64) : "memory"); }
template <int E, int SL = 1> __device__ __forceinline__ unsigned sy_wait(unsigned addr) {
    unsigned v;
    asm volatile("s_branch 2f\n1:\n\ts_sleep %3\n2:\n\tds_read_b32 %0, %1 offset:%2\n\ts_waitcnt lgkmcnt(0)\n\tv_cmp_eq_u32 vcc, 0, %0\n\ts_cbranch_vccnz 1b"
                 : "=&v"(v) : "v"(addr), "n"(E * 64), "n"(SL) : "vcc", "memory");
    return v;
}
template <int OFF, class T> __device__ __forceinline__ void dsrtr_(T& dst, unsigned addr) { asm volatile("ds_read_b64_tr_b16 %0, %1 offset:%2" : "=v"(dst) : "v"(addr), "n"(OFF)); }

constexpr size_t WS_H = 0, WS_KVT = (size_t)10 << 20, WS_ZT = (size_t)15 << 20, WS_E = (size_t)20 << 20, WS_FLAG = WS_E + (2048 << 10);
#define HP 65536
#define KP 8192
#define ZP 4096
constexpr int FL_KV = 0, FL_Z = 65536, FL_H = 131072, FL_EPOCH = 196608;
constexpr unsigned SPIN_CAP = 1u << 17;
#define ABL 0
#define CSPLIT 1
#define MERGE 3
#define CB 13
#define PAUSE 0
#define QLATE 0
#define CVPRIO 0
#define STG_SEL (idx & 3)
#define SP0 3
#define SDP 1
#define LP0 3
#define LDP 2
#define KVS 5
#define KVU 3
#define QIT 1
#define ZB 3
#define OS 2
#define MPRIO 2

#define ROT 0x80A5
#define R2 0x41
#define CPF 0
#define PRE 4
#define PRO 9
#define G1NT 0
#define G1NTS 16
#define TMIX 0
#define TLBT 0
#define CDEF 0
#define CVLATE 1
#define FPRIO 3
#define YS 0
#define DP0 1
#define DP1 5
#define AWB 1
#define PK 0
#define PU 13
#define PC0 13
#define PC1 14
#define PC2 15
#define PC3 16
#define PQ 0
#define SPF 0
#define SPH 0
#define NB1 4
#define EP 1
#define ESL 0
#define CFB 0
#define P1 1
#define P0 1
#define BKE 0
#define EALL 0
#define EREP 0
#define TOUCH 0
#define FILLB 0
#define CPOST 1
#define CEARLY 0
#define NOOWN 0
#define PFIRST 0
#define ZB0 0
#define CATCH 4
#define PWE 0
#define SWAP 1
#define KVM 0x1F0
#define KFD 3
#define ZW 2
#define ZX 1
#define ZP0 3
#define BARM 0x22220
#define NR 6
#define WPS 2
#define CD0 3
#define AWX 8
#define VBM 0
#define ZOWN 1
#define ZPS 4
#define XAB 0
#define QM 1
#define OSG 0
#define KPL 0xF0
#define KPRIO 0
#define GPRIO 1
#define PST 0x7654
#define PLT 0xA864
#define LC 0
#define PRE2 PRE
#define KVD 0
#define PAIR 1
#define G2P1 0
#define EPRIO 0
#define LSW 1
#define EGX 28
#define CR1 0
#define CR2 0
#define CR3 0
#define CRL (2 * NR - CR1 - CR2 - CR3)
#define UE 1
#define UPRIO 0
#define AWN 8
#define CRATE 2
#define ZSB 0
#define ZFB 1
#define ZSL 0
#define OSL 0
constexpr int it_cnt(int p, int k, int last) {
    const int pos[8] = {PK, PU, PC0, PC1, PQ, PU - 1, PC2, PC3}, n[8] = {4, 4, 4, 4, 8, 1, 4, 4};
    int c = 0;
    for (int i = 0; i < 8; ++i) if (pos[i] <= last && (pos[i] > p || (pos[i] == p && i > k))) c += n[i];
    return c;
}
constexpr int nib(int t, int c) { return (t >> (4 * c)) & 15; }
constexpr bool nib_any(int t, int v) { return nib(t, 0) == v || nib(t, 1) == v || nib(t, 2) == v || nib(t, 3) == v; }
constexpr int kv_wait(int late) {
    int t = 0;
    for (int c = 0; c < 4; ++c) t |= ((nib(PLT, c) >= nib(PST, c) && nib(PLT, c) < nib(PST, c) + KFD) == late ? nib(PST, c) + KFD : 15) << (4 * c);
    return t;
}
constexpr int G1STAGE = 32768;
constexpr int g1_slot(int s) { return (NB1 == 5 ? (s + 3) % 5 : s & 3) * G1STAGE; }
constexpr int L_KS = NB1 == 5 ? 65536 : 131072, L_VS = L_KS + 16384;
constexpr int L_TS = 3 * G1STAGE, L_TR = L_TS + 3072;
constexpr int CST = 4288;
constexpr int UOFF = 2 * CST + 128, UPL = 2304;
constexpr int CHSZ = UOFF + 2 * UPL + 32;
constexpr int L_AGG = 4 * CHSZ, L_ES = L_AGG + 15 * 512, L_SY = L_ES + 2048;
constexpr int L_HT = 98304;
constexpr int L_C = 98304, G2STAGE = 16384;
constexpr int L_Y = 16384;
static_assert(L_SY + 384 <= 65536 && L_AGG >= 49152 && UOFF % 16 == 0 && CHSZ % 256 == 32, "lds");

__device__ __forceinline__ int img_off(int r, int c) { return (r >> 1) * 256 + ((((r & 1) * 8 + c) ^ ((r >> 1) & 15)) << 4); }

constexpr int g1_upto(int g) {
    int n = PRO;
    for (int t = 0; t <= g; ++t) {
        int sb = (t + 1) / 2, sub = (t + 1) % 2, allowed = sb >= 1 ? 4 * (sb + NB1 - 1) + 2 * (sub + 1) : 4 * NB1;
        allowed -= LC;
        if (allowed < n) allowed = n;
        if (allowed > 64) allowed = 64;
        if (t >= 2 * LP0 - 1 && t < 2 * LP0 - 1 + PAUSE) continue;
        n = n + CATCH < allowed ? n + CATCH : allowed;
    }
    return n;
}
constexpr int g1_cnt(int s) { return g1_upto(2 * s - 2) - 4 * (s + 1); }
static_assert(g1_upto(28) == 64, "schedule");
constexpr int AW = AWN;
constexpr int g2_place(int s) { return s < 5 ? L_C + ((s + 2) & 3) * G2STAGE : s < 7 ? (s - 1) * G2STAGE : s < 10 ? (9 - s) * G2STAGE : g2_place(s - 9); }
constexpr bool g2_bar(int g) { return (BARM >> g) & 1; }
struct G2Plan { int upto[33], wr[33], ald[64][2], seq[164], use[64]; };
constexpr G2Plan g2_plan() {
    G2Plan p = {};
    int nc = 14 + 2 * NR, nw = 0, nx = 4, lastb = 0, n = 0;
    for (int i = 0; i < 164; ++i) p.seq[i] = 1000;
    for (; nx < 4 + AW; ++nx) p.seq[nx] = ++n;
    for (int t = 0; t < 32; ++t) {
        p.upto[t] = nc; p.wr[t] = nw;
        if (t < 2) continue;
        if (g2_bar(t)) lastb = t;
        const int w = AW + nw < AWX ? AW + nw : AWX;
        for (int f = 2 * t; f < 2 * t + 2; ++f) {
            p.use[f] = n;
            if (nc < 32 && nc == p.upto[t] + (f & 1) && (f & 1) < CRATE && t >= CD0 && lastb >= (nc < 20 ? 1 : (nc | 1) - 18)) p.seq[100 + nc++] = ++n;
            for (int k = 0; k < 2; ++k) {
                p.ald[f][k] = -1;
                if (nx < 64 && nx - f - k < w) { p.ald[f][k] = nx; p.seq[nx++] = ++n; }
            }
        }
        for (int k = 0; k < WPS; ++k) if (nw < 2 * NR && lastb >= (nw < 6 ? 1 : (nw | 1) - 4)) ++nw;
    }
    p.upto[32] = nc; p.wr[32] = nw;
    return p;
}
constexpr G2Plan G2P = g2_plan();
constexpr int g2_upto(int g) { return G2P.upto[g + 1]; }
constexpr int g2_wr(int g) { return G2P.wr[g + 1]; }
constexpr int g2_ald(int f, int k) { return G2P.ald[f][k]; }
constexpr int g2_cnt(int id, int at) { return G2P.use[at] - G2P.seq[id]; }
constexpr int g2_cover(int g) { int s = (g + 1) / 2; while (s < 15 && !g2_bar(2 * s + 1)) ++s; return s; }
static_assert(BARM >> 17 && g2_cover(3) < 7 && CRATE <= 2, "the epilogue needs stage 8 behind a barrier");

constexpr int cv_m(int o) { return (o & 1) ? 7 - (o >> 1) : o >> 1; }
constexpr int cv_first(int o) { int p = 0; for (int i = 0; i < o; ++i) p += 8 - cv_m(i); return p; }
constexpr int cv_grp(int p) { int o = 0; while (cv_first(o + 1) <= p) ++o; return o; }
constexpr int cv_read(int p) {
    int slot[40] = {};
    for (int i = 0; i < 40; ++i) slot[i] = -1;
    slot[0] = 2 * 3 + 1;
    for (int o = 0; o < 8; ++o) {
        int q = cv_first(o + 1) - 1;
        for (int h = 0; h < 2; ++h) { while (slot[q] >= 0) ++q; slot[q] = 2 * (o + 4) + h; }
    }
    const int bs[8] = {1, 3, 5, 11, 12, 13, 19, 21};
    for (int jb = 0; jb < 8; ++jb) slot[bs[jb]] = 32 + jb;
    return slot[p];
}
static_assert(cv_read(35) == 22 && cv_read(36) == 23 && cv_read(37) == -1 && cv_read(10) == 11 && cv_read(15) == 12 && cv_read(18) == 15 && cv_read(24) == 17, "conv schedule");
constexpr int cv_wait(int o) {
    int n = 0;
    for (int q = cv_first(o) - 1;; --q) {
        const int rd = cv_read(q < 0 ? q + 36 : q);
        if (rd == 2 * (o + (q < 0 ? 8 : 0)) + 1) return n;
        if (rd >= 0) ++n;
    }
}

struct Args {
    const char *q, *k, *v, *sk, *ssmk, *ssmD, *x, *lre, *lim, *bre, *bim, *cre, *cim, *logdt, *dskip;
    char *out, *ws;
};

typedef __attribute__((ext_vector_type(8))) unsigned long long u64x8;
__global__ __launch_bounds__(768, 3) void fused(Args) {
    __shared__ __attribute__((aligned(1024))) char shm[163840];
    TS0a();
    u64x8 ka0, ka1; unsigned long long ka2;
    const char* pc0;
    int code_n;
    asm volatile("s_load_dwordx16 %0, %5, 0x0\n\ts_load_dwordx16 %1, %5, 0x40\n\ts_load_dwordx2 %2, %5, 0x80\n\t"
                 "s_getpc_b64 %3\n.Lfk_code_0:\n\ts_mov_b32 %4, .Lfk_code_a-.Lfk_code_0-4\n\ts_max_i32 %4, %4, .Lfk_code_b-.Lfk_code_0-4"
                 : "=&s"(ka0), "=&s"(ka1), "=&s"(ka2), "=&s"(pc0), "=&s"(code_n) : "s"(__builtin_amdgcn_kernarg_segment_ptr()) : "scc");
    const int tid = threadIdx.x, lane = tid & 63, wraw = __builtin_amdgcn_readfirstlane(tid >> 6);
    const int wid = !SWAP ? wraw : wraw < 4 ? wraw + 8 : wraw - 4;
    const int b = blockIdx.x, xcd = b & 7, idx = b >> 3;
    asm volatile("s_cmp_lt_u32 %0, 8\n\ts_cbranch_scc1 1f\n\ts_setprio 3\n\ts_cmp_lg_u32 %0, 8\n\ts_cbranch_scc1 1f\n\t"
                 "v_lshl_add_u32 v161, %1, 7, %2\n\tv_min_i32 v161, %3, v161\n\ts_mov_b64 exec, 0xffff\n\tglobal_load_dword v161, v161, %4\n\ts_mov_b64 exec, -1\n1:"
                 :: "s"(wid), "v"(lane), "s"(idx * 2048), "s"(code_n), "s"(pc0) : "scc", "memory", "v161");
    TS_DECL
    const int tm = (xcd >> 1) * 4 + (idx >> 3), tn = (xcd & 1) * 8 + (idx & 7);
    const int wm = wid & 3, wn = (wid >> 2) & 1, l31 = lane & 31, lh = lane >> 5;
    const unsigned lb = (unsigned)(uintptr_t)shm;
    const unsigned l16 = lane * 16;
    const int l15 = lane & 15, g4 = lane >> 4, sq = 2 * wn + (wm & 1), st = sq * 16 + l15, run = (wm >> 1) * 4 + g4;
    unsigned kvv[4];
    const int trow = idx * 64 * 2048 + xcd * 256;
    if (wid < 8) {
        kvv[0] = trow + wm * 32768 + g4 * 2048 + ((l15 ^ (2 * g4)) << 4);
        kvv[1] = kvv[0] + 8192; kvv[2] = (kvv[0] ^ 128) + 16384; kvv[3] = kvv[2] + 8192;
    } else {
        kvv[0] = trow + (idx ? -4 * 2048 : 0) + g4 * 2048 + wm * 64 + l15 * 4;
        asm volatile("v_writelane_b32 %0, 0, 0\n\tv_writelane_b32 %0, 4, 1\n\tv_writelane_b32 %0, %1, 2" : "+v"(kvv[0]) : "s"((xcd * 64 + idx * 2 + (wm >> 1)) * 4));
        kvv[1] = (xcd * 128 + wm * 32 + l31) * 8;
        kvv[2] = kvv[3] = (tn * 64 + lane) * 2;
    }
    unsigned g1dst = lb + wid * 4096;
    asm volatile("s_waitcnt lgkmcnt(0)" : "+s"(ka0), "+s"(ka1), "+s"(ka2), "+s"(g1dst), "+v"(kvv[0]), "+v"(kvv[1]), "+v"(kvv[2]), "+v"(kvv[3]));
    Args a;
    a.q = (const char*)ka0[0]; a.k = (const char*)ka0[1]; a.v = (const char*)ka0[2]; a.sk = (const char*)ka0[3]; a.ssmk = (const char*)ka0[4]; a.ssmD = (const char*)ka0[5];
    a.x = (const char*)ka0[6]; a.lre = (const char*)ka0[7]; a.lim = (const char*)ka1[0]; a.bre = (const char*)ka1[1]; a.bim = (const char*)ka1[2]; a.cre = (const char*)ka1[3];
    a.cim = (const char*)ka1[4]; a.logdt = (const char*)ka1[5]; a.dskip = (const char*)ka1[6]; a.out = (char*)ka1[7]; a.ws = (char*)ka2;
    char* const ws = a.ws;
    const char* const flg = ws + WS_FLAG;
    TS(26);
    asm volatile("s_cmp_gt_u32 %[w], 7\n\ts_cbranch_scc1 1f\n\t"
                 "s_add_u32 m0, %[m], %[n0]\n\ts_nop 0\n\tglobal_load_lds_dwordx4 %[k0], %[kv]" NTK "\n\ts_add_u32 m0, %[m], %[n0]+1024\n\ts_nop 0\n\tglobal_load_lds_dwordx4 %[k1], %[kv]" NTK "\n\t"
                 "s_add_u32 m0, %[m], %[n0]+2048\n\ts_nop 0\n\tglobal_load_lds_dwordx4 %[k2], %[kv]" NTK "\n\ts_add_u32 m0, %[m], %[n0]+3072\n\ts_nop 0\n\tglobal_load_lds_dwordx4 %[k3], %[kv]" NTK "\n\t"
                 "s_branch 2f\n1:\n\tglobal_load_ushort v164, %[k2], %[c1]\n\tglobal_load_ushort v165, %[k2], %[c2]\n\tglobal_load_ushort v166, %[k2], %[c3]\n\t"
                 "v_mov_b64 v[162:163], %[kv]\n\ts_mov_b64 exec, 7\n\tv_mov_b64 v[162:163], %[d]\n\ts_mov_b64 exec, 3\n\tv_mov_b64 v[162:163], %[e]\n\ts_mov_b64 exec, -1\n\t"
                 "v_mad_u64_u32 v[162:163], vcc, %[k0], 1, v[162:163]\n\tglobal_load_dword v160, v[162:163], off sc1\n\tglobal_load_dwordx2 v[162:163], %[k1], %[sk]\n2:"
                 :
                 : [w] "s"(wid), [c1] "s"(a.logdt), [c2] "s"(a.lre), [c3] "s"(a.lim), [d] "s"(a.ssmD), [e] "s"(flg + FL_EPOCH), [sk] "s"(a.sk),
                   [k0] "v"(kvv[0]), [k1] "v"(kvv[1]), [k2] "v"(kvv[2]), [k3] "v"(kvv[3]), [kv] "s"(wn ? a.v : a.k), [m] "s"(g1dst), [n0] "n"(L_KS)
                 : "scc", "vcc", "memory", "v160", "v161", "v162", "v163", "v164", "v165", "v166");
    TS0b();
    TS(27);
    if (P1) BAR();
#define SLICE_TABLE() unsigned slo, shi; { int ix = idx; asm volatile("" : "+s"(ix)); \
        const int rot2 = (((R2 >> 4) & 15) * (ix >> 3) + (R2 & 15) * (xcd >> 1)) & 15, es = (tn - rot2) & 15; \
        const unsigned long long id = 0xFEDCBA9876543210ull, seq = rot2 ? (id >> (4 * rot2)) | (id << (64 - 4 * rot2)) : id; \
        const unsigned long long lo = (1ull << (4 * es)) - 1, tab = ((seq & lo) << 4) | (seq & ~((lo << 4) | 15)) | tn; \
        slo = (unsigned)tab; shi = (unsigned)(tab >> 32); asm volatile("" : "+s"(slo), "+s"(shi)); }
#define SLICE(st) ((((st) < 8 ? slo : shi) >> (4 * ((st) & 7))) & 15)
    typedef __attribute__((ext_vector_type(4))) short v4i16;
    int role = wid;
    asm volatile("" : "+s"(role));

    if (role >= 8) {
        const int chn = xcd * 128 + idx * 4 + wm;
        const unsigned cb = lb + wm * CHSZ;
        const int r = l15;
        const unsigned fpub = xcd * 256 + idx * 8, fpoll = xcd * 256 + l31 * 8;
        u32x2 fl; u32x4 sw[4], uu[4];
        unsigned tch = 0;
        if (TOUCH) {
            const int ln = wm * 4 + (lane & 3);
            const unsigned fo = (unsigned)(WS_FLAG - WS_E) + (wm == 0 ? FL_H + (tm * 16 + tn) * 128 : (wm == 1 ? FL_KV : FL_Z) + xcd * 256 + (lane & 1) * 128);
            const unsigned o = (lane & 4) && (TOUCH & 2) ? fo : ((tm * 2 + (ln >> 3)) * 1024 + tn * 64) * 16 + (ln & 7) * 128;
            asm volatile("s_mov_b64 exec, 0xff\n\tglobal_load_dword %0, %1, %2\n\ts_mov_b64 exec, -1" : "=&v"(tch) : "v"(o), "s"(ws + WS_E) : "memory");
        }
        SLICE_TABLE();
        TS(1);
        unsigned sm, cl[3]; u32x2 kw0;
        asm volatile("s_waitcnt vmcnt(0)" : "={v160}"(sm), "={v[162:163]}"(kw0), "={v164}"(cl[0]), "={v165}"(cl[1]), "={v166}"(cl[2]) :: "memory", "v161");
        TS(22);
        const unsigned hal = take(sm);
        const unsigned tagA = __builtin_amdgcn_readlane(hal, 0) + 1, tagB = __builtin_amdgcn_readlane(hal, 1) + (tagA == 0 ? 1u : 0u);
        const u32x2 tag = {tagA, tagB};
        f32x2 L1, C1;
        {
            const float dt = rbf(expf(bf2f(take(cl[0]))));
            const float lr = bf2f(take(cl[1])), li = bf2f(take(cl[2]));
            const float e = expf(dt * lr);
            float sn, cs;
            sincosf(dt * li, &sn, &cs);
            L1 = f32x2{e * cs, e * sn};
            const float nr = L1[0] - 1.f, ni = L1[1], den = lr * lr + li * li;
            C1 = f32x2{2.f * ((nr * lr + ni * li) / den), 2.f * ((ni * lr - nr * li) / den)};
            TIE(L1); TIE(C1);
        }
        f32x2 p[11];
        p[0] = L1;
        if (PWE) {
#pragma unroll
            for (int i = 1; i <= 10; ++i) { p[i] = cmul4(p[i - 1], p[i - 1]); TIE(p[i]); }
        }
        {
            if (!PWE) {
#pragma unroll
                for (int i = 1; i <= 10; ++i) p[i] = cmul4(p[i - 1], p[i - 1]);
            }
            const f32x2 one = {1.f, 0.f}, zero = {0.f, 0.f};
            const unsigned ts = lb + L_TS + l16, tr = lb + L_TR + wm * 2048 + l16;
            if (wm == 0) {
                *LDSP(f32x4, ts) = f32x4{L1[0], L1[1], C1[0], C1[1]};
                *LDSP(f32x4, ts + 1024) = f32x4{p[3][0], p[3][1], p[4][0], p[4][1]};
                *LDSP(f32x4, ts + 2048) = f32x4{p[6][0], p[6][1], __uint_as_float(tagA), __uint_as_float(tagB)};
            }
            f32x2 Q[2], V[2], W[2];
            Q[0] = cmul4(cmul4(C1, (wm & 1) ? p[5] : one), (wm & 2) ? p[6] : one); Q[1] = cmul4(Q[0], p[4]);
            V[1] = (wm & 1) ? one : p[5]; V[0] = cmul4(V[1], p[4]);
            W[1] = cmul4((wm & 1) ? one : p[9], (wm & 2) ? one : p[10]); W[0] = cmul4(W[1], p[8]);
#pragma unroll
            for (int i = 0; i < 2; ++i) {
                const f32x2 Q8 = cmul4(Q[i], p[3]), U = 4 * wm + 2 * i >= 16 - tm ? cmul4(W[i], p[7]) : zero, Wz = 4 * wm + 2 * i + 1 >= 16 - tm ? W[i] : zero;
                *LDSP(f32x4, tr + i * 1024) = f32x4{Q[i][0], Q8[0], Q[i][1], Q8[1]};
                *LDSP(f32x4, tr + i * 1024 + 8192) = f32x4{Wz[0], Wz[1], U[0], U[1]};
                *LDSP(f32x2, tr + i * 1024 + 16384) = V[i];
            }
        }
        const unsigned dskl = (__builtin_amdgcn_readlane(hal, 2) >> ((wm & 1) * 16)) & 0xffffu;
        u32x4 kvo[4];
        u32x2 qv[8];
        const char* krow = a.ssmk + (size_t)chn * 4096;
        const char* qs = a.q + (size_t)(idx * 64 + wm * 16) * 2048 + xcd * 256;
        const char* kvd = ws + WS_KVT + (size_t)(xcd * 8 + wm * 2) * 65536 + idx * 2048;
        const int ph4 = (STG_SEL) * 4, ps = (PST >> ph4) & 15, pl = (PLT >> ph4) & 15, pf = wm ? 15 : ps + KFD, pw0 = (kv_wait(0) >> ph4) & 15, pw1 = (kv_wait(1) >> ph4) & 15;
        const unsigned qo = lh * 8192 + l31 * 8 + 4096;
        auto hb = [&](auto nc) {
            constexpr int nb = decltype(nc)::value;
            if constexpr (nib_any(kv_wait(0), nb)) wait_if<nb, 0>(pw0);
            if constexpr (nib_any(kv_wait(1), nb)) wait_if<nb, 12>(pw1);
            BAR();
            if constexpr (nib_any(PST, nb - KFD)) flag_if<nb>(pf, fpub, tag, flg + FL_KV);
            if constexpr (nib_any(PST, nb)) st4_if<nb>(ps, l16, kvo, kvd, kvd + 65536);
            if constexpr (nib_any(PLT, nb)) { ld4_if<nb>(pl, l16, sw, krow); ld8_if<nb>(pl, qo, qv, qs, qs + 16384); }
        };
#define HB(n) static_for<(n) ? (n) + KVD : 0, (n) + KVD + 1>(hb)
        {
            unsigned kw[2][8], ta[2], th[2];
            u32x2 hm[2];
#pragma unroll
            for (int j = 0; j < 2; ++j) {
                const unsigned x = take(kw0[j]);
                const auto y = __builtin_amdgcn_permlane16_swap(x, x, false, false);
#pragma unroll
                for (int p = 0; p < 2; ++p) { kw[p][4 * j] = y[p] & 0xffffu; kw[p][4 * j + 1] = y[p] << 16; kw[p][4 * j + 2] = y[p] >> 16; kw[p][4 * j + 3] = y[p] & 0xffff0000u; }
            }
#pragma unroll
            for (int p = 0; p < 2; ++p) {
                unsigned h[3];
#pragma unroll
                for (int j = 0; j < 3; ++j) h[j] = (__shfl(hal, p * 8 + (r >> 1) + 16 * (j + 1)) >> ((r & 1) * 16)) & 0xffffu;
                hm[p] = idx ? u32x2{h[0] << 16, h[1] | (h[2] << 16)} : u32x2{0u, 0u};
                const int q = r >> 2, pp = r & 3;
                ta[p] = lb + L_KS + (8 * g4 + q) * 256 + (((wm * 4 + p * 2 + (pp >> 1)) ^ (2 * (q | ((g4 & 1) << 2)))) << 4) + (pp & 1) * 8;
                th[p] = (ta[p] ^ 128) - 1024;
                TIE(hm[p]); TIE(ta[p]); TIE(th[p]);
#pragma unroll
                for (int j = 0; j < 8; ++j) TIE(kw[p][j]);
            }
            WAIT_L(0);
            TS(21);
            BAR();
            TS(2);
            u32x2 kt[4][3], vt[4][2];
#pragma unroll
            for (int u = 0; u < 4; ++u) {
                const int p = u >> 1, o = (u & 1) * 8192;
                auto tr = [&](unsigned ad) -> u32x2 { return __builtin_bit_cast(u32x2, __builtin_amdgcn_ds_read_tr16_b64_v4i16(LDSP(v4i16, ad))); };
                kt[u][0] = tr(th[p] + o); kt[u][1] = tr(ta[p] + o); vt[u][0] = tr(ta[p] + o + 16384); kt[u][2] = tr(ta[p] + o + 1024); vt[u][1] = tr(ta[p] + o + 17408);
                SB();
            }
            TS(14);
#pragma unroll
            for (int i = 0; i < 4; ++i) asm volatile("" : "=v"(sw[i]), "=v"(qv[2 * i]), "=v"(qv[2 * i + 1]));
            if (KVM & 1) HB(0);
            static_for<0, 4>([&](auto uc) {
                constexpr int u = decltype(uc)::value, p = u >> 1, oh = u & 1;
                const u32x2 k0 = oh == 0 && g4 == 0 ? hm[p] : kt[u][0];
                unsigned o[4];
                if (KPL && __builtin_ctz(KPL | 256) == 2 * u) __builtin_amdgcn_s_setprio(KPRIO);
                if ((KPL >> (2 * u)) & 1) kv4p(o[3], o[2], k0[0], k0[1], kt[u][1][0], kt[u][1][1], kw[p], vt[u][0][0], vt[u][0][1]);
                else kv4(o[3], o[2], k0[0], k0[1], kt[u][1][0], kt[u][1][1], kw[p], vt[u][0][0], vt[u][0][1]);
                if constexpr ((KVM >> (2 * u + 1)) & 1) { TIE(o[3]); HB(__builtin_popcount(KVM & ((2 << (2 * u)) - 1))); TIE(vt[u][1]); }
                if (KPL && __builtin_ctz(KPL | 256) == 2 * u + 1) __builtin_amdgcn_s_setprio(KPRIO);
                if ((KPL >> (2 * u + 1)) & 1) kv4p(o[1], o[0], kt[u][1][0], kt[u][1][1], kt[u][2][0], kt[u][2][1], kw[p], vt[u][1][0], vt[u][1][1]);
                else kv4(o[1], o[0], kt[u][1][0], kt[u][1][1], kt[u][2][0], kt[u][2][1], kw[p], vt[u][1][0], vt[u][1][1]);
                kvo[u] = u32x4{o[0], o[1], o[2], o[3]};
                TIE(kvo[u]);
                SB();
                TS(15 + u);
                if constexpr (u < 3 && ((KVM >> (2 * u + 2)) & 1)) { HB(__builtin_popcount(KVM & ((4 << (2 * u)) - 1))); TIE(vt[u + 1][0]); }
            });
#pragma unroll
            for (int u = 0; u < 4; ++u) TIE(kvo[u]);
            __builtin_amdgcn_s_setprio(3);
        }
        TS(3);
        const char* usrc = ws + WS_KVT + (size_t)(xcd * 8 + (idx >> 2)) * 65536 + wm * 16384 + (idx & 3) * 64;
        const unsigned uo = (lane >> 2) * 256 + (lane & 3) * 16;
        const char* cs = ((wm & 2) ? a.cim : a.cre) + (size_t)(tn * 64 + (wm & 1) * 2) * 2048;
        unsigned co[4];
#pragma unroll
        for (int i = 0; i < 4; ++i) {
            const int pair = i * 4 + g4, v = r ^ pair, row = pair * 2 + (v >> 3);
            co[i] = ((row & 15) * 4 + (row >> 4)) * 2048 + (v & 7) * 16;
        }
        bool kv_ok = false;
#pragma unroll
        for (int it = 0; it < 4; ++it) asm volatile("" : "=v"(uu[it]));
        auto items = [&](auto pc) {
            constexpr int p = decltype(pc)::value;
            if constexpr (PU == p) {
                WAIT_V(PK == p ? 4 : 0); TIE(fl);
                kv_ok = __all(fl[0] == tagA && fl[1] == tagB);
                const char* us = kv_ok ? usrc : a.x;
                for (int it = 0; it < 4; ++it) GLD_B128_SC1(uu[it], uo, us + it * 4096, 0);
            }
            if constexpr (PC0 == p) { for (int i = 0; i < 4; ++i) DMA(lb + L_C + 2 * G2STAGE + wm * 4096 + i * 1024, co[i], cs + (SLICE(0) << 7)); }
            if constexpr (PC1 == p) { for (int i = 0; i < 4; ++i) DMA(lb + L_C + 3 * G2STAGE + wm * 4096 + i * 1024, co[i], cs + (SLICE(1) << 7)); }
            if constexpr (PU == p + 1) GLD_B64_SC1(fl, fpoll, flg + FL_KV, 0);
            if constexpr (PC2 == p) { for (int i = 0; i < 4; ++i) DMA(lb + g2_place(5) + wm * 4096 + i * 1024, co[i], cs + (SLICE(5) << 7)); }
            if constexpr (PC3 == p) { for (int i = 0; i < 4; ++i) DMA(lb + g2_place(6) + wm * 4096 + i * 1024, co[i], cs + (SLICE(6) << 7)); }
        };
#define ITEMS(p) items(std::integral_constant<int, p>{})
        static_assert(PK == 0 && PQ == 0 && (nib(PST, 0) >= (__builtin_popcount(KVM & 255) + KVD) && nib(PST, 1) >= (__builtin_popcount(KVM & 255) + KVD) && nib(PST, 2) >= (__builtin_popcount(KVM & 255) + KVD) && nib(PST, 3) >= (__builtin_popcount(KVM & 255) + KVD)), "schedule");
        static_assert(nib(PST, 0) + KFD <= 11 && nib(PST, 1) + KFD <= 11 && nib(PST, 2) + KFD <= 11 && nib(PST, 3) + KFD <= 11 && !nib_any(PLT, 12) && !nib_any(PLT, 13), "schedule");
        static_for<(__builtin_popcount(KVM & 255) + KVD), 12>(hb);
        asm volatile("" :: "v"(tch));
        ITEMS(11);
        BAR();
        ITEMS(12);
        BAR();
        ITEMS(13);
        BAR();
        const unsigned ud = lb + (lane & 3) * CHSZ + UOFF + ((lane >> 2) & 1) * UPL + (16 + wm * 32 + (lane >> 3)) * 16;
        auto zero = [&]() {
            const u32x4 z4 = {0, 0, 0, 0};
            if (lane < 24) *LDSP(u32x4, lb + L_SY + l16) = z4;
            if (lane < 56) *LDSP(u32x4, lb + L_AGG + wm * 896 + l16) = z4;
            *LDSP(u32x4, lane < 32 ? cb + ((lane >> 3) & 1) * CST + (lane & 7) * 16 : cb + UOFF + (g4 & 1) * UPL + r * 16) = z4;
        };
        auto fill = [&]() {
#pragma unroll
            for (int it = 0; it < 4; ++it) TIE(sw[it]);
#pragma unroll
            for (int it = 0; it < 4; ++it) {
                const unsigned q = cb + 128 + it * 1024 + l16;
                const unsigned d0 = sw[it][0], d1 = sw[it][1], d2 = sw[it][2], d3 = sw[it][3];
                const unsigned o1 = it ? __builtin_amdgcn_readlane(sw[it ? it - 1 : 0][3], 63) : 0u;
                const unsigned x1 = __builtin_amdgcn_update_dpp(o1, d3, 0x138, 0xf, 0xf, false);
                *LDSP(u32x4, q) = sw[it];
                *LDSP(u32x4, q + CST) = u32x4{__builtin_amdgcn_alignbyte(d0, x1, 2), __builtin_amdgcn_alignbyte(d1, d0, 2), __builtin_amdgcn_alignbyte(d2, d1, 2), __builtin_amdgcn_alignbyte(d3, d2, 2)};
            }
        };
        auto uput = [&]() {
            WAIT_V(it_cnt(PU, 1, UE ? 15 : 16));
#pragma unroll
            for (int it = 0; it < 4; ++it) TIE(uu[it]);
            if (!kv_ok) {
                TSE(23);
                for (unsigned spin = 0; spin < SPIN_CAP; ++spin) {
                    GLD_B64_SC1(fl, fpoll, flg + FL_KV, 0);
                    WAIT_V(0); TIE(fl);
                    if (__all(fl[0] == tagA && fl[1] == tagB)) break;
                    __builtin_amdgcn_s_sleep(8);
                }
#pragma unroll
                for (int it = 0; it < 4; ++it) GLD_B128_SC1(uu[it], uo, usrc + it * 4096, 0);
                WAIT_V(0);
#pragma unroll
                for (int it = 0; it < 4; ++it) TIE(uu[it]);
            }
#pragma unroll
            for (int it = 0; it < 4; ++it) *LDSP(u32x4, ud + it * 128) = uu[it];
        };
        static_assert(PU <= 14, "ssm_k has landed");
        ITEMS(14);
        if (UE) __builtin_amdgcn_s_setprio(UPRIO);
        if (UE >= 2) zero();
        if (UE == 2) fill();
        BAR();
        ITEMS(15);
        TS(4);
        if (UE < 2) zero();
        if (UE & 1) fill();
        if (UE) uput();
        WAIT_L(0);
        TS(5);
        BAR();
        __builtin_amdgcn_s_setprio(FPRIO);
        ITEMS(16);
        TS(11);
        const unsigned pmine = lb + L_SY + 32 + wm * 4, pall = lb + L_SY + 32 + (lane & 3) * 4;
        if (!UE) { fill(); TS(13); uput(); sy_post<0>(pmine); sy_wait<0>(pall); }
        if (!CPOST) { WAIT_V(0); sy_post<1>(pmine); }
        __builtin_amdgcn_s_setprio(CVPRIO);
        TS(6);
        f32x4 ca[8];
        {
            unsigned kb[4];
#pragma unroll
            for (int i = 0; i < 4; ++i) { kb[i] = cb + (~r & 1) * CST + 2 * (64 + 16 + r - 8 * g4 - 7 + (~r & 1)) + i * 1024; TIE(kb[i]); }
            unsigned ub = cb + UOFF + (g4 & 1) * UPL + (15 + r + (g4 >> 1)) * 16 - 64;
#pragma unroll
            for (int i = 0; i < 8; ++i) ca[i] = f32x4{0, 0, 0, 0};
            s16x4 FA[4][2]; bf16x8 FB[2][8];
#define CV_A(s, o, e) dsr2_<16 * ((s) + ((o) >> 3)) + 128 * (cv_m((o) & 7) & 1) + 2 * (e)>(FA[(o) & 3][e], kb[cv_m((o) & 7) >> 1])
#define CV_B(s, jb) DSR128(FB[(s) & 1][jb], ub, 64 + 256 * (jb) - 32 * (s))
            static_for<0, 8>([&](auto ic) { constexpr int n = decltype(ic)::value; CV_B(0, n); CV_A(0, n >> 1, n & 1); });
            WAIT_L(0);
            auto body = [&]() {
                static_for<0, 72>([&](auto ic) {
                    constexpr int s = decltype(ic)::value / 36, p = decltype(ic)::value % 36, o = cv_grp(p), m = cv_m(o), jb = p - cv_first(o);
                    if constexpr (jb == 0) { WAIT_L(cv_wait(o)); TIE(FA[o & 3][0]); TIE(FA[o & 3][1]); }
                    if constexpr (p == 0) { for (int i = 0; i < 8; ++i) TIE(FB[s][i]); }
                    const bf16x8 fa = __builtin_shufflevector(FA[o & 3][0], FA[o & 3][1], 0, 1, 2, 3, 4, 5, 6, 7);
                    ca[jb + m] = __builtin_amdgcn_mfma_f32_16x16x32_bf16(fa, FB[s][jb], ca[jb + m], 0, 0, 0);
                    SB();
                    constexpr int rd = cv_read(p);
                    if constexpr (rd >= 32) CV_B(s + 1, rd - 32);
                    else if constexpr (rd >= 0) CV_A(s, rd >> 1, rd & 1);
                    SB();
                });
                for (int i = 0; i < 4; ++i) { kb[i] += 128; TIE(kb[i]); }
                ub -= 64;
            };
#pragma unroll 1
            for (int it = 0; it < QIT; ++it) body();
            {
                TS(19);
                if (CPOST) { WAIT_V(0); sy_post<1>(pmine); WAIT_L(0); }
                if (CVLATE) sy_wait<1>(lb + L_SY + (CVLATE == 2 ? wm + (lane & 1) * 4 : lane & 7) * 4);
                TS(20);
            }
#pragma unroll 1
            for (int it = QIT; it < 4; ++it) body();
            WAIT_L(0);
#pragma unroll
            for (int i = 0; i < 4; ++i) { TIE(FA[i][0]); TIE(FA[i][1]); }
#pragma unroll
            for (int i = 0; i < 8; ++i) TIE(FB[0][i]);
#undef CV_A
#undef CV_B
        }
        TS(7);
        __builtin_amdgcn_s_setprio(3);
        constexpr int NBAR = __builtin_popcount(BARM & ~VBM);
        unsigned zpub = (xcd * 32 + l31) * 256 + idx * 8, zpoll = ZOWN ? (xcd * 32 + idx) * 256 + l31 * 8 : fpoll;
        {
            const float Dd = bf2f(dskl);
            const char* zd = ws + WS_ZT + (size_t)(xcd * 128 + wm * 32) * 4096 + idx * 128;
            u32x2 kvr[8];
#pragma unroll
            for (int jt = 0; jt < 8; ++jt) kvr[jt] = *LDSP(u32x2, cb + UOFF + (g4 >> 1) * UPL + (16 + 16 * jt + r) * 16 + (1 - (g4 & 1)) * 8);
#pragma unroll
            for (int jt = 0; jt < 8; ++jt) {
                const f32x4 y = ca[jt];
                const u32x2 kq = kvr[jt];
                *LDSP(u32x2, cb + (jt * 4 + (r >> 2)) * 144 + (r & 3) * 32 + g4 * 8) =
                    u32x2{pack2(rb1(y[0]) + rb1(bfhi(kq[1]) * Dd), rb1(y[1]) + rb1(bflo(kq[1]) * Dd)), pack2(rb1(y[2]) + rb1(bfhi(kq[0]) * Dd), rb1(y[3]) + rb1(bflo(kq[0]) * Dd))};
            }
            unsigned za = lb + (r >> 2) * CHSZ + (wm * 8 + (g4 >> 1)) * 144 + ((g4 & 1) * 2 + ((r >> 1) & 1)) * 32 + (r & 1) * 8, zo = (lane >> 3) * 4096 + (lane & 7) * 16;
            TIE(za); TIE(zo); TIE(zpub); TIE(zpoll);
            sy_post<3>(pmine); sy_wait<3, 0>(pall);
            v4i16 t[4][2];
#pragma unroll
            for (int it = 0; it < 4; ++it)
#pragma unroll
                for (int e = 0; e < 2; ++e) t[it][e] = __builtin_amdgcn_ds_read_tr16_b64_v4i16(LDSP(v4i16, za + it * 288 + e * 16));
#pragma unroll
            for (int it = 0; it < 4; ++it) { TIE(t[it][0]); TIE(t[it][1]); }
            if (ZSB) BAR();
#pragma unroll
            for (int it = 0; it < 4; ++it) { const bf16x8 f = __builtin_shufflevector(t[it][0], t[it][1], 0, 1, 2, 3, 4, 5, 6, 7); GST_B128_SC1(zo, f, zd + it * 32768, 0); }
        }
#pragma unroll
        for (int i = ZSB; i < ZFB; ++i) BAR();
        WAIT_V(0);
        sy_post<2>(pmine);
        if (wm == 0) {
            sy_wait<2>(pall);
            if (ZOWN) { if (lane < 32) GST_B64_SC1(zpub, tag, flg + FL_Z); }
            else if (lane == 0) GST_B64_SC1(fpub, tag, flg + FL_Z);
        }
        TS(8);
        unsigned qm[8][4];
        if (QM) {
#pragma unroll
            for (int n = 0; n < 8; ++n) {
                TIE(qv[n]);
                qm[n][0] = qv[n][0] & 0xffffu; qm[n][1] = qv[n][0] & 0xffff0000u; qm[n][2] = qv[n][1] & 0xffffu; qm[n][3] = qv[n][1] & 0xffff0000u;
                TIE(qm[n][0]); TIE(qm[n][1]); TIE(qm[n][2]); TIE(qm[n][3]);
            }
        }
#pragma unroll
        for (int i = ZFB; i < NBAR - 1; ++i) { BAR();
        }
        if (ZSL) __builtin_amdgcn_s_sleep(ZSL);
        asm volatile("s_cmp_lg_u32 %3, 0\n\ts_cbranch_scc1 1f\n\tglobal_load_dwordx2 %0, %1, %2 sc1\n1:" : "=&v"(fl) : "v"(zpoll), "s"(flg + FL_Z), "s"(wm) : "scc", "memory");
        if (NBAR > ZFB) BAR();
        if (wm == 0) {
            unsigned left = SPIN_CAP;
            asm volatile("s_branch 2f\n1:\n\ts_sleep %5\n\tglobal_load_dwordx2 %0, %2, %3 sc1\n2:\n\ts_waitcnt vmcnt(0)\n\tv_cmp_ne_u64 vcc, %4, %0\n\ts_cbranch_vccz 3f\n\ts_sub_u32 %1, %1, 1\n\ts_cbranch_scc0 1b\n3:"
                         : "+v"(fl), "+s"(left) : "v"(zpoll), "s"(flg + FL_Z), "s"(tag), "n"(ZPS) : "vcc", "scc", "memory");
            sy_post<4>(pmine);
        } else sy_wait<4, 2>(lb + L_SY + 32);
        u32x4 zv[4];
        {
            ITEMS(18);
            const char* zsrc = ws + WS_ZT + (size_t)(xcd * 128 + idx * 4 + wm) * 4096;
            static_for<0, 4>([&](auto ic) { constexpr int it = decltype(ic)::value; GLD_B128_SC1(zv[it], (l31 >> 2) * 512 + lh * 64 + (lane & 3) * 16, zsrc, it * 128); });
        }
        WAIT_V(0);
        TS(9);
#pragma unroll
        for (int it = 0; it < 4; ++it) TIE(zv[it]);
        if (!(XAB & 1)) {
        quad_swap<0>(zv[0], zv[1]); quad_swap<0>(zv[2], zv[3]);
        quad_swap<1>(zv[0], zv[2]); quad_swap<1>(zv[1], zv[3]); }
#pragma unroll
        for (int it = 0; it < 4; ++it) TIE(zv[it]);
        char* const oa = a.out + (size_t)(idx * 64 + wm * 16) * 8192 + xcd * 512;
#pragma unroll
        for (int n = 0; n < 8; ++n) {
            if (!QM) TIE(qv[n]);
            const unsigned z0 = zv[n & 3][2 * (n >> 2)], z1 = zv[n & 3][2 * (n >> 2) + 1];
            const f32x4 o = (XAB & 2) ? __builtin_bit_cast(f32x4, zv[n & 3]) : QM ? mul4bf(u32x2{z0, z1}, qm[n]) :
                f32x4{rb1(bflo(qv[n][0]) * bflo(z0)), rb1(bfhi(qv[n][0]) * bfhi(z0)), rb1(bflo(qv[n][1]) * bflo(z1)), rb1(bfhi(qv[n][1]) * bfhi(z1))};
            if (n == 0 && OSG) for (int i = 0; i < (idx & 3); ++i) __builtin_amdgcn_s_sleep(OSG);
            if (!(XAB & 4))
            GST_B128_NT(lh * 32768 + l31 * 16, o, oa + ((n >> 2) * 8 + (n & 3)) * 8192, 0);
            if (OSL) __builtin_amdgcn_s_sleep(OSL);
        }
        asm volatile(".Lfk_code_a:");
        TSE(10);
        TS_FLUSH();
        return;
    }

    u32x2 fl;
    unsigned dv[4];
    {
        const int v = l15 ^ g4, hi = v >> 3;
        const int r0 = wn ? tn * 64 + (wm >> 1) * 32 : tm * 128 + (wm >> 1) * 64 + (wm & 1) * 4;
        const unsigned d0 = (r0 + ((g4 * (wn ? 2 : 9)) & (wn ? 6 : 17))) * 2048 + (v & 7) * 16, odd = wn ? 11 : 14;
        dv[0] = d0 + (hi << odd); dv[2] = d0 + ((hi ^ 1) << odd) + (wn ? 32768 : 4096);
        dv[1] = (dv[0] ^ 64) + (wn ? 16384 : 65536); dv[3] = (dv[2] ^ 64) + (wn ? 16384 : 65536);
    }
    const int rot7 = ((((ROT >> 12) & 15) * (idx & 7) + ((ROT >> 8) & 15) * (idx >> 3) + ((ROT >> 4) & 15) * (xcd >> 1) + (ROT & 15) * (xcd & 1)) & 15) << 7;
    const char* g1src = wn ? ((wm & 1) ? a.bim : a.bre) : a.x;
#define G1_ISSUE(st, i) DMA(g1dst + g1_slot(st) + (i) * 1024, dv[i], g1src + ((((st) << 7) + rot7) & 1920))
    static_for<0, PRE>([&](auto ic) { constexpr int n = decltype(ic)::value; G1_ISSUE(n >> 2, n & 3); });
    TS(14);
    SLICE_TABLE();
    const int j0 = 4 * run - (32 - 2 * tm);
    const int pneg = EALL ? 0 : 16 * (wm >> 1) + 15 - (32 - 2 * tm);
    unsigned eo[2], pes = lb + L_ES + wid * 256 + l15 * 16;
    unsigned plr = lb + L_AGG + st * 8 + run * 512, est = ((tm * 2 + (wm >> 1)) * 1024 + tn * 64 + st) * 16 + (EREP ? g4 * 524288 : 0);
#pragma unroll
    for (int i = 0; i < 2; ++i) { eo[i] = (EALL ? (j0 + 2 * i) & 31 : j0 + 2 * i < 0 ? 0 : j0 + 2 * i) * 16384 + (EREP ? (tm >> 2) * 524288 : 0) + (tn * 64 + st) * 16; TIE(eo[i]); }
    TIE(plr); TIE(pes); TIE(est);
    unsigned hw[2], ta[2];
    {
        const unsigned ht = lb + L_HT + wid * 4096;
#pragma unroll
        for (int j = 0; j < 2; ++j) {
            const int pr = (8 * (g4 & 1) + 4 * j + (l15 >> 2)) ^ ((g4 & 1) * 4), pw = l15 ^ ((l15 >> 3) * 4);
            ta[j] = ht + (g4 >> 1) * 512 + pr * 32 + ((((l15 >> 1) ^ (pr >> 2)) & 1) << 4) + 8 * (lane & 1);
            hw[j] = ht + g4 * 1024 + pw * 32 + (((j ^ (pw >> 2)) & 1) << 4);
            TIE(ta[j]); TIE(hw[j]);
        }
    }
    unsigned po = pes + ((wm & 2) ? -512 : 512);
    unsigned cta = lb + L_TS + st * 16, ctb = lb + L_TR + run * 1024 + st * 16;
    TIE(po); TIE(cta); TIE(ctb);
    f32x4 ct[5]; f32x2 V1;
    const int gm = PAIR ? wid >> 1 : wm, gk = PAIR ? wid & 1 : wn, gs = gk * 4 + (gm & 2);
    unsigned pmine = lb + L_SY + wid * 4, pall = lb + L_SY + (lane & 7) * 4, pown = lb + L_SY + (lane < 4 ? 32 + lane * 4 : gs * 4 + (lane & 4)), pzero = lb + L_SY;
    unsigned g2v[2], cad[2][2], to[2][2];
    unsigned xo = (tm * 128 + gm * 32 + gk * 16 + g4 * 4) * 2048 + tn * 128 + l15 * 8 + 4096, dso = xo & 2047;
    unsigned fst = (tm * 16 + l15) * 128 + tn * 8, fld = (tm * 16 + tn) * 128 + (NOOWN && l15 == tn ? tn ^ 1 : l15) * 8;
#pragma unroll
    for (int i = 0; i < 2; ++i) {
        const int pair = i * 4 + g4, v = l15 ^ pair ^ ((wm & 1) * 8);
        g2v[i] = (pair * 2 + (v >> 3)) * 8192 + (v & 7) * 16 + (tn * 64 + wm) * 2048;
        TIE(g2v[i]);
#pragma unroll
        for (int j = 0; j < 2; ++j) {
            cad[i][j] = lb + L_C + (img_off((g4 >> 1) * 64 + l15, 4 * gk + (g4 & 1)) ^ (i * 32) ^ (j * 128));
            to[i][j] = ta[j] + (i ^ gk) * 1024 + (gm & 1) * 2048 + (gs - wid) * 4096;
            TIE(cad[i][j]); TIE(to[i][j]);
        }
    }
    unsigned cwa = lb + wid * 2048 + l16;
    TIE(cwa);
    TIE(pmine); TIE(pall); TIE(pown); TIE(pzero); TIE(xo); TIE(dso); TIE(fst); TIE(fld);
    TS(1);
    {
        static_for<PRE, PRE2>([&](auto ic) { constexpr int n = decltype(ic)::value; G1_ISSUE(n >> 2, n & 3); });
        WAIT_V(PRE2);
        BAR();
        static_assert(PRO <= 12 && NB1 == 4, "the constants sit in slot 3");
        DSR128(ct[0], cta, 0); DSR128(ct[1], cta, 1024); DSR128(ct[2], cta, 2048); DSR128(ct[3], ctb, 0); DSR128(ct[4], ctb, 8192); DSR64(V1, ctb, 16384);
        static_for<PRE2, PRO>([&](auto ic) { constexpr int n = decltype(ic)::value; G1_ISSUE(n >> 2, n & 3); });
        TS(15);
    }
    WAIT_L(0);
#pragma unroll
    for (int i = 0; i < 5; ++i) TIE(ct[i]);
    TIE(V1);
    float nL1i = -ct[0][1], nC1r = -ct[0][2], nC1i = -ct[0][3];
    TIE(nL1i); TIE(nC1r); TIE(nC1i);
    unsigned tagA = __builtin_amdgcn_readfirstlane(__float_as_uint(ct[2][2])), tagB = __builtin_amdgcn_readfirstlane(__float_as_uint(ct[2][3]));
    asm volatile("" : "+s"(tagA), "+s"(tagB));

    f32x4 acc[4][2];
    {
#pragma unroll
        for (int i = 0; i < 4; ++i) { acc[i][0] = f32x4{0, 0, 0, 0}; acc[i][1] = f32x4{0, 0, 0, 0}; }
        const int aoff = img_off((wm >> 1) * 64 + l15, g4), boff = img_off(128 + wn * 64 + (wm & 1) * 16 + l15, g4);
        unsigned aad[2][2], bad[2];
#pragma unroll
        for (int ks = 0; ks < 2; ++ks) { aad[ks][0] = lb + (aoff ^ (ks * 64)); aad[ks][1] = lb + (aoff ^ (ks * 64) ^ 128); bad[ks] = lb + (boff ^ (ks * 64)); }
        bf16x8 fa[2][4], fb[2][2];
        if (GPRIO) __builtin_amdgcn_s_setprio(GPRIO);
        WAIT_V(PRO - 4); BAR();
        TS(2);
#define G1_FRAG(g, n) do { constexpr int bo_ = g1_slot((g) >> 1), hi_ = bo_ & ~65535, x_ = (n) < 3 ? (n) >> 1 : (n) - 2; \
        if constexpr ((n) == 0 || (n) == 2) DSR128(fb[(g) & 1][x_], bad[(g) & 1] + hi_, bo_ - hi_ + x_ * 4096); \
        else DSR128(fa[(g) & 1][x_], aad[(g) & 1][x_ & 1] + hi_, bo_ - hi_ + x_ * 2048); } while (0)
        static_for<0, 6>([&](auto ic) { constexpr int n = decltype(ic)::value; G1_FRAG(0, n); });
        static_for<0, 32>([&](auto ic) {
            constexpr int g = decltype(ic)::value, gn = g + 1, n0 = g1_upto(g - 1), nd = g1_upto(g) - n0;
            constexpr bool bar = (gn & 1) == 0 && gn < 32;
            if constexpr (bar) {
                static_assert(g1_cnt(gn >> 1) >= 0, "schedule");
                WAIT_V(g1_cnt(gn >> 1)); WAIT_L(0); BAR();
                if constexpr (((gn >> 1) & 3) == 0 || gn >= 26) TS(gn >= 26 ? 23 + (gn >> 1) - 13 : 16 + (gn >> 3));
#pragma unroll
                for (int i = 0; i < 4; ++i) TIE(fa[g & 1][i]);
                TIE(fb[g & 1][0]); TIE(fb[g & 1][1]);
            }
            static_for<0, 8>([&](auto mc) {
                constexpr int m = decltype(mc)::value, i = m >> 1, j = m & 1;
                if constexpr (!bar && (i == 0 || j == 0)) {
                    WAIT_L(5 - (i == 0 ? j + 1 : i + 2) + (gn < 32 ? (m < 6 ? m : 6) : 0));
                    TIE(fa[g & 1][i]); TIE(fb[g & 1][j]);
                }
                acc[i][j] = __builtin_amdgcn_mfma_f32_16x16x32_bf16(fa[g & 1][i], fb[g & 1][j], acc[i][j], 0, 0, 0);
                SB();
                if constexpr (gn < 32 && m < 6) G1_FRAG(gn, m);
                constexpr int d = nd > 2 ? ((m & 1) ? m >> 1 : -1) : (m == DP0 ? 0 : m == DP1 ? 1 : -1);
                if constexpr (d >= 0 && d < nd) G1_ISSUE((n0 + d) >> 2, (n0 + d) & 3);
                SB();
            });
        });
#undef G1_FRAG
    }
    TS(3);
    const unsigned tagG = tagA ^ tagB ^ 0x9E3779B9u;
    __builtin_amdgcn_s_setprio(MPRIO);
    f32x2 ur[8], ui[8], hr, hi;
    const f32x2 L1 = {ct[0][0], ct[0][1]}, C1 = {ct[0][2], ct[0][3]}, L16 = {ct[1][2], ct[1][3]}, Q2r = {ct[3][0], ct[3][1]}, Q2i = {ct[3][2], ct[3][3]};
    const float L8r = ct[1][0], L8i = ct[1][1], L64r = ct[2][0], L64i = ct[2][1];
    u32x4 eg[4], cr[2 * NR + 1];
    const char* g2src = wn ? a.cim : a.cre;
#define CR_LOAD(n0, n1) static_for<n0, n1>([&](auto ic) { constexpr int n = decltype(ic)::value; GLD_B128(cr[n], g2v[n & 1], g2src + (SLICE(7 + (n >> 1)) << 7), 0); })
#define E_LOADS "global_load_dwordx4 v[104:107], %[a0], %[b0] sc1\n\tglobal_load_dwordx4 v[108:111], %[a0], %[b1] sc1\n\t" \
                "global_load_dwordx4 v[112:115], %[a1], %[b0] sc1\n\tglobal_load_dwordx4 v[116:119], %[a1], %[b1] sc1\n"
#define E_POLL() asm volatile("s_cmp_lt_i32 %[on], 0\n\ts_cbranch_scc1 1f\n\t" E_LOADS "1:" : "={v[104:107]}"(eg[0]), "={v[108:111]}"(eg[1]), "={v[112:115]}"(eg[2]), "={v[116:119]}"(eg[3]) \
        : [a0] "v"(eo[0]), [a1] "v"(eo[1]), [b0] "s"(ws + WS_E), [b1] "s"(ws + WS_E + 16384), [on] "s"(pneg) : "scc", "memory")
    {
        asm volatile("s_nop 7" : "+v"(acc[0][0]), "+v"(acc[0][1]), "+v"(acc[1][0]), "+v"(acc[1][1]), "+v"(acc[2][0]), "+v"(acc[2][1]), "+v"(acc[3][0]), "+v"(acc[3][1]));
#pragma unroll
        for (int p = 0; p < 8; ++p) { ur[p] = f32x2{acc[p >> 1][0][2 * (p & 1)], acc[p >> 1][0][2 * (p & 1) + 1]}; ui[p] = f32x2{acc[p >> 1][1][2 * (p & 1)], acc[p >> 1][1][2 * (p & 1) + 1]}; }
        f32x2 gr = ur[0], gi = ui[0];
#pragma unroll
        for (int p = 1; p < 8; ++p) { f32x2 nr, ni; if (PLN & 1) c2_mad(nr, ni, L1[0], nL1i, L1[0], L1[1], gr, gi, ur[p], ui[p]); else pk_cmad(nr, ni, L1, gr, gi, ur[p], ui[p]); gr = nr; gi = ni; }
        float fr, fi;
        cmad(fr, fi, L8r, L8i, gr[0], gi[0], gr[1], gi[1]);
        {
            const f32x2 w = cmul4(V1, f32x2{fr, fi});
            const float wr = sum4(w[0]), wi = sum4(w[1]);
            const u32x4 gq = {__float_as_uint(wr), tagA, __float_as_uint(wi), tagG};
            if (EREP ? (EALL || g4 >= (tm >> 2)) : g4 == 0) GST_B128_SC1(est, gq, ws + WS_E, 0);
        }
        *LDSP(f32x2, plr + 7 * 512) = f32x2{fr, fi};
        *LDSP(unsigned, pes + 8) = 0;
        TS(21);
        if (EP == 2) E_POLL();
        auto coef = [&]() {
#pragma unroll
            for (int p = 0; p < 8; ++p) { f32x2 nr, ni; if (PLN & 2) c2_mul(nr, ni, C1[0], nC1i, nC1r, nC1i, ur[p], ui[p]); else pk_conj_mul(nr, ni, C1, ur[p], ui[p]); ur[p] = nr; ui[p] = ni; TIE(ur[p]); TIE(ui[p]); }
        };
        if (CFB) coef();
        WAIT_L(0); BAR();
        f32x2 e[7];
#pragma unroll
        for (int j = 0; j < 7; ++j) e[j] = *LDSP(f32x2, plr + j * 512);
        if (EP == 1) E_POLL();
        CR_LOAD(0, CR1);
        if (!CFB) coef();
#pragma unroll
        for (int j = 1; j < 7; ++j) { if (PLN & 4) { float xr = e[j][0], xi = e[j][1]; cmac(xr, xi, L16[0], L16[1], e[j - 1][0], e[j - 1][1]); e[j] = f32x2{xr, xi}; } else cmac2(e[j], L16, e[j - 1]); }
        float mr, mi;
        cmad(mr, mi, L8r, L8i, e[6][0], e[6][1], gr[0], gi[0]);
        if (PLN & 4) c2_mul(hr, hi, C1[0], nC1i, nC1r, nC1i, f32x2{e[6][0], mr}, f32x2{e[6][1], mi}); else pk_conj_mul(hr, hi, C1, f32x2{e[6][0], mr}, f32x2{e[6][1], mi});
        TIE(hr); TIE(hi);
        CR_LOAD(CR1, CR1 + CR2);
        TS(4);
    }
    f32x2 G;
    {
        float pr = 0.f, pi = 0.f;
        if (!EP) E_POLL();
        if (pneg >= 0) {
            unsigned t0, t1, t2, left = SPIN_CAP;
            asm volatile("s_waitcnt vmcnt(%[c])\n\ts_branch 2f\n1:\n\ts_sleep %[sl]\n\t" E_LOADS "\ts_waitcnt vmcnt(0)\n2:\n\t"
                         "v_bitop3_b32 %[t0], v105, v109, %[ta] bitop3:0x7e\n\tv_bitop3_b32 %[t1], v113, v117, %[ta] bitop3:0x7e\n\tv_bitop3_b32 %[t2], v107, v111, %[tg] bitop3:0x7e\n\t"
                         "v_or3_b32 %[t0], %[t0], %[t1], %[t2]\n\tv_bitop3_b32 %[t1], v115, v119, %[tg] bitop3:0x7e\n\tv_or_b32 %[t0], %[t0], %[t1]\n\t"
                         "v_cmp_ne_u32 vcc, 0, %[t0]\n\ts_cbranch_vccz 3f\n\ts_sub_u32 %[n], %[n], 1\n\ts_cbranch_scc0 1b\n3:"
                         : "+{v[104:107]}"(eg[0]), "+{v[108:111]}"(eg[1]), "+{v[112:115]}"(eg[2]), "+{v[116:119]}"(eg[3]), [t0] "=&v"(t0), [t1] "=&v"(t1), [t2] "=&v"(t2), [n] "+s"(left)
                         : [a0] "v"(eo[0]), [a1] "v"(eo[1]), [b0] "s"(ws + WS_E), [b1] "s"(ws + WS_E + 16384), [ta] "s"(tagA), [tg] "s"(tagG), [sl] "n"(ESL), [c] "n"(CR1 + CR2) : "vcc", "scc", "memory");
            float ar = __uint_as_float(eg[1][0]), ai = __uint_as_float(eg[1][2]), br = __uint_as_float(eg[3][0]), bi = __uint_as_float(eg[3][2]);
            cmac(ar, ai, L64r, L64i, __uint_as_float(eg[0][0]), __uint_as_float(eg[0][2]));
            cmac(br, bi, L64r, L64i, __uint_as_float(eg[2][0]), __uint_as_float(eg[2][2]));
            if (EALL) { if (j0 < 0) ar = ai = 0.f; if (j0 + 2 < 0) br = bi = 0.f; }
            const f32x2 pu = cmul4(f32x2{ct[4][2], ct[4][3]}, f32x2{ar, ai});
            pr = pu[0]; pi = pu[1];
            cmac(pr, pi, ct[4][0], ct[4][1], br, bi);
            pr = sum4(pr); pi = sum4(pi);
        }
        TS(5);
        *LDSP(u32x4, pes) = u32x4{__float_as_uint(pr), __float_as_uint(pi), 1u, 0u};
        CR_LOAD(CR1 + CR2, CR1 + CR2 + CR3);
        u32x4 o;
        asm volatile("1:\n\tds_read_b128 %0, %1\n\ts_waitcnt lgkmcnt(0)\n\tv_cmp_eq_u32 vcc, 0, v122\n\ts_cbranch_vccnz 1b" : "=&{v[120:123]}"(o) : "v"(po) : "vcc", "memory");
        G = f32x2{pr + __uint_as_float(o[0]), pi + __uint_as_float(o[1])};
    }
    TS(6);
    {
        if (PLN & 8) c2_mac(hr, hi, G[0], -G[1], -G[0], -G[1], Q2r, Q2i); else pk_conj_mac(hr, hi, G, Q2r, Q2i);
#pragma unroll
        for (int p = 0; p < 8; ++p) { if (PLN & 8) c2_mac(ur[p], ui[p], L1[0], L1[1], L1[0], nL1i, hr, hi); else pk_cmac_conj(ur[p], ui[p], L1, hr, hi); hr = ur[p]; hi = ui[p]; }
        const char* hd = ws + WS_H + (size_t)((tm * 4 + (wm & 2)) * 2 + wn) * HP + (tn * 4 + (wm & 1) * 2) * 1024;
#pragma unroll
        for (int e2 = 0; e2 < 2; ++e2)
#pragma unroll
            for (int c = 0; c < 2; ++c) {
                const f32x2* o = c ? ui : ur;
                *LDSP(u32x4, hw[e2] + c * 512) = u32x4{pack2(o[0][e2], o[1][e2]), pack2(o[2][e2], o[3][e2]), pack2(o[4][e2], o[5][e2]), pack2(o[6][e2], o[7][e2])};
            }
        v4i16 t[4][2];
#pragma unroll
        for (int n = 0; n < 4; ++n)
#pragma unroll
            for (int j = 0; j < 2; ++j) t[n][j] = __builtin_amdgcn_ds_read_tr16_b64_v4i16(LDSP(v4i16, ta[j] + n * 1024));
        sy_post<1>(pmine);
        static_for<0, 4>([&](auto ic) {
            constexpr int n = decltype(ic)::value;
            const bf16x8 f = __builtin_shufflevector(t[n][0], t[n][1], 0, 1, 2, 3, 4, 5, 6, 7);
            GST_B128_SC1(l16, f, hd + (n >> 1) * 2 * HP + ((n & 1) ^ wn) * 1024, 0);
        });
    }
    TS(7);
    bf16x8 fa2[64];
    u32x2 xv[4], dsv;
    const char* Aw = ws + WS_H + (size_t)((tm * 4 + gm) * 2 + gk) * HP;
    const unsigned g2dst = lb + wid * 2048;
#define G2_ISSUE(st, i) DMA(g2dst + g2_place(st) + (i) * 1024, g2v[i], g2src + (SLICE(st) << 7))
#define G2_A(g) GLD_B128_SC1(fa2[g], l16, Aw + (SLICE((g) >> 2) << 12), ((g) & 3) * 1024)
    {
        {
            sy_wait<1>(pown);
            s16x4 fo[4][2]; bf16x8 c[8];
            static_for<0, 2>([&](auto kc) {
                constexpr int ks = decltype(kc)::value;
                static_for<0, 4>([&](auto ic) { constexpr int i = decltype(ic)::value; dsrtr_<ks * 4096>(fo[2 * ks + (i >> 1)][i & 1], to[i >> 1][i & 1]); });
                static_for<0, 4>([&](auto ic) { constexpr int i = decltype(ic)::value; DSR128(c[4 * ks + i], cad[ks][i & 1], 2 * G2STAGE + i * 2048); });
            });
            static_for<0, 8>([&](auto ic) {
                constexpr int n = decltype(ic)::value, ks = n >> 2, i = n & 3;
                WAIT_L(ks ? 3 - i : 11 - i);
                TIE(c[n]);
                if constexpr (i == 0) { TIE(fo[2 * ks][0]); TIE(fo[2 * ks][1]); TIE(fo[2 * ks + 1][0]); TIE(fo[2 * ks + 1][1]); }
#pragma unroll
                for (int j = 0; j < 2; ++j)
                    acc[i][j] = __builtin_amdgcn_mfma_f32_16x16x32_bf16(__builtin_shufflevector(fo[2 * ks + j][0], fo[2 * ks + j][1], 0, 1, 2, 3, 4, 5, 6, 7), c[n], ks ? acc[i][j] : f32x4{0, 0, 0, 0}, 0, 0, 0);
                SB();
            });
#pragma unroll
            for (int i = 0; i < 4; ++i) { TIE(acc[i][0]); TIE(acc[i][1]); }
        }
        if (CEARLY) sy_post<0>(pmine);
        static_for<0, 4>([&](auto ic) { constexpr int it = decltype(ic)::value; GLD_B64(xv[it], xo, a.x, it * 2048 - 4096); });
        GLD_B64(dsv, dso, a.dskip, 0);
        if (CEARLY) {
            sy_wait<0>(pall);
            static_for<4, 4 + CEARLY>([&](auto ic) { constexpr int n = decltype(ic)::value; G2_ISSUE(n >> 1, n & 1); });
        }
        TS(16);
        WAIT_V(5 + CEARLY);
        sy_post<2>(pmine);
        if (!CEARLY || wid == (NOOWN ? 1 : 0) || (NOOWN == 1 && wid == 0)) sy_wait<2, 0>(pall);
        const u32x2 tagv = {__float_as_uint(ct[2][2]), __float_as_uint(ct[2][3])};
        if (wid == (NOOWN ? 1 : 0)) asm volatile("s_mov_b64 exec, 0xffff\n\tglobal_store_dwordx2 %0, %1, %2 sc1\n\ts_mov_b64 exec, -1" :: "v"(fst), "v"(tagv), "s"(flg + FL_H) : "memory");
        if (PFIRST) asm volatile("s_cmp_lg_u32 %3, 0\n\ts_cbranch_scc1 1f\n\tglobal_load_dwordx2 %0, %1, %2 sc1\n1:" : "=&v"(fl) : "v"(fld), "s"(flg + FL_H), "s"(wid) : "scc", "memory");
        TS(8);
        static_for<4 + CEARLY, 10>([&](auto ic) { constexpr int n = decltype(ic)::value; G2_ISSUE(n >> 1, n & 1); });
        CR_LOAD(2 * NR - CRL, 2 * NR);
        TS(13);
        if (wid == 0) {
            unsigned left = SPIN_CAP;
            if (PFIRST)
            asm volatile("s_waitcnt vmcnt(6)\n\ts_branch 3f\n1:\n\tglobal_load_dwordx2 %0, %2, %3 sc1\n\ts_waitcnt vmcnt(0)\n3:\n\tv_cmp_ne_u64 vcc, %0, %4\n\ts_cbranch_vccz 2f\n\ts_sub_u32 %1, %1, 1\n\ts_cbranch_scc0 1b\n2:"
                         : "+v"(fl), "+s"(left) : "v"(fld), "s"(flg + FL_H), "v"(tagv) : "vcc", "scc", "memory");
            else
            asm volatile("1:\n\tglobal_load_dwordx2 %0, %2, %3 sc1\n\ts_waitcnt vmcnt(0)\n\tv_cmp_ne_u64 vcc, %0, %4\n\ts_cbranch_vccz 2f\n\ts_sub_u32 %1, %1, 1\n\ts_cbranch_scc0 1b\n2:"
                         : "=&v"(fl), "+s"(left) : "v"(fld), "s"(flg + FL_H), "v"(tagv) : "vcc", "scc", "memory");
            sy_post<3>(pmine);
            if (b == 255 && lane == 0) GST_B64_SC1(0u * lane, tagv, flg + FL_EPOCH);
        } else if (g2_bar(3)) sy_wait<3>(pzero);
        else { WAIT_V(CRL); sy_post<3>(pmine); }
        if (!g2_bar(3)) sy_wait<3>(pall);
        TS(20);
        static_for<4, 4 + AW>([&](auto ic) { constexpr int g = decltype(ic)::value; G2_A(g); });
    }
    TS(9);
    __builtin_amdgcn_s_setprio(0);
    unsigned yw, yr, yp, yo;
    static_assert(PAIR, "yr");
    unsigned dm[4];
    auto dmask = [&]() { TIE(dsv); dm[0] = dsv[0] & 0xffffu; dm[1] = dsv[0] & 0xffff0000u; dm[2] = dsv[1] & 0xffffu; dm[3] = dsv[1] & 0xffff0000u; TIE(dm[0]); TIE(dm[1]); TIE(dm[2]); TIE(dm[3]); };
    auto skip = [&](int it) { TIE(xv[it]); const f32x4 y = mul4bf(xv[it], dm); for (int i = 0; i < 4; ++i) acc[i][0][it] = add1(acc[i][0][it], y[i]); if (it & 1) { for (int i = 0; i < 4; ++i) TIE(acc[i][0]); } };
    {
        bf16x8 fb[2][4];
#define G2_FRAG(g, i) do { constexpr int pl_ = g2_place((g) >> 1), b_ = pl_ >= L_C ? L_C : pl_ >= 32768 ? 32768 : 0; DSR128(fb[(g) & 1][i], cad[(g) & 1][(i) & 1] + (b_ - L_C), pl_ - b_ + (i) * 2048); } while (0)
        static_for<0, 4>([&](auto ic) { constexpr int i = decltype(ic)::value; G2_FRAG(2, i); });
        if (YS == 0) { WAIT_V(AW + 6 + CRL); dmask(); skip(0); skip(1); skip(2); skip(3); }
        static_for<2, 32>([&](auto ic) {
            constexpr int g = decltype(ic)::value, gn = g + 1;
            constexpr bool bar = g2_bar(g) && !((VBM >> g) & 1);
            constexpr int n0 = g2_upto(g - 1), n1 = g2_upto(g);
            if constexpr (bar) {
                constexpr int ca = g2_cnt(2 * g, 2 * g), cs = g2_cover(g) >= 7 + NR ? g2_cnt(100 + 2 * g2_cover(g) + 1, 2 * g) : 63;
                static_assert(cs >= 0 && g2_wr(g - 1) >= 2 * (g2_cover(g) < 6 + NR ? g2_cover(g) - 6 : NR), "schedule");
                WAIT_V(AWB && ca < cs ? ca : cs); WAIT_L(0); BAR();
                if constexpr (!AWB) WAIT_V(ca);
#pragma unroll
                for (int i = 0; i < 4; ++i) TIE(fb[g & 1][i]);
                TIE(fa2[2 * g]);
            }
            static_for<0, 8>([&](auto mc) {
                constexpr int m = decltype(mc)::value, i = m & 3, j = m >> 2, f = 2 * g + j;
                static_assert(g2_cnt(f, f) >= 0, "schedule");
                constexpr int jj = LSW && g == 31 ? 1 - j : j, ff = 2 * g + jj;
                if constexpr (i == 0 && (j || !bar)) { WAIT_V(LSW && g == 31 ? 0 : g2_cnt(f, f)); TIE(fa2[ff]); }
                if constexpr (!bar && j == 0) { WAIT_L((gn < 32 ? 3 : 3 - i) + g2_wr(g - 1) - g2_wr(g - 2) + (g - 1 == EGX)); TIE(fb[g & 1][i]); }
                acc[i][jj] = __builtin_amdgcn_mfma_f32_16x16x32_bf16(fa2[ff], fb[g & 1][i], acc[i][jj], 0, 0, 0);
                SB();
                if constexpr (LSW && g == 31 && j) *LDSP(f32x4, yw + i * 1024) = acc[i][1];
                if constexpr (g == EGX && m == 4) sy_post<5>(pmine);
                if constexpr (g == 29 && m == 5) { yw = cwa + L_Y + wid * 2048; yr = yw ^ (PAIR ? 4096 : 16384); yp = pmine ^ (PAIR ? 4 : 16); yo = g4 * 32768 + l15 * 16; TIE(yw); TIE(yr); TIE(yp); TIE(yo); }
                if constexpr (gn < 32 && m < 4) G2_FRAG(gn, m);
                if constexpr (m >= 4 && g2_wr(g - 1) + m - 4 < g2_wr(g)) {
                    constexpr int n = g2_wr(g - 1) + m - 4, pl = g2_place(7 + (n >> 1)), pb = pl >= L_C ? L_C : 0;
                    asm volatile("ds_write_b128 %0, %1 offset:%2" :: "v"(cwa + pb), "v"(cr[n]), "n"(pl - pb + (n & 1) * 1024) : "memory");
                }
                if constexpr (i == 1 && n0 + j < n1) G2_ISSUE((n0 + j) >> 1, (n0 + j) & 1);
                if constexpr (i >= 2 && g2_ald(f, i & 1) >= 0) G2_A(g2_ald(f, i & 1));
                SB();
            });
        });
#undef G2_FRAG
    }
    TS(11);
    if (EPRIO) __builtin_amdgcn_s_setprio(EPRIO);
    {
#pragma unroll
        for (int i = 0; i < 4; ++i) if (!LSW) *LDSP(f32x4, yw + i * 1024) = acc[i][1];
        sy_post<4>(pmine);
        sy_wait<4, 0>(yp);
        char* const ob = a.out + (size_t)(tm * 128 + gm * 32 + gk * 16) * 8192 + 4096 + tn * 256;
        f32x4 w[4];
#pragma unroll
        for (int i = 0; i < 4; ++i) w[i] = *LDSP(f32x4, yr + i * 1024);
        f32x4 o[4];
#pragma unroll
        for (int r = 0; r < 4; ++r) { o[r] = f32x4{add1(w[0][r], acc[0][0][r]), add1(w[1][r], acc[1][0][r]), add1(w[2][r], acc[2][0][r]), add1(w[3][r], acc[3][0][r])}; if (EGX) TIE(o[r]); }
        if (EGX && !wn) sy_wait<5, 1>(pall);
#pragma unroll
        for (int r = 0; r < 4; ++r) GST_B128_NT(yo, o[r], ob + r * 8192, 0);
    }
    asm volatile(".Lfk_code_b:");
    TSE(12);
    TS_FLUSH();
}

}

extern "C" void kernel_launch(void* const* d_in, const int* in_sizes, int n_in, void* d_out, int out_size, void* d_ws, size_t ws_size,
                              hipStream_t stream) {
    (void)in_sizes; (void)n_in; (void)out_size; (void)ws_size;
    fk::Args a;
    a.q = (const char*)d_in[0]; a.k = (const char*)d_in[1]; a.v = (const char*)d_in[2]; a.sk = (const char*)d_in[3];
    a.ssmk = (const char*)d_in[4]; a.ssmD = (const char*)d_in[5]; a.x = (const char*)d_in[6]; a.lre = (const char*)d_in[7];
    a.lim = (const char*)d_in[8]; a.bre = (const char*)d_in[9]; a.bim = (const char*)d_in[10]; a.cre = (const char*)d_in[11];
    a.cim = (const char*)d_in[12]; a.logdt = (const char*)d_in[13]; a.dskip = (const char*)d_in[14];
    a.out = (char*)d_out; a.ws = (char*)d_ws;
    hipLaunchKernelGGL(fk::fused, dim3(256), dim3(768), 0, stream, a);
}
```

```cpp
#include <hip/hip_runtime.h>
#include <stdint.h>
#include <type_traits>

namespace fk {

constexpr int S = 2048, D = 1024, P = 1024;

typedef __attribute__((ext_vector_type(8))) short bf16x8;
typedef __attribute__((ext_vector_type(4))) short s16x4;
typedef __attribute__((ext_vector_type(16))) float f32x16;
typedef __attribute__((ext_vector_type(4))) float f32x4;
typedef __attribute__((ext_vector_type(2))) float f32x2;
typedef __attribute__((ext_vector_type(4))) unsigned u32x4;
typedef __attribute__((ext_vector_type(2))) unsigned u32x2;
#define LDSP(T, a) ((__attribute__((address_space(3))) T*)(uintptr_t)(unsigned)(a))

__device__ __forceinline__ float bf2f(unsigned v) { return __uint_as_float(v << 16); }
__device__ __forceinline__ float bflo(unsigned w) { return __uint_as_float(w << 16); }
__device__ __forceinline__ float bfhi(unsigned w) { return __uint_as_float(w & 0xffff0000u); }
__device__ __forceinline__ uint16_t f2bf(float f) { __bf16 b = (__bf16)f; return __builtin_bit_cast(uint16_t, b); }
__device__ __forceinline__ float rbf(float f) { return bf2f(f2bf(f)); }
typedef __attribute__((ext_vector_type(2))) __bf16 bf16x2;
__device__ __forceinline__ unsigned pack2(float lo, float hi) { return __builtin_bit_cast(unsigned, __builtin_convertvector(f32x2{lo, hi}, bf16x2)); }
__device__ __forceinline__ float rb1(float x) { return __uint_as_float(pack2(0.f, x)); }
__device__ __forceinline__ void cmac(float& xr, float& xi, float ar, float ai, float hr, float hi) {
    asm("v_fmac_f32 %0, %2, %4\n\tv_fmac_f32 %1, %2, %5\n\tv_fma_f32 %0, -%3, %5, %0\n\tv_fmac_f32 %1, %3, %4" : "+v"(xr), "+v"(xi) : "v"(ar), "v"(ai), "v"(hr), "v"(hi));
}
__device__ __forceinline__ void cmad(float& o_r, float& o_i, float ar, float ai, float hr, float hi, float er, float ei) {
    asm("v_fma_f32 %0, %2, %4, %6\n\tv_fma_f32 %1, %2, %5, %7\n\tv_fma_f32 %0, -%3, %5, %0\n\tv_fmac_f32 %1, %3, %4" : "=&v"(o_r), "=&v"(o_i) : "v"(ar), "v"(ai), "v"(hr), "v"(hi), "v"(er), "v"(ei));
}
__device__ __forceinline__ f32x2 cmul4(f32x2 a, f32x2 h) {
    float o_r, o_i;
    asm("v_mul_f32 %0, %2, %4\n\tv_mul_f32 %1, %2, %5\n\tv_fma_f32 %0, -%3, %5, %0\n\tv_fmac_f32 %1, %3, %4" : "=&v"(o_r), "=&v"(o_i) : "v"(a[0]), "v"(a[1]), "v"(h[0]), "v"(h[1]));
    return f32x2{o_r, o_i};
}

#define PK_LO " op_sel:[0,0,0] op_sel_hi:[0,1,1]"
#define PK_HI " op_sel:[1,0,0] op_sel_hi:[1,1,1]"
#define PK_NEG " neg_lo:[1,0,0] neg_hi:[1,0,0]"
__device__ __forceinline__ void pk_cmad(f32x2& o_r, f32x2& o_i, f32x2 a, f32x2 hr, f32x2 hi, f32x2 er, f32x2 ei) {
    asm("v_pk_fma_f32 %0, %2, %3, %5" PK_LO "\n\tv_pk_fma_f32 %1, %2, %4, %6" PK_LO "\n\tv_pk_fma_f32 %0, %2, %4, %0" PK_HI PK_NEG "\n\tv_pk_fma_f32 %1, %2, %3, %1" PK_HI
        : "=&v"(o_r), "=&v"(o_i) : "v"(a), "v"(hr), "v"(hi), "v"(er), "v"(ei));
}
__device__ __forceinline__ void pk_cmac_conj(f32x2& xr, f32x2& xi, f32x2 a, f32x2 hr, f32x2 hi) {
    asm("v_pk_fma_f32 %0, %2, %3, %0" PK_LO "\n\tv_pk_fma_f32 %1, %2, %4, %1" PK_LO "\n\tv_pk_fma_f32 %0, %2, %4, %0" PK_HI "\n\tv_pk_fma_f32 %1, %2, %3, %1" PK_HI PK_NEG
        : "+v"(xr), "+v"(xi) : "v"(a), "v"(hr), "v"(hi));
}
__device__ __forceinline__ void pk_conj_mul(f32x2& o_r, f32x2& o_i, f32x2 a, f32x2 hr, f32x2 hi) {
    asm("v_pk_mul_f32 %0, %2, %3 op_sel:[0,0] op_sel_hi:[0,1]\n\tv_pk_mul_f32 %1, %2, %4 op_sel:[0,0] op_sel_hi:[0,1] neg_lo:[1,0] neg_hi:[1,0]\n\t"
        "v_pk_fma_f32 %0, %2, %4, %0" PK_HI PK_NEG "\n\tv_pk_fma_f32 %1, %2, %3, %1" PK_HI PK_NEG : "=&v"(o_r), "=&v"(o_i) : "v"(a), "v"(hr), "v"(hi));
}
__device__ __forceinline__ void pk_conj_mac(f32x2& xr, f32x2& xi, f32x2 a, f32x2 hr, f32x2 hi) {
    asm("v_pk_fma_f32 %0, %2, %3, %0" PK_LO "\n\tv_pk_fma_f32 %1, %2, %4, %1" PK_LO PK_NEG "\n\tv_pk_fma_f32 %0, %2, %4, %0" PK_HI PK_NEG "\n\tv_pk_fma_f32 %1, %2, %3, %1" PK_HI PK_NEG
        : "+v"(xr), "+v"(xi) : "v"(a), "v"(hr), "v"(hi));
}
__device__ __forceinline__ void cmac2(f32x2& x, f32x2 a, f32x2 h) {
    asm("v_pk_fma_f32 %0, %1, %2, %0" PK_LO "\n\tv_pk_fma_f32 %0, %1, %2, %0 op_sel:[1,1,0] op_sel_hi:[1,0,1] neg_lo:[1,0,0]" : "+v"(x) : "v"(a), "v"(h));
}
#define PLN 0
__device__ __forceinline__ void c2_mac(f32x2& xr, f32x2& xi, float p, float q, float r, float s, f32x2 hr, f32x2 hi) {
    float a = xr[0], b = xi[0], c = xr[1], d = xi[1];
    asm("v_fmac_f32 %0, %4, %8\n\tv_fmac_f32 %1, %6, %9\n\tv_fmac_f32 %2, %4, %10\n\tv_fmac_f32 %3, %6, %11\n\t"
        "v_fmac_f32 %0, %5, %9\n\tv_fmac_f32 %1, %7, %8\n\tv_fmac_f32 %2, %5, %11\n\tv_fmac_f32 %3, %7, %10"
        : "+v"(a), "+v"(b), "+v"(c), "+v"(d) : "v"(p), "v"(q), "v"(r), "v"(s), "v"(hr[0]), "v"(hi[0]), "v"(hr[1]), "v"(hi[1]));
    xr = f32x2{a, c}; xi = f32x2{b, d};
}
__device__ __forceinline__ void c2_mad(f32x2& xr, f32x2& xi, float p, float q, float r, float s, f32x2 hr, f32x2 hi, f32x2 er, f32x2 ei) {
    float a, b, c, d;
    asm("v_fma_f32 %0, %4, %8, %12\n\tv_fma_f32 %1, %6, %9, %13\n\tv_fma_f32 %2, %4, %10, %14\n\tv_fma_f32 %3, %6, %11, %15\n\t"
        "v_fmac_f32 %0, %5, %9\n\tv_fmac_f32 %1, %7, %8\n\tv_fmac_f32 %2, %5, %11\n\tv_fmac_f32 %3, %7, %10"
        : "=&v"(a), "=&v"(b), "=&v"(c), "=&v"(d) : "v"(p), "v"(q), "v"(r), "v"(s), "v"(hr[0]), "v"(hi[0]), "v"(hr[1]), "v"(hi[1]), "v"(er[0]), "v"(ei[0]), "v"(er[1]), "v"(ei[1]));
    xr = f32x2{a, c}; xi = f32x2{b, d};
}
__device__ __forceinline__ void c2_mul(f32x2& xr, f32x2& xi, float p, float q, float r, float s, f32x2 hr, f32x2 hi) {
    float a, b, c, d;
    asm("v_mul_f32 %0, %4, %8\n\tv_mul_f32 %1, %6, %9\n\tv_mul_f32 %2, %4, %10\n\tv_mul_f32 %3, %6, %11\n\t"
        "v_fmac_f32 %0, %5, %9\n\tv_fmac_f32 %1, %7, %8\n\tv_fmac_f32 %2, %5, %11\n\tv_fmac_f32 %3, %7, %10"
        : "=&v"(a), "=&v"(b), "=&v"(c), "=&v"(d) : "v"(p), "v"(q), "v"(r), "v"(s), "v"(hr[0]), "v"(hi[0]), "v"(hr[1]), "v"(hi[1]));
    xr = f32x2{a, c}; xi = f32x2{b, d};
}
__device__ __forceinline__ float add1(float a, float b) { float c; asm("v_add_f32 %0, %1, %2" : "=v"(c) : "v"(a), "v"(b)); return c; }
__device__ __forceinline__ void mfma_(f32x4& c, bf16x8 a, bf16x8 b) { asm volatile("v_mfma_f32_16x16x32_bf16 %0, %1, %2, %0" : "+v"(c) : "v"(a), "v"(b)); }
__device__ __forceinline__ float sum4(float x) {
    const auto p = __builtin_amdgcn_permlane32_swap(__float_as_uint(x), __float_as_uint(x), false, false);
    x = __uint_as_float(p[0]) + __uint_as_float(p[1]);
    const auto q = __builtin_amdgcn_permlane16_swap(__float_as_uint(x), __float_as_uint(x), false, false);
    return __uint_as_float(q[0]) + __uint_as_float(q[1]);
}

template <int I, int N, class F>
__device__ __forceinline__ void static_for(F&& f) { if constexpr (I < N) { f(std::integral_constant<int, I>{}); static_for<I + 1, N>(f); } }

#define TS_DECL
#define TS0a()
#define TS0b()
#define TSE(i)
#define TS(i)
#define TS_FLUSH()

#define WAIT_V(n) asm volatile("s_waitcnt vmcnt(%0)" ::"n"((n) > 63 ? 63 : (n)) : "memory")
#define WAIT_L(n) asm volatile("s_waitcnt lgkmcnt(%0)" ::"n"((n) > 15 ? 15 : (n)) : "memory")
#define BW 1
constexpr int wenc(int v, int l) { v = v > 63 ? 63 : v; l = l > 15 ? 15 : l; return (v & 15) | 0x70 | (l << 8) | ((v >> 4) << 14); }
#define WAIT_VB(n) do { if constexpr (BW) __builtin_amdgcn_s_waitcnt(wenc(n, 15)); else WAIT_V(n); } while (0)
#define WAIT_LB(n) do { if constexpr (BW) __builtin_amdgcn_s_waitcnt(wenc(63, n)); else WAIT_L(n); } while (0)
#define BAR() asm volatile("s_barrier" ::: "memory")
#define SB() __builtin_amdgcn_sched_barrier(0)
template <class T> __device__ __forceinline__ void tie_(T& x) { asm volatile("" : "+v"(x)); }
#define TIE(x) tie_(x)
__device__ __forceinline__ unsigned take(unsigned x) { unsigned y; asm volatile("v_mov_b32 %0, %1" : "=v"(y) : "v"(x)); return y; }
#define DEF_LD(name, ins) template <int OFF, class T> __device__ __forceinline__ void name(T& dst, unsigned voff, const char* sb) { \
    asm volatile(ins : "=v"(dst) : "v"(voff), "s"(sb), "n"(OFF) : "memory"); }
DEF_LD(gld_u16_, "global_load_ushort %0, %1, %2 offset:%3")
DEF_LD(gld_b32_, "global_load_dword %0, %1, %2 offset:%3")
DEF_LD(gld_b64_, "global_load_dwordx2 %0, %1, %2 offset:%3")
DEF_LD(gld_b128_, "global_load_dwordx4 %0, %1, %2 offset:%3")
#define NTQ " nt"
#define NTK " nt"
DEF_LD(gld_b64_nt_, "global_load_dwordx2 %0, %1, %2 offset:%3" NTQ)
DEF_LD(gld_b128_nt_, "global_load_dwordx4 %0, %1, %2 offset:%3" NTQ)
DEF_LD(gld_b64_sc1_, "global_load_dwordx2 %0, %1, %2 offset:%3 sc1")
DEF_LD(gld_b128_sc1_, "global_load_dwordx4 %0, %1, %2 offset:%3 sc1")
#define DEF_ST(name, ins) template <int OFF, class T> __device__ __forceinline__ void name(unsigned voff, const T& v, const char* sb) { \
    asm volatile(ins :: "v"(voff), "v"(v), "s"(sb), "n"(OFF) : "memory"); }
DEF_ST(gst_b128_, "global_store_dwordx4 %0, %1, %2 offset:%3\n\ts_nop 1")
DEF_ST(gst_b128_nt_, "global_store_dwordx4 %0, %1, %2 offset:%3 nt\n\ts_nop 1")
DEF_ST(gst_b128_sc1_, "global_store_dwordx4 %0, %1, %2 offset:%3 sc1\n\ts_nop 1")
DEF_ST(gst_b64_sc1_, "global_store_dwordx2 %0, %1, %2 offset:%3 sc1")
#define GLD_U16(dst, voff, sb) gld_u16_<0>(dst, voff, sb)
#define GLD_B32(dst, voff, sb, off) gld_b32_<(off)>(dst, voff, sb)
#define GLD_B64(dst, voff, sb, off) gld_b64_<(off)>(dst, voff, sb)
#define GLD_B128(dst, voff, sb, off) gld_b128_<(off)>(dst, voff, sb)
#define GLD_B64_NT(dst, voff, sb, off) gld_b64_nt_<(off)>(dst, voff, sb)
#define GLD_B128_NT(dst, voff, sb, off) gld_b128_nt_<(off)>(dst, voff, sb)
#define GLD_B64_SC1(dst, voff, sb, off) gld_b64_sc1_<(off)>(dst, voff, sb)
#define GLD_B128_SC1(dst, voff, sb, off) gld_b128_sc1_<(off)>(dst, voff, sb)
#define GST_B128(voff, v, sb, off) gst_b128_<(off)>(voff, v, sb)
#define GST_B128_NT(voff, v, sb, off) gst_b128_nt_<(off)>(voff, v, sb)
#define GST_B128_SC1(voff, v, sb, off) gst_b128_sc1_<(off)>(voff, v, sb)
#define GST_B64_SC1(voff, v, sb) gst_b64_sc1_<0>(voff, v, sb)
__device__ __forceinline__ void dma_(unsigned ldsaddr, unsigned voff, const char* sb) {
    asm volatile("s_mov_b32 m0, %0\n\ts_nop 0\n\tglobal_load_lds_dwordx4 %1, %2" :: "s"(ldsaddr), "v"(voff), "s"(sb) : "memory"); }
__device__ __forceinline__ void dma_nt_(unsigned ldsaddr, unsigned voff, const char* sb) {
    asm volatile("s_mov_b32 m0, %0\n\ts_nop 0\n\tglobal_load_lds_dwordx4 %1, %2 nt" :: "s"(ldsaddr), "v"(voff), "s"(sb) : "memory"); }
#define DMA(ldsaddr, voff, sb) dma_(ldsaddr, voff, sb)
template <int OFF> __device__ __forceinline__ void dmao_(unsigned voff, const char* sb) {
    asm volatile("global_load_lds_dwordx4 %0, %1 offset:%2" :: "v"(voff), "s"(sb), "n"(OFF) : "memory"); }
template <int OFF, class T> __device__ __forceinline__ void dsr128_(T& dst, unsigned addr) { asm volatile("ds_read_b128 %0, %1 offset:%2" : "=v"(dst) : "v"(addr), "n"(OFF)); }
template <int OFF, class T> __device__ __forceinline__ void dsr64_(T& dst, unsigned addr) { asm volatile("ds_read_b64 %0, %1 offset:%2" : "=v"(dst) : "v"(addr), "n"(OFF)); }
template <int OFF, class T> __device__ __forceinline__ void dsr2_(T& dst, unsigned addr) { asm volatile("ds_read2_b32 %0, %1 offset0:%2 offset1:%3" : "=v"(dst) : "v"(addr), "n"(OFF), "n"(OFF + 1)); }
#define DSR128(dst, addr, off) dsr128_<(off)>(dst, addr)
#define DSR64(dst, addr, off) dsr64_<(off)>(dst, addr)

__device__ __forceinline__ void kv4(unsigned& o01, unsigned& o23, unsigned ka, unsigned kb, unsigned kc, unsigned kd, const unsigned* w, unsigned v0, unsigned v1) {
    unsigned t1, t3, p0, p1, p2, p3;
    asm(
        "v_dot2_f32_bf16 %0, %8, %19, 0\n\tv_dot2_f32_bf16 %1, %9, %18, 0\n\tv_dot2_f32_bf16 %2, %9, %19, 0\n\tv_dot2_f32_bf16 %3, %10, %18, 0\n\t"
        "v_dot2_f32_bf16 %4, %9, %16, 0\n\tv_dot2_f32_bf16 %5, %9, %17, 0\n\tv_dot2_f32_bf16 %6, %10, %16, 0\n\tv_dot2_f32_bf16 %7, %10, %17, 0\n\t"
        "v_cvt_pk_bf16_f32 %0, %0, %4\n\tv_cvt_pk_bf16_f32 %1, %1, %5\n\tv_cvt_pk_bf16_f32 %2, %2, %6\n\tv_cvt_pk_bf16_f32 %3, %3, %7\n\t"
        "v_dot2_f32_bf16 %4, %9, %15, 0\n\tv_dot2_f32_bf16 %5, %10, %14, 0\n\tv_dot2_f32_bf16 %6, %10, %15, 0\n\tv_dot2_f32_bf16 %7, %11, %14, 0\n\t"
        "v_dot2_f32_bf16 %0, %0, %22, 0\n\tv_dot2_f32_bf16 %1, %1, %22, 0\n\tv_dot2_f32_bf16 %2, %2, %22, 0\n\tv_dot2_f32_bf16 %3, %3, %22, 0\n\t"
        "v_cvt_pk_bf16_f32 %0, %0, %4\n\tv_cvt_pk_bf16_f32 %1, %1, %5\n\tv_cvt_pk_bf16_f32 %2, %2, %6\n\tv_cvt_pk_bf16_f32 %3, %3, %7\n\t"
        "v_dot2_f32_bf16 %4, %10, %12, 0\n\tv_dot2_f32_bf16 %5, %10, %13, 0\n\tv_dot2_f32_bf16 %6, %11, %12, 0\n\tv_dot2_f32_bf16 %7, %11, %13, 0\n\t"
        "v_dot2_f32_bf16 %0, %0, %22, 0\n\tv_dot2_f32_bf16 %1, %1, %22, 0\n\tv_dot2_f32_bf16 %2, %2, %22, 0\n\tv_dot2_f32_bf16 %3, %3, %22, 0\n\t"
        "v_cvt_pk_bf16_f32 %0, %0, %4\n\tv_cvt_pk_bf16_f32 %1, %1, %5\n\tv_cvt_pk_bf16_f32 %2, %2, %6\n\tv_cvt_pk_bf16_f32 %3, %3, %7\n\t"
        "v_dot2_f32_bf16 %0, %0, %22, 0\n\tv_dot2_f32_bf16 %1, %1, %22, 0\n\tv_dot2_f32_bf16 %2, %2, %22, 0\n\tv_dot2_f32_bf16 %3, %3, %22, 0\n\t"
        "v_cvt_pk_bf16_f32 %0, %0, 0\n\tv_cvt_pk_bf16_f32 %1, 0, %1\n\tv_cvt_pk_bf16_f32 %2, %2, 0\n\tv_cvt_pk_bf16_f32 %3, 0, %3\n\t"
        "v_dot2_f32_bf16 %0, %0, %20, 0\n\tv_dot2_f32_bf16 %1, %1, %20, 0\n\tv_dot2_f32_bf16 %2, %2, %21, 0\n\tv_dot2_f32_bf16 %3, %3, %21, 0\n\t"
        "s_nop 1\n\tv_cvt_pk_bf16_f32 %0, %1, %0\n\tv_cvt_pk_bf16_f32 %2, %3, %2\n\t"
        : "=&v"(o01), "=&v"(t1), "=&v"(o23), "=&v"(t3), "=&v"(p0), "=&v"(p1), "=&v"(p2), "=&v"(p3)
        : "v"(ka), "v"(kb), "v"(kc), "v"(kd), "v"(w[0]), "v"(w[1]), "v"(w[2]), "v"(w[3]), "v"(w[4]), "v"(w[5]), "v"(w[6]), "v"(w[7]), "v"(v0), "v"(v1), "s"(0x3f803f80u));
}
__device__ __forceinline__ void kv4p(unsigned& o01, unsigned& o23, unsigned ka, unsigned kb, unsigned kc, unsigned kd, const unsigned* w, unsigned v0, unsigned v1) {
    unsigned t1, t3, p0, p1, p2, p3, e0, e1, e2, e3, e4, e5, e6;
    asm(
        "v_and_b32 %8, %25, %15\n\tv_lshlrev_b32 %9, 16, %16\n\tv_and_b32 %10, %25, %16\n\tv_lshlrev_b32 %11, 16, %17\n\t"
        "v_and_b32 %12, %25, %17\n\tv_mul_f32 %0, %22, %8\n\tv_mul_f32 %1, %22, %9\n\tv_mul_f32 %2, %22, %10\n\t"
        "v_mul_f32 %3, %22, %11\n\tv_mul_f32 %4, %21, %9\n\tv_mul_f32 %5, %21, %10\n\tv_mul_f32 %6, %21, %11\n\t"
        "v_mul_f32 %7, %21, %12\n\tv_lshlrev_b32 %13, 16, %18\n\tv_and_b32 %14, %25, %18\n\tv_cvt_pk_bf16_f32 %0, 0, %0\n\t"
        "v_cvt_pk_bf16_f32 %1, 0, %1\n\tv_cvt_pk_bf16_f32 %2, 0, %2\n\tv_cvt_pk_bf16_f32 %3, 0, %3\n\tv_cvt_pk_bf16_f32 %4, 0, %4\n\t"
        "v_cvt_pk_bf16_f32 %5, 0, %5\n\tv_cvt_pk_bf16_f32 %6, 0, %6\n\tv_cvt_pk_bf16_f32 %7, 0, %7\n\tv_add_f32 %0, %0, %4\n\t"
        "v_add_f32 %1, %1, %5\n\tv_add_f32 %2, %2, %6\n\tv_add_f32 %3, %3, %7\n\tv_mul_f32 %4, %20, %10\n\t"
        "v_mul_f32 %5, %20, %11\n\tv_mul_f32 %6, %20, %12\n\tv_mul_f32 %7, %20, %13\n\tv_cvt_pk_bf16_f32 %0, 0, %0\n\t"
        "v_cvt_pk_bf16_f32 %1, 0, %1\n\tv_cvt_pk_bf16_f32 %2, 0, %2\n\tv_cvt_pk_bf16_f32 %3, 0, %3\n\tv_cvt_pk_bf16_f32 %4, 0, %4\n\t"
        "v_cvt_pk_bf16_f32 %5, 0, %5\n\tv_cvt_pk_bf16_f32 %6, 0, %6\n\tv_cvt_pk_bf16_f32 %7, 0, %7\n\tv_add_f32 %0, %0, %4\n\t"
        "v_add_f32 %1, %1, %5\n\tv_add_f32 %2, %2, %6\n\tv_add_f32 %3, %3, %7\n\tv_mul_f32 %4, %19, %11\n\t"
        "v_mul_f32 %5, %19, %12\n\tv_mul_f32 %6, %19, %13\n\tv_mul_f32 %7, %19, %14\n\tv_cvt_pk_bf16_f32 %0, 0, %0\n\t"
        "v_cvt_pk_bf16_f32 %1, 0, %1\n\tv_cvt_pk_bf16_f32 %2, 0, %2\n\tv_cvt_pk_bf16_f32 %3, 0, %3\n\tv_cvt_pk_bf16_f32 %4, 0, %4\n\t"
        "v_cvt_pk_bf16_f32 %5, 0, %5\n\tv_cvt_pk_bf16_f32 %6, 0, %6\n\tv_cvt_pk_bf16_f32 %7, 0, %7\n\tv_add_f32 %0, %0, %4\n\t"
        "v_add_f32 %1, %1, %5\n\tv_add_f32 %2, %2, %6\n\tv_add_f32 %3, %3, %7\n\tv_lshlrev_b32 %4, 16, %23\n\t"
        "v_and_b32 %5, %25, %23\n\tv_lshlrev_b32 %6, 16, %24\n\tv_and_b32 %7, %25, %24\n\tv_cvt_pk_bf16_f32 %0, 0, %0\n\t"
        "v_cvt_pk_bf16_f32 %1, 0, %1\n\tv_cvt_pk_bf16_f32 %2, 0, %2\n\tv_cvt_pk_bf16_f32 %3, 0, %3\n\tv_mul_f32 %0, %0, %4\n\t"
        "v_mul_f32 %1, %1, %5\n\tv_mul_f32 %2, %2, %6\n\tv_mul_f32 %3, %3, %7\n\tv_cvt_pk_bf16_f32 %0, %1, %0\n\t"
        "v_cvt_pk_bf16_f32 %2, %3, %2\n\t"
        : "=&v"(o01), "=&v"(t1), "=&v"(o23), "=&v"(t3), "=&v"(p0), "=&v"(p1), "=&v"(p2), "=&v"(p3), "=&v"(e0), "=&v"(e1), "=&v"(e2), "=&v"(e3), "=&v"(e4), "=&v"(e5), "=&v"(e6)
        : "v"(ka), "v"(kb), "v"(kc), "v"(kd), "v"(w[1]), "v"(w[3]), "v"(w[5]), "v"(w[7]), "v"(v0), "v"(v1), "s"(0xffff0000u));
}
__device__ __forceinline__ f32x4 mul4bf(u32x2 x, const unsigned* d) {
    float a, b, c, e;
    asm("v_dot2_f32_bf16 %0, %4, %6, 0\n\tv_dot2_f32_bf16 %1, %4, %7, 0\n\tv_dot2_f32_bf16 %2, %5, %8, 0\n\tv_dot2_f32_bf16 %3, %5, %9, 0\n\t"
        "v_cvt_pk_bf16_f32 %0, 0, %0\n\tv_cvt_pk_bf16_f32 %1, 0, %1\n\tv_cvt_pk_bf16_f32 %2, 0, %2\n\tv_cvt_pk_bf16_f32 %3, 0, %3"
        : "=&v"(a), "=&v"(b), "=&v"(c), "=&v"(e) : "v"(x[0]), "v"(x[1]), "v"(d[0]), "v"(d[1]), "v"(d[2]), "v"(d[3]));
    return f32x4{a, b, c, e};
}
template <int BIT> __device__ __forceinline__ void quad_swap(u32x4& a, u32x4& b) {
    unsigned n0, n1, n2, n3, b0 = b[0], b1 = b[1], b2 = b[2], b3 = b[3];
#define QS_(d, s, k) "v_cndmask_b32_dpp " d ", " s ", " k ", vcc quad_perm:[%14,%15,%16,%17] row_mask:0xf bank_mask:0xf\n\t"
    asm("s_mov_b64 vcc, %12\n\ts_nop 0\n\t" QS_("%0", "%4", "%8") QS_("%1", "%5", "%9") QS_("%2", "%6", "%10") QS_("%3", "%7", "%11")
        "s_mov_b64 vcc, %13\n\t" QS_("%4", "%8", "%4") QS_("%5", "%9", "%5") QS_("%6", "%10", "%6") QS_("%7", "%11", "%7")
        : "=&v"(n0), "=&v"(n1), "=&v"(n2), "=&v"(n3), "+v"(b0), "+v"(b1), "+v"(b2), "+v"(b3)
        : "v"(a[0]), "v"(a[1]), "v"(a[2]), "v"(a[3]), "s"(BIT ? 0x3333333333333333ull : 0x5555555555555555ull), "s"(BIT ? 0xccccccccccccccccull : 0xaaaaaaaaaaaaaaaaull),
          "n"(BIT ? 2 : 1), "n"(BIT ? 3 : 0), "n"(BIT ? 0 : 3), "n"(BIT ? 1 : 2) : "vcc");
#undef QS_
    a = u32x4{n0, n1, n2, n3}; b = u32x4{b0, b1, b2, b3};
}
#define IF_(c, C) "s_cmp_lg_u32 " c ", " C "\n\ts_cbranch_scc1 1f\n\t"
template <int C, int N> __device__ __forceinline__ void wait_if(int c) { asm volatile(IF_("%0", "%1") "s_waitcnt vmcnt(%2)\n1:" :: "s"(c), "n"(C), "n"(N) : "scc", "memory"); }
template <int C> __device__ __forceinline__ void flag_if(int c, unsigned voff, u32x2 v, const char* sb) {
    asm volatile(IF_("%0", "%1") "s_mov_b64 exec, 1\n\tglobal_store_dwordx2 %2, %3, %4 sc1\n\ts_mov_b64 exec, -1\n1:" :: "s"(c), "n"(C), "v"(voff), "v"(v), "s"(sb) : "scc", "memory"); }
template <int C> __device__ __forceinline__ void st4_if(int c, unsigned voff, const u32x4* v, const char* s0, const char* s1) {
    asm volatile(IF_("%0", "%1") "global_store_dwordx4 %2, %3, %7 sc1\n\tglobal_store_dwordx4 %2, %4, %7 offset:1024 sc1\n\tglobal_store_dwordx4 %2, %5, %8 sc1\n\tglobal_store_dwordx4 %2, %6, %8 offset:1024 sc1\n\ts_nop 1\n1:"
                 :: "s"(c), "n"(C), "v"(voff), "v"(v[0]), "v"(v[1]), "v"(v[2]), "v"(v[3]), "s"(s0), "s"(s1) : "scc", "memory"); }
template <int C> __device__ __forceinline__ void ld4_if(int c, unsigned voff, u32x4* v, const char* s0) {
    asm volatile(IF_("%4", "%5") "global_load_dwordx4 %0, %6, %7 nt\n\tglobal_load_dwordx4 %1, %6, %7 offset:1024 nt\n\tglobal_load_dwordx4 %2, %6, %7 offset:2048 nt\n\tglobal_load_dwordx4 %3, %6, %7 offset:3072 nt\n1:"
                 : "+v"(v[0]), "+v"(v[1]), "+v"(v[2]), "+v"(v[3]) : "s"(c), "n"(C), "v"(voff), "s"(s0) : "scc", "memory"); }
template <int C> __device__ __forceinline__ void ld8_if(int c, unsigned voff, u32x2* v, const char* s0, const char* s1) {
    asm volatile(IF_("%8", "%9") "global_load_dwordx2 %0, %10, %11 offset:-4096 nt\n\tglobal_load_dwordx2 %1, %10, %11 offset:-2048 nt\n\tglobal_load_dwordx2 %2, %10, %11 nt\n\tglobal_load_dwordx2 %3, %10, %11 offset:2048 nt\n\t"
                 "global_load_dwordx2 %4, %10, %12 offset:-4096 nt\n\tglobal_load_dwordx2 %5, %10, %12 offset:-2048 nt\n\tglobal_load_dwordx2 %6, %10, %12 nt\n\tglobal_load_dwordx2 %7, %10, %12 offset:2048 nt\n1:"
                 : "+v"(v[0]), "+v"(v[1]), "+v"(v[2]), "+v"(v[3]), "+v"(v[4]), "+v"(v[5]), "+v"(v[6]), "+v"(v[7]) : "s"(c), "n"(C), "v"(voff), "s"(s0), "s"(s1) : "scc", "memory"); }
template <int E> __device__ __forceinline__ void sy_post(unsigned myaddr) { asm volatile("ds_write_b32 %0, %0 offset:%1" :: "v"(myaddr), "n"(E * 64) : "memory"); }
template <int E, int SL = 1> __device__ __forceinline__ unsigned sy_wait(unsigned addr) {
    unsigned v;
    asm volatile("s_branch 2f\n1:\n\ts_sleep %3\n2:\n\tds_read_b32 %0, %1 offset:%2\n\ts_waitcnt lgkmcnt(0)\n\tv_cmp_eq_u32 vcc, 0, %0\n\ts_cbranch_vccnz 1b"
                 : "=&v"(v) : "v"(addr), "n"(E * 64), "n"(SL) : "vcc", "memory");
    return v;
}
template <int OFF, class T> __device__ __forceinline__ void dsrtr_(T& dst, unsigned addr) { asm volatile("ds_read_b64_tr_b16 %0, %1 offset:%2" : "=v"(dst) : "v"(addr), "n"(OFF)); }

constexpr size_t WS_H = 0, WS_KVT = (size_t)10 << 20, WS_ZT = (size_t)15 << 20, WS_E = (size_t)20 << 20, WS_FLAG = WS_E + (2048 << 10);
#define HP 65536
#define KP 8192
#define ZP 4096
constexpr int FL_KV = 0, FL_Z = 65536, FL_H = 131072, FL_EPOCH = 196608;
constexpr unsigned SPIN_CAP = 1u << 17;
#define ABL 0
#define CSPLIT 1
#define MERGE 3
#define CB 13
#define PAUSE 0
#define QLATE 0
#define CVPRIO 0
#define STG_SEL (idx & 3)
#define SP0 3
#define SDP 1
#define LP0 3
#define LDP 2
#define KVS 5
#define KVU 3
#define QIT 1
#define ZB 3
#define OS 2
#define MPRIO 2

#define ROT 0x80A5
#define R2 0x41
#define CPF 0
#define PRE 4
#define PRO 9
#define G1NT 0
#define G1NTS 16
#define TMIX 0
#define TLBT 0
#define CDEF 0
#define CVLATE 1
#define FPRIO 3
#define YS 0
#define DP0 1
#define DP1 5
#define AWB 1
#define PK 0
#define PU 13
#define PC0 13
#define PC1 14
#define PC2 15
#define PC3 16
#define PQ 0
#define SPF 0
#define SPH 0
#define NB1 4
#define EP 1
#define ESL 0
#define CFB 0
#define P1 1
#define P0 1
#define BKE 0
#define EALL 0
#define EREP 0
#define TOUCH 0
#define FILLB 0
#define CPOST 1
#define CEARLY 0
#define NOOWN 0
#define PFIRST 0
#define ZB0 0
#define CATCH 4
#define PWE 0
#define SWAP 1
#define KVM 0x1F0
#define KFD 3
#define ZW 2
#define ZX 1
#define ZP0 3
#define BARM 0x22220
#define NR 6
#define WPS 2
#define CD0 3
#define AWX 8
#define VBM 0
#define ZOWN 1
#define ZPS 4
#define XAB 0
#define QM 1
#define OSG 0
#define KPL 0xF0
#define KPRIO 0
#define GPRIO 1
#define PST 0x7654
#define PLT 0xA864
#define LC 0
#define PRE2 PRE
#define KVD 0
#define PAIR 1
#define G2P1 0
#define EPRIO 0
#define LSW 1
#define EGX 28
#define CR1 0
#define CR2 0
#define CR3 4
#define CRL (2 * NR - CR1 - CR2 - CR3)
#define UE 1
#define UPRIO 0
#define AMF 7
#define M1 1
#define W2 1
#define W1 0
#define AWN 8
#define CRATE 2
#define ZSB 0
#define ZFB 1
#define ZSL 0
#define OSL 0
constexpr int it_cnt(int p, int k, int last) {
    const int pos[8] = {PK, PU, PC0, PC1, PQ, PU - 1, PC2, PC3}, n[8] = {4, 4, 4, 4, 8, 1, 4, 4};
    int c = 0;
    for (int i = 0; i < 8; ++i) if (pos[i] <= last && (pos[i] > p || (pos[i] == p && i > k))) c += n[i];
    return c;
}
constexpr int nib(int t, int c) { return (t >> (4 * c)) & 15; }
constexpr bool nib_any(int t, int v) { return nib(t, 0) == v || nib(t, 1) == v || nib(t, 2) == v || nib(t, 3) == v; }
constexpr int kv_wait(int late) {
    int t = 0;
    for (int c = 0; c < 4; ++c) t |= ((nib(PLT, c) >= nib(PST, c) && nib(PLT, c) < nib(PST, c) + KFD) == late ? nib(PST, c) + KFD : 15) << (4 * c);
    return t;
}
constexpr int G1STAGE = 32768;
constexpr int g1_slot(int s) { return (NB1 == 5 ? (s + 3) % 5 : s & 3) * G1STAGE; }
constexpr int L_KS = NB1 == 5 ? 65536 : 131072, L_VS = L_KS + 16384;
constexpr int L_TS = 3 * G1STAGE, L_TR = L_TS + 3072;
constexpr int CST = 4288;
constexpr int UOFF = 2 * CST + 128, UPL = 2304;
constexpr int CHSZ = UOFF + 2 * UPL + 32;
constexpr int L_AGG = 4 * CHSZ, L_ES = L_AGG + 15 * 512, L_SY = L_ES + 2048;
constexpr int L_HT = 98304;
constexpr int L_C = 98304, G2STAGE = 16384;
constexpr int L_Y = 16384;
static_assert(L_SY + 384 <= 65536 && L_AGG >= 49152 && UOFF % 16 == 0 && CHSZ % 256 == 32, "lds");

__device__ __forceinline__ int img_off(int r, int c) { return (r >> 1) * 256 + ((((r & 1) * 8 + c) ^ ((r >> 1) & 15)) << 4); }

constexpr int g1_upto(int g) {
    int n = PRO;
    for (int t = 0; t <= g; ++t) {
        int sb = (t + 1) / 2, sub = (t + 1) % 2, allowed = sb >= 1 ? 4 * (sb + NB1 - 1) + 2 * (sub + 1) : 4 * NB1;
        allowed -= LC;
        if (allowed < n) allowed = n;
        if (allowed > 64) allowed = 64;
        if (t >= 2 * LP0 - 1 && t < 2 * LP0 - 1 + PAUSE) continue;
        n = n + CATCH < allowed ? n + CATCH : allowed;
    }
    return n;
}
constexpr int g1_cnt(int s) { return g1_upto(2 * s - 2) - 4 * (s + 1); }
static_assert(g1_upto(28) == 64, "schedule");
constexpr int AW = AWN;
constexpr int g2_place(int s) { return s < 5 ? L_C + ((s + 2) & 3) * G2STAGE : s < 7 ? (s - 1) * G2STAGE : s < 10 ? (9 - s) * G2STAGE : g2_place(s - 9); }
constexpr bool g2_bar(int g) { return (BARM >> g) & 1; }
struct G2Plan { int upto[33], wr[33], ald[64][2], seq[164], use[64]; };
constexpr G2Plan g2_plan() {
    G2Plan p = {};
    int nc = 14 + 2 * NR, nw = 0, nx = 4, lastb = 0, n = 0;
    for (int i = 0; i < 164; ++i) p.seq[i] = 1000;
    for (; nx < 4 + AW; ++nx) p.seq[nx] = ++n;
    for (int t = 0; t < 32; ++t) {
        p.upto[t] = nc; p.wr[t] = nw;
        if (t < 2) continue;
        if (g2_bar(t)) lastb = t;
        const int w = AW + nw < AWX ? AW + nw : AWX;
        for (int f = 2 * t; f < 2 * t + 2; ++f) {
            p.use[f] = n;
            if (nc < 32 && nc == p.upto[t] + (f & 1) && (f & 1) < CRATE && t >= CD0 && lastb >= (nc < 20 ? 1 : (nc | 1) - 18)) p.seq[100 + nc++] = ++n;
            for (int k = 0; k < 2; ++k) {
                p.ald[f][k] = -1;
                if (nx < 64 && nx - f - k < w) { p.ald[f][k] = nx; p.seq[nx++] = ++n; }
            }
        }
        for (int k = 0; k < WPS; ++k) if (nw < 2 * NR && lastb >= (nw < 6 ? 1 : (nw | 1) - 4)) ++nw;
    }
    p.upto[32] = nc; p.wr[32] = nw;
    return p;
}
constexpr G2Plan G2P = g2_plan();
constexpr int g2_upto(int g) { return G2P.upto[g + 1]; }
constexpr int g2_wr(int g) { return G2P.wr[g + 1]; }
constexpr int g2_ald(int f, int k) { return G2P.ald[f][k]; }
constexpr int g2_cnt(int id, int at) { return G2P.use[at] - G2P.seq[id]; }
constexpr int g2_cover(int g) { int s = (g + 1) / 2; while (s < 15 && !g2_bar(2 * s + 1)) ++s; return s; }
static_assert(BARM >> 17 && g2_cover(3) < 7 && CRATE <= 2, "the epilogue needs stage 8 behind a barrier");

constexpr int cv_m(int o) { return (o & 1) ? 7 - (o >> 1) : o >> 1; }
constexpr int cv_first(int o) { int p = 0; for (int i = 0; i < o; ++i) p += 8 - cv_m(i); return p; }
constexpr int cv_grp(int p) { int o = 0; while (cv_first(o + 1) <= p) ++o; return o; }
constexpr int cv_read(int p) {
    int slot[40] = {};
    for (int i = 0; i < 40; ++i) slot[i] = -1;
    slot[0] = 2 * 3 + 1;
    for (int o = 0; o < 8; ++o) {
        int q = cv_first(o + 1) - 1;
        for (int h = 0; h < 2; ++h) { while (slot[q] >= 0) ++q; slot[q] = 2 * (o + 4) + h; }
    }
    const int bs[8] = {1, 3, 5, 11, 12, 13, 19, 21};
    for (int jb = 0; jb < 8; ++jb) slot[bs[jb]] = 32 + jb;
    return slot[p];
}
static_assert(cv_read(35) == 22 && cv_read(36) == 23 && cv_read(37) == -1 && cv_read(10) == 11 && cv_read(15) == 12 && cv_read(18) == 15 && cv_read(24) == 17, "conv schedule");
constexpr int cv_wait(int o) {
    int n = 0;
    for (int q = cv_first(o) - 1;; --q) {
        const int rd = cv_read(q < 0 ? q + 36 : q);
        if (rd == 2 * (o + (q < 0 ? 8 : 0)) + 1) return n;
        if (rd >= 0) ++n;
    }
}

struct Args {
    const char *q, *k, *v, *sk, *ssmk, *ssmD, *x, *lre, *lim, *bre, *bim, *cre, *cim, *logdt, *dskip;
    char *out, *ws;
};

typedef __attribute__((ext_vector_type(8))) unsigned long long u64x8;
__global__ __launch_bounds__(768, 3) void fused(Args) {
    __shared__ __attribute__((aligned(1024))) char shm[163840];
    TS0a();
    u64x8 ka0, ka1; unsigned long long ka2;
    const char* pc0;
    int code_n;
    asm volatile("s_load_dwordx16 %0, %5, 0x0\n\ts_load_dwordx16 %1, %5, 0x40\n\ts_load_dwordx2 %2, %5, 0x80\n\t"
                 "s_getpc_b64 %3\n.Lfk_code_0:\n\ts_mov_b32 %4, .Lfk_code_a-.Lfk_code_0-4\n\ts_max_i32 %4, %4, .Lfk_code_b-.Lfk_code_0-4"
                 : "=&s"(ka0), "=&s"(ka1), "=&s"(ka2), "=&s"(pc0), "=&s"(code_n) : "s"(__builtin_amdgcn_kernarg_segment_ptr()) : "scc");
    const int tid = threadIdx.x, lane = tid & 63, wraw = __builtin_amdgcn_readfirstlane(tid >> 6);
    const int wid = !SWAP ? wraw : wraw < 4 ? wraw + 8 : wraw - 4;
    const int b = blockIdx.x, xcd = b & 7, idx = b >> 3;
    asm volatile("s_cmp_lt_u32 %0, 8\n\ts_cbranch_scc1 1f\n\ts_setprio 3\n\ts_cmp_lg_u32 %0, 8\n\ts_cbranch_scc1 1f\n\t"
                 "v_lshl_add_u32 v161, %1, 7, %2\n\tv_min_i32 v161, %3, v161\n\ts_mov_b64 exec, 0xffff\n\tglobal_load_dword v161, v161, %4\n\ts_mov_b64 exec, -1\n1:"
                 :: "s"(wid), "v"(lane), "s"(idx * 2048), "s"(code_n), "s"(pc0) : "scc", "memory", "v161");
    TS_DECL
    const int tm = (xcd >> 1) * 4 + (idx >> 3), tn = (xcd & 1) * 8 + (idx & 7);
    const int wm = wid & 3, wn = (wid >> 2) & 1, l31 = lane & 31, lh = lane >> 5;
    const unsigned lb = (unsigned)(uintptr_t)shm;
    const unsigned l16 = lane * 16;
    const int l15 = lane & 15, g4 = lane >> 4, sq = 2 * wn + (wm & 1), st = sq * 16 + l15, run = (wm >> 1) * 4 + g4;
    unsigned kvv[4];
    const int trow = idx * 64 * 2048 + xcd * 256;
    if (wid < 8) {
        kvv[0] = trow + wm * 32768 + g4 * 2048 + ((l15 ^ (2 * g4)) << 4);
        kvv[1] = kvv[0] + 7168; kvv[2] = (kvv[0] ^ 128) + 14336; kvv[3] = kvv[2] + 7168;
    } else {
        kvv[0] = trow + (idx ? -4 * 2048 : 0) + g4 * 2048 + wm * 64 + l15 * 4;
        asm volatile("v_writelane_b32 %0, 0, 0\n\tv_writelane_b32 %0, 4, 1\n\tv_writelane_b32 %0, %1, 2" : "+v"(kvv[0]) : "s"((xcd * 64 + idx * 2 + (wm >> 1)) * 4));
        kvv[1] = (xcd * 128 + wm * 32 + l31) * 8;
        kvv[2] = kvv[3] = (tn * 64 + lane) * 2;
    }
    unsigned g1dst = lb + wid * 4096;
    asm volatile("s_waitcnt lgkmcnt(0)" : "+s"(ka0), "+s"(ka1), "+s"(ka2), "+s"(g1dst), "+v"(kvv[0]), "+v"(kvv[1]), "+v"(kvv[2]), "+v"(kvv[3]));
    Args a;
    a.q = (const char*)ka0[0]; a.k = (const char*)ka0[1]; a.v = (const char*)ka0[2]; a.sk = (const char*)ka0[3]; a.ssmk = (const char*)ka0[4]; a.ssmD = (const char*)ka0[5];
    a.x = (const char*)ka0[6]; a.lre = (const char*)ka0[7]; a.lim = (const char*)ka1[0]; a.bre = (const char*)ka1[1]; a.bim = (const char*)ka1[2]; a.cre = (const char*)ka1[3];
    a.cim = (const char*)ka1[4]; a.logdt = (const char*)ka1[5]; a.dskip = (const char*)ka1[6]; a.out = (char*)ka1[7]; a.ws = (char*)ka2;
    char* const ws = a.ws;
    const char* const flg = ws + WS_FLAG;
    TS(26);
    asm volatile("s_add_u32 m0, %[m], %[n0]\n\ts_cmp_gt_u32 %[w], 7\n\ts_cbranch_scc1 1f\n\t"
                 "global_load_lds_dwordx4 %[k0], %[kv]" NTK "\n\tglobal_load_lds_dwordx4 %[k1], %[kv] offset:1024" NTK "\n\t"
                 "global_load_lds_dwordx4 %[k2], %[kv] offset:2048" NTK "\n\tglobal_load_lds_dwordx4 %[k3], %[kv] offset:3072" NTK "\n\t"
                 "s_branch 2f\n1:\n\tglobal_load_ushort v164, %[k2], %[c1]\n\tglobal_load_ushort v165, %[k2], %[c2]\n\tglobal_load_ushort v166, %[k2], %[c3]\n\t"
                 "v_mov_b64 v[162:163], %[kv]\n\ts_mov_b64 exec, 7\n\tv_mov_b64 v[162:163], %[d]\n\ts_mov_b64 exec, 3\n\tv_mov_b64 v[162:163], %[e]\n\ts_mov_b64 exec, -1\n\t"
                 "v_mad_u64_u32 v[162:163], vcc, %[k0], 1, v[162:163]\n\tglobal_load_dword v160, v[162:163], off sc1\n\tglobal_load_dwordx2 v[162:163], %[k1], %[sk]\n2:"
                 :
                 : [w] "s"(wid), [c1] "s"(a.logdt), [c2] "s"(a.lre), [c3] "s"(a.lim), [d] "s"(a.ssmD), [e] "s"(flg + FL_EPOCH), [sk] "s"(a.sk),
                   [k0] "v"(kvv[0]), [k1] "v"(kvv[1]), [k2] "v"(kvv[2]), [k3] "v"(kvv[3]), [kv] "s"(wn ? a.v : a.k), [m] "s"(g1dst), [n0] "n"(L_KS)
                 : "scc", "vcc", "memory", "v160", "v161", "v162", "v163", "v164", "v165", "v166");
    TS0b();
    TS(27);
    if (P1) BAR();
#define SLICE_TABLE() unsigned slo, shi; { int ix = idx; asm volatile("" : "+s"(ix)); \
        const int rot2 = (((R2 >> 4) & 15) * (ix >> 3) + (R2 & 15) * (xcd >> 1)) & 15, es = (tn - rot2) & 15; \
        const unsigned long long id = 0xFEDCBA9876543210ull, seq = rot2 ? (id >> (4 * rot2)) | (id << (64 - 4 * rot2)) : id; \
        const unsigned long long lo = (1ull << (4 * es)) - 1, tab = ((seq & lo) << 4) | (seq & ~((lo << 4) | 15)) | tn; \
        slo = (unsigned)tab; shi = (unsigned)(tab >> 32); asm volatile("" : "+s"(slo), "+s"(shi)); }
#define SLICE(st) ((((st) < 8 ? slo : shi) >> (4 * ((st) & 7))) & 15)
    typedef __attribute__((ext_vector_type(4))) short v4i16;
    int role = wid;
    asm volatile("" : "+s"(role));

    if (role >= 8) {
        const int chn = xcd * 128 + idx * 4 + wm;
        const unsigned cb = lb + wm * CHSZ;
        const int r = l15;
        const unsigned fpub = xcd * 256 + idx * 8, fpoll = xcd * 256 + l31 * 8;
        u32x2 fl; u32x4 sw[4], uu[4];
        unsigned tch = 0;
        if (TOUCH) {
            const int ln = wm * 4 + (lane & 3);
            const unsigned fo = (unsigned)(WS_FLAG - WS_E) + (wm == 0 ? FL_H + (tm * 16 + tn) * 128 : (wm == 1 ? FL_KV : FL_Z) + xcd * 256 + (lane & 1) * 128);
            const unsigned o = (lane & 4) && (TOUCH & 2) ? fo : ((tm * 2 + (ln >> 3)) * 1024 + tn * 64) * 16 + (ln & 7) * 128;
            asm volatile("s_mov_b64 exec, 0xff\n\tglobal_load_dword %0, %1, %2\n\ts_mov_b64 exec, -1" : "=&v"(tch) : "v"(o), "s"(ws + WS_E) : "memory");
        }
        SLICE_TABLE();
        TS(1);
        unsigned sm, cl[3]; u32x2 kw0;
        asm volatile("s_waitcnt vmcnt(0)" : "={v160}"(sm), "={v[162:163]}"(kw0), "={v164}"(cl[0]), "={v165}"(cl[1]), "={v166}"(cl[2]) :: "memory", "v161");
        TS(22);
        const unsigned hal = take(sm);
        const unsigned tagA = __builtin_amdgcn_readlane(hal, 0) + 1, tagB = __builtin_amdgcn_readlane(hal, 1) + (tagA == 0 ? 1u : 0u);
        const u32x2 tag = {tagA, tagB};
        f32x2 L1, C1;
        {
            const float dt = rbf(expf(bf2f(take(cl[0]))));
            const float lr = bf2f(take(cl[1])), li = bf2f(take(cl[2]));
            const float e = expf(dt * lr);
            float sn, cs;
            sincosf(dt * li, &sn, &cs);
            L1 = f32x2{e * cs, e * sn};
            const float nr = L1[0] - 1.f, ni = L1[1], den = lr * lr + li * li;
            C1 = f32x2{2.f * ((nr * lr + ni * li) / den), 2.f * ((ni * lr - nr * li) / den)};
            TIE(L1); TIE(C1);
        }
        f32x2 p[11];
        p[0] = L1;
        if (PWE) {
#pragma unroll
            for (int i = 1; i <= 10; ++i) { p[i] = cmul4(p[i - 1], p[i - 1]); TIE(p[i]); }
        }
        {
            if (!PWE) {
#pragma unroll
                for (int i = 1; i <= 10; ++i) p[i] = cmul4(p[i - 1], p[i - 1]);
            }
            const f32x2 one = {1.f, 0.f}, zero = {0.f, 0.f};
            const unsigned ts = lb + L_TS + l16, tr = lb + L_TR + wm * 2048 + l16;
            if (wm == 0) {
                *LDSP(f32x4, ts) = f32x4{L1[0], L1[1], C1[0], C1[1]};
                *LDSP(f32x4, ts + 1024) = f32x4{p[3][0], p[3][1], p[4][0], p[4][1]};
                *LDSP(f32x4, ts + 2048) = f32x4{p[6][0], p[6][1], __uint_as_float(tagA), __uint_as_float(tagB)};
            }
            f32x2 Q[2], V[2], W[2];
            Q[0] = cmul4(cmul4(C1, (wm & 1) ? p[5] : one), (wm & 2) ? p[6] : one); Q[1] = cmul4(Q[0], p[4]);
            V[1] = (wm & 1) ? one : p[5]; V[0] = cmul4(V[1], p[4]);
            W[1] = cmul4((wm & 1) ? one : p[9], (wm & 2) ? one : p[10]); W[0] = cmul4(W[1], p[8]);
#pragma unroll
            for (int i = 0; i < 2; ++i) {
                const f32x2 Q8 = cmul4(Q[i], p[3]), U = 4 * wm + 2 * i >= 16 - tm ? cmul4(W[i], p[7]) : zero, Wz = 4 * wm + 2 * i + 1 >= 16 - tm ? W[i] : zero;
                *LDSP(f32x4, tr + i * 1024) = f32x4{Q[i][0], Q8[0], Q[i][1], Q8[1]};
                *LDSP(f32x4, tr + i * 1024 + 8192) = f32x4{Wz[0], Wz[1], U[0], U[1]};
                *LDSP(f32x2, tr + i * 1024 + 16384) = V[i];
            }
        }
        const unsigned dskl = (__builtin_amdgcn_readlane(hal, 2) >> ((wm & 1) * 16)) & 0xffffu;
        u32x4 kvo[4];
        u32x2 qv[8];
        const char* krow = a.ssmk + (size_t)chn * 4096;
        const char* qs = a.q + (size_t)(idx * 64 + wm * 16) * 2048 + xcd * 256;
        const char* kvd = ws + WS_KVT + (size_t)(xcd * 8 + wm * 2) * 65536 + idx * 2048;
        const int ph4 = (STG_SEL) * 4, ps = (PST >> ph4) & 15, pl = (PLT >> ph4) & 15, pf = wm ? 15 : ps + KFD, pw0 = (kv_wait(0) >> ph4) & 15, pw1 = (kv_wait(1) >> ph4) & 15;
        const unsigned qo = lh * 8192 + l31 * 8 + 4096;
        auto hb = [&](auto nc) {
            constexpr int nb = decltype(nc)::value;
            if constexpr (nib_any(kv_wait(0), nb)) wait_if<nb, 0>(pw0);
            if constexpr (nib_any(kv_wait(1), nb)) wait_if<nb, 12>(pw1);
            BAR();
            if constexpr (nib_any(PST, nb - KFD)) flag_if<nb>(pf, fpub, tag, flg + FL_KV);
            if constexpr (nib_any(PST, nb)) st4_if<nb>(ps, l16, kvo, kvd, kvd + 65536);
            if constexpr (nib_any(PLT, nb)) { ld4_if<nb>(pl, l16, sw, krow); ld8_if<nb>(pl, qo, qv, qs, qs + 16384); }
        };
#define HB(n) static_for<(n) ? (n) + KVD : 0, (n) + KVD + 1>(hb)
        {
            unsigned kw[2][8], ta[2], th[2];
            u32x2 hm[2];
#pragma unroll
            for (int j = 0; j < 2; ++j) {
                const unsigned x = take(kw0[j]);
                const auto y = __builtin_amdgcn_permlane16_swap(x, x, false, false);
#pragma unroll
                for (int p = 0; p < 2; ++p) { kw[p][4 * j] = y[p] & 0xffffu; kw[p][4 * j + 1] = y[p] << 16; kw[p][4 * j + 2] = y[p] >> 16; kw[p][4 * j + 3] = y[p] & 0xffff0000u; }
            }
#pragma unroll
            for (int p = 0; p < 2; ++p) {
                unsigned h[3];
#pragma unroll
                for (int j = 0; j < 3; ++j) h[j] = (__shfl(hal, p * 8 + (r >> 1) + 16 * (j + 1)) >> ((r & 1) * 16)) & 0xffffu;
                hm[p] = idx ? u32x2{h[0] << 16, h[1] | (h[2] << 16)} : u32x2{0u, 0u};
                const int q = r >> 2, pp = r & 3;
                ta[p] = lb + L_KS + (8 * g4 + q) * 256 + (((wm * 4 + p * 2 + (pp >> 1)) ^ (2 * (q | ((g4 & 1) << 2)))) << 4) + (pp & 1) * 8;
                th[p] = (ta[p] ^ 128) - 1024;
                TIE(hm[p]); TIE(ta[p]); TIE(th[p]);
#pragma unroll
                for (int j = 0; j < 8; ++j) TIE(kw[p][j]);
            }
            WAIT_L(0);
            TS(21);
            BAR();
            TS(2);
            u32x2 kt[4][3], vt[4][2];
#pragma unroll
            for (int u = 0; u < 4; ++u) {
                const int p = u >> 1, o = (u & 1) * 8192;
                auto tr = [&](unsigned ad) -> u32x2 { return __builtin_bit_cast(u32x2, __builtin_amdgcn_ds_read_tr16_b64_v4i16(LDSP(v4i16, ad))); };
                kt[u][0] = tr(th[p] + o); kt[u][1] = tr(ta[p] + o); vt[u][0] = tr(ta[p] + o + 16384); kt[u][2] = tr(ta[p] + o + 1024); vt[u][1] = tr(ta[p] + o + 17408);
                SB();
            }
            TS(14);
#pragma unroll
            for (int i = 0; i < 4; ++i) asm volatile("" : "=v"(sw[i]), "=v"(qv[2 * i]), "=v"(qv[2 * i + 1]));
            if (KVM & 1) HB(0);
            static_for<0, 4>([&](auto uc) {
                constexpr int u = decltype(uc)::value, p = u >> 1, oh = u & 1;
                const u32x2 k0 = oh == 0 && g4 == 0 ? hm[p] : kt[u][0];
                unsigned o[4];
                if (KPL && __builtin_ctz(KPL | 256) == 2 * u) __builtin_amdgcn_s_setprio(KPRIO);
                if ((KPL >> (2 * u)) & 1) kv4p(o[3], o[2], k0[0], k0[1], kt[u][1][0], kt[u][1][1], kw[p], vt[u][0][0], vt[u][0][1]);
                else kv4(o[3], o[2], k0[0], k0[1], kt[u][1][0], kt[u][1][1], kw[p], vt[u][0][0], vt[u][0][1]);
                if constexpr ((KVM >> (2 * u + 1)) & 1) { TIE(o[3]); HB(__builtin_popcount(KVM & ((2 << (2 * u)) - 1))); TIE(vt[u][1]); }
                if (KPL && __builtin_ctz(KPL | 256) == 2 * u + 1) __builtin_amdgcn_s_setprio(KPRIO);
                if ((KPL >> (2 * u + 1)) & 1) kv4p(o[1], o[0], kt[u][1][0], kt[u][1][1], kt[u][2][0], kt[u][2][1], kw[p], vt[u][1][0], vt[u][1][1]);
                else kv4(o[1], o[0], kt[u][1][0], kt[u][1][1], kt[u][2][0], kt[u][2][1], kw[p], vt[u][1][0], vt[u][1][1]);
                kvo[u] = u32x4{o[0], o[1], o[2], o[3]};
                TIE(kvo[u]);
                SB();
                TS(15 + u);
                if constexpr (u < 3 && ((KVM >> (2 * u + 2)) & 1)) { HB(__builtin_popcount(KVM & ((4 << (2 * u)) - 1))); TIE(vt[u + 1][0]); }
            });
#pragma unroll
            for (int u = 0; u < 4; ++u) TIE(kvo[u]);
            __builtin_amdgcn_s_setprio(3);
        }
        TS(3);
        const char* usrc = ws + WS_KVT + (size_t)(xcd * 8 + (idx >> 2)) * 65536 + wm * 16384 + (idx & 3) * 64;
        const unsigned uo = (lane >> 2) * 256 + (lane & 3) * 16;
        const char* cs = ((wm & 2) ? a.cim : a.cre) + (size_t)(tn * 64 + (wm & 1) * 2) * 2048;
        unsigned co[4];
#pragma unroll
        for (int i = 0; i < 4; ++i) {
            const int pair = i * 4 + g4, v = r ^ pair, row = pair * 2 + (v >> 3);
            co[i] = ((row & 15) * 4 + (row >> 4)) * 2048 + (v & 7) * 16 - i * 1024;
        }
        bool kv_ok = false;
#pragma unroll
        for (int it = 0; it < 4; ++it) asm volatile("" : "=v"(uu[it]));
        auto cst = [&](int place, int slice) { const char* s = cs + (slice << 7); DMA(lb + place + wm * 4096, co[0], s); dmao_<1024>(co[1], s); dmao_<2048>(co[2], s); dmao_<3072>(co[3], s); };
        auto items = [&](auto pc) {
            constexpr int p = decltype(pc)::value;
            if constexpr (PU == p) {
                WAIT_V(PK == p ? 4 : 0); TIE(fl);
                kv_ok = __all(fl[0] == tagA && fl[1] == tagB);
                const char* us = kv_ok ? usrc : a.x;
                for (int it = 0; it < 4; ++it) GLD_B128_SC1(uu[it], uo, us + it * 4096, 0);
            }
            if constexpr (PC0 == p) cst(g2_place(0), SLICE(0));
            if constexpr (PC1 == p) cst(g2_place(1), SLICE(1));
            if constexpr (PU == p + 1) GLD_B64_SC1(fl, fpoll, flg + FL_KV, 0);
            if constexpr (PC2 == p) cst(g2_place(5), SLICE(5));
            if constexpr (PC3 == p) cst(g2_place(6), SLICE(6));
        };
#define ITEMS(p) items(std::integral_constant<int, p>{})
        static_assert(PK == 0 && PQ == 0 && (nib(PST, 0) >= (__builtin_popcount(KVM & 255) + KVD) && nib(PST, 1) >= (__builtin_popcount(KVM & 255) + KVD) && nib(PST, 2) >= (__builtin_popcount(KVM & 255) + KVD) && nib(PST, 3) >= (__builtin_popcount(KVM & 255) + KVD)), "schedule");
        static_assert(nib(PST, 0) + KFD <= 11 && nib(PST, 1) + KFD <= 11 && nib(PST, 2) + KFD <= 11 && nib(PST, 3) + KFD <= 11 && !nib_any(PLT, 12) && !nib_any(PLT, 13), "schedule");
        static_for<(__builtin_popcount(KVM & 255) + KVD), 12>(hb);
        asm volatile("" :: "v"(tch));
        ITEMS(11);
        BAR();
        ITEMS(12);
        BAR();
        ITEMS(13);
        BAR();
        const unsigned ud = lb + (lane & 3) * CHSZ + UOFF + ((lane >> 2) & 1) * UPL + (16 + wm * 32 + (lane >> 3)) * 16;
        auto zero = [&]() {
            const u32x4 z4 = {0, 0, 0, 0};
            if (lane < 24) *LDSP(u32x4, lb + L_SY + l16) = z4;
            if (lane < 56) *LDSP(u32x4, lb + L_AGG + wm * 896 + l16) = z4;
            *LDSP(u32x4, lane < 32 ? cb + ((lane >> 3) & 1) * CST + (lane & 7) * 16 : cb + UOFF + (g4 & 1) * UPL + r * 16) = z4;
        };
        auto fill = [&]() {
#pragma unroll
            for (int it = 0; it < 4; ++it) TIE(sw[it]);
#pragma unroll
            for (int it = 0; it < 4; ++it) {
                const unsigned q = cb + 128 + it * 1024 + l16;
                const unsigned d0 = sw[it][0], d1 = sw[it][1], d2 = sw[it][2], d3 = sw[it][3];
                const unsigned o1 = it ? __builtin_amdgcn_readlane(sw[it ? it - 1 : 0][3], 63) : 0u;
                const unsigned x1 = __builtin_amdgcn_update_dpp(o1, d3, 0x138, 0xf, 0xf, false);
                *LDSP(u32x4, q) = sw[it];
                *LDSP(u32x4, q + CST) = u32x4{__builtin_amdgcn_alignbyte(d0, x1, 2), __builtin_amdgcn_alignbyte(d1, d0, 2), __builtin_amdgcn_alignbyte(d2, d1, 2), __builtin_amdgcn_alignbyte(d3, d2, 2)};
            }
        };
        auto uput = [&]() {
            WAIT_V(it_cnt(PU, 1, UE ? 15 : 16));
#pragma unroll
            for (int it = 0; it < 4; ++it) TIE(uu[it]);
            if (!kv_ok) {
                TSE(23);
                for (unsigned spin = 0; spin < SPIN_CAP; ++spin) {
                    GLD_B64_SC1(fl, fpoll, flg + FL_KV, 0);
                    WAIT_V(0); TIE(fl);
                    if (__all(fl[0] == tagA && fl[1] == tagB)) break;
                    __builtin_amdgcn_s_sleep(8);
                }
#pragma unroll
                for (int it = 0; it < 4; ++it) GLD_B128_SC1(uu[it], uo, usrc + it * 4096, 0);
                WAIT_V(0);
#pragma unroll
                for (int it = 0; it < 4; ++it) TIE(uu[it]);
            }
#pragma unroll
            for (int it = 0; it < 4; ++it) *LDSP(u32x4, ud + it * 128) = uu[it];
        };
        static_assert(PU <= 14, "ssm_k has landed");
        ITEMS(14);
        if (UE) __builtin_amdgcn_s_setprio(UPRIO);
        if (UE >= 2) zero();
        if (UE == 2) fill();
        BAR();
        ITEMS(15);
        TS(4);
        if (UE < 2) zero();
        if (UE & 1) fill();
        if (UE) uput();
        WAIT_L(0);
        TS(5);
        BAR();
        __builtin_amdgcn_s_setprio(FPRIO);
        ITEMS(16);
        TS(11);
        const unsigned pmine = lb + L_SY + 32 + wm * 4, pall = lb + L_SY + 32 + (lane & 3) * 4;
        if (!UE) { fill(); TS(13); uput(); sy_post<0>(pmine); sy_wait<0>(pall); }
        if (!CPOST) { WAIT_V(0); sy_post<1>(pmine); }
        __builtin_amdgcn_s_setprio(CVPRIO);
        TS(6);
        f32x4 ca[8];
        {
            unsigned kb[4];
#pragma unroll
            for (int i = 0; i < 4; ++i) { kb[i] = cb + (~r & 1) * CST + 2 * (64 + 16 + r - 8 * g4 - 7 + (~r & 1)) + i * 1024; TIE(kb[i]); }
            unsigned ub = cb + UOFF + (g4 & 1) * UPL + (15 + r + (g4 >> 1)) * 16 - 64;
#pragma unroll
            for (int i = 0; i < 8; ++i) { ca[i] = f32x4{0, 0, 0, 0}; TIE(ca[i]); }
            s16x4 FA[4][2]; bf16x8 FB[2][8];
#define CV_A(s, o, e) dsr2_<16 * ((s) + ((o) >> 3)) + 128 * (cv_m((o) & 7) & 1) + 2 * (e)>(FA[(o) & 3][e], kb[cv_m((o) & 7) >> 1])
#define CV_B(s, jb) DSR128(FB[(s) & 1][jb], ub, 64 + 256 * (jb) - 32 * (s))
            static_for<0, 8>([&](auto ic) { constexpr int n = decltype(ic)::value; CV_B(0, n); CV_A(0, n >> 1, n & 1); });
            WAIT_L(0);
            auto body = [&]() {
                static_for<0, 72>([&](auto ic) {
                    constexpr int s = decltype(ic)::value / 36, p = decltype(ic)::value % 36, o = cv_grp(p), m = cv_m(o), jb = p - cv_first(o);
                    if constexpr (jb == 0) { WAIT_LB(cv_wait(o)); if constexpr (!(AMF & 1)) { TIE(FA[o & 3][0]); TIE(FA[o & 3][1]); } }
                    if constexpr (p == 0 && !(AMF & 1)) { for (int i = 0; i < 8; ++i) TIE(FB[s][i]); }
                    const bf16x8 fa = __builtin_shufflevector(FA[o & 3][0], FA[o & 3][1], 0, 1, 2, 3, 4, 5, 6, 7);
                    if constexpr (AMF & 1) mfma_(ca[jb + m], fa, FB[s][jb]); else
                    ca[jb + m] = __builtin_amdgcn_mfma_f32_16x16x32_bf16(fa, FB[s][jb], ca[jb + m], 0, 0, 0);
                    SB();
                    constexpr int rd = cv_read(p);
                    if constexpr (rd >= 32) CV_B(s + 1, rd - 32);
                    else if constexpr (rd >= 0) CV_A(s, rd >> 1, rd & 1);
                    SB();
                });
                for (int i = 0; i < 4; ++i) { kb[i] += 128; TIE(kb[i]); }
                ub -= 64;
            };
#pragma unroll 1
            for (int it = 0; it < QIT; ++it) body();
            {
                TS(19);
                if (CPOST) { WAIT_V(0); sy_post<1>(pmine); WAIT_L(0); }
                if (CVLATE) sy_wait<1>(lb + L_SY + (CVLATE == 2 ? wm + (lane & 1) * 4 : lane & 7) * 4);
                TS(20);
            }
#pragma unroll 1
            for (int it = QIT; it < 4; ++it) body();
            WAIT_L(0);
            if (AMF & 1) asm volatile("s_nop 7\n\ts_nop 7" : "+v"(ca[0]), "+v"(ca[1]), "+v"(ca[2]), "+v"(ca[3]), "+v"(ca[4]), "+v"(ca[5]), "+v"(ca[6]), "+v"(ca[7]));
#pragma unroll
            for (int i = 0; i < 4; ++i) { TIE(FA[i][0]); TIE(FA[i][1]); }
#pragma unroll
            for (int i = 0; i < 8; ++i) TIE(FB[0][i]);
#undef CV_A
#undef CV_B
        }
        TS(7);
        __builtin_amdgcn_s_setprio(3);
        constexpr int NBAR = __builtin_popcount(BARM & ~VBM);
        unsigned zpub = (xcd * 32 + l31) * 256 + idx * 8, zpoll = ZOWN ? (xcd * 32 + idx) * 256 + l31 * 8 : fpoll;
        {
            const float Dd = bf2f(dskl);
            const char* zd = ws + WS_ZT + (size_t)(xcd * 128 + wm * 32) * 4096 + idx * 128;
            u32x2 kvr[8];
#pragma unroll
            for (int jt = 0; jt < 8; ++jt) kvr[jt] = *LDSP(u32x2, cb + UOFF + (g4 >> 1) * UPL + (16 + 16 * jt + r) * 16 + (1 - (g4 & 1)) * 8);
#pragma unroll
            for (int jt = 0; jt < 8; ++jt) {
                const f32x4 y = ca[jt];
                const u32x2 kq = kvr[jt];
                *LDSP(u32x2, cb + (jt * 4 + (r >> 2)) * 144 + (r & 3) * 32 + g4 * 8) =
                    u32x2{pack2(rb1(y[0]) + rb1(bfhi(kq[1]) * Dd), rb1(y[1]) + rb1(bflo(kq[1]) * Dd)), pack2(rb1(y[2]) + rb1(bfhi(kq[0]) * Dd), rb1(y[3]) + rb1(bflo(kq[0]) * Dd))};
            }
            unsigned za = lb + (r >> 2) * CHSZ + (wm * 8 + (g4 >> 1)) * 144 + ((g4 & 1) * 2 + ((r >> 1) & 1)) * 32 + (r & 1) * 8, zo = (lane >> 3) * 4096 + (lane & 7) * 16;
            TIE(za); TIE(zo); TIE(zpub); TIE(zpoll);
            sy_post<3>(pmine); sy_wait<3, 0>(pall);
            v4i16 t[4][2];
#pragma unroll
            for (int it = 0; it < 4; ++it)
#pragma unroll
                for (int e = 0; e < 2; ++e) t[it][e] = __builtin_amdgcn_ds_read_tr16_b64_v4i16(LDSP(v4i16, za + it * 288 + e * 16));
#pragma unroll
            for (int it = 0; it < 4; ++it) { TIE(t[it][0]); TIE(t[it][1]); }
            if (ZSB) BAR();
#pragma unroll
            for (int it = 0; it < 4; ++it) { const bf16x8 f = __builtin_shufflevector(t[it][0], t[it][1], 0, 1, 2, 3, 4, 5, 6, 7); GST_B128_SC1(zo, f, zd + it * 32768, 0); }
        }
#pragma unroll
        for (int i = ZSB; i < ZFB; ++i) BAR();
        WAIT_V(0);
        sy_post<2>(pmine);
        if (wm == 0) {
            sy_wait<2>(pall);
            if (ZOWN) { if (lane < 32) GST_B64_SC1(zpub, tag, flg + FL_Z); }
            else if (lane == 0) GST_B64_SC1(fpub, tag, flg + FL_Z);
        }
        TS(8);
        unsigned qm[8][4];
        if (QM) {
#pragma unroll
            for (int n = 0; n < 8; ++n) {
                TIE(qv[n]);
                qm[n][0] = qv[n][0] & 0xffffu; qm[n][1] = qv[n][0] & 0xffff0000u; qm[n][2] = qv[n][1] & 0xffffu; qm[n][3] = qv[n][1] & 0xffff0000u;
                TIE(qm[n][0]); TIE(qm[n][1]); TIE(qm[n][2]); TIE(qm[n][3]);
            }
        }
#pragma unroll
        for (int i = ZFB; i < NBAR - 1; ++i) { BAR();
        }
        if (ZSL) __builtin_amdgcn_s_sleep(ZSL);
        asm volatile("s_cmp_lg_u32 %3, 0\n\ts_cbranch_scc1 1f\n\tglobal_load_dwordx2 %0, %1, %2 sc1\n1:" : "=&v"(fl) : "v"(zpoll), "s"(flg + FL_Z), "s"(wm) : "scc", "memory");
        if (NBAR > ZFB) BAR();
        if (wm == 0) {
            unsigned left = SPIN_CAP;
            asm volatile("s_branch 2f\n1:\n\ts_sleep %5\n\tglobal_load_dwordx2 %0, %2, %3 sc1\n2:\n\ts_waitcnt vmcnt(0)\n\tv_cmp_ne_u64 vcc, %4, %0\n\ts_cbranch_vccz 3f\n\ts_sub_u32 %1, %1, 1\n\ts_cbranch_scc0 1b\n3:"
                         : "+v"(fl), "+s"(left) : "v"(zpoll), "s"(flg + FL_Z), "s"(tag), "n"(ZPS) : "vcc", "scc", "memory");
            sy_post<4>(pmine);
        } else sy_wait<4, 2>(lb + L_SY + 32);
        u32x4 zv[4];
        {
            ITEMS(18);
            const char* zsrc = ws + WS_ZT + (size_t)(xcd * 128 + idx * 4 + wm) * 4096;
            static_for<0, 4>([&](auto ic) { constexpr int it = decltype(ic)::value; GLD_B128_SC1(zv[it], (l31 >> 2) * 512 + lh * 64 + (lane & 3) * 16, zsrc, it * 128); });
        }
        WAIT_V(0);
        TS(9);
#pragma unroll
        for (int it = 0; it < 4; ++it) TIE(zv[it]);
        if (!(XAB & 1)) {
        quad_swap<0>(zv[0], zv[1]); quad_swap<0>(zv[2], zv[3]);
        quad_swap<1>(zv[0], zv[2]); quad_swap<1>(zv[1], zv[3]); }
#pragma unroll
        for (int it = 0; it < 4; ++it) TIE(zv[it]);
        char* const oa = a.out + (size_t)(idx * 64 + wm * 16) * 8192 + xcd * 512;
#pragma unroll
        for (int n = 0; n < 8; ++n) {
            if (!QM) TIE(qv[n]);
            const unsigned z0 = zv[n & 3][2 * (n >> 2)], z1 = zv[n & 3][2 * (n >> 2) + 1];
            const f32x4 o = (XAB & 2) ? __builtin_bit_cast(f32x4, zv[n & 3]) : QM ? mul4bf(u32x2{z0, z1}, qm[n]) :
                f32x4{rb1(bflo(qv[n][0]) * bflo(z0)), rb1(bfhi(qv[n][0]) * bfhi(z0)), rb1(bflo(qv[n][1]) * bflo(z1)), rb1(bfhi(qv[n][1]) * bfhi(z1))};
            if (n == 0 && OSG) for (int i = 0; i < (idx & 3); ++i) __builtin_amdgcn_s_sleep(OSG);
            if (!(XAB & 4))
            GST_B128_NT(lh * 32768 + l31 * 16, o, oa + ((n >> 2) * 8 + (n & 3)) * 8192, 0);
            if (OSL) __builtin_amdgcn_s_sleep(OSL);
        }
        asm volatile(".Lfk_code_a:");
        TSE(10);
        TS_FLUSH();
        return;
    }

    u32x2 fl;
    unsigned dv[4];
    {
        const int v = l15 ^ g4, hi = v >> 3;
        const int r0 = wn ? tn * 64 + (wm >> 1) * 32 : tm * 128 + (wm >> 1) * 64 + (wm & 1) * 4;
        const unsigned d0 = (r0 + ((g4 * (wn ? 2 : 9)) & (wn ? 6 : 17))) * 2048 + (v & 7) * 16, odd = wn ? 11 : 14;
        dv[0] = d0 + (hi << odd); dv[2] = d0 + ((hi ^ 1) << odd) + (wn ? 32768 - 2048 : 4096 - 2048);
        dv[1] = (dv[0] ^ 64) + (wn ? 16384 - 1024 : 65536 - 1024); dv[3] = (dv[2] ^ 64) + (wn ? 16384 - 1024 : 65536 - 1024);
    }
    const int rot7 = ((((ROT >> 12) & 15) * (idx & 7) + ((ROT >> 8) & 15) * (idx >> 3) + ((ROT >> 4) & 15) * (xcd >> 1) + (ROT & 15) * (xcd & 1)) & 15) << 7;
    const char* g1src = wn ? ((wm & 1) ? a.bim : a.bre) : a.x;
#define G1_ISSUE(st, i) do { if constexpr (M1 && (i) > 0) dmao_<(i) * 1024>(dv[i], g1src + ((((st) << 7) + rot7) & 1920)); \
        else DMA(g1dst + g1_slot(st) + (i) * 1024, dv[i], g1src + ((((st) << 7) + rot7) & 1920)); } while (0)
    static_for<0, PRE>([&](auto ic) { constexpr int n = decltype(ic)::value; G1_ISSUE(n >> 2, n & 3); });
    TS(14);
    SLICE_TABLE();
    const int j0 = 4 * run - (32 - 2 * tm);
    const int pneg = EALL ? 0 : 16 * (wm >> 1) + 15 - (32 - 2 * tm);
    unsigned eo[2], pes = lb + L_ES + wid * 256 + l15 * 16;
    unsigned plr = lb + L_AGG + st * 8 + run * 512, est = ((tm * 2 + (wm >> 1)) * 1024 + tn * 64 + st) * 16 + (EREP ? g4 * 524288 : 0);
#pragma unroll
    for (int i = 0; i < 2; ++i) { eo[i] = (EALL ? (j0 + 2 * i) & 31 : j0 + 2 * i < 0 ? 0 : j0 + 2 * i) * 16384 + (EREP ? (tm >> 2) * 524288 : 0) + (tn * 64 + st) * 16; TIE(eo[i]); }
    TIE(plr); TIE(pes); TIE(est);
    unsigned hw[2], ta[2];
    {
        const unsigned ht = lb + L_HT + wid * 4096;
#pragma unroll
        for (int j = 0; j < 2; ++j) {
            const int pr = (8 * (g4 & 1) + 4 * j + (l15 >> 2)) ^ ((g4 & 1) * 4), pw = l15 ^ ((l15 >> 3) * 4);
            ta[j] = ht + (g4 >> 1) * 512 + pr * 32 + ((((l15 >> 1) ^ (pr >> 2)) & 1) << 4) + 8 * (lane & 1);
            hw[j] = ht + g4 * 1024 + pw * 32 + (((j ^ (pw >> 2)) & 1) << 4);
            TIE(ta[j]); TIE(hw[j]);
        }
    }
    unsigned po = pes + ((wm & 2) ? -512 : 512);
    unsigned cta = lb + L_TS + st * 16, ctb = lb + L_TR + run * 1024 + st * 16;
    TIE(po); TIE(cta); TIE(ctb);
    f32x4 ct[5]; f32x2 V1;
    const int gm = PAIR ? wid >> 1 : wm, gk = PAIR ? wid & 1 : wn, gs = gk * 4 + (gm & 2);
    unsigned pmine = lb + L_SY + wid * 4, pall = lb + L_SY + (lane & 7) * 4, pown = lb + L_SY + (lane < 4 ? 32 + lane * 4 : gs * 4 + (lane & 4)), pzero = lb + L_SY;
    unsigned g2v[2], cad[2][2], to[2][2];
    unsigned xo = (tm * 128 + gm * 32 + gk * 16 + g4 * 4) * 2048 + tn * 128 + l15 * 8 + 4096, dso = xo & 2047;
    unsigned fst = (tm * 16 + l15) * 128 + tn * 8, fld = (tm * 16 + tn) * 128 + (NOOWN && l15 == tn ? tn ^ 1 : l15) * 8;
#pragma unroll
    for (int i = 0; i < 2; ++i) {
        const int pair = i * 4 + g4, v = l15 ^ pair ^ ((wm & 1) * 8);
        g2v[i] = (pair * 2 + (v >> 3)) * 8192 + (v & 7) * 16 + (tn * 64 + wm) * 2048 - i * 1024;
        TIE(g2v[i]);
#pragma unroll
        for (int j = 0; j < 2; ++j) {
            cad[i][j] = lb + L_C + (img_off((g4 >> 1) * 64 + l15, 4 * gk + (g4 & 1)) ^ (i * 32) ^ (j * 128));
            to[i][j] = ta[j] + (i ^ gk) * 1024 + (gm & 1) * 2048 + (gs - wid) * 4096;
            TIE(cad[i][j]); TIE(to[i][j]);
        }
    }
    unsigned cwa = lb + wid * 2048 + l16;
    TIE(cwa);
    TIE(pmine); TIE(pall); TIE(pown); TIE(pzero); TIE(xo); TIE(dso); TIE(fst); TIE(fld);
    TS(1);
    {
        static_for<PRE, PRE2>([&](auto ic) { constexpr int n = decltype(ic)::value; G1_ISSUE(n >> 2, n & 3); });
        WAIT_V(PRE2);
        BAR();
        static_assert(PRO <= 12 && NB1 == 4, "the constants sit in slot 3");
        DSR128(ct[0], cta, 0); DSR128(ct[1], cta, 1024); DSR128(ct[2], cta, 2048); DSR128(ct[3], ctb, 0); DSR128(ct[4], ctb, 8192); DSR64(V1, ctb, 16384);
        static_for<PRE2, PRO>([&](auto ic) { constexpr int n = decltype(ic)::value; G1_ISSUE(n >> 2, n & 3); });
        TS(15);
    }
    WAIT_L(0);
#pragma unroll
    for (int i = 0; i < 5; ++i) TIE(ct[i]);
    TIE(V1);
    float nL1i = -ct[0][1], nC1r = -ct[0][2], nC1i = -ct[0][3];
    TIE(nL1i); TIE(nC1r); TIE(nC1i);
    unsigned tagA = __builtin_amdgcn_readfirstlane(__float_as_uint(ct[2][2])), tagB = __builtin_amdgcn_readfirstlane(__float_as_uint(ct[2][3]));
    asm volatile("" : "+s"(tagA), "+s"(tagB));

    f32x4 acc[4][2];
    {
#pragma unroll
        for (int i = 0; i < 4; ++i) { acc[i][0] = f32x4{0, 0, 0, 0}; acc[i][1] = f32x4{0, 0, 0, 0}; }
        const int aoff = img_off((wm >> 1) * 64 + l15, g4), boff = img_off(128 + wn * 64 + (wm & 1) * 16 + l15, g4);
        unsigned aad[2][2], bad[2];
#pragma unroll
        for (int ks = 0; ks < 2; ++ks) { aad[ks][0] = lb + (aoff ^ (ks * 64)); aad[ks][1] = lb + (aoff ^ (ks * 64) ^ 128); bad[ks] = lb + (boff ^ (ks * 64)); }
        bf16x8 fa[2][4], fb[2][2];
        if (GPRIO) __builtin_amdgcn_s_setprio(GPRIO);
        WAIT_V(PRO - 4); BAR();
        TS(2);
#define G1_FRAG(g, n) do { constexpr int bo_ = g1_slot((g) >> 1), hi_ = bo_ & ~65535, x_ = (n) < 3 ? (n) >> 1 : (n) - 2; \
        if constexpr ((n) == 0 || (n) == 2) DSR128(fb[(g) & 1][x_], bad[(g) & 1] + hi_, bo_ - hi_ + x_ * 4096); \
        else DSR128(fa[(g) & 1][x_], aad[(g) & 1][x_ & 1] + hi_, bo_ - hi_ + x_ * 2048); } while (0)
        static_for<0, 6>([&](auto ic) { constexpr int n = decltype(ic)::value; G1_FRAG(0, n); });
        static_for<0, 32>([&](auto ic) {
            constexpr int g = decltype(ic)::value, gn = g + 1, n0 = g1_upto(g - 1), nd = g1_upto(g) - n0;
            constexpr bool bar = (gn & 1) == 0 && gn < 32;
            if constexpr (bar) {
                static_assert(g1_cnt(gn >> 1) >= 0, "schedule");
                WAIT_VB(g1_cnt(gn >> 1)); WAIT_LB(0); BAR();
                if constexpr (((gn >> 1) & 3) == 0 || gn >= 26) TS(gn >= 26 ? 23 + (gn >> 1) - 13 : 16 + (gn >> 3));
#pragma unroll
                for (int i = 0; i < 4; ++i) if (!(AMF & 2)) TIE(fa[g & 1][i]);
                if (!(AMF & 2)) { TIE(fb[g & 1][0]); TIE(fb[g & 1][1]); }
            }
            static_for<0, 8>([&](auto mc) {
                constexpr int m = decltype(mc)::value, i = m >> 1, j = m & 1;
                if constexpr (W1 && !bar && g > 0 && g < 31) {
                    if constexpr (m == 0) WAIT_LB(W1 == 1 ? 3 : 2);
                    if constexpr (m == 2 && W1 == 1) WAIT_LB(4);
                    if constexpr (m == 4) WAIT_LB(4);
                } else
                if constexpr (!bar && (i == 0 || j == 0)) {
                    WAIT_LB(5 - (i == 0 ? j + 1 : i + 2) + (gn < 32 ? (m < 6 ? m : 6) : 0));
                    if constexpr (!(AMF & 2) || g == 0) { TIE(fa[g & 1][i]); TIE(fb[g & 1][j]); }
                }
                if constexpr ((AMF & 2) && g > 0) mfma_(acc[i][j], fa[g & 1][i], fb[g & 1][j]); else
                acc[i][j] = __builtin_amdgcn_mfma_f32_16x16x32_bf16(fa[g & 1][i], fb[g & 1][j], acc[i][j], 0, 0, 0);
                SB();
                if constexpr (gn < 32 && m < 6) G1_FRAG(gn, m);
                constexpr int d = nd > 2 ? ((m & 1) ? m >> 1 : -1) : (m == DP0 ? 0 : m == DP1 ? 1 : -1);
                if constexpr (d >= 0 && d < nd) G1_ISSUE((n0 + d) >> 2, (n0 + d) & 3);
                SB();
            });
        });
#undef G1_FRAG
    }
    TS(3);
    const unsigned tagG = tagA ^ tagB ^ 0x9E3779B9u;
    __builtin_amdgcn_s_setprio(MPRIO);
    f32x2 ur[8], ui[8], hr, hi;
    const f32x2 L1 = {ct[0][0], ct[0][1]}, C1 = {ct[0][2], ct[0][3]}, L16 = {ct[1][2], ct[1][3]}, Q2r = {ct[3][0], ct[3][1]}, Q2i = {ct[3][2], ct[3][3]};
    const float L8r = ct[1][0], L8i = ct[1][1], L64r = ct[2][0], L64i = ct[2][1];
    u32x4 eg[4], cr[2 * NR + 1];
    const char* g2src = wn ? a.cim : a.cre;
#define CR_LOAD(n0, n1) static_for<n0, n1>([&](auto ic) { constexpr int n = decltype(ic)::value; GLD_B128(cr[n], g2v[n & 1], g2src + (SLICE(7 + (n >> 1)) << 7), (n & 1) * 1024); })
#define E_LOADS "global_load_dwordx4 v[104:107], %[a0], %[b0] sc1\n\tglobal_load_dwordx4 v[108:111], %[a0], %[b1] sc1\n\t" \
                "global_load_dwordx4 v[112:115], %[a1], %[b0] sc1\n\tglobal_load_dwordx4 v[116:119], %[a1], %[b1] sc1\n"
#define E_POLL() asm volatile("s_cmp_lt_i32 %[on], 0\n\ts_cbranch_scc1 1f\n\t" E_LOADS "1:" : "={v[104:107]}"(eg[0]), "={v[108:111]}"(eg[1]), "={v[112:115]}"(eg[2]), "={v[116:119]}"(eg[3]) \
        : [a0] "v"(eo[0]), [a1] "v"(eo[1]), [b0] "s"(ws + WS_E), [b1] "s"(ws + WS_E + 16384), [on] "s"(pneg) : "scc", "memory")
    {
        asm volatile("s_nop 7\n\ts_nop 3" : "+v"(acc[0][0]), "+v"(acc[0][1]), "+v"(acc[1][0]), "+v"(acc[1][1]), "+v"(acc[2][0]), "+v"(acc[2][1]), "+v"(acc[3][0]), "+v"(acc[3][1]));
#pragma unroll
        for (int p = 0; p < 8; ++p) { ur[p] = f32x2{acc[p >> 1][0][2 * (p & 1)], acc[p >> 1][0][2 * (p & 1) + 1]}; ui[p] = f32x2{acc[p >> 1][1][2 * (p & 1)], acc[p >> 1][1][2 * (p & 1) + 1]}; }
        f32x2 gr = ur[0], gi = ui[0];
#pragma unroll
        for (int p = 1; p < 8; ++p) { f32x2 nr, ni; if (PLN & 1) c2_mad(nr, ni, L1[0], nL1i, L1[0], L1[1], gr, gi, ur[p], ui[p]); else pk_cmad(nr, ni, L1, gr, gi, ur[p], ui[p]); gr = nr; gi = ni; }
        float fr, fi;
        cmad(fr, fi, L8r, L8i, gr[0], gi[0], gr[1], gi[1]);
        {
            const f32x2 w = cmul4(V1, f32x2{fr, fi});
            const float wr = sum4(w[0]), wi = sum4(w[1]);
            const u32x4 gq = {__float_as_uint(wr), tagA, __float_as_uint(wi), tagG};
            if (EREP ? (EALL || g4 >= (tm >> 2)) : g4 == 0) GST_B128_SC1(est, gq, ws + WS_E, 0);
        }
        *LDSP(f32x2, plr + 7 * 512) = f32x2{fr, fi};
        *LDSP(unsigned, pes + 8) = 0;
        TS(21);
        if (EP == 2) E_POLL();
        auto coef = [&]() {
#pragma unroll
            for (int p = 0; p < 8; ++p) { f32x2 nr, ni; if (PLN & 2) c2_mul(nr, ni, C1[0], nC1i, nC1r, nC1i, ur[p], ui[p]); else pk_conj_mul(nr, ni, C1, ur[p], ui[p]); ur[p] = nr; ui[p] = ni; TIE(ur[p]); TIE(ui[p]); }
        };
        if (CFB) coef();
        WAIT_L(0); BAR();
        f32x2 e[7];
#pragma unroll
        for (int j = 0; j < 7; ++j) e[j] = *LDSP(f32x2, plr + j * 512);
        if (EP == 1) E_POLL();
        CR_LOAD(0, CR1);
        if (!CFB) coef();
#pragma unroll
        for (int j = 1; j < 7; ++j) { if (PLN & 4) { float xr = e[j][0], xi = e[j][1]; cmac(xr, xi, L16[0], L16[1], e[j - 1][0], e[j - 1][1]); e[j] = f32x2{xr, xi}; } else cmac2(e[j], L16, e[j - 1]); }
        float mr, mi;
        cmad(mr, mi, L8r, L8i, e[6][0], e[6][1], gr[0], gi[0]);
        if (PLN & 4) c2_mul(hr, hi, C1[0], nC1i, nC1r, nC1i, f32x2{e[6][0], mr}, f32x2{e[6][1], mi}); else pk_conj_mul(hr, hi, C1, f32x2{e[6][0], mr}, f32x2{e[6][1], mi});
        TIE(hr); TIE(hi);
        CR_LOAD(CR1, CR1 + CR2);
        TS(4);
    }
    f32x2 G;
    {
        float pr = 0.f, pi = 0.f;
        if (!EP) E_POLL();
        if (pneg >= 0) {
            unsigned t0, t1, t2, left = SPIN_CAP;
            asm volatile("s_waitcnt vmcnt(%[c])\n\ts_branch 2f\n1:\n\ts_sleep %[sl]\n\t" E_LOADS "\ts_waitcnt vmcnt(0)\n2:\n\t"
                         "v_bitop3_b32 %[t0], v105, v109, %[ta] bitop3:0x7e\n\tv_bitop3_b32 %[t1], v113, v117, %[ta] bitop3:0x7e\n\tv_bitop3_b32 %[t2], v107, v111, %[tg] bitop3:0x7e\n\t"
                         "v_or3_b32 %[t0], %[t0], %[t1], %[t2]\n\tv_bitop3_b32 %[t1], v115, v119, %[tg] bitop3:0x7e\n\tv_or_b32 %[t0], %[t0], %[t1]\n\t"
                         "v_cmp_ne_u32 vcc, 0, %[t0]\n\ts_cbranch_vccz 3f\n\ts_sub_u32 %[n], %[n], 1\n\ts_cbranch_scc0 1b\n3:"
                         : "+{v[104:107]}"(eg[0]), "+{v[108:111]}"(eg[1]), "+{v[112:115]}"(eg[2]), "+{v[116:119]}"(eg[3]), [t0] "=&v"(t0), [t1] "=&v"(t1), [t2] "=&v"(t2), [n] "+s"(left)
                         : [a0] "v"(eo[0]), [a1] "v"(eo[1]), [b0] "s"(ws + WS_E), [b1] "s"(ws + WS_E + 16384), [ta] "s"(tagA), [tg] "s"(tagG), [sl] "n"(ESL), [c] "n"(CR1 + CR2) : "vcc", "scc", "memory");
            float ar = __uint_as_float(eg[1][0]), ai = __uint_as_float(eg[1][2]), br = __uint_as_float(eg[3][0]), bi = __uint_as_float(eg[3][2]);
            cmac(ar, ai, L64r, L64i, __uint_as_float(eg[0][0]), __uint_as_float(eg[0][2]));
            cmac(br, bi, L64r, L64i, __uint_as_float(eg[2][0]), __uint_as_float(eg[2][2]));
            if (EALL) { if (j0 < 0) ar = ai = 0.f; if (j0 + 2 < 0) br = bi = 0.f; }
            const f32x2 pu = cmul4(f32x2{ct[4][2], ct[4][3]}, f32x2{ar, ai});
            pr = pu[0]; pi = pu[1];
            cmac(pr, pi, ct[4][0], ct[4][1], br, bi);
            pr = sum4(pr); pi = sum4(pi);
        }
        TS(5);
        *LDSP(u32x4, pes) = u32x4{__float_as_uint(pr), __float_as_uint(pi), 1u, 0u};
        CR_LOAD(CR1 + CR2, CR1 + CR2 + CR3);
        u32x4 o;
        asm volatile("1:\n\tds_read_b128 %0, %1\n\ts_waitcnt lgkmcnt(0)\n\tv_cmp_eq_u32 vcc, 0, v122\n\ts_cbranch_vccnz 1b" : "=&{v[120:123]}"(o) : "v"(po) : "vcc", "memory");
        G = f32x2{pr + __uint_as_float(o[0]), pi + __uint_as_float(o[1])};
    }
    TS(6);
    {
        if (PLN & 8) c2_mac(hr, hi, G[0], -G[1], -G[0], -G[1], Q2r, Q2i); else pk_conj_mac(hr, hi, G, Q2r, Q2i);
#pragma unroll
        for (int p = 0; p < 8; ++p) { if (PLN & 8) c2_mac(ur[p], ui[p], L1[0], L1[1], L1[0], nL1i, hr, hi); else pk_cmac_conj(ur[p], ui[p], L1, hr, hi); hr = ur[p]; hi = ui[p]; }
        const char* hd = ws + WS_H + (size_t)((tm * 4 + (wm & 2)) * 2 + wn) * HP + (tn * 4 + (wm & 1) * 2) * 1024;
#pragma unroll
        for (int e2 = 0; e2 < 2; ++e2)
#pragma unroll
            for (int c = 0; c < 2; ++c) {
                const f32x2* o = c ? ui : ur;
                *LDSP(u32x4, hw[e2] + c * 512) = u32x4{pack2(o[0][e2], o[1][e2]), pack2(o[2][e2], o[3][e2]), pack2(o[4][e2], o[5][e2]), pack2(o[6][e2], o[7][e2])};
            }
        v4i16 t[4][2];
#pragma unroll
        for (int n = 0; n < 4; ++n)
#pragma unroll
            for (int j = 0; j < 2; ++j) t[n][j] = __builtin_amdgcn_ds_read_tr16_b64_v4i16(LDSP(v4i16, ta[j] + n * 1024));
        sy_post<1>(pmine);
        static_for<0, 4>([&](auto ic) {
            constexpr int n = decltype(ic)::value;
            const bf16x8 f = __builtin_shufflevector(t[n][0], t[n][1], 0, 1, 2, 3, 4, 5, 6, 7);
            GST_B128_SC1(l16, f, hd + (n >> 1) * 2 * HP + ((n & 1) ^ wn) * 1024, 0);
        });
    }
    TS(7);
    bf16x8 fa2[64];
    u32x2 xv[4], dsv;
    const char* Aw = ws + WS_H + (size_t)((tm * 4 + gm) * 2 + gk) * HP;
    const unsigned g2dst = lb + wid * 2048;
#define G2_ISSUE(st, i) do { if constexpr (i) dmao_<1024>(g2v[1], g2src + (SLICE(st) << 7)); else DMA(g2dst + g2_place(st), g2v[0], g2src + (SLICE(st) << 7)); } while (0)
#define G2_A(g) GLD_B128_SC1(fa2[g], l16, Aw + (SLICE((g) >> 2) << 12), ((g) & 3) * 1024)
    {
        {
            sy_wait<1>(pown);
            s16x4 fo[4][2]; bf16x8 c[8];
            static_for<0, 2>([&](auto kc) {
                constexpr int ks = decltype(kc)::value;
                static_for<0, 4>([&](auto ic) { constexpr int i = decltype(ic)::value; dsrtr_<ks * 4096>(fo[2 * ks + (i >> 1)][i & 1], to[i >> 1][i & 1]); });
                static_for<0, 4>([&](auto ic) { constexpr int i = decltype(ic)::value; DSR128(c[4 * ks + i], cad[ks][i & 1], 2 * G2STAGE + i * 2048); });
            });
            static_for<0, 8>([&](auto ic) {
                constexpr int n = decltype(ic)::value, ks = n >> 2, i = n & 3;
                WAIT_L(ks ? 3 - i : 11 - i);
                TIE(c[n]);
                if constexpr (i == 0) { TIE(fo[2 * ks][0]); TIE(fo[2 * ks][1]); TIE(fo[2 * ks + 1][0]); TIE(fo[2 * ks + 1][1]); }
#pragma unroll
                for (int j = 0; j < 2; ++j)
                    acc[i][j] = __builtin_amdgcn_mfma_f32_16x16x32_bf16(__builtin_shufflevector(fo[2 * ks + j][0], fo[2 * ks + j][1], 0, 1, 2, 3, 4, 5, 6, 7), c[n], ks ? acc[i][j] : f32x4{0, 0, 0, 0}, 0, 0, 0);
                SB();
            });
#pragma unroll
            for (int i = 0; i < 4; ++i) { TIE(acc[i][0]); TIE(acc[i][1]); }
        }
        if (CEARLY) sy_post<0>(pmine);
        static_for<0, 4>([&](auto ic) { constexpr int it = decltype(ic)::value; GLD_B64(xv[it], xo, a.x, it * 2048 - 4096); });
        GLD_B64(dsv, dso, a.dskip, 0);
        if (CEARLY) {
            sy_wait<0>(pall);
            static_for<4, 4 + CEARLY>([&](auto ic) { constexpr int n = decltype(ic)::value; G2_ISSUE(n >> 1, n & 1); });
        }
        TS(16);
        WAIT_V(5 + CEARLY);
        sy_post<2>(pmine);
        if (!CEARLY || wid == (NOOWN ? 1 : 0) || (NOOWN == 1 && wid == 0)) sy_wait<2, 0>(pall);
        const u32x2 tagv = {__float_as_uint(ct[2][2]), __float_as_uint(ct[2][3])};
        if (wid == (NOOWN ? 1 : 0)) asm volatile("s_mov_b64 exec, 0xffff\n\tglobal_store_dwordx2 %0, %1, %2 sc1\n\ts_mov_b64 exec, -1" :: "v"(fst), "v"(tagv), "s"(flg + FL_H) : "memory");
        if (PFIRST) asm volatile("s_cmp_lg_u32 %3, 0\n\ts_cbranch_scc1 1f\n\tglobal_load_dwordx2 %0, %1, %2 sc1\n1:" : "=&v"(fl) : "v"(fld), "s"(flg + FL_H), "s"(wid) : "scc", "memory");
        TS(8);
        static_for<4 + CEARLY, 10>([&](auto ic) { constexpr int n = decltype(ic)::value; G2_ISSUE(n >> 1, n & 1); });
        CR_LOAD(2 * NR - CRL, 2 * NR);
        TS(13);
        if (wid == 0) {
            unsigned left = SPIN_CAP;
            if (PFIRST)
            asm volatile("s_waitcnt vmcnt(6)\n\ts_branch 3f\n1:\n\tglobal_load_dwordx2 %0, %2, %3 sc1\n\ts_waitcnt vmcnt(0)\n3:\n\tv_cmp_ne_u64 vcc, %0, %4\n\ts_cbranch_vccz 2f\n\ts_sub_u32 %1, %1, 1\n\ts_cbranch_scc0 1b\n2:"
                         : "+v"(fl), "+s"(left) : "v"(fld), "s"(flg + FL_H), "v"(tagv) : "vcc", "scc", "memory");
            else
            asm volatile("1:\n\tglobal_load_dwordx2 %0, %2, %3 sc1\n\ts_waitcnt vmcnt(0)\n\tv_cmp_ne_u64 vcc, %0, %4\n\ts_cbranch_vccz 2f\n\ts_sub_u32 %1, %1, 1\n\ts_cbranch_scc0 1b\n2:"
                         : "=&v"(fl), "+s"(left) : "v"(fld), "s"(flg + FL_H), "v"(tagv) : "vcc", "scc", "memory");
            sy_post<3>(pmine);
            if (b == 255 && lane == 0) GST_B64_SC1(0u * lane, tagv, flg + FL_EPOCH);
        } else if (g2_bar(3)) sy_wait<3>(pzero);
        else { WAIT_V(CRL); sy_post<3>(pmine); }
        if (!g2_bar(3)) sy_wait<3>(pall);
        TS(20);
        static_for<4, 4 + AW>([&](auto ic) { constexpr int g = decltype(ic)::value; G2_A(g); });
    }
    TS(9);
    __builtin_amdgcn_s_setprio(0);
    unsigned yw, yr, yp, yo;
    static_assert(PAIR, "yr");
    unsigned dm[4];
    auto dmask = [&]() { TIE(dsv); dm[0] = dsv[0] & 0xffffu; dm[1] = dsv[0] & 0xffff0000u; dm[2] = dsv[1] & 0xffffu; dm[3] = dsv[1] & 0xffff0000u; TIE(dm[0]); TIE(dm[1]); TIE(dm[2]); TIE(dm[3]); };
    auto skip = [&](int it) { TIE(xv[it]); const f32x4 y = mul4bf(xv[it], dm); for (int i = 0; i < 4; ++i) acc[i][0][it] = add1(acc[i][0][it], y[i]); if (it & 1) { for (int i = 0; i < 4; ++i) TIE(acc[i][0]); } };
    {
        bf16x8 fb[2][4];
#define G2_FRAG(g, i) do { constexpr int pl_ = g2_place((g) >> 1), b_ = pl_ >= L_C ? L_C : pl_ >= 32768 ? 32768 : 0; DSR128(fb[(g) & 1][i], cad[(g) & 1][(i) & 1] + (b_ - L_C), pl_ - b_ + (i) * 2048); } while (0)
        static_for<0, 4>([&](auto ic) { constexpr int i = decltype(ic)::value; G2_FRAG(2, i); });
        if (YS == 0) { WAIT_V(AW + 6 + CRL); dmask(); skip(0); skip(1); skip(2); skip(3); }
        static_for<2, 32>([&](auto ic) {
            constexpr int g = decltype(ic)::value, gn = g + 1;
            constexpr bool bar = g2_bar(g) && !((VBM >> g) & 1);
            constexpr int n0 = g2_upto(g - 1), n1 = g2_upto(g);
            if constexpr (bar) {
                constexpr int ca = g2_cnt(2 * g, 2 * g), cs = g2_cover(g) >= 7 + NR ? g2_cnt(100 + 2 * g2_cover(g) + 1, 2 * g) : 63;
                static_assert(cs >= 0 && g2_wr(g - 1) >= 2 * (g2_cover(g) < 6 + NR ? g2_cover(g) - 6 : NR), "schedule");
                WAIT_VB(AWB && ca < cs ? ca : cs); WAIT_LB(0); BAR();
                if constexpr (!AWB) WAIT_V(ca);
#pragma unroll
                for (int i = 0; i < 4; ++i) if (!(AMF & 4)) TIE(fb[g & 1][i]);
                if (!(AMF & 4)) TIE(fa2[2 * g]);
            }
            static_for<0, 8>([&](auto mc) {
                constexpr int m = decltype(mc)::value, i = m & 3, j = m >> 2, f = 2 * g + j;
                static_assert(g2_cnt(f, f) >= 0, "schedule");
                constexpr int jj = LSW && g == 31 ? 1 - j : j, ff = 2 * g + jj;
                if constexpr (i == 0 && (j || !bar)) { WAIT_VB(LSW && g == 31 ? 0 : g2_cnt(f, f)); if constexpr (!(AMF & 4) || g == 2 || g == 31) TIE(fa2[ff]); }
                if constexpr (W2 && g > 2 && g < 31) { if constexpr (!bar && j == 0 && (i & W2) == 0) WAIT_LB(3 - W2 + g2_wr(g - 1) - g2_wr(g - 2) + (g - 1 == EGX)); } else
                if constexpr (!bar && j == 0) { WAIT_LB((gn < 32 ? 3 : 3 - i) + g2_wr(g - 1) - g2_wr(g - 2) + (g - 1 == EGX)); if constexpr (!(AMF & 4) || g == 2 || g == 31) TIE(fb[g & 1][i]); }
                if constexpr ((AMF & 4) && g > 2 && g < 31) mfma_(acc[i][jj], fa2[ff], fb[g & 1][i]); else
                acc[i][jj] = __builtin_amdgcn_mfma_f32_16x16x32_bf16(fa2[ff], fb[g & 1][i], acc[i][jj], 0, 0, 0);
                SB();
                if constexpr (LSW && g == 31 && j) *LDSP(f32x4, yw + i * 1024) = acc[i][1];
                if constexpr (g == EGX && m == 4) sy_post<5>(pmine);
                if constexpr (g == 29 && m == 5) { yw = cwa + L_Y + wid * 2048; yr = yw ^ (PAIR ? 4096 : 16384); yp = pmine ^ (PAIR ? 4 : 16); yo = g4 * 32768 + l15 * 16; TIE(yw); TIE(yr); TIE(yp); TIE(yo); }
                if constexpr (gn < 32 && m < 4) G2_FRAG(gn, m);
                if constexpr (m >= 4 && g2_wr(g - 1) + m - 4 < g2_wr(g)) {
                    constexpr int n = g2_wr(g - 1) + m - 4, pl = g2_place(7 + (n >> 1)), pb = pl >= L_C ? L_C : 0;
                    asm volatile("ds_write_b128 %0, %1 offset:%2" :: "v"(cwa + pb), "v"(cr[n]), "n"(pl - pb + (n & 1) * 1024) : "memory");
                }
                if constexpr (i == 1 && n0 + j < n1) G2_ISSUE((n0 + j) >> 1, (n0 + j) & 1);
                if constexpr (i >= 2 && g2_ald(f, i & 1) >= 0) G2_A(g2_ald(f, i & 1));
                SB();
            });
        });
#undef G2_FRAG
    }
    TS(11);
    if (EPRIO) __builtin_amdgcn_s_setprio(EPRIO);
    {
#pragma unroll
        for (int i = 0; i < 4; ++i) if (!LSW) *LDSP(f32x4, yw + i * 1024) = acc[i][1];
        sy_post<4>(pmine);
        sy_wait<4, 0>(yp);
        char* const ob = a.out + (size_t)(tm * 128 + gm * 32 + gk * 16) * 8192 + 4096 + tn * 256;
        f32x4 w[4];
#pragma unroll
        for (int i = 0; i < 4; ++i) w[i] = *LDSP(f32x4, yr + i * 1024);
        f32x4 o[4];
#pragma unroll
        for (int r = 0; r < 4; ++r) { o[r] = f32x4{add1(w[0][r], acc[0][0][r]), add1(w[1][r], acc[1][0][r]), add1(w[2][r], acc[2][0][r]), add1(w[3][r], acc[3][0][r])}; if (EGX) TIE(o[r]); }
        if (EGX && !wn) sy_wait<5, 1>(pall);
#pragma unroll
        for (int r = 0; r < 4; ++r) GST_B128_NT(yo, o[r], ob + r * 8192, 0);
    }
    asm volatile(".Lfk_code_b:");
    TSE(12);
    TS_FLUSH();
}

}

extern "C" void kernel_launch(void* const* d_in, const int* in_sizes, int n_in, void* d_out, int out_size, void* d_ws, size_t ws_size,
                              hipStream_t stream) {
    (void)in_sizes; (void)n_in; (void)out_size; (void)ws_size;
    fk::Args a;
    a.q = (const char*)d_in[0]; a.k = (const char*)d_in[1]; a.v = (const char*)d_in[2]; a.sk = (const char*)d_in[3];
    a.ssmk = (const char*)d_in[4]; a.ssmD = (const char*)d_in[5]; a.x = (const char*)d_in[6]; a.lre = (const char*)d_in[7];
    a.lim = (const char*)d_in[8]; a.bre = (const char*)d_in[9]; a.bim = (const char*)d_in[10]; a.cre = (const char*)d_in[11];
    a.cim = (const char*)d_in[12]; a.logdt = (const char*)d_in[13]; a.dskip = (const char*)d_in[14];
    a.out = (char*)d_out; a.ws = (char*)d_ws;
    hipLaunchKernelGGL(fk::fused, dim3(256), dim3(768), 0, stream, a);
}
```

```cpp
#include <hip/hip_runtime.h>
#include <stdint.h>
#include <type_traits>

namespace fk {

constexpr int S = 2048, D = 1024, P = 1024;

typedef __attribute__((ext_vector_type(8))) short bf16x8;
typedef __attribute__((ext_vector_type(4))) short s16x4;
typedef __attribute__((ext_vector_type(16))) float f32x16;
typedef __attribute__((ext_vector_type(4))) float f32x4;
typedef __attribute__((ext_vector_type(2))) float f32x2;
typedef __attribute__((ext_vector_type(4))) unsigned u32x4;
typedef __attribute__((ext_vector_type(2))) unsigned u32x2;
#define LDSP(T, a) ((__attribute__((address_space(3))) T*)(uintptr_t)(unsigned)(a))

__device__ __forceinline__ float bf2f(unsigned v) { return __uint_as_float(v << 16); }
__device__ __forceinline__ float bflo(unsigned w) { return __uint_as_float(w << 16); }
__device__ __forceinline__ float bfhi(unsigned w) { return __uint_as_float(w & 0xffff0000u); }
__device__ __forceinline__ uint16_t f2bf(float f) { __bf16 b = (__bf16)f; return __builtin_bit_cast(uint16_t, b); }
__device__ __forceinline__ float rbf(float f) { return bf2f(f2bf(f)); }
typedef __attribute__((ext_vector_type(2))) __bf16 bf16x2;
__device__ __forceinline__ unsigned pack2(float lo, float hi) { return __builtin_bit_cast(unsigned, __builtin_convertvector(f32x2{lo, hi}, bf16x2)); }
__device__ __forceinline__ float rb1(float x) { return __uint_as_float(pack2(0.f, x)); }
__device__ __forceinline__ void cmac(float& xr, float& xi, float ar, float ai, float hr, float hi) {
    asm("v_fmac_f32 %0, %2, %4\n\tv_fmac_f32 %1, %2, %5\n\tv_fma_f32 %0, -%3, %5, %0\n\tv_fmac_f32 %1, %3, %4" : "+v"(xr), "+v"(xi) : "v"(ar), "v"(ai), "v"(hr), "v"(hi));
}
__device__ __forceinline__ void cmad(float& o_r, float& o_i, float ar, float ai, float hr, float hi, float er, float ei) {
    asm("v_fma_f32 %0, %2, %4, %6\n\tv_fma_f32 %1, %2, %5, %7\n\tv_fma_f32 %0, -%3, %5, %0\n\tv_fmac_f32 %1, %3, %4" : "=&v"(o_r), "=&v"(o_i) : "v"(ar), "v"(ai), "v"(hr), "v"(hi), "v"(er), "v"(ei));
}
__device__ __forceinline__ f32x2 cmul4(f32x2 a, f32x2 h) {
    float o_r, o_i;
    asm("v_mul_f32 %0, %2, %4\n\tv_mul_f32 %1, %2, %5\n\tv_fma_f32 %0, -%3, %5, %0\n\tv_fmac_f32 %1, %3, %4" : "=&v"(o_r), "=&v"(o_i) : "v"(a[0]), "v"(a[1]), "v"(h[0]), "v"(h[1]));
    return f32x2{o_r, o_i};
}

#define PK_LO " op_sel:[0,0,0] op_sel_hi:[0,1,1]"
#define PK_HI " op_sel:[1,0,0] op_sel_hi:[1,1,1]"
#define PK_NEG " neg_lo:[1,0,0] neg_hi:[1,0,0]"
__device__ __forceinline__ void pk_cmad(f32x2& o_r, f32x2& o_i, f32x2 a, f32x2 hr, f32x2 hi, f32x2 er, f32x2 ei) {
    asm("v_pk_fma_f32 %0, %2, %3, %5" PK_LO "\n\tv_pk_fma_f32 %1, %2, %4, %6" PK_LO "\n\tv_pk_fma_f32 %0, %2, %4, %0" PK_HI PK_NEG "\n\tv_pk_fma_f32 %1, %2, %3, %1" PK_HI
        : "=&v"(o_r), "=&v"(o_i) : "v"(a), "v"(hr), "v"(hi), "v"(er), "v"(ei));
}
__device__ __forceinline__ void pk_cmac_conj(f32x2& xr, f32x2& xi, f32x2 a, f32x2 hr, f32x2 hi) {
    asm("v_pk_fma_f32 %0, %2, %3, %0" PK_LO "\n\tv_pk_fma_f32 %1, %2, %4, %1" PK_LO "\n\tv_pk_fma_f32 %0, %2, %4, %0" PK_HI "\n\tv_pk_fma_f32 %1, %2, %3, %1" PK_HI PK_NEG
        : "+v"(xr), "+v"(xi) : "v"(a), "v"(hr), "v"(hi));
}
__device__ __forceinline__ void pk_conj_mul(f32x2& o_r, f32x2& o_i, f32x2 a, f32x2 hr, f32x2 hi) {
    asm("v_pk_mul_f32 %0, %2, %3 op_sel:[0,0] op_sel_hi:[0,1]\n\tv_pk_mul_f32 %1, %2, %4 op_sel:[0,0] op_sel_hi:[0,1] neg_lo:[1,0] neg_hi:[1,0]\n\t"
        "v_pk_fma_f32 %0, %2, %4, %0" PK_HI PK_NEG "\n\tv_pk_fma_f32 %1, %2, %3, %1" PK_HI PK_NEG : "=&v"(o_r), "=&v"(o_i) : "v"(a), "v"(hr), "v"(hi));
}
__device__ __forceinline__ void pk_conj_mac(f32x2& xr, f32x2& xi, f32x2 a, f32x2 hr, f32x2 hi) {
    asm("v_pk_fma_f32 %0, %2, %3, %0" PK_LO "\n\tv_pk_fma_f32 %1, %2, %4, %1" PK_LO PK_NEG "\n\tv_pk_fma_f32 %0, %2, %4, %0" PK_HI PK_NEG "\n\tv_pk_fma_f32 %1, %2, %3, %1" PK_HI PK_NEG
        : "+v"(xr), "+v"(xi) : "v"(a), "v"(hr), "v"(hi));
}
__device__ __forceinline__ void cmac2(f32x2& x, f32x2 a, f32x2 h) {
    asm("v_pk_fma_f32 %0, %1, %2, %0" PK_LO "\n\tv_pk_fma_f32 %0, %1, %2, %0 op_sel:[1,1,0] op_sel_hi:[1,0,1] neg_lo:[1,0,0]" : "+v"(x) : "v"(a), "v"(h));
}
#define PLN 0
__device__ __forceinline__ void c2_mac(f32x2& xr, f32x2& xi, float p, float q, float r, float s, f32x2 hr, f32x2 hi) {
    float a = xr[0], b = xi[0], c = xr[1], d = xi[1];
    asm("v_fmac_f32 %0, %4, %8\n\tv_fmac_f32 %1, %6, %9\n\tv_fmac_f32 %2, %4, %10\n\tv_fmac_f32 %3, %6, %11\n\t"
        "v_fmac_f32 %0, %5, %9\n\tv_fmac_f32 %1, %7, %8\n\tv_fmac_f32 %2, %5, %11\n\tv_fmac_f32 %3, %7, %10"
        : "+v"(a), "+v"(b), "+v"(c), "+v"(d) : "v"(p), "v"(q), "v"(r), "v"(s), "v"(hr[0]), "v"(hi[0]), "v"(hr[1]), "v"(hi[1]));
    xr = f32x2{a, c}; xi = f32x2{b, d};
}
__device__ __forceinline__ void c2_mad(f32x2& xr, f32x2& xi, float p, float q, float r, float s, f32x2 hr, f32x2 hi, f32x2 er, f32x2 ei) {
    float a, b, c, d;
    asm("v_fma_f32 %0, %4, %8, %12\n\tv_fma_f32 %1, %6, %9, %13\n\tv_fma_f32 %2, %4, %10, %14\n\tv_fma_f32 %3, %6, %11, %15\n\t"
        "v_fmac_f32 %0, %5, %9\n\tv_fmac_f32 %1, %7, %8\n\tv_fmac_f32 %2, %5, %11\n\tv_fmac_f32 %3, %7, %10"
        : "=&v"(a), "=&v"(b), "=&v"(c), "=&v"(d) : "v"(p), "v"(q), "v"(r), "v"(s), "v"(hr[0]), "v"(hi[0]), "v"(hr[1]), "v"(hi[1]), "v"(er[0]), "v"(ei[0]), "v"(er[1]), "v"(ei[1]));
    xr = f32x2{a, c}; xi = f32x2{b, d};
}
__device__ __forceinline__ void c2_mul(f32x2& xr, f32x2& xi, float p, float q, float r, float s, f32x2 hr, f32x2 hi) {
    float a, b, c, d;
    asm("v_mul_f32 %0, %4, %8\n\tv_mul_f32 %1, %6, %9\n\tv_mul_f32 %2, %4, %10\n\tv_mul_f32 %3, %6, %11\n\t"
        "v_fmac_f32 %0, %5, %9\n\tv_fmac_f32 %1, %7, %8\n\tv_fmac_f32 %2, %5, %11\n\tv_fmac_f32 %3, %7, %10"
        : "=&v"(a), "=&v"(b), "=&v"(c), "=&v"(d) : "v"(p), "v"(q), "v"(r), "v"(s), "v"(hr[0]), "v"(hi[0]), "v"(hr[1]), "v"(hi[1]));
    xr = f32x2{a, c}; xi = f32x2{b, d};
}
__device__ __forceinline__ float add1(float a, float b) { float c; asm("v_add_f32 %0, %1, %2" : "=v"(c) : "v"(a), "v"(b)); return c; }
__device__ __forceinline__ void mfma_(f32x4& c, bf16x8 a, bf16x8 b) { asm volatile("v_mfma_f32_16x16x32_bf16 %0, %1, %2, %0" : "+v"(c) : "v"(a), "v"(b)); }
__device__ __forceinline__ float sum4(float x) {
    const auto p = __builtin_amdgcn_permlane32_swap(__float_as_uint(x), __float_as_uint(x), false, false);
    x = __uint_as_float(p[0]) + __uint_as_float(p[1]);
    const auto q = __builtin_amdgcn_permlane16_swap(__float_as_uint(x), __float_as_uint(x), false, false);
    return __uint_as_float(q[0]) + __uint_as_float(q[1]);
}

template <int I, int N, class F>
__device__ __forceinline__ void static_for(F&& f) { if constexpr (I < N) { f(std::integral_constant<int, I>{}); static_for<I + 1, N>(f); } }

#define TS_DECL
#define TS0a()
#define TS0b()
#define TSE(i)
#define TS(i)
#define TS_FLUSH()

#define WAIT_V(n) asm volatile("s_waitcnt vmcnt(%0)" ::"n"((n) > 63 ? 63 : (n)) : "memory")
#define WAIT_L(n) asm volatile("s_waitcnt lgkmcnt(%0)" ::"n"((n) > 15 ? 15 : (n)) : "memory")
#define BW 1
constexpr int wenc(int v, int l) { v = v > 63 ? 63 : v; l = l > 15 ? 15 : l; return (v & 15) | 0x70 | (l << 8) | ((v >> 4) << 14); }
#define WAIT_VB(n) do { if constexpr (BW) __builtin_amdgcn_s_waitcnt(wenc(n, 15)); else WAIT_V(n); } while (0)
#define WAIT_LB(n) do { if constexpr (BW) __builtin_amdgcn_s_waitcnt(wenc(63, n)); else WAIT_L(n); } while (0)
#define BAR() asm volatile("s_barrier" ::: "memory")
#define SB() __builtin_amdgcn_sched_barrier(0)
template <class T> __device__ __forceinline__ void tie_(T& x) { asm volatile("" : "+v"(x)); }
#define TIE(x) tie_(x)
template <class T> __device__ __forceinline__ void ties_(T& x) { asm volatile("" : "+s"(x)); }
__device__ __forceinline__ unsigned take(unsigned x) { unsigned y; asm volatile("v_mov_b32 %0, %1" : "=v"(y) : "v"(x)); return y; }
#define DEF_LD(name, ins) template <int OFF, class T> __device__ __forceinline__ void name(T& dst, unsigned voff, const char* sb) { \
    asm volatile(ins : "=v"(dst) : "v"(voff), "s"(sb), "n"(OFF) : "memory"); }
DEF_LD(gld_u16_, "global_load_ushort %0, %1, %2 offset:%3")
DEF_LD(gld_b32_, "global_load_dword %0, %1, %2 offset:%3")
DEF_LD(gld_b64_, "global_load_dwordx2 %0, %1, %2 offset:%3")
DEF_LD(gld_b128_, "global_load_dwordx4 %0, %1, %2 offset:%3")
#define NTQ " nt"
#define NTK " nt"
DEF_LD(gld_b64_nt_, "global_load_dwordx2 %0, %1, %2 offset:%3" NTQ)
DEF_LD(gld_b128_nt_, "global_load_dwordx4 %0, %1, %2 offset:%3" NTQ)
DEF_LD(gld_b64_sc1_, "global_load_dwordx2 %0, %1, %2 offset:%3 sc1")
DEF_LD(gld_b128_sc1_, "global_load_dwordx4 %0, %1, %2 offset:%3 sc1")
#define DEF_ST(name, ins) template <int OFF, class T> __device__ __forceinline__ void name(unsigned voff, const T& v, const char* sb) { \
    asm volatile(ins :: "v"(voff), "v"(v), "s"(sb), "n"(OFF) : "memory"); }
DEF_ST(gst_b128_, "global_store_dwordx4 %0, %1, %2 offset:%3\n\ts_nop 1")
DEF_ST(gst_b128_nt_, "global_store_dwordx4 %0, %1, %2 offset:%3 nt\n\ts_nop 1")
DEF_ST(gst_b128_sc1_, "global_store_dwordx4 %0, %1, %2 offset:%3 sc1\n\ts_nop 1")
DEF_ST(gst_b64_sc1_, "global_store_dwordx2 %0, %1, %2 offset:%3 sc1")
#define GLD_U16(dst, voff, sb) gld_u16_<0>(dst, voff, sb)
#define GLD_B32(dst, voff, sb, off) gld_b32_<(off)>(dst, voff, sb)
#define GLD_B64(dst, voff, sb, off) gld_b64_<(off)>(dst, voff, sb)
#define GLD_B128(dst, voff, sb, off) gld_b128_<(off)>(dst, voff, sb)
#define GLD_B64_NT(dst, voff, sb, off) gld_b64_nt_<(off)>(dst, voff, sb)
#define GLD_B128_NT(dst, voff, sb, off) gld_b128_nt_<(off)>(dst, voff, sb)
#define GLD_B64_SC1(dst, voff, sb, off) gld_b64_sc1_<(off)>(dst, voff, sb)
#define GLD_B128_SC1(dst, voff, sb, off) gld_b128_sc1_<(off)>(dst, voff, sb)
#define GST_B128(voff, v, sb, off) gst_b128_<(off)>(voff, v, sb)
#define GST_B128_NT(voff, v, sb, off) gst_b128_nt_<(off)>(voff, v, sb)
#define GST_B128_SC1(voff, v, sb, off) gst_b128_sc1_<(off)>(voff, v, sb)
#define GST_B64_SC1(voff, v, sb) gst_b64_sc1_<0>(voff, v, sb)
__device__ __forceinline__ void dma_(unsigned ldsaddr, unsigned voff, const char* sb) {
    asm volatile("s_mov_b32 m0, %0\n\ts_nop 0\n\tglobal_load_lds_dwordx4 %1, %2" :: "s"(ldsaddr), "v"(voff), "s"(sb) : "memory"); }
__device__ __forceinline__ void dma_nt_(unsigned ldsaddr, unsigned voff, const char* sb) {
    asm volatile("s_mov_b32 m0, %0\n\ts_nop 0\n\tglobal_load_lds_dwordx4 %1, %2 nt" :: "s"(ldsaddr), "v"(voff), "s"(sb) : "memory"); }
#define DF 0
#define DFN 4
#define FL_0 ""
#define FL_1 " nt"
#define FL_2 " sc1"
#define FL_3 " sc1 nt"
#define FL_4 " sc0 sc1"
#define FL_5 " sc0 sc1 nt"
#define FL_6 " sc0 nt"
#define FLX_(x) FL_##x
#define FLX(x) FLX_(x)
__device__ __forceinline__ void dmaf_(unsigned ldsaddr, unsigned voff, const char* sb) {
    asm volatile("s_mov_b32 m0, %0\n\ts_nop 0\n\tglobal_load_lds_dwordx4 %1, %2" FLX(DF) :: "s"(ldsaddr), "v"(voff), "s"(sb) : "memory"); }
template <int OFF> __device__ __forceinline__ void dmaof_(unsigned voff, const char* sb) {
    asm volatile("global_load_lds_dwordx4 %0, %1 offset:%2" FLX(DF) :: "v"(voff), "s"(sb), "n"(OFF) : "memory"); }
#define DMA(ldsaddr, voff, sb) dma_(ldsaddr, voff, sb)
template <int OFF> __device__ __forceinline__ void dmao_(unsigned voff, const char* sb) {
    asm volatile("global_load_lds_dwordx4 %0, %1 offset:%2" :: "v"(voff), "s"(sb), "n"(OFF) : "memory"); }
template <int OFF, class T> __device__ __forceinline__ void dsr128_(T& dst, unsigned addr) { asm volatile("ds_read_b128 %0, %1 offset:%2" : "=v"(dst) : "v"(addr), "n"(OFF)); }
template <int OFF, class T> __device__ __forceinline__ void dsr64_(T& dst, unsigned addr) { asm volatile("ds_read_b64 %0, %1 offset:%2" : "=v"(dst) : "v"(addr), "n"(OFF)); }
template <int OFF, class T> __device__ __forceinline__ void dsr2_(T& dst, unsigned addr) { asm volatile("ds_read2_b32 %0, %1 offset0:%2 offset1:%3" : "=v"(dst) : "v"(addr), "n"(OFF), "n"(OFF + 1)); }
#define DSR128(dst, addr, off) dsr128_<(off)>(dst, addr)
#define DSR64(dst, addr, off) dsr64_<(off)>(dst, addr)

__device__ __forceinline__ void kv4(unsigned& o01, unsigned& o23, unsigned ka, unsigned kb, unsigned kc, unsigned kd, const unsigned* w, unsigned v0, unsigned v1) {
    unsigned t1, t3, p0, p1, p2, p3;
    asm(
        "v_dot2_f32_bf16 %0, %8, %19, 0\n\tv_dot2_f32_bf16 %1, %9, %18, 0\n\tv_dot2_f32_bf16 %2, %9, %19, 0\n\tv_dot2_f32_bf16 %3, %10, %18, 0\n\t"
        "v_dot2_f32_bf16 %4, %9, %16, 0\n\tv_dot2_f32_bf16 %5, %9, %17, 0\n\tv_dot2_f32_bf16 %6, %10, %16, 0\n\tv_dot2_f32_bf16 %7, %10, %17, 0\n\t"
        "v_cvt_pk_bf16_f32 %0, %0, %4\n\tv_cvt_pk_bf16_f32 %1, %1, %5\n\tv_cvt_pk_bf16_f32 %2, %2, %6\n\tv_cvt_pk_bf16_f32 %3, %3, %7\n\t"
        "v_dot2_f32_bf16 %4, %9, %15, 0\n\tv_dot2_f32_bf16 %5, %10, %14, 0\n\tv_dot2_f32_bf16 %6, %10, %15, 0\n\tv_dot2_f32_bf16 %7, %11, %14, 0\n\t"
        "v_dot2_f32_bf16 %0, %0, %22, 0\n\tv_dot2_f32_bf16 %1, %1, %22, 0\n\tv_dot2_f32_bf16 %2, %2, %22, 0\n\tv_dot2_f32_bf16 %3, %3, %22, 0\n\t"
        "v_cvt_pk_bf16_f32 %0, %0, %4\n\tv_cvt_pk_bf16_f32 %1, %1, %5\n\tv_cvt_pk_bf16_f32 %2, %2, %6\n\tv_cvt_pk_bf16_f32 %3, %3, %7\n\t"
        "v_dot2_f32_bf16 %4, %10, %12, 0\n\tv_dot2_f32_bf16 %5, %10, %13, 0\n\tv_dot2_f32_bf16 %6, %11, %12, 0\n\tv_dot2_f32_bf16 %7, %11, %13, 0\n\t"
        "v_dot2_f32_bf16 %0, %0, %22, 0\n\tv_dot2_f32_bf16 %1, %1, %22, 0\n\tv_dot2_f32_bf16 %2, %2, %22, 0\n\tv_dot2_f32_bf16 %3, %3, %22, 0\n\t"
        "v_cvt_pk_bf16_f32 %0, %0, %4\n\tv_cvt_pk_bf16_f32 %1, %1, %5\n\tv_cvt_pk_bf16_f32 %2, %2, %6\n\tv_cvt_pk_bf16_f32 %3, %3, %7\n\t"
        "v_dot2_f32_bf16 %0, %0, %22, 0\n\tv_dot2_f32_bf16 %1, %1, %22, 0\n\tv_dot2_f32_bf16 %2, %2, %22, 0\n\tv_dot2_f32_bf16 %3, %3, %22, 0\n\t"
        "v_cvt_pk_bf16_f32 %0, %0, 0\n\tv_cvt_pk_bf16_f32 %1, 0, %1\n\tv_cvt_pk_bf16_f32 %2, %2, 0\n\tv_cvt_pk_bf16_f32 %3, 0, %3\n\t"
        "v_dot2_f32_bf16 %0, %0, %20, 0\n\tv_dot2_f32_bf16 %1, %1, %20, 0\n\tv_dot2_f32_bf16 %2, %2, %21, 0\n\tv_dot2_f32_bf16 %3, %3, %21, 0\n\t"
        "s_nop 1\n\tv_cvt_pk_bf16_f32 %0, %1, %0\n\tv_cvt_pk_bf16_f32 %2, %3, %2\n\t"
        : "=&v"(o01), "=&v"(t1), "=&v"(o23), "=&v"(t3), "=&v"(p0), "=&v"(p1), "=&v"(p2), "=&v"(p3)
        : "v"(ka), "v"(kb), "v"(kc), "v"(kd), "v"(w[0]), "v"(w[1]), "v"(w[2]), "v"(w[3]), "v"(w[4]), "v"(w[5]), "v"(w[6]), "v"(w[7]), "v"(v0), "v"(v1), "s"(0x3f803f80u));
}
__device__ __forceinline__ void kv4a(unsigned* t, unsigned* e, unsigned ka, unsigned kb, unsigned kc, unsigned kd, const unsigned* w) {
    asm volatile(
        "v_and_b32 %8, %21, %15\n\tv_lshlrev_b32 %9, 16, %16\n\tv_and_b32 %10, %21, %16\n\tv_lshlrev_b32 %11, 16, %17\n\t"
        "v_and_b32 %12, %21, %17\n\tv_mul_f32 %0, %20, %8\n\tv_mul_f32 %1, %20, %9\n\tv_mul_f32 %2, %20, %10\n\t"
        "v_mul_f32 %3, %20, %11\n\tv_mul_f32 %4, %19, %9\n\tv_mul_f32 %5, %19, %10\n\tv_mul_f32 %6, %19, %11\n\t"
        "v_mul_f32 %7, %19, %12\n\tv_lshlrev_b32 %13, 16, %18\n\tv_and_b32 %14, %21, %18\n\tv_cvt_pk_bf16_f32 %0, 0, %0\n\t"
        "v_cvt_pk_bf16_f32 %1, 0, %1\n\tv_cvt_pk_bf16_f32 %2, 0, %2\n\tv_cvt_pk_bf16_f32 %3, 0, %3\n\tv_cvt_pk_bf16_f32 %4, 0, %4\n\t"
        "v_cvt_pk_bf16_f32 %5, 0, %5\n\tv_cvt_pk_bf16_f32 %6, 0, %6\n\tv_cvt_pk_bf16_f32 %7, 0, %7\n\t"
        : "=&v"(t[0]), "=&v"(t[1]), "=&v"(t[2]), "=&v"(t[3]), "=&v"(t[4]), "=&v"(t[5]), "=&v"(t[6]), "=&v"(t[7]), "=&v"(e[0]), "=&v"(e[1]), "=&v"(e[2]), "=&v"(e[3]), "=&v"(e[4]), "=&v"(e[5]), "=&v"(e[6])
        : "v"(ka), "v"(kb), "v"(kc), "v"(kd), "v"(w[2]), "v"(w[3]), "s"(0xffff0000u));
}
__device__ __forceinline__ void kv4b(unsigned* t, const unsigned* e, const unsigned* w) {
    asm volatile(
        "v_add_f32 %0, %0, %4\n\tv_add_f32 %1, %1, %5\n\tv_add_f32 %2, %2, %6\n\tv_add_f32 %3, %3, %7\n\t"
        "v_mul_f32 %4, %14, %8\n\tv_mul_f32 %5, %14, %9\n\tv_mul_f32 %6, %14, %10\n\tv_mul_f32 %7, %14, %11\n\t"
        "v_cvt_pk_bf16_f32 %0, 0, %0\n\tv_cvt_pk_bf16_f32 %1, 0, %1\n\tv_cvt_pk_bf16_f32 %2, 0, %2\n\tv_cvt_pk_bf16_f32 %3, 0, %3\n\t"
        "v_cvt_pk_bf16_f32 %4, 0, %4\n\tv_cvt_pk_bf16_f32 %5, 0, %5\n\tv_cvt_pk_bf16_f32 %6, 0, %6\n\tv_cvt_pk_bf16_f32 %7, 0, %7\n\t"
        "v_add_f32 %0, %0, %4\n\tv_add_f32 %1, %1, %5\n\tv_add_f32 %2, %2, %6\n\tv_add_f32 %3, %3, %7\n\t"
        "v_mul_f32 %4, %13, %9\n\tv_mul_f32 %5, %13, %10\n\tv_mul_f32 %6, %13, %11\n\tv_mul_f32 %7, %13, %12\n\t"
        : "+v"(t[0]), "+v"(t[1]), "+v"(t[2]), "+v"(t[3]), "+v"(t[4]), "+v"(t[5]), "+v"(t[6]), "+v"(t[7]) : "v"(e[2]), "v"(e[3]), "v"(e[4]), "v"(e[5]), "v"(e[6]), "v"(w[0]), "v"(w[1]));
}
__device__ __forceinline__ void kv4c(unsigned* t, unsigned v0, unsigned v1) {
    asm volatile(
        "v_cvt_pk_bf16_f32 %0, 0, %0\n\tv_cvt_pk_bf16_f32 %1, 0, %1\n\tv_cvt_pk_bf16_f32 %2, 0, %2\n\tv_cvt_pk_bf16_f32 %3, 0, %3\n\t"
        "v_cvt_pk_bf16_f32 %4, 0, %4\n\tv_cvt_pk_bf16_f32 %5, 0, %5\n\tv_cvt_pk_bf16_f32 %6, 0, %6\n\tv_cvt_pk_bf16_f32 %7, 0, %7\n\t"
        "v_add_f32 %0, %0, %4\n\tv_add_f32 %1, %1, %5\n\tv_add_f32 %2, %2, %6\n\tv_add_f32 %3, %3, %7\n\t"
        "v_lshlrev_b32 %4, 16, %8\n\tv_and_b32 %5, %10, %8\n\tv_lshlrev_b32 %6, 16, %9\n\tv_and_b32 %7, %10, %9\n\t"
        "v_cvt_pk_bf16_f32 %0, 0, %0\n\tv_cvt_pk_bf16_f32 %1, 0, %1\n\tv_cvt_pk_bf16_f32 %2, 0, %2\n\tv_cvt_pk_bf16_f32 %3, 0, %3\n\t"
        "v_mul_f32 %0, %0, %4\n\tv_mul_f32 %1, %1, %5\n\tv_mul_f32 %2, %2, %6\n\tv_mul_f32 %3, %3, %7\n\t"
        "v_cvt_pk_bf16_f32 %0, %1, %0\n\tv_cvt_pk_bf16_f32 %2, %3, %2\n\t"
        : "+v"(t[0]), "+v"(t[1]), "+v"(t[2]), "+v"(t[3]), "+v"(t[4]), "+v"(t[5]), "+v"(t[6]), "+v"(t[7]) : "v"(v0), "v"(v1), "s"(0xffff0000u));
}
__device__ __forceinline__ f32x4 mul4bf(u32x2 x, const unsigned* d) {
    float a, b, c, e;
    asm("v_dot2_f32_bf16 %0, %4, %6, 0\n\tv_dot2_f32_bf16 %1, %4, %7, 0\n\tv_dot2_f32_bf16 %2, %5, %8, 0\n\tv_dot2_f32_bf16 %3, %5, %9, 0\n\t"
        "v_cvt_pk_bf16_f32 %0, 0, %0\n\tv_cvt_pk_bf16_f32 %1, 0, %1\n\tv_cvt_pk_bf16_f32 %2, 0, %2\n\tv_cvt_pk_bf16_f32 %3, 0, %3"
        : "=&v"(a), "=&v"(b), "=&v"(c), "=&v"(e) : "v"(x[0]), "v"(x[1]), "v"(d[0]), "v"(d[1]), "v"(d[2]), "v"(d[3]));
    return f32x4{a, b, c, e};
}
template <int BIT> __device__ __forceinline__ void quad_swap(u32x4& a, u32x4& b) {
    unsigned n0, n1, n2, n3, b0 = b[0], b1 = b[1], b2 = b[2], b3 = b[3];
#define QS_(d, s, k) "v_cndmask_b32_dpp " d ", " s ", " k ", vcc quad_perm:[%14,%15,%16,%17] row_mask:0xf bank_mask:0xf\n\t"
    asm("s_mov_b64 vcc, %12\n\ts_nop 0\n\t" QS_("%0", "%4", "%8") QS_("%1", "%5", "%9") QS_("%2", "%6", "%10") QS_("%3", "%7", "%11")
        "s_mov_b64 vcc, %13\n\t" QS_("%4", "%8", "%4") QS_("%5", "%9", "%5") QS_("%6", "%10", "%6") QS_("%7", "%11", "%7")
        : "=&v"(n0), "=&v"(n1), "=&v"(n2), "=&v"(n3), "+v"(b0), "+v"(b1), "+v"(b2), "+v"(b3)
        : "v"(a[0]), "v"(a[1]), "v"(a[2]), "v"(a[3]), "s"(BIT ? 0x3333333333333333ull : 0x5555555555555555ull), "s"(BIT ? 0xccccccccccccccccull : 0xaaaaaaaaaaaaaaaaull),
          "n"(BIT ? 2 : 1), "n"(BIT ? 3 : 0), "n"(BIT ? 0 : 3), "n"(BIT ? 1 : 2) : "vcc");
#undef QS_
    a = u32x4{n0, n1, n2, n3}; b = u32x4{b0, b1, b2, b3};
}
#define IF_(c, C) "s_cmp_lg_u32 " c ", " C "\n\ts_cbranch_scc1 1f\n\t"
template <int C, int N> __device__ __forceinline__ void wait_if(int c) { asm volatile(IF_("%0", "%1") "s_waitcnt vmcnt(%2)\n1:" :: "s"(c), "n"(C), "n"(N) : "scc", "memory"); }
template <int C, int M> __device__ __forceinline__ void flag_if(int c, unsigned voff, u32x2 v, const char* sb) {
    asm volatile(IF_("%0", "%1") "s_mov_b32 exec_lo, %5\n\ts_mov_b32 exec_hi, 0\n\tglobal_store_dwordx2 %2, %3, %4 sc1\n\ts_mov_b64 exec, -1\n1:" :: "s"(c), "n"(C), "v"(voff), "v"(v), "s"(sb), "n"(M) : "scc", "memory"); }
template <int C> __device__ __forceinline__ void st4_if(int c, unsigned voff, const u32x4* v, const char* s0, const char* s1) {
    asm volatile(IF_("%0", "%1") "global_store_dwordx4 %2, %3, %7 sc1\n\tglobal_store_dwordx4 %2, %4, %7 offset:1024 sc1\n\tglobal_store_dwordx4 %2, %5, %8 sc1\n\tglobal_store_dwordx4 %2, %6, %8 offset:1024 sc1\n\ts_nop 1\n1:"
                 :: "s"(c), "n"(C), "v"(voff), "v"(v[0]), "v"(v[1]), "v"(v[2]), "v"(v[3]), "s"(s0), "s"(s1) : "scc", "memory"); }
template <int C> __device__ __forceinline__ void ld4_if(int c, unsigned voff, u32x4* v, const char* s0) {
    asm volatile(IF_("%4", "%5") "global_load_dwordx4 %0, %6, %7 nt\n\tglobal_load_dwordx4 %1, %6, %7 offset:1024 nt\n\tglobal_load_dwordx4 %2, %6, %7 offset:2048 nt\n\tglobal_load_dwordx4 %3, %6, %7 offset:3072 nt\n1:"
                 : "+v"(v[0]), "+v"(v[1]), "+v"(v[2]), "+v"(v[3]) : "s"(c), "n"(C), "v"(voff), "s"(s0) : "scc", "memory"); }
template <int C> __device__ __forceinline__ void ld8_if(int c, unsigned voff, u32x2* v, const char* s0, const char* s1) {
    asm volatile(IF_("%8", "%9") "global_load_dwordx2 %0, %10, %11 offset:-4096 nt\n\tglobal_load_dwordx2 %1, %10, %11 offset:-2048 nt\n\tglobal_load_dwordx2 %2, %10, %11 nt\n\tglobal_load_dwordx2 %3, %10, %11 offset:2048 nt\n\t"
                 "global_load_dwordx2 %4, %10, %12 offset:-4096 nt\n\tglobal_load_dwordx2 %5, %10, %12 offset:-2048 nt\n\tglobal_load_dwordx2 %6, %10, %12 nt\n\tglobal_load_dwordx2 %7, %10, %12 offset:2048 nt\n1:"
                 : "+v"(v[0]), "+v"(v[1]), "+v"(v[2]), "+v"(v[3]), "+v"(v[4]), "+v"(v[5]), "+v"(v[6]), "+v"(v[7]) : "s"(c), "n"(C), "v"(voff), "s"(s0), "s"(s1) : "scc", "memory"); }
template <int E> __device__ __forceinline__ void sy_post(unsigned myaddr) { asm volatile("ds_write_b32 %0, %0 offset:%1" :: "v"(myaddr), "n"(E * 64) : "memory"); }
template <int E, int SL = 1> __device__ __forceinline__ unsigned sy_wait(unsigned addr) {
    unsigned v;
    asm volatile("s_branch 2f\n1:\n\ts_sleep %3\n2:\n\tds_read_b32 %0, %1 offset:%2\n\ts_waitcnt lgkmcnt(0)\n\tv_cmp_eq_u32 vcc, 0, %0\n\ts_cbranch_vccnz 1b"
                 : "=&v"(v) : "v"(addr), "n"(E * 64), "n"(SL) : "vcc", "memory");
    return v;
}
template <int OFF, class T> __device__ __forceinline__ void dsrtr_(T& dst, unsigned addr) { asm volatile("ds_read_b64_tr_b16 %0, %1 offset:%2" : "=v"(dst) : "v"(addr), "n"(OFF)); }

constexpr size_t WS_H = 0, WS_KVT = (size_t)10 << 20, WS_ZT = (size_t)15 << 20, WS_E = (size_t)20 << 20, WS_FLAG = WS_E + (2048 << 10);
#define HP 65536
#define KP 8192
#define ZP 4096
constexpr int FL_KV = 0, FL_Z = 65536, FL_H = 131072, FL_EPOCH = 196608;
constexpr unsigned SPIN_CAP = 1u << 17;
#define ABL 0
#define CSPLIT 1
#define MERGE 3
#define CB 13
#define PAUSE 0
#define QLATE 0
#define CVPRIO 0
#define STG_SEL (idx & 3)
#define SP0 3
#define SDP 1
#define LP0 3
#define LDP 2
#define KVS 5
#define KVU 3
#define QIT 1
#define ZB 3
#define OS 2
#define MPRIO 2

#define ROT 0x4042
#define R2 0x41
#define CPF 0
#define PRE 4
#define PRO 9
#define G1NT 0
#define G1NTS 16
#define TMIX 0
#define TLBT 0
#define CDEF 0
#define CVLATE 1
#define FPRIO 3
#define YS 0
#define DP0 1
#define DP1 5
#define AWB 1
#define PK 0
#define PU 13
#define PC0 13
#define PC1 14
#define PC2 15
#define PC3 16
#define PQ 0
#define SPF 0
#define SPH 0
#define NB1 4
#define EP 1
#define ESL 0
#define CFB 0
#define P1 1
#define P0 1
#define BKE 0
#define EALL 0
#define EREP 0
#define TOUCH 0
#define FILLB 0
#define CPOST 1
#define CEARLY 0
#define NOOWN 0
#define PFIRST 0
#define ZB0 0
#define CATCH 4
#define PWE 0
#define SWAP 1
#define KVM 0x0FE
#define KFD 3
#define ZW 2
#define ZX 1
#define ZP0 3
#define BARM 0x22220
#define NR 6
#define WPS 2
#define CD0 3
#define AWX 8
#define VBM 0
#define ZOWN 1
#define ZPS 4
#define XAB 0
#define QM 1
#define OSG 0
#define KPL 0xFE
#define KPRIO 0
#define GPRIO 1
#define PST 0x7654
#define PLT 0xA864
#define LC 0
#define PRE2 PRE
#define KVD 0
#define PAIR 1
#define G2P1 0
#define EPRIO 0
#define LSW 1
#define EGX 28
#define CR1 0
#define CR2 0
#define CR3 0
#define CRL (2 * NR - CR1 - CR2 - CR3)
#define UE 1
#define UPRIO 0
#define AMF 7
#define M1 1
#define W2 1
#define W1 0
#define NBK 3
#define XB 4
#define KFB 9
#define APRE 0
#define CB0 0
#define AP0 8
#define HTSE(i)
#define KAT 2
#define SD 2
#define KV3 0x088888
#define KOWN 1
#define AWN 8
#define CRATE 2
#define ZSB 0
#define ZFB 1
#define ZSL 0
#define OSL 0
constexpr int it_cnt(int p, int k, int last) {
    const int pos[8] = {PK, PU, PC0, PC1, PQ, PU - 1, PC2, PC3}, n[8] = {4, 4, 4, 4, 8, 1, 4, 4};
    int c = 0;
    for (int i = 0; i < 8; ++i) if (pos[i] <= last && (pos[i] > p || (pos[i] == p && i > k))) c += n[i];
    return c;
}
constexpr int nib(int t, int c) { return (t >> (4 * c)) & 15; }
constexpr bool nib_any(int t, int v) { return nib(t, 0) == v || nib(t, 1) == v || nib(t, 2) == v || nib(t, 3) == v; }
constexpr int kv_wait(int late) {
    int t = 0;
    for (int c = 0; c < 4; ++c) t |= ((nib(PLT, c) >= nib(PST, c) && nib(PLT, c) < nib(PST, c) + KFD) == late ? nib(PST, c) + KFD : 15) << (4 * c);
    return t;
}
constexpr int G1STAGE = 32768;
constexpr int g1_slot(int s) { return (NB1 == 5 ? (s + 3) % 5 : s & 3) * G1STAGE; }
constexpr int L_KS = NB1 == 5 ? 65536 : 131072, L_VS = L_KS + 16384;
constexpr int L_TS = 3 * G1STAGE, L_TR = L_TS + 3072;
constexpr int CST = 4288;
constexpr int UOFF = 2 * CST + 128, UPL = 2304;
constexpr int CHSZ = UOFF + 2 * UPL + 32;
constexpr int L_AGG = 4 * CHSZ, L_ES = L_AGG + 15 * 512, L_SY = L_ES + 2048;
constexpr int L_HT = 98304;
constexpr int L_C = 98304, G2STAGE = 16384;
constexpr int L_Y = 16384;
static_assert(L_SY + 384 <= 65536 && L_AGG >= 49152 && UOFF % 16 == 0 && CHSZ % 256 == 32, "lds");

__device__ __forceinline__ int img_off(int r, int c) { return (r >> 1) * 256 + ((((r & 1) * 8 + c) ^ ((r >> 1) & 15)) << 4); }

constexpr int g1_upto(int g) {
    int n = 4 * NBK;
    for (int t = SD; t <= g; ++t) {
        int sb = (t + 1) / 2, sub = (t + 1) % 2, allowed = sb ? 4 * (sb + 3) + 2 * (sub + 1) : 12;
        if (allowed > 64) allowed = 64;
        n = n + CATCH < allowed ? n + CATCH : allowed;
    }
    return n;
}
constexpr int g1_cnt(int s) { return s >= KAT && s < NBK ? g1_upto(2 * s - 2) - 4 * NBK : g1_upto(2 * s - 2) - 4 * (s + 1) + (s < NBK ? 4 : 0); }
static_assert(KAT >= 1 && KAT <= NBK, "schedule");
static_assert(g1_upto(28) == 64, "schedule");
constexpr int AW = AWN;
constexpr int g2_place(int s) { return s < 5 ? L_C + ((s + 2) & 3) * G2STAGE : s < 7 ? (s - 1) * G2STAGE : s < 10 ? (9 - s) * G2STAGE : g2_place(s - 9); }
constexpr bool g2_bar(int g) { return (BARM >> g) & 1; }
struct G2Plan { int upto[33], wr[33], ald[64][2], seq[164], use[64]; };
constexpr G2Plan g2_plan() {
    G2Plan p = {};
    int nc = 14 + 2 * NR, nw = 0, nx = 4, lastb = 0, n = 0;
    for (int i = 0; i < 164; ++i) p.seq[i] = 1000;
    for (; nx < 4 + AW; ++nx) p.seq[nx] = ++n;
    for (int t = 0; t < 32; ++t) {
        p.upto[t] = nc; p.wr[t] = nw;
        if (t < 2) continue;
        if (g2_bar(t)) lastb = t;
        const int w = AW + nw < AWX ? AW + nw : AWX;
        for (int f = 2 * t; f < 2 * t + 2; ++f) {
            p.use[f] = n;
            if (nc < 32 && nc == p.upto[t] + (f & 1) && (f & 1) < CRATE && t >= CD0 && lastb >= (nc < 20 ? 1 : (nc | 1) - 18)) p.seq[100 + nc++] = ++n;
            for (int k = 0; k < 2; ++k) {
                p.ald[f][k] = -1;
                if (nx < 64 && nx - f - k < w) { p.ald[f][k] = nx; p.seq[nx++] = ++n; }
            }
        }
        for (int k = 0; k < WPS; ++k) if (nw < 2 * NR && lastb >= (nw < 6 ? 1 : (nw | 1) - 4)) ++nw;
    }
    p.upto[32] = nc; p.wr[32] = nw;
    return p;
}
constexpr G2Plan G2P = g2_plan();
constexpr int g2_upto(int g) { return G2P.upto[g + 1]; }
constexpr int g2_wr(int g) { return G2P.wr[g + 1]; }
constexpr int g2_ald(int f, int k) { return G2P.ald[f][k]; }
constexpr int g2_cnt(int id, int at) { return G2P.use[at] - G2P.seq[id]; }
constexpr int g2_cover(int g) { int s = (g + 1) / 2; while (s < 15 && !g2_bar(2 * s + 1)) ++s; return s; }
static_assert(BARM >> 17 && g2_cover(3) < 7 && CRATE <= 2, "the epilogue needs stage 8 behind a barrier");

constexpr int cv_m(int o) { return (o & 1) ? 7 - (o >> 1) : o >> 1; }
constexpr int cv_first(int o) { int p = 0; for (int i = 0; i < o; ++i) p += 8 - cv_m(i); return p; }
constexpr int cv_grp(int p) { int o = 0; while (cv_first(o + 1) <= p) ++o; return o; }
constexpr int cv_read(int p) {
    int slot[40] = {};
    for (int i = 0; i < 40; ++i) slot[i] = -1;
    slot[0] = 2 * 3 + 1;
    for (int o = 0; o < 8; ++o) {
        int q = cv_first(o + 1) - 1;
        for (int h = 0; h < 2; ++h) { while (slot[q] >= 0) ++q; slot[q] = 2 * (o + 4) + h; }
    }
    const int bs[8] = {1, 3, 5, 11, 12, 13, 19, 21};
    for (int jb = 0; jb < 8; ++jb) slot[bs[jb]] = 32 + jb;
    return slot[p];
}
static_assert(cv_read(35) == 22 && cv_read(36) == 23 && cv_read(37) == -1 && cv_read(10) == 11 && cv_read(15) == 12 && cv_read(18) == 15 && cv_read(24) == 17, "conv schedule");
constexpr int cv_wait(int o) {
    int n = 0;
    for (int q = cv_first(o) - 1;; --q) {
        const int rd = cv_read(q < 0 ? q + 36 : q);
        if (rd == 2 * (o + (q < 0 ? 8 : 0)) + 1) return n;
        if (rd >= 0) ++n;
    }
}

struct Args {
    const char *q, *k, *v, *sk, *ssmk, *ssmD, *x, *lre, *lim, *bre, *bim, *cre, *cim, *logdt, *dskip;
    char *out, *ws;
};

typedef __attribute__((ext_vector_type(8))) unsigned long long u64x8;
__global__ __launch_bounds__(768, 3) void fused(Args) {
    __shared__ __attribute__((aligned(1024))) char shm[163840];
    TS0a();
    u64x8 ka0, ka1; unsigned long long ka2;
    const char* pc0;
    int code_n;
    asm volatile("s_load_dwordx16 %0, %5, 0x0\n\ts_load_dwordx16 %1, %5, 0x40\n\ts_load_dwordx2 %2, %5, 0x80\n\t"
                 "s_getpc_b64 %3\n.Lfk_code_0:\n\ts_mov_b32 %4, .Lfk_code_a-.Lfk_code_0-4\n\ts_max_i32 %4, %4, .Lfk_code_b-.Lfk_code_0-4"
                 : "=&s"(ka0), "=&s"(ka1), "=&s"(ka2), "=&s"(pc0), "=&s"(code_n) : "s"(__builtin_amdgcn_kernarg_segment_ptr()) : "scc");
    const int tid = threadIdx.x, lane = tid & 63, wraw = __builtin_amdgcn_readfirstlane(tid >> 6);
    const int wid = !SWAP ? wraw : wraw < 4 ? wraw + 8 : wraw - 4;
    const int b = blockIdx.x, xcd = b & 7, idx = b >> 3;
    asm volatile("s_cmp_lt_u32 %0, 8\n\ts_cbranch_scc1 1f\n\ts_setprio 3\n\ts_cmp_lg_u32 %0, 8\n\ts_cbranch_scc1 1f\n\t"
                 "v_lshl_add_u32 v161, %1, 7, %2\n\tv_min_i32 v161, %3, v161\n\ts_mov_b64 exec, 0xffff\n\tglobal_load_dword v161, v161, %4\n\ts_mov_b64 exec, -1\n1:"
                 :: "s"(wid), "v"(lane), "s"(idx * 2048), "s"(code_n), "s"(pc0) : "scc", "memory", "v161");
    TS_DECL
    const int tm = (xcd >> 1) * 4 + (idx >> 3), tn = (xcd & 1) * 8 + (idx & 7);
    const int wm = wid & 3, wn = (wid >> 2) & 1, l31 = lane & 31, lh = lane >> 5;
    const unsigned lb = (unsigned)(uintptr_t)shm;
    const unsigned l16 = lane * 16;
    const int l15 = lane & 15, g4 = lane >> 4, sq = 2 * wn + (wm & 1), st = sq * 16 + l15, run = (wm >> 1) * 4 + g4;
    unsigned kvv[4];
    const int trow = idx * 64 * 2048 + xcd * 256;
    if (wid < 8) {
        const int v = l15 ^ g4, hi = v >> 3;
        const int r0 = wn ? tn * 64 + (wm >> 1) * 32 : tm * 128 + (wm >> 1) * 64 + (wm & 1) * 4;
        const unsigned d0 = (r0 + ((g4 * (wn ? 2 : 9)) & (wn ? 6 : 17))) * 2048 + (v & 7) * 16, odd = wn ? 11 : 14;
        kvv[0] = d0 + (hi << odd); kvv[2] = d0 + ((hi ^ 1) << odd) + (wn ? 32768 - 2048 : 4096 - 2048);
        kvv[1] = (kvv[0] ^ 64) + (wn ? 16384 - 1024 : 65536 - 1024); kvv[3] = (kvv[2] ^ 64) + (wn ? 16384 - 1024 : 65536 - 1024);
    } else {
        kvv[0] = trow + (idx ? -4 * 2048 : 0) + g4 * 2048 + wm * 64 + l15 * 4;
        asm volatile("v_writelane_b32 %0, 0, 0\n\tv_writelane_b32 %0, 4, 1\n\tv_writelane_b32 %0, %1, 2" : "+v"(kvv[0]) : "s"((xcd * 64 + idx * 2 + (wm >> 1)) * 4));
        kvv[1] = (xcd * 128 + wm * 32 + l31) * 8;
        kvv[2] = kvv[3] = (tn * 64 + lane) * 2;
    }
    unsigned g1dst = lb + wid * 4096;
    int rot7 = ((((ROT >> 12) & 15) * (idx & 7) + ((ROT >> 8) & 15) * (idx >> 3) + ((ROT >> 4) & 15) * (xcd >> 1) + (ROT & 15) * (xcd & 1)) & 15) << 7;
    asm volatile("s_waitcnt lgkmcnt(0)" : "+s"(ka0), "+s"(ka1), "+s"(ka2), "+s"(g1dst), "+s"(rot7), "+v"(kvv[0]), "+v"(kvv[1]), "+v"(kvv[2]), "+v"(kvv[3]));
    Args a;
    a.q = (const char*)ka0[0]; a.k = (const char*)ka0[1]; a.v = (const char*)ka0[2]; a.sk = (const char*)ka0[3]; a.ssmk = (const char*)ka0[4]; a.ssmD = (const char*)ka0[5];
    a.x = (const char*)ka0[6]; a.lre = (const char*)ka0[7]; a.lim = (const char*)ka1[0]; a.bre = (const char*)ka1[1]; a.bim = (const char*)ka1[2]; a.cre = (const char*)ka1[3];
    a.cim = (const char*)ka1[4]; a.logdt = (const char*)ka1[5]; a.dskip = (const char*)ka1[6]; a.out = (char*)ka1[7]; a.ws = (char*)ka2;
    char* const ws = a.ws;
    const char* const flg = ws + WS_FLAG;
    TS(26);
    const char* g1src = wn ? ((wm & 1) ? a.bim : a.bre) : a.x;
    asm volatile("s_mov_b32 m0, %[m]\n\ts_cmp_gt_u32 %[w], 7\n\ts_cbranch_scc1 1f\n\t"
                 "global_load_lds_dwordx4 %[k0], %[g]" FLX(DF) "\n\tglobal_load_lds_dwordx4 %[k1], %[g] offset:1024" FLX(DF) "\n\t"
                 "global_load_lds_dwordx4 %[k2], %[g] offset:2048" FLX(DF) "\n\tglobal_load_lds_dwordx4 %[k3], %[g] offset:3072" FLX(DF) "\n\t"
                 "s_branch 2f\n1:\n\tglobal_load_ushort v164, %[k2], %[c1]\n\tglobal_load_ushort v165, %[k2], %[c2]\n\tglobal_load_ushort v166, %[k2], %[c3]\n\t"
                 "v_mov_b64 v[162:163], %[kv]\n\ts_mov_b64 exec, 7\n\tv_mov_b64 v[162:163], %[d]\n\ts_mov_b64 exec, 3\n\tv_mov_b64 v[162:163], %[e]\n\ts_mov_b64 exec, -1\n\t"
                 "v_mad_u64_u32 v[162:163], vcc, %[k0], 1, v[162:163]\n\tglobal_load_dword v160, v[162:163], off sc1\n\tglobal_load_dwordx2 v[162:163], %[k1], %[sk]\n2:"
                 :
                 : [w] "s"(wid), [c1] "s"(a.logdt), [c2] "s"(a.lre), [c3] "s"(a.lim), [d] "s"(a.ssmD), [e] "s"(flg + FL_EPOCH), [sk] "s"(a.sk),
                   [k0] "v"(kvv[0]), [k1] "v"(kvv[1]), [k2] "v"(kvv[2]), [k3] "v"(kvv[3]), [kv] "s"(a.k), [g] "s"(g1src + rot7), [m] "s"(g1dst)
                 : "scc", "vcc", "memory", "v160", "v161", "v162", "v163", "v164", "v165", "v166");
    TS0b();
    TS(27);
    if (P1) BAR();
#define SLICE_TABLE() unsigned slo, shi; { int ix = idx; asm volatile("" : "+s"(ix)); \
        const int rot2 = (((R2 >> 4) & 15) * (ix >> 3) + (R2 & 15) * (xcd >> 1)) & 15, es = (tn - rot2) & 15; \
        const unsigned long long id = 0xFEDCBA9876543210ull, seq = rot2 ? (id >> (4 * rot2)) | (id << (64 - 4 * rot2)) : id; \
        const unsigned long long lo = (1ull << (4 * es)) - 1, tab = ((seq & lo) << 4) | (seq & ~((lo << 4) | 15)) | tn; \
        slo = (unsigned)tab; shi = (unsigned)(tab >> 32); asm volatile("" : "+s"(slo), "+s"(shi)); }
#define SLICE(st) ((((st) < 8 ? slo : shi) >> (4 * ((st) & 7))) & 15)
    typedef __attribute__((ext_vector_type(4))) short v4i16;
    int role = wid;
    asm volatile("" : "+s"(role));

    if (role >= 8) {
        int lane;
        asm volatile("v_mbcnt_lo_u32_b32 %0, -1, 0\n\tv_mbcnt_hi_u32_b32 %0, -1, %0" : "=v"(lane));
        __builtin_assume(lane >= 0 && lane < 64);
        const int l31 = lane & 31, lh = lane >> 5, l15 = lane & 15, g4 = lane >> 4;
        const unsigned l16 = lane * 16;
        const int chn = xcd * 128 + idx * 4 + wm;
        const unsigned cb = lb + wm * CHSZ;
        const int r = l15;
        const unsigned fpub = KOWN ? (xcd * 32 + l31) * 256 + idx * 8 : xcd * 256 + idx * 8, fpoll = KOWN ? (xcd * 32 + idx) * 256 + l31 * 8 : xcd * 256 + l31 * 8;
        u32x2 fl; u32x4 sw[4], uu[4];
        unsigned tch = 0;
        if (TOUCH) {
            const int ln = wm * 4 + (lane & 3);
            const unsigned fo = (unsigned)(WS_FLAG - WS_E) + (wm == 0 ? FL_H + (tm * 16 + tn) * 128 : (wm == 1 ? FL_KV : FL_Z) + xcd * 256 + (lane & 1) * 128);
            const unsigned o = (lane & 4) && (TOUCH & 2) ? fo : ((tm * 2 + (ln >> 3)) * 1024 + tn * 64) * 16 + (ln & 7) * 128;
            asm volatile("s_mov_b64 exec, 0xff\n\tglobal_load_dword %0, %1, %2\n\ts_mov_b64 exec, -1" : "=&v"(tch) : "v"(o), "s"(ws + WS_E) : "memory");
        }
        SLICE_TABLE();
        TS(1);
        unsigned sm, cl[3]; u32x2 kw0;
        asm volatile("s_waitcnt vmcnt(0)" : "={v160}"(sm), "={v[162:163]}"(kw0), "={v164}"(cl[0]), "={v165}"(cl[1]), "={v166}"(cl[2]) :: "memory", "v161");
        TS(22);
        const unsigned hal = take(sm);
        const unsigned tagA = __builtin_amdgcn_readlane(hal, 0) + 1, tagB = __builtin_amdgcn_readlane(hal, 1) + (tagA == 0 ? 1u : 0u);
        const u32x2 tag = {tagA, tagB};
        f32x2 L1, C1;
        {
            const float dt = rbf(expf(bf2f(take(cl[0]))));
            const float lr = bf2f(take(cl[1])), li = bf2f(take(cl[2]));
            const float e = expf(dt * lr);
            float sn, cs;
            sincosf(dt * li, &sn, &cs);
            L1 = f32x2{e * cs, e * sn};
            const float nr = L1[0] - 1.f, ni = L1[1], den = lr * lr + li * li;
            C1 = f32x2{2.f * ((nr * lr + ni * li) / den), 2.f * ((ni * lr - nr * li) / den)};
            TIE(L1); TIE(C1);
        }

        f32x2 p[11];
        p[0] = L1;
        if (PWE) {
#pragma unroll
            for (int i = 1; i <= 10; ++i) { p[i] = cmul4(p[i - 1], p[i - 1]); TIE(p[i]); }
        }
        {
            if (!PWE) {
#pragma unroll
                for (int i = 1; i <= 10; ++i) p[i] = cmul4(p[i - 1], p[i - 1]);
            }
            const f32x2 one = {1.f, 0.f}, zero = {0.f, 0.f};
            const unsigned ts = lb + L_TS + l16, tr = lb + L_TR + wm * 2048 + l16;
            if (wm == 0) {
                *LDSP(f32x4, ts) = f32x4{L1[0], L1[1], C1[0], C1[1]};
                *LDSP(f32x4, ts + 1024) = f32x4{p[3][0], p[3][1], p[4][0], p[4][1]};
                *LDSP(f32x4, ts + 2048) = f32x4{p[6][0], p[6][1], __uint_as_float(tagA), __uint_as_float(tagB)};
            }
            f32x2 Q[2], V[2], W[2];
            Q[0] = cmul4(cmul4(C1, (wm & 1) ? p[5] : one), (wm & 2) ? p[6] : one); Q[1] = cmul4(Q[0], p[4]);
            V[1] = (wm & 1) ? one : p[5]; V[0] = cmul4(V[1], p[4]);
            W[1] = cmul4((wm & 1) ? one : p[9], (wm & 2) ? one : p[10]); W[0] = cmul4(W[1], p[8]);
#pragma unroll
            for (int i = 0; i < 2; ++i) {
                const f32x2 Q8 = cmul4(Q[i], p[3]), U = 4 * wm + 2 * i >= 16 - tm ? cmul4(W[i], p[7]) : zero, Wz = 4 * wm + 2 * i + 1 >= 16 - tm ? W[i] : zero;
                *LDSP(f32x4, tr + i * 1024) = f32x4{Q[i][0], Q8[0], Q[i][1], Q8[1]};
                *LDSP(f32x4, tr + i * 1024 + 8192) = f32x4{Wz[0], Wz[1], U[0], U[1]};
                *LDSP(f32x2, tr + i * 1024 + 16384) = V[i];
            }
        }
        if (CB0 == 0) { WAIT_L(0); BAR(); HTSE(1); }
        const unsigned dskl = (__builtin_amdgcn_readlane(hal, 2) >> ((wm & 1) * 16)) & 0xffffu;
        u32x4 kvo[4];
        u32x2 qv[8];
        const char* krow = a.ssmk + (size_t)chn * 4096;
        const char* qs = a.q + (size_t)(idx * 64 + wm * 16) * 2048 + xcd * 256;
        const char* kvd = ws + WS_KVT + (size_t)(xcd * 8 + wm * 2) * 65536 + idx * 2048;
        const int pl = (PLT >> ((STG_SEL) * 4)) & 15;
        const unsigned qo = lh * 8192 + l31 * 8 + 4096;
        auto hb = [&](auto nc) {
            constexpr int nb = decltype(nc)::value;
            if constexpr (nb == KFB) { WAIT_V(12); int lm = (1 << KFB) - (2 << (KAT + __builtin_popcount(KV3))); ties_(lm); wait_if<0, 0>((lm >> pl) & 1); }
            BAR();
            if constexpr (nb == KFB) flag_if<0, KOWN ? -1 : 1>(wm, fpub, tag, flg + FL_KV);
            if constexpr (nib_any(PLT, nb)) { ld4_if<nb>(pl, l16, sw, krow); ld8_if<nb>(pl, qo, qv, qs, qs + 16384); }
        };
#define HB(n) hb(std::integral_constant<int, (n) + NBK>{})
        {
            unsigned kw[2][4], ta[2], th[2];
            u32x2 hm[2];
#pragma unroll
            for (int j = 0; j < 2; ++j) {
                const unsigned x = take(kw0[j]);
                const auto y = __builtin_amdgcn_permlane16_swap(x, x, false, false);
#pragma unroll
                for (int p = 0; p < 2; ++p) { kw[p][2 * j] = y[p] << 16; kw[p][2 * j + 1] = y[p] & 0xffff0000u; }
            }
#pragma unroll
            for (int p = 0; p < 2; ++p) {
                unsigned h[3];
#pragma unroll
                for (int j = 0; j < 3; ++j) h[j] = (__shfl(hal, p * 8 + (r >> 1) + 16 * (j + 1)) >> ((r & 1) * 16)) & 0xffffu;
                hm[p] = idx ? u32x2{h[0] << 16, h[1] | (h[2] << 16)} : u32x2{0u, 0u};
                const int q = r >> 2, pp = r & 3;
                ta[p] = lb + L_KS + (8 * g4 + q) * 256 + (((wm * 4 + p * 2 + (pp >> 1)) ^ (2 * (q | ((g4 & 1) << 2)))) << 4) + (pp & 1) * 8;
                th[p] = (ta[p] ^ 128) - 1024;
                TIE(hm[p]); TIE(ta[p]); TIE(th[p]);
#pragma unroll
                for (int j = 0; j < 4; ++j) TIE(kw[p][j]);
            }
            TS(21);
            if (CB0 == 1) { WAIT_L(0); BAR(); HTSE(1); }
#pragma unroll
            for (int i = 0; i < 4; ++i) asm volatile("" : "=v"(sw[i]), "=v"(qv[2 * i]), "=v"(qv[2 * i + 1]));
            static_for<1, KAT + 1>(hb);
            TS(2);
            u32x2 kt[4][3], vt[4][2];
#pragma unroll
            for (int u = 0; u < 4; ++u) {
                const int p = u >> 1, o = (u & 1) * 8192;
                auto tr = [&](unsigned ad) -> u32x2 { return __builtin_bit_cast(u32x2, __builtin_amdgcn_ds_read_tr16_b64_v4i16(LDSP(v4i16, ad))); };
                kt[u][0] = tr(th[p] + o); kt[u][1] = tr(ta[p] + o); vt[u][0] = tr(ta[p] + o + 16384); kt[u][2] = tr(ta[p] + o + 1024); vt[u][1] = tr(ta[p] + o + 17408);
                SB();
            }
            TS(14);
            __builtin_amdgcn_s_setprio(KPRIO);
            static_for<0, 4>([&](auto uc) {
                constexpr int u = decltype(uc)::value, p = u >> 1, oh = u & 1;
                const u32x2 k0 = oh == 0 && g4 == 0 ? hm[p] : kt[u][0];
                unsigned t0[8], t1[8], e[7];
#define K3(n) do { if constexpr ((KV3 >> (6 * u + n)) & 1) hb(std::integral_constant<int, KAT + 1 + __builtin_popcount(KV3 & ((1 << (6 * u + n)) - 1))>{}); } while (0)
                kv4a(t0, e, k0[0], k0[1], kt[u][1][0], kt[u][1][1], kw[p]); K3(0); kv4b(t0, e, kw[p]); K3(1); kv4c(t0, vt[u][0][0], vt[u][0][1]); K3(2);
                kv4a(t1, e, kt[u][1][0], kt[u][1][1], kt[u][2][0], kt[u][2][1], kw[p]); K3(3); kv4b(t1, e, kw[p]); K3(4); kv4c(t1, vt[u][1][0], vt[u][1][1]);
                kvo[u] = u32x4{t1[2], t1[0], t0[2], t0[0]};
                GST_B128_SC1(l16, kvo[u], kvd + (u >> 1) * 65536, (u & 1) * 1024);
                K3(5);
#undef K3
            });
            hb(std::integral_constant<int, KAT + 1 + __builtin_popcount(KV3)>{});
            __builtin_amdgcn_s_setprio(3);
        }
        TS(3);
        const char* usrc = ws + WS_KVT + (size_t)(xcd * 8 + (idx >> 2)) * 65536 + wm * 16384 + (idx & 3) * 64;
        const unsigned uo = (lane >> 2) * 256 + (lane & 3) * 16;
        const char* cs = ((wm & 2) ? a.cim : a.cre) + (size_t)(tn * 64 + (wm & 1) * 2) * 2048;
        unsigned co[4];
#pragma unroll
        for (int i = 0; i < 4; ++i) {
            const int pair = i * 4 + g4, v = r ^ pair, row = pair * 2 + (v >> 3);
            co[i] = ((row & 15) * 4 + (row >> 4)) * 2048 + (v & 7) * 16 - i * 1024;
        }
        bool kv_ok = false;
#pragma unroll
        for (int it = 0; it < 4; ++it) asm volatile("" : "=v"(uu[it]));
        auto cst = [&](int place, int slice) { const char* s = cs + (slice << 7); DMA(lb + place + wm * 4096, co[0], s); dmao_<1024>(co[1], s); dmao_<2048>(co[2], s); dmao_<3072>(co[3], s); };
        auto items = [&](auto pc) {
            constexpr int p = decltype(pc)::value;
            if constexpr (PU == p) {
                WAIT_V(it_cnt(PU - 1, 5, PU - 1)); TIE(fl);
                kv_ok = __all(fl[0] == tagA && fl[1] == tagB);
                const char* us = kv_ok ? usrc : a.x;
                for (int it = 0; it < 4; ++it) GLD_B128_SC1(uu[it], uo, us + it * 4096, 0);
            }
            if constexpr (PC0 == p) cst(g2_place(0), SLICE(0));
            if constexpr (PC1 == p) cst(g2_place(1), SLICE(1));
            if constexpr (PU == p + 1) GLD_B64_SC1(fl, fpoll, flg + FL_KV, 0);
            if constexpr (PC2 == p) cst(g2_place(5), SLICE(5));
            if constexpr (PC3 == p) cst(g2_place(6), SLICE(6));
        };
#define ITEMS(p) items(std::integral_constant<int, p>{})
        static_assert(KFB > KAT + 1 + __builtin_popcount(KV3) && KV3 < (1 << 23) && KFB <= 13 && KFB <= PC0 && KFB < PU - 1 && nib(PLT, 0) >= 1 && nib(PLT, 3) < PU - 1, "schedule");
        static_for<KAT + 2 + __builtin_popcount(KV3), 15>([&](auto nc) { hb(nc); if constexpr (decltype(nc)::value < 14) items(nc); });
        const unsigned ud = lb + (lane & 3) * CHSZ + UOFF + ((lane >> 2) & 1) * UPL + (16 + wm * 32 + (lane >> 3)) * 16;
        auto zero = [&]() {
            const u32x4 z4 = {0, 0, 0, 0};
            if (lane < 24) *LDSP(u32x4, lb + L_SY + l16) = z4;
            if (lane < 56) *LDSP(u32x4, lb + L_AGG + wm * 896 + l16) = z4;
            *LDSP(u32x4, lane < 32 ? cb + ((lane >> 3) & 1) * CST + (lane & 7) * 16 : cb + UOFF + (g4 & 1) * UPL + r * 16) = z4;
        };
        auto fill = [&]() {
#pragma unroll
            for (int it = 0; it < 4; ++it) TIE(sw[it]);
#pragma unroll
            for (int it = 0; it < 4; ++it) {
                const unsigned q = cb + 128 + it * 1024 + l16;
                const unsigned d0 = sw[it][0], d1 = sw[it][1], d2 = sw[it][2], d3 = sw[it][3];
                const unsigned o1 = it ? __builtin_amdgcn_readlane(sw[it ? it - 1 : 0][3], 63) : 0u;
                const unsigned x1 = __builtin_amdgcn_update_dpp(o1, d3, 0x138, 0xf, 0xf, false);
                *LDSP(u32x4, q) = sw[it];
                *LDSP(u32x4, q + CST) = u32x4{__builtin_amdgcn_alignbyte(d0, x1, 2), __builtin_amdgcn_alignbyte(d1, d0, 2), __builtin_amdgcn_alignbyte(d2, d1, 2), __builtin_amdgcn_alignbyte(d3, d2, 2)};
            }
        };
        auto uput = [&]() {
            WAIT_V(it_cnt(PU, 1, UE ? 15 : 16));
#pragma unroll
            for (int it = 0; it < 4; ++it) TIE(uu[it]);
            if (!kv_ok) {
                TSE(23);
                for (unsigned spin = 0; spin < SPIN_CAP; ++spin) {
                    GLD_B64_SC1(fl, fpoll, flg + FL_KV, 0);
                    WAIT_V(0); TIE(fl);
                    if (__all(fl[0] == tagA && fl[1] == tagB)) break;
                    __builtin_amdgcn_s_sleep(8);
                }
#pragma unroll
                for (int it = 0; it < 4; ++it) GLD_B128_SC1(uu[it], uo, usrc + it * 4096, 0);
                WAIT_V(0);
#pragma unroll
                for (int it = 0; it < 4; ++it) TIE(uu[it]);
            }
#pragma unroll
            for (int it = 0; it < 4; ++it) *LDSP(u32x4, ud + it * 128) = uu[it];
        };
        static_assert(PU <= (UE ? 15 : 16), "u");
        ITEMS(14);
        if (UE) __builtin_amdgcn_s_setprio(UPRIO);
        if (UE >= 2) zero();
        if (UE == 2) fill();
        BAR();
        ITEMS(15);
        TS(4);
        if (UE < 2) zero();
        if (UE & 1) fill();
        if (UE) uput();
        WAIT_L(0);
        TS(5);
        BAR();
        __builtin_amdgcn_s_setprio(FPRIO);
        ITEMS(16);
        TS(11);
        const unsigned pmine = lb + L_SY + 32 + wm * 4, pall = lb + L_SY + 32 + (lane & 3) * 4;
        if (!UE) { fill(); TS(13); uput(); sy_post<0>(pmine); sy_wait<0>(pall); }
        if (!CPOST) { WAIT_V(0); sy_post<1>(pmine); }
        __builtin_amdgcn_s_setprio(CVPRIO);
        TS(6);
        f32x4 ca[8];
        {
            unsigned kb[4];
#pragma unroll
            for (int i = 0; i < 4; ++i) { kb[i] = cb + (~r & 1) * CST + 2 * (64 + 16 + r - 8 * g4 - 7 + (~r & 1)) + i * 1024; TIE(kb[i]); }
            unsigned ub = cb + UOFF + (g4 & 1) * UPL + (15 + r + (g4 >> 1)) * 16 - 64;
#pragma unroll
            for (int i = 0; i < 8; ++i) { ca[i] = f32x4{0, 0, 0, 0}; TIE(ca[i]); }
            s16x4 FA[4][2]; bf16x8 FB[2][8];
#define CV_A(s, o, e) dsr2_<16 * ((s) + ((o) >> 3)) + 128 * (cv_m((o) & 7) & 1) + 2 * (e)>(FA[(o) & 3][e], kb[cv_m((o) & 7) >> 1])
#define CV_B(s, jb) DSR128(FB[(s) & 1][jb], ub, 64 + 256 * (jb) - 32 * (s))
            static_for<0, 8>([&](auto ic) { constexpr int n = decltype(ic)::value; CV_B(0, n); CV_A(0, n >> 1, n & 1); });
            WAIT_L(0);
            auto body = [&]() {
                static_for<0, 72>([&](auto ic) {
                    constexpr int s = decltype(ic)::value / 36, p = decltype(ic)::value % 36, o = cv_grp(p), m = cv_m(o), jb = p - cv_first(o);
                    if constexpr (jb == 0) { WAIT_LB(cv_wait(o)); if constexpr (!(AMF & 1)) { TIE(FA[o & 3][0]); TIE(FA[o & 3][1]); } }
                    if constexpr (p == 0 && !(AMF & 1)) { for (int i = 0; i < 8; ++i) TIE(FB[s][i]); }
                    const bf16x8 fa = __builtin_shufflevector(FA[o & 3][0], FA[o & 3][1], 0, 1, 2, 3, 4, 5, 6, 7);
                    if constexpr (AMF & 1) mfma_(ca[jb + m], fa, FB[s][jb]); else
                    ca[jb + m] = __builtin_amdgcn_mfma_f32_16x16x32_bf16(fa, FB[s][jb], ca[jb + m], 0, 0, 0);
                    SB();
                    constexpr int rd = cv_read(p);
                    if constexpr (rd >= 32) CV_B(s + 1, rd - 32);
                    else if constexpr (rd >= 0) CV_A(s, rd >> 1, rd & 1);
                    SB();
                });
                for (int i = 0; i < 4; ++i) { kb[i] += 128; TIE(kb[i]); }
                ub -= 64;
            };
#pragma unroll 1
            for (int it = 0; it < QIT; ++it) body();
            {
                TS(19);
                if (CPOST) { WAIT_V(0); sy_post<1>(pmine); WAIT_L(0); }
                if (CVLATE) sy_wait<1>(lb + L_SY + (CVLATE == 2 ? wm + (lane & 1) * 4 : lane & 7) * 4);
                TS(20);
            }
#pragma unroll 1
            for (int it = QIT; it < 4; ++it) body();
            WAIT_L(0);
            if (AMF & 1) asm volatile("s_nop 7\n\ts_nop 7" : "+v"(ca[0]), "+v"(ca[1]), "+v"(ca[2]), "+v"(ca[3]), "+v"(ca[4]), "+v"(ca[5]), "+v"(ca[6]), "+v"(ca[7]));
#pragma unroll
            for (int i = 0; i < 4; ++i) { TIE(FA[i][0]); TIE(FA[i][1]); }
#pragma unroll
            for (int i = 0; i < 8; ++i) TIE(FB[0][i]);
#undef CV_A
#undef CV_B
        }
        TS(7);
        __builtin_amdgcn_s_setprio(3);
        constexpr int NBAR = __builtin_popcount(BARM & ~VBM);
        unsigned zpub = (xcd * 32 + l31) * 256 + idx * 8, zpoll = ZOWN ? (xcd * 32 + idx) * 256 + l31 * 8 : fpoll;
        {
            const float Dd = bf2f(dskl);
            const char* zd = ws + WS_ZT + (size_t)(xcd * 128 + wm * 32) * 4096 + idx * 128;
            u32x2 kvr[8];
#pragma unroll
            for (int jt = 0; jt < 8; ++jt) kvr[jt] = *LDSP(u32x2, cb + UOFF + (g4 >> 1) * UPL + (16 + 16 * jt + r) * 16 + (1 - (g4 & 1)) * 8);
#pragma unroll
            for (int jt = 0; jt < 8; ++jt) {
                const f32x4 y = ca[jt];
                const u32x2 kq = kvr[jt];
                *LDSP(u32x2, cb + (jt * 4 + (r >> 2)) * 144 + (r & 3) * 32 + g4 * 8) =
                    u32x2{pack2(rb1(y[0]) + rb1(bfhi(kq[1]) * Dd), rb1(y[1]) + rb1(bflo(kq[1]) * Dd)), pack2(rb1(y[2]) + rb1(bfhi(kq[0]) * Dd), rb1(y[3]) + rb1(bflo(kq[0]) * Dd))};
            }
            unsigned za = lb + (r >> 2) * CHSZ + (wm * 8 + (g4 >> 1)) * 144 + ((g4 & 1) * 2 + ((r >> 1) & 1)) * 32 + (r & 1) * 8, zo = (lane >> 3) * 4096 + (lane & 7) * 16;
            TIE(za); TIE(zo); TIE(zpub); TIE(zpoll);
            sy_post<3>(pmine); sy_wait<3, 0>(pall);
            v4i16 t[4][2];
#pragma unroll
            for (int it = 0; it < 4; ++it)
#pragma unroll
                for (int e = 0; e < 2; ++e) t[it][e] = __builtin_amdgcn_ds_read_tr16_b64_v4i16(LDSP(v4i16, za + it * 288 + e * 16));
#pragma unroll
            for (int it = 0; it < 4; ++it) { TIE(t[it][0]); TIE(t[it][1]); }
            if (ZSB) BAR();
#pragma unroll
            for (int it = 0; it < 4; ++it) { const bf16x8 f = __builtin_shufflevector(t[it][0], t[it][1], 0, 1, 2, 3, 4, 5, 6, 7); GST_B128_SC1(zo, f, zd + it * 32768, 0); }
        }
#pragma unroll
        for (int i = ZSB; i < ZFB; ++i) BAR();
        WAIT_V(0);
        sy_post<2>(pmine);
        if (wm == 0) {
            sy_wait<2>(pall);
            if (ZOWN) { if (lane < 32) GST_B64_SC1(zpub, tag, flg + FL_Z); }
            else if (lane == 0) GST_B64_SC1(fpub, tag, flg + FL_Z);
        }
        TS(8);
        unsigned qm[8][4];
        if (QM) {
#pragma unroll
            for (int n = 0; n < 8; ++n) {
                TIE(qv[n]);
                qm[n][0] = qv[n][0] & 0xffffu; qm[n][1] = qv[n][0] & 0xffff0000u; qm[n][2] = qv[n][1] & 0xffffu; qm[n][3] = qv[n][1] & 0xffff0000u;
                TIE(qm[n][0]); TIE(qm[n][1]); TIE(qm[n][2]); TIE(qm[n][3]);
            }
        }
#pragma unroll
        for (int i = ZFB; i < NBAR - 1; ++i) { BAR();
        }
        if (ZSL) __builtin_amdgcn_s_sleep(ZSL);
        asm volatile("s_cmp_lg_u32 %3, 0\n\ts_cbranch_scc1 1f\n\tglobal_load_dwordx2 %0, %1, %2 sc1\n1:" : "=&v"(fl) : "v"(zpoll), "s"(flg + FL_Z), "s"(wm) : "scc", "memory");
        if (NBAR > ZFB) BAR();
        if (wm == 0) {
            unsigned left = SPIN_CAP;
            asm volatile("s_branch 2f\n1:\n\ts_sleep %5\n\tglobal_load_dwordx2 %0, %2, %3 sc1\n2:\n\ts_waitcnt vmcnt(0)\n\tv_cmp_ne_u64 vcc, %4, %0\n\ts_cbranch_vccz 3f\n\ts_sub_u32 %1, %1, 1\n\ts_cbranch_scc0 1b\n3:"
                         : "+v"(fl), "+s"(left) : "v"(zpoll), "s"(flg + FL_Z), "s"(tag), "n"(ZPS) : "vcc", "scc", "memory");
            sy_post<4>(pmine);
        } else sy_wait<4, 2>(lb + L_SY + 32);
        u32x4 zv[4];
        {
            ITEMS(18);
            const char* zsrc = ws + WS_ZT + (size_t)(xcd * 128 + idx * 4 + wm) * 4096;
            static_for<0, 4>([&](auto ic) { constexpr int it = decltype(ic)::value; GLD_B128_SC1(zv[it], (l31 >> 2) * 512 + lh * 64 + (lane & 3) * 16, zsrc, it * 128); });
        }
        WAIT_V(0);
        TS(9);
#pragma unroll
        for (int it = 0; it < 4; ++it) TIE(zv[it]);
        if (!(XAB & 1)) {
        quad_swap<0>(zv[0], zv[1]); quad_swap<0>(zv[2], zv[3]);
        quad_swap<1>(zv[0], zv[2]); quad_swap<1>(zv[1], zv[3]); }
#pragma unroll
        for (int it = 0; it < 4; ++it) TIE(zv[it]);
        char* const oa = a.out + (size_t)(idx * 64 + wm * 16) * 8192 + xcd * 512;
#pragma unroll
        for (int n = 0; n < 8; ++n) {
            if (!QM) TIE(qv[n]);
            const unsigned z0 = zv[n & 3][2 * (n >> 2)], z1 = zv[n & 3][2 * (n >> 2) + 1];
            const f32x4 o = (XAB & 2) ? __builtin_bit_cast(f32x4, zv[n & 3]) : QM ? mul4bf(u32x2{z0, z1}, qm[n]) :
                f32x4{rb1(bflo(qv[n][0]) * bflo(z0)), rb1(bfhi(qv[n][0]) * bfhi(z0)), rb1(bflo(qv[n][1]) * bflo(z1)), rb1(bfhi(qv[n][1]) * bfhi(z1))};
            if (n == 0 && OSG) for (int i = 0; i < (idx & 3); ++i) __builtin_amdgcn_s_sleep(OSG);
            if (!(XAB & 4))
            GST_B128_NT(lh * 32768 + l31 * 16, o, oa + ((n >> 2) * 8 + (n & 3)) * 8192, 0);
            if (OSL) __builtin_amdgcn_s_sleep(OSL);
        }
        asm volatile(".Lfk_code_a:");
        TSE(10);
        TS_FLUSH();
        return;
    }

    u32x2 fl;
    const unsigned* dv = kvv;
#define G1_ISSUE(st, i) do { if constexpr ((st) < DFN) { if constexpr ((i) > 0) dmaof_<(i) * 1024>(dv[i], g1src + ((((st) << 7) + rot7) & 1920)); else dmaf_(g1dst + g1_slot(st), dv[i], g1src + ((((st) << 7) + rot7) & 1920)); } else if constexpr (M1 && (i) > 0) dmao_<(i) * 1024>(dv[i], g1src + ((((st) << 7) + rot7) & 1920)); \
        else DMA(g1dst + g1_slot(st) + (i) * 1024, dv[i], g1src + ((((st) << 7) + rot7) & 1920)); } while (0)
    static_for<4, AP0>([&](auto ic) { constexpr int n = decltype(ic)::value; G1_ISSUE(n >> 2, n & 3); });
    TS(14);
    SLICE_TABLE();
    const int j0 = 4 * run - (32 - 2 * tm);
    const int pneg = EALL ? 0 : 16 * (wm >> 1) + 15 - (32 - 2 * tm);
    unsigned eo[2], pes, plr, est, hw[2], ta[2], po, pmine, pall, pown, pzero, g2v[2], cad[2][2], to[2][2], xo, dso, fst, fld, cwa;
    unsigned cta = lb + L_TS + st * 16, ctb = lb + L_TR + run * 1024 + st * 16;
    TIE(cta); TIE(ctb);
    f32x4 ct[5]; f32x2 V1;
    const int gm = PAIR ? wid >> 1 : wm, gk = PAIR ? wid & 1 : wn, gs = gk * 4 + (gm & 2);
    auto addrs = [&]() {
        int ln = lane;
        TIE(ln);
        const int l15 = ln & 15, g4 = ln >> 4, st = sq * 16 + l15, run = (wm >> 1) * 4 + g4, j0 = 4 * run - (32 - 2 * tm);
        pes = lb + L_ES + wid * 256 + l15 * 16;
        plr = lb + L_AGG + st * 8 + run * 512; est = ((tm * 2 + (wm >> 1)) * 1024 + tn * 64 + st) * 16;
#pragma unroll
        for (int i = 0; i < 2; ++i) { eo[i] = (j0 + 2 * i < 0 ? 0 : j0 + 2 * i) * 16384 + (tn * 64 + st) * 16; TIE(eo[i]); }
        TIE(plr); TIE(pes); TIE(est);
        const unsigned ht = lb + L_HT + wid * 4096;
#pragma unroll
        for (int j = 0; j < 2; ++j) {
            const int pr = (8 * (g4 & 1) + 4 * j + (l15 >> 2)) ^ ((g4 & 1) * 4), pw = l15 ^ ((l15 >> 3) * 4);
            ta[j] = ht + (g4 >> 1) * 512 + pr * 32 + ((((l15 >> 1) ^ (pr >> 2)) & 1) << 4) + 8 * (ln & 1);
            hw[j] = ht + g4 * 1024 + pw * 32 + (((j ^ (pw >> 2)) & 1) << 4);
            TIE(ta[j]); TIE(hw[j]);
        }
        po = pes + ((wm & 2) ? -512 : 512);
        pmine = lb + L_SY + wid * 4; pall = lb + L_SY + (ln & 7) * 4; pown = lb + L_SY + (ln < 4 ? 32 + ln * 4 : gs * 4 + (ln & 4)); pzero = lb + L_SY;
        xo = (tm * 128 + gm * 32 + gk * 16 + g4 * 4) * 2048 + tn * 128 + l15 * 8 + 4096; dso = xo & 2047;
        fst = (tm * 16 + l15) * 128 + tn * 8; fld = (tm * 16 + tn) * 128 + l15 * 8;
#pragma unroll
        for (int i = 0; i < 2; ++i) {
            const int pair = i * 4 + g4, v = l15 ^ pair ^ ((wm & 1) * 8);
            g2v[i] = (pair * 2 + (v >> 3)) * 8192 + (v & 7) * 16 + (tn * 64 + wm) * 2048 - i * 1024;
            TIE(g2v[i]);
#pragma unroll
            for (int j = 0; j < 2; ++j) {
                cad[i][j] = lb + L_C + (img_off((g4 >> 1) * 64 + l15, 4 * gk + (g4 & 1)) ^ (i * 32) ^ (j * 128));
                to[i][j] = ta[j] + (i ^ gk) * 1024 + (gm & 1) * 2048 + (gs - wid) * 4096;
                TIE(cad[i][j]); TIE(to[i][j]);
            }
        }
        cwa = lb + wid * 2048 + ln * 16;
        TIE(cwa); TIE(po);
        TIE(pmine); TIE(pall); TIE(pown); TIE(pzero); TIE(xo); TIE(dso); TIE(fst); TIE(fld);
    };
    if (!APRE) addrs();
    static_for<AP0, 4 * NBK>([&](auto ic) { constexpr int n = decltype(ic)::value; G1_ISSUE(n >> 2, n & 3); });
    {
        const unsigned k0 = trow + wm * 32768 + g4 * 2048 + ((l15 ^ (2 * g4)) << 4), k2 = (k0 ^ 128) + 14336;
        asm volatile("s_add_u32 m0, %5, %6\n\ts_nop 0\n\tglobal_load_lds_dwordx4 %0, %4" NTK "\n\tglobal_load_lds_dwordx4 %1, %4 offset:1024" NTK "\n\t"
                     "global_load_lds_dwordx4 %2, %4 offset:2048" NTK "\n\tglobal_load_lds_dwordx4 %3, %4 offset:3072" NTK
                     :: "v"(k0), "v"(k0 + 7168), "v"(k2), "v"(k2 + 7168), "s"(wn ? a.v : a.k), "s"(g1dst), "n"(L_KS) : "scc", "memory");
    }
    TS(1);
    float nL1i, nC1r, nC1i;
    unsigned tagA, tagB;
    f32x4 acc[4][2];
    {
#pragma unroll
        for (int i = 0; i < 4; ++i) { acc[i][0] = f32x4{0, 0, 0, 0}; acc[i][1] = f32x4{0, 0, 0, 0}; }
        const int aoff = img_off((wm >> 1) * 64 + l15, g4), boff = img_off(128 + wn * 64 + (wm & 1) * 16 + l15, g4);
        unsigned aad[2][2], bad[2];
#pragma unroll
        for (int ks = 0; ks < 2; ++ks) { aad[ks][0] = lb + (aoff ^ (ks * 64)); aad[ks][1] = lb + (aoff ^ (ks * 64) ^ 128); bad[ks] = lb + (boff ^ (ks * 64)); }
        bf16x8 fa[2][4], fb[2][2];
        if (GPRIO) __builtin_amdgcn_s_setprio(GPRIO);
        WAIT_V(g1_cnt(0)); BAR();
        TS(2);
#define G1_FRAG(g, n) do { constexpr int bo_ = g1_slot((g) >> 1), hi_ = bo_ & ~65535, x_ = (n) < 3 ? (n) >> 1 : (n) - 2; \
        if constexpr ((n) == 0 || (n) == 2) DSR128(fb[(g) & 1][x_], bad[(g) & 1] + hi_, bo_ - hi_ + x_ * 4096); \
        else DSR128(fa[(g) & 1][x_], aad[(g) & 1][x_ & 1] + hi_, bo_ - hi_ + x_ * 2048); } while (0)
        static_for<0, 6>([&](auto ic) { constexpr int n = decltype(ic)::value; G1_FRAG(0, n); });
        static_for<0, 32>([&](auto ic) {
            constexpr int g = decltype(ic)::value, gn = g + 1, n0 = g1_upto(g - 1), nd = g1_upto(g) - n0;
            constexpr bool bar = (gn & 1) == 0 && gn < 32;
            if constexpr (g == 1) {
                DSR128(ct[0], cta, 0); DSR128(ct[1], cta, 1024); DSR128(ct[2], cta, 2048); DSR128(ct[3], ctb, 0); DSR128(ct[4], ctb, 8192); DSR64(V1, ctb, 16384);
            }
            if constexpr (g == 3) {
#pragma unroll
                for (int i = 0; i < 5; ++i) TIE(ct[i]);
                TIE(V1);
                nL1i = -ct[0][1]; nC1r = -ct[0][2]; nC1i = -ct[0][3];
                TIE(nL1i); TIE(nC1r); TIE(nC1i);
                tagA = __builtin_amdgcn_readfirstlane(__float_as_uint(ct[2][2])); tagB = __builtin_amdgcn_readfirstlane(__float_as_uint(ct[2][3]));
                ties_(tagA); ties_(tagB);
            }
            if constexpr (bar) {
                static_assert(g1_cnt(gn >> 1) >= 0, "schedule");
                WAIT_VB(g1_cnt(gn >> 1)); WAIT_LB(0); BAR();
                if constexpr (((gn >> 1) & 3) == 0 || gn >= 26) TS(gn >= 26 ? 23 + (gn >> 1) - 13 : 16 + (gn >> 3));
#pragma unroll
                for (int i = 0; i < 4; ++i) if (!(AMF & 2)) TIE(fa[g & 1][i]);
                if (!(AMF & 2)) { TIE(fb[g & 1][0]); TIE(fb[g & 1][1]); }
            }
            static_for<0, 8>([&](auto mc) {
                constexpr int m = decltype(mc)::value, i = m >> 1, j = m & 1;
                if constexpr (W1 && !bar && g > 0 && g < 31) {
                    if constexpr (m == 0) WAIT_LB(W1 == 1 ? 3 : 2);
                    if constexpr (m == 2 && W1 == 1) WAIT_LB(4);
                    if constexpr (m == 4) WAIT_LB(4);
                } else
                if constexpr (!bar && (i == 0 || j == 0)) {
                    WAIT_LB(5 - (i == 0 ? j + 1 : i + 2) + (gn < 32 ? (m < 6 ? m : 6) : 0));
                    if constexpr (!(AMF & 2) || g == 0) { TIE(fa[g & 1][i]); TIE(fb[g & 1][j]); }
                }
                if constexpr ((AMF & 2) && g > 0) mfma_(acc[i][j], fa[g & 1][i], fb[g & 1][j]); else
                acc[i][j] = __builtin_amdgcn_mfma_f32_16x16x32_bf16(fa[g & 1][i], fb[g & 1][j], acc[i][j], 0, 0, 0);
                SB();
                if constexpr (gn < 32 && m < 6) G1_FRAG(gn, m);
                constexpr int d = nd > 2 ? ((m & 1) ? m >> 1 : -1) : (m == DP0 ? 0 : m == DP1 ? 1 : -1);
                if constexpr (d >= 0 && d < nd) G1_ISSUE((n0 + d) >> 2, (n0 + d) & 3);
                SB();
            });
        });
#undef G1_FRAG
    }
    TS(3);
    const unsigned tagG = tagA ^ tagB ^ 0x9E3779B9u;
    __builtin_amdgcn_s_setprio(MPRIO);
    f32x2 ur[8], ui[8], hr, hi;
    const f32x2 L1 = {ct[0][0], ct[0][1]}, C1 = {ct[0][2], ct[0][3]}, L16 = {ct[1][2], ct[1][3]}, Q2r = {ct[3][0], ct[3][1]}, Q2i = {ct[3][2], ct[3][3]};
    const float L8r = ct[1][0], L8i = ct[1][1], L64r = ct[2][0], L64i = ct[2][1];
    u32x4 eg[4], cr[2 * NR + 1];
    const char* g2src = wn ? a.cim : a.cre;
#define CR_LOAD(n0, n1) static_for<n0, n1>([&](auto ic) { constexpr int n = decltype(ic)::value; GLD_B128(cr[n], g2v[n & 1], g2src + (SLICE(7 + (n >> 1)) << 7), (n & 1) * 1024); })
#define E_LOADS "global_load_dwordx4 v[104:107], %[a0], %[b0] sc1\n\tglobal_load_dwordx4 v[108:111], %[a0], %[b1] sc1\n\t" \
                "global_load_dwordx4 v[112:115], %[a1], %[b0] sc1\n\tglobal_load_dwordx4 v[116:119], %[a1], %[b1] sc1\n"
#define E_POLL() asm volatile("s_cmp_lt_i32 %[on], 0\n\ts_cbranch_scc1 1f\n\t" E_LOADS "1:" : "={v[104:107]}"(eg[0]), "={v[108:111]}"(eg[1]), "={v[112:115]}"(eg[2]), "={v[116:119]}"(eg[3]) \
        : [a0] "v"(eo[0]), [a1] "v"(eo[1]), [b0] "s"(ws + WS_E), [b1] "s"(ws + WS_E + 16384), [on] "s"(pneg) : "scc", "memory")
    {
        asm volatile("s_nop 7\n\ts_nop 3" : "+v"(acc[0][0]), "+v"(acc[0][1]), "+v"(acc[1][0]), "+v"(acc[1][1]), "+v"(acc[2][0]), "+v"(acc[2][1]), "+v"(acc[3][0]), "+v"(acc[3][1]));
#pragma unroll
        for (int p = 0; p < 8; ++p) { ur[p] = f32x2{acc[p >> 1][0][2 * (p & 1)], acc[p >> 1][0][2 * (p & 1) + 1]}; ui[p] = f32x2{acc[p >> 1][1][2 * (p & 1)], acc[p >> 1][1][2 * (p & 1) + 1]}; }
        f32x2 gr = ur[0], gi = ui[0];
#pragma unroll
        for (int p = 1; p < 8; ++p) { f32x2 nr, ni; if (PLN & 1) c2_mad(nr, ni, L1[0], nL1i, L1[0], L1[1], gr, gi, ur[p], ui[p]); else pk_cmad(nr, ni, L1, gr, gi, ur[p], ui[p]); gr = nr; gi = ni; }
        float fr, fi;
        cmad(fr, fi, L8r, L8i, gr[0], gi[0], gr[1], gi[1]);
        {
            const f32x2 w = cmul4(V1, f32x2{fr, fi});
            const float wr = sum4(w[0]), wi = sum4(w[1]);
            const u32x4 gq = {__float_as_uint(wr), tagA, __float_as_uint(wi), tagG};
            if (EREP ? (EALL || g4 >= (tm >> 2)) : g4 == 0) GST_B128_SC1(est, gq, ws + WS_E, 0);
        }
        *LDSP(f32x2, plr + 7 * 512) = f32x2{fr, fi};
        *LDSP(unsigned, pes + 8) = 0;
        TS(21);
        if (EP == 2) E_POLL();
        auto coef = [&]() {
#pragma unroll
            for (int p = 0; p < 8; ++p) { f32x2 nr, ni; if (PLN & 2) c2_mul(nr, ni, C1[0], nC1i, nC1r, nC1i, ur[p], ui[p]); else pk_conj_mul(nr, ni, C1, ur[p], ui[p]); ur[p] = nr; ui[p] = ni; TIE(ur[p]); TIE(ui[p]); }
        };
        if (CFB) coef();
        WAIT_L(0); BAR();
        f32x2 e[7];
#pragma unroll
        for (int j = 0; j < 7; ++j) e[j] = *LDSP(f32x2, plr + j * 512);
        if (EP == 1) E_POLL();
        CR_LOAD(0, CR1);
        if (!CFB) coef();
#pragma unroll
        for (int j = 1; j < 7; ++j) { if (PLN & 4) { float xr = e[j][0], xi = e[j][1]; cmac(xr, xi, L16[0], L16[1], e[j - 1][0], e[j - 1][1]); e[j] = f32x2{xr, xi}; } else cmac2(e[j], L16, e[j - 1]); }
        float mr, mi;
        cmad(mr, mi, L8r, L8i, e[6][0], e[6][1], gr[0], gi[0]);
        if (PLN & 4) c2_mul(hr, hi, C1[0], nC1i, nC1r, nC1i, f32x2{e[6][0], mr}, f32x2{e[6][1], mi}); else pk_conj_mul(hr, hi, C1, f32x2{e[6][0], mr}, f32x2{e[6][1], mi});
        TIE(hr); TIE(hi);
        CR_LOAD(CR1, CR1 + CR2);
        TS(4);
    }
    f32x2 G;
    {
        float pr = 0.f, pi = 0.f;
        if (!EP) E_POLL();
        if (pneg >= 0) {
            unsigned t0, t1, t2, left = SPIN_CAP;
            asm volatile("s_waitcnt vmcnt(%[c])\n\ts_branch 2f\n1:\n\ts_sleep %[sl]\n\t" E_LOADS "\ts_waitcnt vmcnt(0)\n2:\n\t"
                         "v_bitop3_b32 %[t0], v105, v109, %[ta] bitop3:0x7e\n\tv_bitop3_b32 %[t1], v113, v117, %[ta] bitop3:0x7e\n\tv_bitop3_b32 %[t2], v107, v111, %[tg] bitop3:0x7e\n\t"
                         "v_or3_b32 %[t0], %[t0], %[t1], %[t2]\n\tv_bitop3_b32 %[t1], v115, v119, %[tg] bitop3:0x7e\n\tv_or_b32 %[t0], %[t0], %[t1]\n\t"
                         "v_cmp_ne_u32 vcc, 0, %[t0]\n\ts_cbranch_vccz 3f\n\ts_sub_u32 %[n], %[n], 1\n\ts_cbranch_scc0 1b\n3:"
                         : "+{v[104:107]}"(eg[0]), "+{v[108:111]}"(eg[1]), "+{v[112:115]}"(eg[2]), "+{v[116:119]}"(eg[3]), [t0] "=&v"(t0), [t1] "=&v"(t1), [t2] "=&v"(t2), [n] "+s"(left)
                         : [a0] "v"(eo[0]), [a1] "v"(eo[1]), [b0] "s"(ws + WS_E), [b1] "s"(ws + WS_E + 16384), [ta] "s"(tagA), [tg] "s"(tagG), [sl] "n"(ESL), [c] "n"(CR1 + CR2) : "vcc", "scc", "memory");
            float ar = __uint_as_float(eg[1][0]), ai = __uint_as_float(eg[1][2]), br = __uint_as_float(eg[3][0]), bi = __uint_as_float(eg[3][2]);
            cmac(ar, ai, L64r, L64i, __uint_as_float(eg[0][0]), __uint_as_float(eg[0][2]));
            cmac(br, bi, L64r, L64i, __uint_as_float(eg[2][0]), __uint_as_float(eg[2][2]));
            if (EALL) { if (j0 < 0) ar = ai = 0.f; if (j0 + 2 < 0) br = bi = 0.f; }
            const f32x2 pu = cmul4(f32x2{ct[4][2], ct[4][3]}, f32x2{ar, ai});
            pr = pu[0]; pi = pu[1];
            cmac(pr, pi, ct[4][0], ct[4][1], br, bi);
            pr = sum4(pr); pi = sum4(pi);
        }
        TS(5);
        *LDSP(u32x4, pes) = u32x4{__float_as_uint(pr), __float_as_uint(pi), 1u, 0u};
        CR_LOAD(CR1 + CR2, CR1 + CR2 + CR3);
        u32x4 o;
        asm volatile("1:\n\tds_read_b128 %0, %1\n\ts_waitcnt lgkmcnt(0)\n\tv_cmp_eq_u32 vcc, 0, v122\n\ts_cbranch_vccnz 1b" : "=&{v[120:123]}"(o) : "v"(po) : "vcc", "memory");
        G = f32x2{pr + __uint_as_float(o[0]), pi + __uint_as_float(o[1])};
    }
    TS(6);
    {
        if (PLN & 8) c2_mac(hr, hi, G[0], -G[1], -G[0], -G[1], Q2r, Q2i); else pk_conj_mac(hr, hi, G, Q2r, Q2i);
#pragma unroll
        for (int p = 0; p < 8; ++p) { if (PLN & 8) c2_mac(ur[p], ui[p], L1[0], L1[1], L1[0], nL1i, hr, hi); else pk_cmac_conj(ur[p], ui[p], L1, hr, hi); hr = ur[p]; hi = ui[p]; }
        const char* hd = ws + WS_H + (size_t)((tm * 4 + (wm & 2)) * 2 + wn) * HP + (tn * 4 + (wm & 1) * 2) * 1024;
#pragma unroll
        for (int e2 = 0; e2 < 2; ++e2)
#pragma unroll
            for (int c = 0; c < 2; ++c) {
                const f32x2* o = c ? ui : ur;
                *LDSP(u32x4, hw[e2] + c * 512) = u32x4{pack2(o[0][e2], o[1][e2]), pack2(o[2][e2], o[3][e2]), pack2(o[4][e2], o[5][e2]), pack2(o[6][e2], o[7][e2])};
            }
        v4i16 t[4][2];
#pragma unroll
        for (int n = 0; n < 4; ++n)
#pragma unroll
            for (int j = 0; j < 2; ++j) t[n][j] = __builtin_amdgcn_ds_read_tr16_b64_v4i16(LDSP(v4i16, ta[j] + n * 1024));
        sy_post<1>(pmine);
        static_for<0, 4>([&](auto ic) {
            constexpr int n = decltype(ic)::value;
            const bf16x8 f = __builtin_shufflevector(t[n][0], t[n][1], 0, 1, 2, 3, 4, 5, 6, 7);
            GST_B128_SC1(l16, f, hd + (n >> 1) * 2 * HP + ((n & 1) ^ wn) * 1024, 0);
        });
    }
    TS(7);
    bf16x8 fa2[64];
    u32x2 xv[4], dsv;
    const char* Aw = ws + WS_H + (size_t)((tm * 4 + gm) * 2 + gk) * HP;
    const unsigned g2dst = lb + wid * 2048;
#define G2_ISSUE(st, i) do { if constexpr (i) dmao_<1024>(g2v[1], g2src + (SLICE(st) << 7)); else DMA(g2dst + g2_place(st), g2v[0], g2src + (SLICE(st) << 7)); } while (0)
#define G2_A(g) GLD_B128_SC1(fa2[g], l16, Aw + (SLICE((g) >> 2) << 12), ((g) & 3) * 1024)
    {
        {
            sy_wait<1>(pown);
            s16x4 fo[4][2]; bf16x8 c[8];
            static_for<0, 2>([&](auto kc) {
                constexpr int ks = decltype(kc)::value;
                static_for<0, 4>([&](auto ic) { constexpr int i = decltype(ic)::value; dsrtr_<ks * 4096>(fo[2 * ks + (i >> 1)][i & 1], to[i >> 1][i & 1]); });
                static_for<0, 4>([&](auto ic) { constexpr int i = decltype(ic)::value; DSR128(c[4 * ks + i], cad[ks][i & 1], 2 * G2STAGE + i * 2048); });
            });
            static_for<0, 8>([&](auto ic) {
                constexpr int n = decltype(ic)::value, ks = n >> 2, i = n & 3;
                WAIT_L(ks ? 3 - i : 11 - i);
                TIE(c[n]);
                if constexpr (i == 0) { TIE(fo[2 * ks][0]); TIE(fo[2 * ks][1]); TIE(fo[2 * ks + 1][0]); TIE(fo[2 * ks + 1][1]); }
#pragma unroll
                for (int j = 0; j < 2; ++j)
                    acc[i][j] = __builtin_amdgcn_mfma_f32_16x16x32_bf16(__builtin_shufflevector(fo[2 * ks + j][0], fo[2 * ks + j][1], 0, 1, 2, 3, 4, 5, 6, 7), c[n], ks ? acc[i][j] : f32x4{0, 0, 0, 0}, 0, 0, 0);
                SB();
            });
#pragma unroll
            for (int i = 0; i < 4; ++i) { TIE(acc[i][0]); TIE(acc[i][1]); }
        }
        if (CEARLY) sy_post<0>(pmine);
        static_for<0, 4>([&](auto ic) { constexpr int it = decltype(ic)::value; GLD_B64(xv[it], xo, a.x, it * 2048 - 4096); });
        GLD_B64(dsv, dso, a.dskip, 0);
        if (CEARLY) {
            sy_wait<0>(pall);
            static_for<4, 4 + CEARLY>([&](auto ic) { constexpr int n = decltype(ic)::value; G2_ISSUE(n >> 1, n & 1); });
        }
        TS(16);
        WAIT_V(5 + CEARLY);
        sy_post<2>(pmine);
        if (!CEARLY || wid == (NOOWN ? 1 : 0) || (NOOWN == 1 && wid == 0)) sy_wait<2, 0>(pall);
        const u32x2 tagv = {__float_as_uint(ct[2][2]), __float_as_uint(ct[2][3])};
        if (wid == (NOOWN ? 1 : 0)) asm volatile("s_mov_b64 exec, 0xffff\n\tglobal_store_dwordx2 %0, %1, %2 sc1\n\ts_mov_b64 exec, -1" :: "v"(fst), "v"(tagv), "s"(flg + FL_H) : "memory");
        if (PFIRST) asm volatile("s_cmp_lg_u32 %3, 0\n\ts_cbranch_scc1 1f\n\tglobal_load_dwordx2 %0, %1, %2 sc1\n1:" : "=&v"(fl) : "v"(fld), "s"(flg + FL_H), "s"(wid) : "scc", "memory");
        TS(8);
        static_for<4 + CEARLY, 10>([&](auto ic) { constexpr int n = decltype(ic)::value; G2_ISSUE(n >> 1, n & 1); });
        CR_LOAD(2 * NR - CRL, 2 * NR);
        TS(13);
        if (wid == 0) {
            unsigned left = SPIN_CAP;
            if (PFIRST)
            asm volatile("s_waitcnt vmcnt(6)\n\ts_branch 3f\n1:\n\tglobal_load_dwordx2 %0, %2, %3 sc1\n\ts_waitcnt vmcnt(0)\n3:\n\tv_cmp_ne_u64 vcc, %0, %4\n\ts_cbranch_vccz 2f\n\ts_sub_u32 %1, %1, 1\n\ts_cbranch_scc0 1b\n2:"
                         : "+v"(fl), "+s"(left) : "v"(fld), "s"(flg + FL_H), "v"(tagv) : "vcc", "scc", "memory");
            else
            asm volatile("1:\n\tglobal_load_dwordx2 %0, %2, %3 sc1\n\ts_waitcnt vmcnt(0)\n\tv_cmp_ne_u64 vcc, %0, %4\n\ts_cbranch_vccz 2f\n\ts_sub_u32 %1, %1, 1\n\ts_cbranch_scc0 1b\n2:"
                         : "=&v"(fl), "+s"(left) : "v"(fld), "s"(flg + FL_H), "v"(tagv) : "vcc", "scc", "memory");
            sy_post<3>(pmine);
            if (b == 255 && lane == 0) GST_B64_SC1(0u * lane, tagv, flg + FL_EPOCH);
        } else if (g2_bar(3)) sy_wait<3>(pzero);
        else { WAIT_V(CRL); sy_post<3>(pmine); }
        if (!g2_bar(3)) sy_wait<3>(pall);
        TS(20);
        static_for<4, 4 + AW>([&](auto ic) { constexpr int g = decltype(ic)::value; G2_A(g); });
    }
    TS(9);
    __builtin_amdgcn_s_setprio(0);
    unsigned yw, yr, yp, yo;
    static_assert(PAIR, "yr");
    unsigned dm[4];
    auto dmask = [&]() { TIE(dsv); dm[0] = dsv[0] & 0xffffu; dm[1] = dsv[0] & 0xffff0000u; dm[2] = dsv[1] & 0xffffu; dm[3] = dsv[1] & 0xffff0000u; TIE(dm[0]); TIE(dm[1]); TIE(dm[2]); TIE(dm[3]); };
    auto skip = [&](int it) { TIE(xv[it]); const f32x4 y = mul4bf(xv[it], dm); for (int i = 0; i < 4; ++i) acc[i][0][it] = add1(acc[i][0][it], y[i]); if (it & 1) { for (int i = 0; i < 4; ++i) TIE(acc[i][0]); } };
    {
        bf16x8 fb[2][4];
#define G2_FRAG(g, i) do { constexpr int pl_ = g2_place((g) >> 1), b_ = pl_ >= L_C ? L_C : pl_ >= 32768 ? 32768 : 0; DSR128(fb[(g) & 1][i], cad[(g) & 1][(i) & 1] + (b_ - L_C), pl_ - b_ + (i) * 2048); } while (0)
        static_for<0, 4>([&](auto ic) { constexpr int i = decltype(ic)::value; G2_FRAG(2, i); });
        if (YS == 0) { WAIT_V(AW + 6 + CRL); dmask(); skip(0); skip(1); skip(2); skip(3); }
        static_for<2, 32>([&](auto ic) {
            constexpr int g = decltype(ic)::value, gn = g + 1;
            constexpr bool bar = g2_bar(g) && !((VBM >> g) & 1);
            constexpr int n0 = g2_upto(g - 1), n1 = g2_upto(g);
            if constexpr (bar) {
                constexpr int ca = g2_cnt(2 * g, 2 * g), cs = g2_cover(g) >= 7 + NR ? g2_cnt(100 + 2 * g2_cover(g) + 1, 2 * g) : 63;
                static_assert(cs >= 0 && g2_wr(g - 1) >= 2 * (g2_cover(g) < 6 + NR ? g2_cover(g) - 6 : NR), "schedule");
                WAIT_VB(AWB && ca < cs ? ca : cs); WAIT_LB(0); BAR();
                if constexpr (!AWB) WAIT_V(ca);
#pragma unroll
                for (int i = 0; i < 4; ++i) if (!(AMF & 4)) TIE(fb[g & 1][i]);
                if (!(AMF & 4)) TIE(fa2[2 * g]);
            }
            static_for<0, 8>([&](auto mc) {
                constexpr int m = decltype(mc)::value, i = m & 3, j = m >> 2, f = 2 * g + j;
                static_assert(g2_cnt(f, f) >= 0, "schedule");
                constexpr int jj = LSW && g == 31 ? 1 - j : j, ff = 2 * g + jj;
                if constexpr (i == 0 && (j || !bar)) { WAIT_VB(LSW && g == 31 ? 0 : g2_cnt(f, f)); if constexpr (!(AMF & 4) || g == 2 || g == 31) TIE(fa2[ff]); }
                if constexpr (W2 && g > 2 && g < 31) { if constexpr (!bar && j == 0 && (i & W2) == 0) WAIT_LB(3 - W2 + g2_wr(g - 1) - g2_wr(g - 2) + (g - 1 == EGX)); } else
                if constexpr (!bar && j == 0) { WAIT_LB((gn < 32 ? 3 : 3 - i) + g2_wr(g - 1) - g2_wr(g - 2) + (g - 1 == EGX)); if constexpr (!(AMF & 4) || g == 2 || g == 31) TIE(fb[g & 1][i]); }
                if constexpr ((AMF & 4) && g > 2 && g < 31) mfma_(acc[i][jj], fa2[ff], fb[g & 1][i]); else
                acc[i][jj] = __builtin_amdgcn_mfma_f32_16x16x32_bf16(fa2[ff], fb[g & 1][i], acc[i][jj], 0, 0, 0);
                SB();
                if constexpr (LSW && g == 31 && j) *LDSP(f32x4, yw + i * 1024) = acc[i][1];
                if constexpr (g == EGX && m == 4) sy_post<5>(pmine);
                if constexpr (g == 29 && m == 5) { yw = cwa + L_Y + wid * 2048; yr = yw ^ (PAIR ? 4096 : 16384); yp = pmine ^ (PAIR ? 4 : 16); yo = (cwa & 255) + ((cwa & 768) << 7); TIE(yw); TIE(yr); TIE(yp); TIE(yo); }
                if constexpr (gn < 32 && m < 4) G2_FRAG(gn, m);
                if constexpr (m >= 4 && g2_wr(g - 1) + m - 4 < g2_wr(g)) {
                    constexpr int n = g2_wr(g - 1) + m - 4, pl = g2_place(7 + (n >> 1)), pb = pl >= L_C ? L_C : 0;
                    asm volatile("ds_write_b128 %0, %1 offset:%2" :: "v"(cwa + pb), "v"(cr[n]), "n"(pl - pb + (n & 1) * 1024) : "memory");
                }
                if constexpr (i == 1 && n0 + j < n1) G2_ISSUE((n0 + j) >> 1, (n0 + j) & 1);
                if constexpr (i >= 2 && g2_ald(f, i & 1) >= 0) G2_A(g2_ald(f, i & 1));
                SB();
            });
        });
#undef G2_FRAG
    }
    TS(11);
    if (EPRIO) __builtin_amdgcn_s_setprio(EPRIO);
    {
#pragma unroll
        for (int i = 0; i < 4; ++i) if (!LSW) *LDSP(f32x4, yw + i * 1024) = acc[i][1];
        sy_post<4>(pmine);
        sy_wait<4, 0>(yp);
        char* const ob = a.out + (size_t)(tm * 128 + gm * 32 + gk * 16) * 8192 + 4096 + tn * 256;
        f32x4 w[4];
#pragma unroll
        for (int i = 0; i < 4; ++i) w[i] = *LDSP(f32x4, yr + i * 1024);
        f32x4 o[4];
#pragma unroll
        for (int r = 0; r < 4; ++r) { o[r] = f32x4{add1(w[0][r], acc[0][0][r]), add1(w[1][r], acc[1][0][r]), add1(w[2][r], acc[2][0][r]), add1(w[3][r], acc[3][0][r])}; if (EGX) TIE(o[r]); }
        if (EGX && !wn) sy_wait<5, 1>(pall);
#pragma unroll
        for (int r = 0; r < 4; ++r) GST_B128_NT(yo, o[r], ob + r * 8192, 0);
    }
    asm volatile(".Lfk_code_b:");
    TSE(12);
    TS_FLUSH();
}

}

extern "C" void kernel_launch(void* const* d_in, const int* in_sizes, int n_in, void* d_out, int out_size, void* d_ws, size_t ws_size,
                              hipStream_t stream) {
    (void)in_sizes; (void)n_in; (void)out_size; (void)ws_size;
    fk::Args a;
    a.q = (const char*)d_in[0]; a.k = (const char*)d_in[1]; a.v = (const char*)d_in[2]; a.sk = (const char*)d_in[3];
    a.ssmk = (const char*)d_in[4]; a.ssmD = (const char*)d_in[5]; a.x = (const char*)d_in[6]; a.lre = (const char*)d_in[7];
    a.lim = (const char*)d_in[8]; a.bre = (const char*)d_in[9]; a.bim = (const char*)d_in[10]; a.cre = (const char*)d_in[11];
    a.cim = (const char*)d_in[12]; a.logdt = (const char*)d_in[13]; a.dskip = (const char*)d_in[14];
    a.out = (char*)d_out; a.ws = (char*)d_ws;
    hipLaunchKernelGGL(fk::fused, dim3(256), dim3(768), 0, stream, a);
}
```

```cpp
#include <hip/hip_runtime.h>
#include <stdint.h>
#include <type_traits>

namespace fk {

constexpr int S = 2048, D = 1024, P = 1024;

typedef __attribute__((ext_vector_type(8))) short bf16x8;
typedef __attribute__((ext_vector_type(4))) short s16x4;
typedef __attribute__((ext_vector_type(16))) float f32x16;
typedef __attribute__((ext_vector_type(4))) float f32x4;
typedef __attribute__((ext_vector_type(2))) float f32x2;
typedef __attribute__((ext_vector_type(4))) unsigned u32x4;
typedef __attribute__((ext_vector_type(2))) unsigned u32x2;
#define LDSP(T, a) ((__attribute__((address_space(3))) T*)(uintptr_t)(unsigned)(a))

__device__ __forceinline__ float bf2f(unsigned v) { return __uint_as_float(v << 16); }
__device__ __forceinline__ float bflo(unsigned w) { return __uint_as_float(w << 16); }
__device__ __forceinline__ float bfhi(unsigned w) { return __uint_as_float(w & 0xffff0000u); }
__device__ __forceinline__ uint16_t f2bf(float f) { __bf16 b = (__bf16)f; return __builtin_bit_cast(uint16_t, b); }
__device__ __forceinline__ float rbf(float f) { return bf2f(f2bf(f)); }
typedef __attribute__((ext_vector_type(2))) __bf16 bf16x2;
__device__ __forceinline__ unsigned pack2(float lo, float hi) { return __builtin_bit_cast(unsigned, __builtin_convertvector(f32x2{lo, hi}, bf16x2)); }
__device__ __forceinline__ float rb1(float x) { return __uint_as_float(pack2(0.f, x)); }
__device__ __forceinline__ void cmac(float& xr, float& xi, float ar, float ai, float hr, float hi) {
    asm("v_fmac_f32 %0, %2, %4\n\tv_fmac_f32 %1, %2, %5\n\tv_fma_f32 %0, -%3, %5, %0\n\tv_fmac_f32 %1, %3, %4" : "+v"(xr), "+v"(xi) : "v"(ar), "v"(ai), "v"(hr), "v"(hi));
}
__device__ __forceinline__ void cmad(float& o_r, float& o_i, float ar, float ai, float hr, float hi, float er, float ei) {
    asm("v_fma_f32 %0, %2, %4, %6\n\tv_fma_f32 %1, %2, %5, %7\n\tv_fma_f32 %0, -%3, %5, %0\n\tv_fmac_f32 %1, %3, %4" : "=&v"(o_r), "=&v"(o_i) : "v"(ar), "v"(ai), "v"(hr), "v"(hi), "v"(er), "v"(ei));
}
__device__ __forceinline__ f32x2 cmul4(f32x2 a, f32x2 h) {
    float o_r, o_i;
    asm("v_mul_f32 %0, %2, %4\n\tv_mul_f32 %1, %2, %5\n\tv_fma_f32 %0, -%3, %5, %0\n\tv_fmac_f32 %1, %3, %4" : "=&v"(o_r), "=&v"(o_i) : "v"(a[0]), "v"(a[1]), "v"(h[0]), "v"(h[1]));
    return f32x2{o_r, o_i};
}

#define PK_LO " op_sel:[0,0,0] op_sel_hi:[0,1,1]"
#define PK_HI " op_sel:[1,0,0] op_sel_hi:[1,1,1]"
#define PK_NEG " neg_lo:[1,0,0] neg_hi:[1,0,0]"
__device__ __forceinline__ void pk_cmad(f32x2& o_r, f32x2& o_i, f32x2 a, f32x2 hr, f32x2 hi, f32x2 er, f32x2 ei) {
    asm("v_pk_fma_f32 %0, %2, %3, %5" PK_LO "\n\tv_pk_fma_f32 %1, %2, %4, %6" PK_LO "\n\tv_pk_fma_f32 %0, %2, %4, %0" PK_HI PK_NEG "\n\tv_pk_fma_f32 %1, %2, %3, %1" PK_HI
        : "=&v"(o_r), "=&v"(o_i) : "v"(a), "v"(hr), "v"(hi), "v"(er), "v"(ei));
}
__device__ __forceinline__ void pk_cmac_conj(f32x2& xr, f32x2& xi, f32x2 a, f32x2 hr, f32x2 hi) {
    asm("v_pk_fma_f32 %0, %2, %3, %0" PK_LO "\n\tv_pk_fma_f32 %1, %2, %4, %1" PK_LO "\n\tv_pk_fma_f32 %0, %2, %4, %0" PK_HI "\n\tv_pk_fma_f32 %1, %2, %3, %1" PK_HI PK_NEG
        : "+v"(xr), "+v"(xi) : "v"(a), "v"(hr), "v"(hi));
}
__device__ __forceinline__ void pk_conj_mul(f32x2& o_r, f32x2& o_i, f32x2 a, f32x2 hr, f32x2 hi) {
    asm("v_pk_mul_f32 %0, %2, %3 op_sel:[0,0] op_sel_hi:[0,1]\n\tv_pk_mul_f32 %1, %2, %4 op_sel:[0,0] op_sel_hi:[0,1] neg_lo:[1,0] neg_hi:[1,0]\n\t"
        "v_pk_fma_f32 %0, %2, %4, %0" PK_HI PK_NEG "\n\tv_pk_fma_f32 %1, %2, %3, %1" PK_HI PK_NEG : "=&v"(o_r), "=&v"(o_i) : "v"(a), "v"(hr), "v"(hi));
}
__device__ __forceinline__ void pk_conj_mac(f32x2& xr, f32x2& xi, f32x2 a, f32x2 hr, f32x2 hi) {
    asm("v_pk_fma_f32 %0, %2, %3, %0" PK_LO "\n\tv_pk_fma_f32 %1, %2, %4, %1" PK_LO PK_NEG "\n\tv_pk_fma_f32 %0, %2, %4, %0" PK_HI PK_NEG "\n\tv_pk_fma_f32 %1, %2, %3, %1" PK_HI PK_NEG
        : "+v"(xr), "+v"(xi) : "v"(a), "v"(hr), "v"(hi));
}
__device__ __forceinline__ void cmac2(f32x2& x, f32x2 a, f32x2 h) {
    asm("v_pk_fma_f32 %0, %1, %2, %0" PK_LO "\n\tv_pk_fma_f32 %0, %1, %2, %0 op_sel:[1,1,0] op_sel_hi:[1,0,1] neg_lo:[1,0,0]" : "+v"(x) : "v"(a), "v"(h));
}
#define PLN 0
__device__ __forceinline__ void c2_mac(f32x2& xr, f32x2& xi, float p, float q, float r, float s, f32x2 hr, f32x2 hi) {
    float a = xr[0], b = xi[0], c = xr[1], d = xi[1];
    asm("v_fmac_f32 %0, %4, %8\n\tv_fmac_f32 %1, %6, %9\n\tv_fmac_f32 %2, %4, %10\n\tv_fmac_f32 %3, %6, %11\n\t"
        "v_fmac_f32 %0, %5, %9\n\tv_fmac_f32 %1, %7, %8\n\tv_fmac_f32 %2, %5, %11\n\tv_fmac_f32 %3, %7, %10"
        : "+v"(a), "+v"(b), "+v"(c), "+v"(d) : "v"(p), "v"(q), "v"(r), "v"(s), "v"(hr[0]), "v"(hi[0]), "v"(hr[1]), "v"(hi[1]));
    xr = f32x2{a, c}; xi = f32x2{b, d};
}
__device__ __forceinline__ void c2_mad(f32x2& xr, f32x2& xi, float p, float q, float r, float s, f32x2 hr, f32x2 hi, f32x2 er, f32x2 ei) {
    float a, b, c, d;
    asm("v_fma_f32 %0, %4, %8, %12\n\tv_fma_f32 %1, %6, %9, %13\n\tv_fma_f32 %2, %4, %10, %14\n\tv_fma_f32 %3, %6, %11, %15\n\t"
        "v_fmac_f32 %0, %5, %9\n\tv_fmac_f32 %1, %7, %8\n\tv_fmac_f32 %2, %5, %11\n\tv_fmac_f32 %3, %7, %10"
        : "=&v"(a), "=&v"(b), "=&v"(c), "=&v"(d) : "v"(p), "v"(q), "v"(r), "v"(s), "v"(hr[0]), "v"(hi[0]), "v"(hr[1]), "v"(hi[1]), "v"(er[0]), "v"(ei[0]), "v"(er[1]), "v"(ei[1]));
    xr = f32x2{a, c}; xi = f32x2{b, d};
}
__device__ __forceinline__ void c2_mul(f32x2& xr, f32x2& xi, float p, float q, float r, float s, f32x2 hr, f32x2 hi) {
    float a, b, c, d;
    asm("v_mul_f32 %0, %4, %8\n\tv_mul_f32 %1, %6, %9\n\tv_mul_f32 %2, %4, %10\n\tv_mul_f32 %3, %6, %11\n\t"
        "v_fmac_f32 %0, %5, %9\n\tv_fmac_f32 %1, %7, %8\n\tv_fmac_f32 %2, %5, %11\n\tv_fmac_f32 %3, %7, %10"
        : "=&v"(a), "=&v"(b), "=&v"(c), "=&v"(d) : "v"(p), "v"(q), "v"(r), "v"(s), "v"(hr[0]), "v"(hi[0]), "v"(hr[1]), "v"(hi[1]));
    xr = f32x2{a, c}; xi = f32x2{b, d};
}
__device__ __forceinline__ float add1(float a, float b) { float c; asm("v_add_f32 %0, %1, %2" : "=v"(c) : "v"(a), "v"(b)); return c; }
__device__ __forceinline__ void mfma_(f32x4& c, bf16x8 a, bf16x8 b) { asm volatile("v_mfma_f32_16x16x32_bf16 %0, %1, %2, %0" : "+v"(c) : "v"(a), "v"(b)); }
__device__ __forceinline__ float sum4(float x) {
    const auto p = __builtin_amdgcn_permlane32_swap(__float_as_uint(x), __float_as_uint(x), false, false);
    x = __uint_as_float(p[0]) + __uint_as_float(p[1]);
    const auto q = __builtin_amdgcn_permlane16_swap(__float_as_uint(x), __float_as_uint(x), false, false);
    return __uint_as_float(q[0]) + __uint_as_float(q[1]);
}

template <int I, int N, class F>
__device__ __forceinline__ void static_for(F&& f) { if constexpr (I < N) { f(std::integral_constant<int, I>{}); static_for<I + 1, N>(f); } }

#define TS_DECL
#define TS0a()
#define TS0b()
#define TSE(i)
#define TS(i)
#define TS_FLUSH()

#define WAIT_V(n) asm volatile("s_waitcnt vmcnt(%0)" ::"n"((n) > 63 ? 63 : (n)) : "memory")
#define WAIT_L(n) asm volatile("s_waitcnt lgkmcnt(%0)" ::"n"((n) > 15 ? 15 : (n)) : "memory")
#define BW 1
constexpr int wenc(int v, int l) { v = v > 63 ? 63 : v; l = l > 15 ? 15 : l; return (v & 15) | 0x70 | (l << 8) | ((v >> 4) << 14); }
#define WAIT_VB(n) do { if constexpr (BW) __builtin_amdgcn_s_waitcnt(wenc(n, 15)); else WAIT_V(n); } while (0)
#define WAIT_LB(n) do { if constexpr (BW) __builtin_amdgcn_s_waitcnt(wenc(63, n)); else WAIT_L(n); } while (0)
#define BAR() asm volatile("s_barrier" ::: "memory")
#define SB() __builtin_amdgcn_sched_barrier(0)
template <class T> __device__ __forceinline__ void tie_(T& x) { asm volatile("" : "+v"(x)); }
#define TIE(x) tie_(x)
template <class T> __device__ __forceinline__ void ties_(T& x) { asm volatile("" : "+s"(x)); }
__device__ __forceinline__ unsigned take(unsigned x) { unsigned y; asm volatile("v_mov_b32 %0, %1" : "=v"(y) : "v"(x)); return y; }
#define DEF_LD(name, ins) template <int OFF, class T> __device__ __forceinline__ void name(T& dst, unsigned voff, const char* sb) { \
    asm volatile(ins : "=v"(dst) : "v"(voff), "s"(sb), "n"(OFF) : "memory"); }
DEF_LD(gld_u16_, "global_load_ushort %0, %1, %2 offset:%3")
DEF_LD(gld_b32_, "global_load_dword %0, %1, %2 offset:%3")
DEF_LD(gld_b64_, "global_load_dwordx2 %0, %1, %2 offset:%3")
DEF_LD(gld_b128_, "global_load_dwordx4 %0, %1, %2 offset:%3")
#define NTQ " nt"
#define NTK " nt"
DEF_LD(gld_b64_nt_, "global_load_dwordx2 %0, %1, %2 offset:%3" NTQ)
DEF_LD(gld_b128_nt_, "global_load_dwordx4 %0, %1, %2 offset:%3" NTQ)
DEF_LD(gld_b64_sc1_, "global_load_dwordx2 %0, %1, %2 offset:%3 sc1")
DEF_LD(gld_b128_sc1_, "global_load_dwordx4 %0, %1, %2 offset:%3 sc1")
#define DEF_ST(name, ins) template <int OFF, class T> __device__ __forceinline__ void name(unsigned voff, const T& v, const char* sb) { \
    asm volatile(ins :: "v"(voff), "v"(v), "s"(sb), "n"(OFF) : "memory"); }
DEF_ST(gst_b128_, "global_store_dwordx4 %0, %1, %2 offset:%3\n\ts_nop 1")
DEF_ST(gst_b128_nt_, "global_store_dwordx4 %0, %1, %2 offset:%3 nt\n\ts_nop 1")
DEF_ST(gst_b128_sc1_, "global_store_dwordx4 %0, %1, %2 offset:%3 sc1\n\ts_nop 1")
DEF_ST(gst_b64_sc1_, "global_store_dwordx2 %0, %1, %2 offset:%3 sc1")
#define GLD_U16(dst, voff, sb) gld_u16_<0>(dst, voff, sb)
#define GLD_B32(dst, voff, sb, off) gld_b32_<(off)>(dst, voff, sb)
#define GLD_B64(dst, voff, sb, off) gld_b64_<(off)>(dst, voff, sb)
#define GLD_B128(dst, voff, sb, off) gld_b128_<(off)>(dst, voff, sb)
#define GLD_B64_NT(dst, voff, sb, off) gld_b64_nt_<(off)>(dst, voff, sb)
#define GLD_B128_NT(dst, voff, sb, off) gld_b128_nt_<(off)>(dst, voff, sb)
#define GLD_B64_SC1(dst, voff, sb, off) gld_b64_sc1_<(off)>(dst, voff, sb)
#define GLD_B128_SC1(dst, voff, sb, off) gld_b128_sc1_<(off)>(dst, voff, sb)
#define GST_B128(voff, v, sb, off) gst_b128_<(off)>(voff, v, sb)
#define GST_B128_NT(voff, v, sb, off) gst_b128_nt_<(off)>(voff, v, sb)
#define GST_B128_SC1(voff, v, sb, off) gst_b128_sc1_<(off)>(voff, v, sb)
#define GST_B64_SC1(voff, v, sb) gst_b64_sc1_<0>(voff, v, sb)
__device__ __forceinline__ void dma_(unsigned ldsaddr, unsigned voff, const char* sb) {
    asm volatile("s_mov_b32 m0, %0\n\ts_nop 0\n\tglobal_load_lds_dwordx4 %1, %2" :: "s"(ldsaddr), "v"(voff), "s"(sb) : "memory"); }
__device__ __forceinline__ void dma_nt_(unsigned ldsaddr, unsigned voff, const char* sb) {
    asm volatile("s_mov_b32 m0, %0\n\ts_nop 0\n\tglobal_load_lds_dwordx4 %1, %2 nt" :: "s"(ldsaddr), "v"(voff), "s"(sb) : "memory"); }
#define DF 0
#define DFN 4
#define FL_0 ""
#define FL_1 " nt"
#define FL_2 " sc1"
#define FL_3 " sc1 nt"
#define FL_4 " sc0 sc1"
#define FL_5 " sc0 sc1 nt"
#define FL_6 " sc0 nt"
#define FLX_(x) FL_##x
#define FLX(x) FLX_(x)
__device__ __forceinline__ void dmaf_(unsigned ldsaddr, unsigned voff, const char* sb) {
    asm volatile("s_mov_b32 m0, %0\n\ts_nop 0\n\tglobal_load_lds_dwordx4 %1, %2" FLX(DF) :: "s"(ldsaddr), "v"(voff), "s"(sb) : "memory"); }
template <int OFF> __device__ __forceinline__ void dmaof_(unsigned voff, const char* sb) {
    asm volatile("global_load_lds_dwordx4 %0, %1 offset:%2" FLX(DF) :: "v"(voff), "s"(sb), "n"(OFF) : "memory"); }
#define DMA(ldsaddr, voff, sb) dma_(ldsaddr, voff, sb)
template <int OFF> __device__ __forceinline__ void dmao_(unsigned voff, const char* sb) {
    asm volatile("global_load_lds_dwordx4 %0, %1 offset:%2" :: "v"(voff), "s"(sb), "n"(OFF) : "memory"); }
template <int OFF, class T> __device__ __forceinline__ void dsr128_(T& dst, unsigned addr) { asm volatile("ds_read_b128 %0, %1 offset:%2" : "=v"(dst) : "v"(addr), "n"(OFF)); }
template <int OFF, class T> __device__ __forceinline__ void dsr64_(T& dst, unsigned addr) { asm volatile("ds_read_b64 %0, %1 offset:%2" : "=v"(dst) : "v"(addr), "n"(OFF)); }
template <int OFF, class T> __device__ __forceinline__ void dsr2_(T& dst, unsigned addr) { asm volatile("ds_read2_b32 %0, %1 offset0:%2 offset1:%3" : "=v"(dst) : "v"(addr), "n"(OFF), "n"(OFF + 1)); }
#define DSR128(dst, addr, off) dsr128_<(off)>(dst, addr)
#define DSR64(dst, addr, off) dsr64_<(off)>(dst, addr)

__device__ __forceinline__ void kv4(unsigned& o01, unsigned& o23, unsigned ka, unsigned kb, unsigned kc, unsigned kd, const unsigned* w, unsigned v0, unsigned v1) {
    unsigned t1, t3, p0, p1, p2, p3;
    asm(
        "v_dot2_f32_bf16 %0, %8, %19, 0\n\tv_dot2_f32_bf16 %1, %9, %18, 0\n\tv_dot2_f32_bf16 %2, %9, %19, 0\n\tv_dot2_f32_bf16 %3, %10, %18, 0\n\t"
        "v_dot2_f32_bf16 %4, %9, %16, 0\n\tv_dot2_f32_bf16 %5, %9, %17, 0\n\tv_dot2_f32_bf16 %6, %10, %16, 0\n\tv_dot2_f32_bf16 %7, %10, %17, 0\n\t"
        "v_cvt_pk_bf16_f32 %0, %0, %4\n\tv_cvt_pk_bf16_f32 %1, %1, %5\n\tv_cvt_pk_bf16_f32 %2, %2, %6\n\tv_cvt_pk_bf16_f32 %3, %3, %7\n\t"
        "v_dot2_f32_bf16 %4, %9, %15, 0\n\tv_dot2_f32_bf16 %5, %10, %14, 0\n\tv_dot2_f32_bf16 %6, %10, %15, 0\n\tv_dot2_f32_bf16 %7, %11, %14, 0\n\t"
        "v_dot2_f32_bf16 %0, %0, %22, 0\n\tv_dot2_f32_bf16 %1, %1, %22, 0\n\tv_dot2_f32_bf16 %2, %2, %22, 0\n\tv_dot2_f32_bf16 %3, %3, %22, 0\n\t"
        "v_cvt_pk_bf16_f32 %0, %0, %4\n\tv_cvt_pk_bf16_f32 %1, %1, %5\n\tv_cvt_pk_bf16_f32 %2, %2, %6\n\tv_cvt_pk_bf16_f32 %3, %3, %7\n\t"
        "v_dot2_f32_bf16 %4, %10, %12, 0\n\tv_dot2_f32_bf16 %5, %10, %13, 0\n\tv_dot2_f32_bf16 %6, %11, %12, 0\n\tv_dot2_f32_bf16 %7, %11, %13, 0\n\t"
        "v_dot2_f32_bf16 %0, %0, %22, 0\n\tv_dot2_f32_bf16 %1, %1, %22, 0\n\tv_dot2_f32_bf16 %2, %2, %22, 0\n\tv_dot2_f32_bf16 %3, %3, %22, 0\n\t"
        "v_cvt_pk_bf16_f32 %0, %0, %4\n\tv_cvt_pk_bf16_f32 %1, %1, %5\n\tv_cvt_pk_bf16_f32 %2, %2, %6\n\tv_cvt_pk_bf16_f32 %3, %3, %7\n\t"
        "v_dot2_f32_bf16 %0, %0, %22, 0\n\tv_dot2_f32_bf16 %1, %1, %22, 0\n\tv_dot2_f32_bf16 %2, %2, %22, 0\n\tv_dot2_f32_bf16 %3, %3, %22, 0\n\t"
        "v_cvt_pk_bf16_f32 %0, %0, 0\n\tv_cvt_pk_bf16_f32 %1, 0, %1\n\tv_cvt_pk_bf16_f32 %2, %2, 0\n\tv_cvt_pk_bf16_f32 %3, 0, %3\n\t"
        "v_dot2_f32_bf16 %0, %0, %20, 0\n\tv_dot2_f32_bf16 %1, %1, %20, 0\n\tv_dot2_f32_bf16 %2, %2, %21, 0\n\tv_dot2_f32_bf16 %3, %3, %21, 0\n\t"
        "s_nop 1\n\tv_cvt_pk_bf16_f32 %0, %1, %0\n\tv_cvt_pk_bf16_f32 %2, %3, %2\n\t"
        : "=&v"(o01), "=&v"(t1), "=&v"(o23), "=&v"(t3), "=&v"(p0), "=&v"(p1), "=&v"(p2), "=&v"(p3)
        : "v"(ka), "v"(kb), "v"(kc), "v"(kd), "v"(w[0]), "v"(w[1]), "v"(w[2]), "v"(w[3]), "v"(w[4]), "v"(w[5]), "v"(w[6]), "v"(w[7]), "v"(v0), "v"(v1), "s"(0x3f803f80u));
}
__device__ __forceinline__ void kv4a(unsigned* t, unsigned* e, unsigned ka, unsigned kb, unsigned kc, unsigned kd, const unsigned* w) {
    asm volatile(
        "v_and_b32 %8, %21, %15\n\tv_lshlrev_b32 %9, 16, %16\n\tv_and_b32 %10, %21, %16\n\tv_lshlrev_b32 %11, 16, %17\n\t"
        "v_and_b32 %12, %21, %17\n\tv_mul_f32 %0, %20, %8\n\tv_mul_f32 %1, %20, %9\n\tv_mul_f32 %2, %20, %10\n\t"
        "v_mul_f32 %3, %20, %11\n\tv_mul_f32 %4, %19, %9\n\tv_mul_f32 %5, %19, %10\n\tv_mul_f32 %6, %19, %11\n\t"
        "v_mul_f32 %7, %19, %12\n\tv_lshlrev_b32 %13, 16, %18\n\tv_and_b32 %14, %21, %18\n\tv_cvt_pk_bf16_f32 %0, 0, %0\n\t"
        "v_cvt_pk_bf16_f32 %1, 0, %1\n\tv_cvt_pk_bf16_f32 %2, 0, %2\n\tv_cvt_pk_bf16_f32 %3, 0, %3\n\tv_cvt_pk_bf16_f32 %4, 0, %4\n\t"
        "v_cvt_pk_bf16_f32 %5, 0, %5\n\tv_cvt_pk_bf16_f32 %6, 0, %6\n\tv_cvt_pk_bf16_f32 %7, 0, %7\n\t"
        : "=&v"(t[0]), "=&v"(t[1]), "=&v"(t[2]), "=&v"(t[3]), "=&v"(t[4]), "=&v"(t[5]), "=&v"(t[6]), "=&v"(t[7]), "=&v"(e[0]), "=&v"(e[1]), "=&v"(e[2]), "=&v"(e[3]), "=&v"(e[4]), "=&v"(e[5]), "=&v"(e[6])
        : "v"(ka), "v"(kb), "v"(kc), "v"(kd), "v"(w[2]), "v"(w[3]), "s"(0xffff0000u));
}
__device__ __forceinline__ void kv4b(unsigned* t, const unsigned* e, const unsigned* w) {
    asm volatile(
        "v_add_f32 %0, %0, %4\n\tv_add_f32 %1, %1, %5\n\tv_add_f32 %2, %2, %6\n\tv_add_f32 %3, %3, %7\n\t"
        "v_mul_f32 %4, %14, %8\n\tv_mul_f32 %5, %14, %9\n\tv_mul_f32 %6, %14, %10\n\tv_mul_f32 %7, %14, %11\n\t"
        "v_cvt_pk_bf16_f32 %0, 0, %0\n\tv_cvt_pk_bf16_f32 %1, 0, %1\n\tv_cvt_pk_bf16_f32 %2, 0, %2\n\tv_cvt_pk_bf16_f32 %3, 0, %3\n\t"
        "v_cvt_pk_bf16_f32 %4, 0, %4\n\tv_cvt_pk_bf16_f32 %5, 0, %5\n\tv_cvt_pk_bf16_f32 %6, 0, %6\n\tv_cvt_pk_bf16_f32 %7, 0, %7\n\t"
        "v_add_f32 %0, %0, %4\n\tv_add_f32 %1, %1, %5\n\tv_add_f32 %2, %2, %6\n\tv_add_f32 %3, %3, %7\n\t"
        "v_mul_f32 %4, %13, %9\n\tv_mul_f32 %5, %13, %10\n\tv_mul_f32 %6, %13, %11\n\tv_mul_f32 %7, %13, %12\n\t"
        : "+v"(t[0]), "+v"(t[1]), "+v"(t[2]), "+v"(t[3]), "+v"(t[4]), "+v"(t[5]), "+v"(t[6]), "+v"(t[7]) : "v"(e[2]), "v"(e[3]), "v"(e[4]), "v"(e[5]), "v"(e[6]), "v"(w[0]), "v"(w[1]));
}
__device__ __forceinline__ void kv4c(unsigned* t, unsigned v0, unsigned v1) {
    asm volatile(
        "v_cvt_pk_bf16_f32 %0, 0, %0\n\tv_cvt_pk_bf16_f32 %1, 0, %1\n\tv_cvt_pk_bf16_f32 %2, 0, %2\n\tv_cvt_pk_bf16_f32 %3, 0, %3\n\t"
        "v_cvt_pk_bf16_f32 %4, 0, %4\n\tv_cvt_pk_bf16_f32 %5, 0, %5\n\tv_cvt_pk_bf16_f32 %6, 0, %6\n\tv_cvt_pk_bf16_f32 %7, 0, %7\n\t"
        "v_add_f32 %0, %0, %4\n\tv_add_f32 %1, %1, %5\n\tv_add_f32 %2, %2, %6\n\tv_add_f32 %3, %3, %7\n\t"
        "v_lshlrev_b32 %4, 16, %8\n\tv_and_b32 %5, %10, %8\n\tv_lshlrev_b32 %6, 16, %9\n\tv_and_b32 %7, %10, %9\n\t"
        "v_cvt_pk_bf16_f32 %0, 0, %0\n\tv_cvt_pk_bf16_f32 %1, 0, %1\n\tv_cvt_pk_bf16_f32 %2, 0, %2\n\tv_cvt_pk_bf16_f32 %3, 0, %3\n\t"
        "v_mul_f32 %0, %0, %4\n\tv_mul_f32 %1, %1, %5\n\tv_mul_f32 %2, %2, %6\n\tv_mul_f32 %3, %3, %7\n\t"
        "v_cvt_pk_bf16_f32 %0, %1, %0\n\tv_cvt_pk_bf16_f32 %2, %3, %2\n\t"
        : "+v"(t[0]), "+v"(t[1]), "+v"(t[2]), "+v"(t[3]), "+v"(t[4]), "+v"(t[5]), "+v"(t[6]), "+v"(t[7]) : "v"(v0), "v"(v1), "s"(0xffff0000u));
}
__device__ __forceinline__ f32x4 mul4bf(u32x2 x, const unsigned* d) {
    float a, b, c, e;
    asm("v_dot2_f32_bf16 %0, %4, %6, 0\n\tv_dot2_f32_bf16 %1, %4, %7, 0\n\tv_dot2_f32_bf16 %2, %5, %8, 0\n\tv_dot2_f32_bf16 %3, %5, %9, 0\n\t"
        "v_cvt_pk_bf16_f32 %0, 0, %0\n\tv_cvt_pk_bf16_f32 %1, 0, %1\n\tv_cvt_pk_bf16_f32 %2, 0, %2\n\tv_cvt_pk_bf16_f32 %3, 0, %3"
        : "=&v"(a), "=&v"(b), "=&v"(c), "=&v"(e) : "v"(x[0]), "v"(x[1]), "v"(d[0]), "v"(d[1]), "v"(d[2]), "v"(d[3]));
    return f32x4{a, b, c, e};
}
template <int BIT> __device__ __forceinline__ void quad_swap(u32x4& a, u32x4& b) {
    unsigned n0, n1, n2, n3, b0 = b[0], b1 = b[1], b2 = b[2], b3 = b[3];
#define QS_(d, s, k) "v_cndmask_b32_dpp " d ", " s ", " k ", vcc quad_perm:[%14,%15,%16,%17] row_mask:0xf bank_mask:0xf\n\t"
    asm("s_mov_b64 vcc, %12\n\ts_nop 0\n\t" QS_("%0", "%4", "%8") QS_("%1", "%5", "%9") QS_("%2", "%6", "%10") QS_("%3", "%7", "%11")
        "s_mov_b64 vcc, %13\n\t" QS_("%4", "%8", "%4") QS_("%5", "%9", "%5") QS_("%6", "%10", "%6") QS_("%7", "%11", "%7")
        : "=&v"(n0), "=&v"(n1), "=&v"(n2), "=&v"(n3), "+v"(b0), "+v"(b1), "+v"(b2), "+v"(b3)
        : "v"(a[0]), "v"(a[1]), "v"(a[2]), "v"(a[3]), "s"(BIT ? 0x3333333333333333ull : 0x5555555555555555ull), "s"(BIT ? 0xccccccccccccccccull : 0xaaaaaaaaaaaaaaaaull),
          "n"(BIT ? 2 : 1), "n"(BIT ? 3 : 0), "n"(BIT ? 0 : 3), "n"(BIT ? 1 : 2) : "vcc");
#undef QS_
    a = u32x4{n0, n1, n2, n3}; b = u32x4{b0, b1, b2, b3};
}
__device__ __forceinline__ void kv_dma(unsigned ldsaddr, unsigned k0, unsigned k1, unsigned k2, unsigned k3, const char* sb) {
    asm volatile("s_mov_b32 m0, %5\n\ts_nop 0\n\tglobal_load_lds_dwordx4 %0, %4" NTK "\n\tglobal_load_lds_dwordx4 %1, %4 offset:1024" NTK "\n\t"
                 "global_load_lds_dwordx4 %2, %4 offset:2048" NTK "\n\tglobal_load_lds_dwordx4 %3, %4 offset:3072" NTK
                 :: "v"(k0), "v"(k1), "v"(k2), "v"(k3), "s"(sb), "s"(ldsaddr) : "memory");
}
#define IF_(c, C) "s_cmp_lg_u32 " c ", " C "\n\ts_cbranch_scc1 1f\n\t"
template <int C, int N> __device__ __forceinline__ void wait_if(int c) { asm volatile(IF_("%0", "%1") "s_waitcnt vmcnt(%2)\n1:" :: "s"(c), "n"(C), "n"(N) : "scc", "memory"); }
template <int C, int M> __device__ __forceinline__ void flag_if(int c, unsigned voff, u32x2 v, const char* sb) {
    asm volatile(IF_("%0", "%1") "s_mov_b32 exec_lo, %5\n\ts_mov_b32 exec_hi, 0\n\tglobal_store_dwordx2 %2, %3, %4 sc1\n\ts_mov_b64 exec, -1\n1:" :: "s"(c), "n"(C), "v"(voff), "v"(v), "s"(sb), "n"(M) : "scc", "memory"); }
template <int C> __device__ __forceinline__ void st4_if(int c, unsigned voff, const u32x4* v, const char* s0, const char* s1) {
    asm volatile(IF_("%0", "%1") "global_store_dwordx4 %2, %3, %7 sc1\n\tglobal_store_dwordx4 %2, %4, %7 offset:1024 sc1\n\tglobal_store_dwordx4 %2, %5, %8 sc1\n\tglobal_store_dwordx4 %2, %6, %8 offset:1024 sc1\n\ts_nop 1\n1:"
                 :: "s"(c), "n"(C), "v"(voff), "v"(v[0]), "v"(v[1]), "v"(v[2]), "v"(v[3]), "s"(s0), "s"(s1) : "scc", "memory"); }
template <int C> __device__ __forceinline__ void ld4_if(int c, unsigned voff, u32x4* v, const char* s0) {
    asm volatile(IF_("%4", "%5") "global_load_dwordx4 %0, %6, %7 nt\n\tglobal_load_dwordx4 %1, %6, %7 offset:1024 nt\n\tglobal_load_dwordx4 %2, %6, %7 offset:2048 nt\n\tglobal_load_dwordx4 %3, %6, %7 offset:3072 nt\n1:"
                 : "+v"(v[0]), "+v"(v[1]), "+v"(v[2]), "+v"(v[3]) : "s"(c), "n"(C), "v"(voff), "s"(s0) : "scc", "memory"); }
template <int C> __device__ __forceinline__ void ld8_if(int c, unsigned voff, u32x2* v, const char* s0, const char* s1) {
    asm volatile(IF_("%8", "%9") "global_load_dwordx2 %0, %10, %11 offset:-4096 nt\n\tglobal_load_dwordx2 %1, %10, %11 offset:-2048 nt\n\tglobal_load_dwordx2 %2, %10, %11 nt\n\tglobal_load_dwordx2 %3, %10, %11 offset:2048 nt\n\t"
                 "global_load_dwordx2 %4, %10, %12 offset:-4096 nt\n\tglobal_load_dwordx2 %5, %10, %12 offset:-2048 nt\n\tglobal_load_dwordx2 %6, %10, %12 nt\n\tglobal_load_dwordx2 %7, %10, %12 offset:2048 nt\n1:"
                 : "+v"(v[0]), "+v"(v[1]), "+v"(v[2]), "+v"(v[3]), "+v"(v[4]), "+v"(v[5]), "+v"(v[6]), "+v"(v[7]) : "s"(c), "n"(C), "v"(voff), "s"(s0), "s"(s1) : "scc", "memory"); }
template <int E> __device__ __forceinline__ void sy_post(unsigned myaddr) { asm volatile("ds_write_b32 %0, %0 offset:%1" :: "v"(myaddr), "n"(E * 64) : "memory"); }
template <int E, int SL = 1> __device__ __forceinline__ unsigned sy_wait(unsigned addr) {
    unsigned v;
    asm volatile("s_branch 2f\n1:\n\ts_sleep %3\n2:\n\tds_read_b32 %0, %1 offset:%2\n\ts_waitcnt lgkmcnt(0)\n\tv_cmp_eq_u32 vcc, 0, %0\n\ts_cbranch_vccnz 1b"
                 : "=&v"(v) : "v"(addr), "n"(E * 64), "n"(SL) : "vcc", "memory");
    return v;
}
template <int OFF, class T> __device__ __forceinline__ void dsrtr_(T& dst, unsigned addr) { asm volatile("ds_read_b64_tr_b16 %0, %1 offset:%2" : "=v"(dst) : "v"(addr), "n"(OFF)); }

constexpr size_t WS_H = 0, WS_KVT = (size_t)10 << 20, WS_ZT = (size_t)15 << 20, WS_E = (size_t)20 << 20, WS_FLAG = WS_E + (2048 << 10);
#define HP 65536
#define KP 8192
#define ZP 4096
constexpr int FL_KV = 0, FL_Z = 65536, FL_H = 131072, FL_EPOCH = 196608;
constexpr unsigned SPIN_CAP = 1u << 17;
#define ABL 0
#define CSPLIT 1
#define MERGE 3
#define CB 13
#define PAUSE 0
#define QLATE 0
#define CVPRIO 0
#define STG_SEL (idx & 3)
#define SP0 3
#define SDP 1
#define LP0 3
#define LDP 2
#define KVS 5
#define KVU 3
#define QIT 1
#define ZB 3
#define OS 2
#define MPRIO 2

#define ROT 0x4042
#define R2 0x41
#define CPF 0
#define PRE 4
#define PRO 9
#define G1NT 0
#define G1NTS 16
#define TMIX 0
#define TLBT 0
#define CDEF 0
#define CVLATE 1
#define FPRIO 3
#define YS 0
#define DP0 1
#define DP1 5
#define AWB 1
#define PK 0
#define PU 14
#define PC0 13
#define PC1 14
#define PC2 15
#define PC3 16
#define PQ 0
#define SPF 0
#define SPH 0
#define NB1 4
#define EP 1
#define ESL 0
#define CFB 0
#define P1 1
#define P0 1
#define BKE 0
#define EALL 0
#define EREP 0
#define TOUCH 0
#define FILLB 0
#define CPOST 1
#define CEARLY 0
#define NOOWN 0
#define PFIRST 0
#define ZB0 0
#define CATCH 4
#define PWE 0
#define SWAP 1
#define KVM 0x0FE
#define KFD 3
#define ZW 2
#define ZX 1
#define ZP0 3
#define BARM 0x22220
#define NR 6
#define WPS 2
#define CD0 3
#define AWX 8
#define VBM 0
#define ZOWN 1
#define ZPS 4
#define XAB 0
#define QM 1
#define OSG 0
#define KPL 0xFE
#define KPRIO 0
#define GPRIO 1
#define PST 0x7654
#define PLT 0xB964
#define LC 0
#define PRE2 PRE
#define KVD 0
#define PAIR 1
#define G2P1 0
#define EPRIO 0
#define LSW 1
#define EGX 28
#define CR1 0
#define CR2 0
#define CR3 0
#define CRL (2 * NR - CR1 - CR2 - CR3)
#define UE 1
#define UPRIO 0
#define AMF 7
#define M1 1
#define W2 1
#define W1 0
#define NBK 3
#define XB 4
#define KFB 9
#define APRE 0
#define CB0 1
#define AP0 8
#define HTSE(i)
#define KAT 2
#define SD 2
#define KV3 0x088888
#define KOWN 1
#define HKV 0
#define KVP NBK
#define AWN 8
#define CRATE 2
#define ZSB 0
#define ZFB 1
#define ZSL 0
#define OSL 0
constexpr int it_cnt(int p, int k, int last) {
    const int pos[8] = {PK, PU, PC0, PC1, PQ, PU - 1, PC2, PC3}, n[8] = {4, 4, 4, 4, 8, 1, 4, 4};
    int c = 0;
    for (int i = 0; i < 8; ++i) if (pos[i] <= last && (pos[i] > p || (pos[i] == p && i > k))) c += n[i];
    return c;
}
constexpr int nib(int t, int c) { return (t >> (4 * c)) & 15; }
constexpr bool nib_any(int t, int v) { return nib(t, 0) == v || nib(t, 1) == v || nib(t, 2) == v || nib(t, 3) == v; }
constexpr int kv_wait(int late) {
    int t = 0;
    for (int c = 0; c < 4; ++c) t |= ((nib(PLT, c) >= nib(PST, c) && nib(PLT, c) < nib(PST, c) + KFD) == late ? nib(PST, c) + KFD : 15) << (4 * c);
    return t;
}
constexpr int G1STAGE = 32768;
constexpr int g1_slot(int s) { return (NB1 == 5 ? (s + 3) % 5 : s & 3) * G1STAGE; }
constexpr int L_KS = NB1 == 5 ? 65536 : 131072, L_VS = L_KS + 16384;
constexpr int L_TS = 3 * G1STAGE, L_TR = L_TS + 3072;
constexpr int CST = 4288;
constexpr int UOFF = 2 * CST + 128, UPL = 2304;
constexpr int CHSZ = UOFF + 2 * UPL + 32;
constexpr int L_AGG = 4 * CHSZ, L_ES = L_AGG + 15 * 512, L_SY = L_ES + 2048;
constexpr int L_HT = 98304;
constexpr int L_C = 98304, G2STAGE = 16384;
constexpr int L_Y = 16384;
static_assert(L_SY + 384 <= 65536 && L_AGG >= 49152 && UOFF % 16 == 0 && CHSZ % 256 == 32, "lds");

__device__ __forceinline__ int img_off(int r, int c) { return (r >> 1) * 256 + ((((r & 1) * 8 + c) ^ ((r >> 1) & 15)) << 4); }

constexpr int g1_upto(int g) {
    int n = 4 * NBK;
    for (int t = SD; t <= g; ++t) {
        int sb = (t + 1) / 2, sub = (t + 1) % 2, allowed = sb ? 4 * (sb + 3) + 2 * (sub + 1) : 12;
        if (allowed > 64) allowed = 64;
        n = n + CATCH < allowed ? n + CATCH : allowed;
    }
    return n;
}
constexpr int g1_cnt(int s) { return HKV || s >= KVP ? g1_upto(2 * s - 2) - 4 * (s + 1) : s >= KAT ? g1_upto(2 * s - 2) - 4 * KVP : g1_upto(2 * s - 2) - 4 * (s + 1) + 4; }
static_assert(KVP >= 1 && KVP <= NBK && AP0 % 4 == 0, "schedule");
static_assert(g1_upto(28) == 64, "schedule");
constexpr int AW = AWN;
constexpr int g2_place(int s) { return s < 5 ? L_C + ((s + 2) & 3) * G2STAGE : s < 7 ? (s - 1) * G2STAGE : s < 10 ? (9 - s) * G2STAGE : g2_place(s - 9); }
constexpr bool g2_bar(int g) { return (BARM >> g) & 1; }
struct G2Plan { int upto[33], wr[33], ald[64][2], seq[164], use[64]; };
constexpr G2Plan g2_plan() {
    G2Plan p = {};
    int nc = 14 + 2 * NR, nw = 0, nx = 4, lastb = 0, n = 0;
    for (int i = 0; i < 164; ++i) p.seq[i] = 1000;
    for (; nx < 4 + AW; ++nx) p.seq[nx] = ++n;
    for (int t = 0; t < 32; ++t) {
        p.upto[t] = nc; p.wr[t] = nw;
        if (t < 2) continue;
        if (g2_bar(t)) lastb = t;
        const int w = AW + nw < AWX ? AW + nw : AWX;
        for (int f = 2 * t; f < 2 * t + 2; ++f) {
            p.use[f] = n;
            if (nc < 32 && nc == p.upto[t] + (f & 1) && (f & 1) < CRATE && t >= CD0 && lastb >= (nc < 20 ? 1 : (nc | 1) - 18)) p.seq[100 + nc++] = ++n;
            for (int k = 0; k < 2; ++k) {
                p.ald[f][k] = -1;
                if (nx < 64 && nx - f - k < w) { p.ald[f][k] = nx; p.seq[nx++] = ++n; }
            }
        }
        for (int k = 0; k < WPS; ++k) if (nw < 2 * NR && lastb >= (nw < 6 ? 1 : (nw | 1) - 4)) ++nw;
    }
    p.upto[32] = nc; p.wr[32] = nw;
    return p;
}
constexpr G2Plan G2P = g2_plan();
constexpr int g2_upto(int g) { return G2P.upto[g + 1]; }
constexpr int g2_wr(int g) { return G2P.wr[g + 1]; }
constexpr int g2_ald(int f, int k) { return G2P.ald[f][k]; }
constexpr int g2_cnt(int id, int at) { return G2P.use[at] - G2P.seq[id]; }
constexpr int g2_cover(int g) { int s = (g + 1) / 2; while (s < 15 && !g2_bar(2 * s + 1)) ++s; return s; }
static_assert(BARM >> 17 && g2_cover(3) < 7 && CRATE <= 2, "the epilogue needs stage 8 behind a barrier");

constexpr int cv_m(int o) { return (o & 1) ? 7 - (o >> 1) : o >> 1; }
constexpr int cv_first(int o) { int p = 0; for (int i = 0; i < o; ++i) p += 8 - cv_m(i); return p; }
constexpr int cv_grp(int p) { int o = 0; while (cv_first(o + 1) <= p) ++o; return o; }
constexpr int cv_read(int p) {
    int slot[40] = {};
    for (int i = 0; i < 40; ++i) slot[i] = -1;
    slot[0] = 2 * 3 + 1;
    for (int o = 0; o < 8; ++o) {
        int q = cv_first(o + 1) - 1;
        for (int h = 0; h < 2; ++h) { while (slot[q] >= 0) ++q; slot[q] = 2 * (o + 4) + h; }
    }
    const int bs[8] = {1, 3, 5, 11, 12, 13, 19, 21};
    for (int jb = 0; jb < 8; ++jb) slot[bs[jb]] = 32 + jb;
    return slot[p];
}
static_assert(cv_read(35) == 22 && cv_read(36) == 23 && cv_read(37) == -1 && cv_read(10) == 11 && cv_read(15) == 12 && cv_read(18) == 15 && cv_read(24) == 17, "conv schedule");
constexpr int cv_wait(int o) {
    int n = 0;
    for (int q = cv_first(o) - 1;; --q) {
        const int rd = cv_read(q < 0 ? q + 36 : q);
        if (rd == 2 * (o + (q < 0 ? 8 : 0)) + 1) return n;
        if (rd >= 0) ++n;
    }
}

struct Args {
    const char *q, *k, *v, *sk, *ssmk, *ssmD, *x, *lre, *lim, *bre, *bim, *cre, *cim, *logdt, *dskip;
    char *out, *ws;
};

typedef __attribute__((ext_vector_type(8))) unsigned long long u64x8;
__global__ __launch_bounds__(768, 3) void fused(Args) {
    __shared__ __attribute__((aligned(1024))) char shm[163840];
    TS0a();
    u64x8 ka0, ka1; unsigned long long ka2;
    const char* pc0;
    int code_n;
    asm volatile("s_load_dwordx16 %0, %5, 0x0\n\ts_load_dwordx16 %1, %5, 0x40\n\ts_load_dwordx2 %2, %5, 0x80\n\t"
                 "s_getpc_b64 %3\n.Lfk_code_0:\n\ts_mov_b32 %4, .Lfk_code_a-.Lfk_code_0-4\n\ts_max_i32 %4, %4, .Lfk_code_b-.Lfk_code_0-4"
                 : "=&s"(ka0), "=&s"(ka1), "=&s"(ka2), "=&s"(pc0), "=&s"(code_n) : "s"(__builtin_amdgcn_kernarg_segment_ptr()) : "scc");
    const int tid = threadIdx.x, lane = tid & 63, wraw = __builtin_amdgcn_readfirstlane(tid >> 6);
    const int wid = !SWAP ? wraw : wraw < 4 ? wraw + 8 : wraw - 4;
    const int b = blockIdx.x, xcd = b & 7, idx = b >> 3;
    asm volatile("s_cmp_lt_u32 %0, 8\n\ts_cbranch_scc1 1f\n\ts_setprio 3\n\ts_cmp_lg_u32 %0, 8\n\ts_cbranch_scc1 1f\n\t"
                 "v_lshl_add_u32 v161, %1, 7, %2\n\tv_min_i32 v161, %3, v161\n\ts_mov_b64 exec, 0xffff\n\tglobal_load_dword v161, v161, %4\n\ts_mov_b64 exec, -1\n1:"
                 :: "s"(wid), "v"(lane), "s"(idx * 2048), "s"(code_n), "s"(pc0) : "scc", "memory", "v161");
    TS_DECL
    const int tm = (xcd >> 1) * 4 + (idx >> 3), tn = (xcd & 1) * 8 + (idx & 7);
    const int wm = wid & 3, wn = (wid >> 2) & 1, l31 = lane & 31, lh = lane >> 5;
    const unsigned lb = (unsigned)(uintptr_t)shm;
    const unsigned l16 = lane * 16;
    const int l15 = lane & 15, g4 = lane >> 4, sq = 2 * wn + (wm & 1), st = sq * 16 + l15, run = (wm >> 1) * 4 + g4;
    unsigned kvv[4];
    const int trow = idx * 64 * 2048 + xcd * 256;
    if (wid < 8) {
        const int v = l15 ^ g4, hi = v >> 3;
        const int r0 = wn ? tn * 64 + (wm >> 1) * 32 : tm * 128 + (wm >> 1) * 64 + (wm & 1) * 4;
        const unsigned d0 = (r0 + ((g4 * (wn ? 2 : 9)) & (wn ? 6 : 17))) * 2048 + (v & 7) * 16, odd = wn ? 11 : 14;
        kvv[0] = d0 + (hi << odd); kvv[2] = d0 + ((hi ^ 1) << odd) + (wn ? 32768 - 2048 : 4096 - 2048);
        kvv[1] = (kvv[0] ^ 64) + (wn ? 16384 - 1024 : 65536 - 1024); kvv[3] = (kvv[2] ^ 64) + (wn ? 16384 - 1024 : 65536 - 1024);
    } else {
        kvv[0] = trow + (idx ? -4 * 2048 : 0) + g4 * 2048 + wm * 64 + l15 * 4;
        asm volatile("v_writelane_b32 %0, 0, 0\n\tv_writelane_b32 %0, 4, 1\n\tv_writelane_b32 %0, %1, 2" : "+v"(kvv[0]) : "s"((xcd * 64 + idx * 2 + (wm >> 1)) * 4));
        kvv[1] = (xcd * 128 + wm * 32 + l31) * 8;
        kvv[2] = kvv[3] = (tn * 64 + lane) * 2;
    }
    unsigned g1dst = lb + wid * 4096;
    int rot7 = ((((ROT >> 12) & 15) * (idx & 7) + ((ROT >> 8) & 15) * (idx >> 3) + ((ROT >> 4) & 15) * (xcd >> 1) + (ROT & 15) * (xcd & 1)) & 15) << 7;
    asm volatile("s_waitcnt lgkmcnt(0)" : "+s"(ka0), "+s"(ka1), "+s"(ka2), "+s"(g1dst), "+s"(rot7), "+v"(kvv[0]), "+v"(kvv[1]), "+v"(kvv[2]), "+v"(kvv[3]));
    Args a;
    a.q = (const char*)ka0[0]; a.k = (const char*)ka0[1]; a.v = (const char*)ka0[2]; a.sk = (const char*)ka0[3]; a.ssmk = (const char*)ka0[4]; a.ssmD = (const char*)ka0[5];
    a.x = (const char*)ka0[6]; a.lre = (const char*)ka0[7]; a.lim = (const char*)ka1[0]; a.bre = (const char*)ka1[1]; a.bim = (const char*)ka1[2]; a.cre = (const char*)ka1[3];
    a.cim = (const char*)ka1[4]; a.logdt = (const char*)ka1[5]; a.dskip = (const char*)ka1[6]; a.out = (char*)ka1[7]; a.ws = (char*)ka2;
    char* const ws = a.ws;
    const char* const flg = ws + WS_FLAG;
    TS(26);
    const char* g1src = wn ? ((wm & 1) ? a.bim : a.bre) : a.x;
    asm volatile("s_mov_b32 m0, %[m]\n\ts_cmp_gt_u32 %[w], 7\n\ts_cbranch_scc1 1f\n\t"
                 "global_load_lds_dwordx4 %[k0], %[g]" FLX(DF) "\n\tglobal_load_lds_dwordx4 %[k1], %[g] offset:1024" FLX(DF) "\n\t"
                 "global_load_lds_dwordx4 %[k2], %[g] offset:2048" FLX(DF) "\n\tglobal_load_lds_dwordx4 %[k3], %[g] offset:3072" FLX(DF) "\n\t"
                 "s_branch 2f\n1:\n\tglobal_load_ushort v164, %[k2], %[c1]\n\tglobal_load_ushort v165, %[k2], %[c2]\n\tglobal_load_ushort v166, %[k2], %[c3]\n\t"
                 "v_mov_b64 v[162:163], %[kv]\n\ts_mov_b64 exec, 7\n\tv_mov_b64 v[162:163], %[d]\n\ts_mov_b64 exec, 3\n\tv_mov_b64 v[162:163], %[e]\n\ts_mov_b64 exec, -1\n\t"
                 "v_mad_u64_u32 v[162:163], vcc, %[k0], 1, v[162:163]\n\tglobal_load_dword v160, v[162:163], off sc1\n\tglobal_load_dwordx2 v[162:163], %[k1], %[sk]\n2:"
                 :
                 : [w] "s"(wid), [c1] "s"(a.logdt), [c2] "s"(a.lre), [c3] "s"(a.lim), [d] "s"(a.ssmD), [e] "s"(flg + FL_EPOCH), [sk] "s"(a.sk),
                   [k0] "v"(kvv[0]), [k1] "v"(kvv[1]), [k2] "v"(kvv[2]), [k3] "v"(kvv[3]), [kv] "s"(a.k), [g] "s"(g1src + rot7), [m] "s"(g1dst)
                 : "scc", "vcc", "memory", "v160", "v161", "v162", "v163", "v164", "v165", "v166");
    TS0b();
    TS(27);
    if (P1) BAR();
#define SLICE_TABLE() unsigned slo, shi; { int ix = idx; asm volatile("" : "+s"(ix)); \
        const int rot2 = (((R2 >> 4) & 15) * (ix >> 3) + (R2 & 15) * (xcd >> 1)) & 15, es = (tn - rot2) & 15; \
        const unsigned long long id = 0xFEDCBA9876543210ull, seq = rot2 ? (id >> (4 * rot2)) | (id << (64 - 4 * rot2)) : id; \
        const unsigned long long lo = (1ull << (4 * es)) - 1, tab = ((seq & lo) << 4) | (seq & ~((lo << 4) | 15)) | tn; \
        slo = (unsigned)tab; shi = (unsigned)(tab >> 32); asm volatile("" : "+s"(slo), "+s"(shi)); }
#define SLICE(st) ((((st) < 8 ? slo : shi) >> (4 * ((st) & 7))) & 15)
    typedef __attribute__((ext_vector_type(4))) short v4i16;
    int role = wid;
    asm volatile("" : "+s"(role));

    if (role >= 8) {
        int lane;
        asm volatile("v_mbcnt_lo_u32_b32 %0, -1, 0\n\tv_mbcnt_hi_u32_b32 %0, -1, %0" : "=v"(lane));
        __builtin_assume(lane >= 0 && lane < 64);
        const int l31 = lane & 31, lh = lane >> 5, l15 = lane & 15, g4 = lane >> 4;
        const unsigned l16 = lane * 16;
        const int chn = xcd * 128 + idx * 4 + wm;
        const unsigned cb = lb + wm * CHSZ;
        const int r = l15;
        const unsigned fpub = KOWN ? (xcd * 32 + l31) * 256 + idx * 8 : xcd * 256 + idx * 8, fpoll = KOWN ? (xcd * 32 + idx) * 256 + l31 * 8 : xcd * 256 + l31 * 8;
        u32x2 fl; u32x4 sw[4], uu[4];
        unsigned tch = 0;
        if (TOUCH) {
            const int ln = wm * 4 + (lane & 3);
            const unsigned fo = (unsigned)(WS_FLAG - WS_E) + (wm == 0 ? FL_H + (tm * 16 + tn) * 128 : (wm == 1 ? FL_KV : FL_Z) + xcd * 256 + (lane & 1) * 128);
            const unsigned o = (lane & 4) && (TOUCH & 2) ? fo : ((tm * 2 + (ln >> 3)) * 1024 + tn * 64) * 16 + (ln & 7) * 128;
            asm volatile("s_mov_b64 exec, 0xff\n\tglobal_load_dword %0, %1, %2\n\ts_mov_b64 exec, -1" : "=&v"(tch) : "v"(o), "s"(ws + WS_E) : "memory");
        }
        SLICE_TABLE();
        TS(1);
        unsigned sm, cl[3]; u32x2 kw0;
        asm volatile("s_waitcnt vmcnt(0)" : "={v160}"(sm), "={v[162:163]}"(kw0), "={v164}"(cl[0]), "={v165}"(cl[1]), "={v166}"(cl[2]) :: "memory", "v161");
        TS(22);
        const unsigned hal = take(sm);
        const unsigned tagA = __builtin_amdgcn_readlane(hal, 0) + 1, tagB = __builtin_amdgcn_readlane(hal, 1) + (tagA == 0 ? 1u : 0u);
        const u32x2 tag = {tagA, tagB};
        f32x2 L1, C1;
        {
            const float dt = rbf(expf(bf2f(take(cl[0]))));
            const float lr = bf2f(take(cl[1])), li = bf2f(take(cl[2]));
            const float e = expf(dt * lr);
            float sn, cs;
            sincosf(dt * li, &sn, &cs);
            L1 = f32x2{e * cs, e * sn};
            const float nr = L1[0] - 1.f, ni = L1[1], den = lr * lr + li * li;
            C1 = f32x2{2.f * ((nr * lr + ni * li) / den), 2.f * ((ni * lr - nr * li) / den)};
            TIE(L1); TIE(C1);
        }

        f32x2 p[11];
        p[0] = L1;
        if (PWE) {
#pragma unroll
            for (int i = 1; i <= 10; ++i) { p[i] = cmul4(p[i - 1], p[i - 1]); TIE(p[i]); }
        }
        {
            if (!PWE) {
#pragma unroll
                for (int i = 1; i <= 10; ++i) p[i] = cmul4(p[i - 1], p[i - 1]);
            }
            const f32x2 one = {1.f, 0.f}, zero = {0.f, 0.f};
            const unsigned ts = lb + L_TS + l16, tr = lb + L_TR + wm * 2048 + l16;
            if (wm == 0) {
                *LDSP(f32x4, ts) = f32x4{L1[0], L1[1], C1[0], C1[1]};
                *LDSP(f32x4, ts + 1024) = f32x4{p[3][0], p[3][1], p[4][0], p[4][1]};
                *LDSP(f32x4, ts + 2048) = f32x4{p[6][0], p[6][1], __uint_as_float(tagA), __uint_as_float(tagB)};
            }
            f32x2 Q[2], V[2], W[2];
            Q[0] = cmul4(cmul4(C1, (wm & 1) ? p[5] : one), (wm & 2) ? p[6] : one); Q[1] = cmul4(Q[0], p[4]);
            V[1] = (wm & 1) ? one : p[5]; V[0] = cmul4(V[1], p[4]);
            W[1] = cmul4((wm & 1) ? one : p[9], (wm & 2) ? one : p[10]); W[0] = cmul4(W[1], p[8]);
#pragma unroll
            for (int i = 0; i < 2; ++i) {
                const f32x2 Q8 = cmul4(Q[i], p[3]), U = 4 * wm + 2 * i >= 16 - tm ? cmul4(W[i], p[7]) : zero, Wz = 4 * wm + 2 * i + 1 >= 16 - tm ? W[i] : zero;
                *LDSP(f32x4, tr + i * 1024) = f32x4{Q[i][0], Q8[0], Q[i][1], Q8[1]};
                *LDSP(f32x4, tr + i * 1024 + 8192) = f32x4{Wz[0], Wz[1], U[0], U[1]};
                *LDSP(f32x2, tr + i * 1024 + 16384) = V[i];
            }
        }
        auto b0 = [&]() {
            unsigned k0 = trow + wm * 32768 + g4 * 2048 + ((l15 ^ (2 * g4)) << 4), k1 = k0 + 7168, k2 = (k0 ^ 128) + 14336, k3 = k2 + 7168;
            if (HKV) { TIE(k0); TIE(k1); TIE(k2); TIE(k3); }
            if (HKV == 1) { kv_dma(lb + L_KS + wm * 4096, k0, k1, k2, k3, a.k); kv_dma(lb + L_VS + wm * 4096, k0, k1, k2, k3, a.v); }
            WAIT_L(0); BAR(); HTSE(1);
            if (HKV == 2) { kv_dma(lb + L_KS + wm * 4096, k0, k1, k2, k3, a.k); kv_dma(lb + L_VS + wm * 4096, k0, k1, k2, k3, a.v); }
        };
        if (CB0 == 0) b0();
        const unsigned dskl = (__builtin_amdgcn_readlane(hal, 2) >> ((wm & 1) * 16)) & 0xffffu;
        u32x4 kvo[4];
        u32x2 qv[8];
        const char* krow = a.ssmk + (size_t)chn * 4096;
        const char* qs = a.q + (size_t)(idx * 64 + wm * 16) * 2048 + xcd * 256;
        const char* kvd = ws + WS_KVT + (size_t)(xcd * 8 + wm * 2) * 65536 + idx * 2048;
        const int pl = (PLT >> ((STG_SEL) * 4)) & 15;
        const unsigned qo = lh * 8192 + l31 * 8 + 4096;
        auto hb = [&](auto nc) {
            constexpr int nb = decltype(nc)::value;
            if constexpr (nb == KAT && HKV) WAIT_V(0);
            if constexpr (nb == KFB) { WAIT_V(12); int lm = (1 << KFB) - (2 << (KAT + __builtin_popcount(KV3))); ties_(lm); wait_if<0, 0>((lm >> pl) & 1); }
            BAR();
            if constexpr (nb == KFB) flag_if<0, KOWN ? -1 : 1>(wm, fpub, tag, flg + FL_KV);
            if constexpr (nib_any(PLT, nb)) { ld4_if<nb>(pl, l16, sw, krow); ld8_if<nb>(pl, qo, qv, qs, qs + 16384); }
        };
#define HB(n) hb(std::integral_constant<int, (n) + NBK>{})
        {
            unsigned kw[2][4], ta[2], th[2];
            u32x2 hm[2];
#pragma unroll
            for (int j = 0; j < 2; ++j) {
                const unsigned x = take(kw0[j]);
                const auto y = __builtin_amdgcn_permlane16_swap(x, x, false, false);
#pragma unroll
                for (int p = 0; p < 2; ++p) { kw[p][2 * j] = y[p] << 16; kw[p][2 * j + 1] = y[p] & 0xffff0000u; }
            }
#pragma unroll
            for (int p = 0; p < 2; ++p) {
                unsigned h[3];
#pragma unroll
                for (int j = 0; j < 3; ++j) h[j] = (__shfl(hal, p * 8 + (r >> 1) + 16 * (j + 1)) >> ((r & 1) * 16)) & 0xffffu;
                hm[p] = idx ? u32x2{h[0] << 16, h[1] | (h[2] << 16)} : u32x2{0u, 0u};
                const int q = r >> 2, pp = r & 3;
                ta[p] = lb + L_KS + (8 * g4 + q) * 256 + (((wm * 4 + p * 2 + (pp >> 1)) ^ (2 * (q | ((g4 & 1) << 2)))) << 4) + (pp & 1) * 8;
                th[p] = (ta[p] ^ 128) - 1024;
                TIE(hm[p]); TIE(ta[p]); TIE(th[p]);
#pragma unroll
                for (int j = 0; j < 4; ++j) TIE(kw[p][j]);
            }
            TS(21);
            if (CB0 == 1) b0();
#pragma unroll
            for (int i = 0; i < 4; ++i) asm volatile("" : "=v"(sw[i]), "=v"(qv[2 * i]), "=v"(qv[2 * i + 1]));
            static_for<1, KAT + 1>(hb);
            TS(2);
            u32x2 kt[4][3], vt[4][2];
#pragma unroll
            for (int u = 0; u < 4; ++u) {
                const int p = u >> 1, o = (u & 1) * 8192;
                auto tr = [&](unsigned ad) -> u32x2 { return __builtin_bit_cast(u32x2, __builtin_amdgcn_ds_read_tr16_b64_v4i16(LDSP(v4i16, ad))); };
                kt[u][0] = tr(th[p] + o); kt[u][1] = tr(ta[p] + o); vt[u][0] = tr(ta[p] + o + 16384); kt[u][2] = tr(ta[p] + o + 1024); vt[u][1] = tr(ta[p] + o + 17408);
                SB();
            }
            TS(14);
            __builtin_amdgcn_s_setprio(KPRIO);
            static_for<0, 4>([&](auto uc) {
                constexpr int u = decltype(uc)::value, p = u >> 1, oh = u & 1;
                const u32x2 k0 = oh == 0 && g4 == 0 ? hm[p] : kt[u][0];
                unsigned t0[8], t1[8], e[7];
#define K3(n) do { if constexpr ((KV3 >> (6 * u + n)) & 1) hb(std::integral_constant<int, KAT + 1 + __builtin_popcount(KV3 & ((1 << (6 * u + n)) - 1))>{}); } while (0)
                kv4a(t0, e, k0[0], k0[1], kt[u][1][0], kt[u][1][1], kw[p]); K3(0); kv4b(t0, e, kw[p]); K3(1); kv4c(t0, vt[u][0][0], vt[u][0][1]); K3(2);
                kv4a(t1, e, kt[u][1][0], kt[u][1][1], kt[u][2][0], kt[u][2][1], kw[p]); K3(3); kv4b(t1, e, kw[p]); K3(4); kv4c(t1, vt[u][1][0], vt[u][1][1]);
                kvo[u] = u32x4{t1[2], t1[0], t0[2], t0[0]};
                GST_B128_SC1(l16, kvo[u], kvd + (u >> 1) * 65536, (u & 1) * 1024);
                K3(5);
#undef K3
            });
            hb(std::integral_constant<int, KAT + 1 + __builtin_popcount(KV3)>{});
            __builtin_amdgcn_s_setprio(3);
        }
        TS(3);
        const char* usrc = ws + WS_KVT + (size_t)(xcd * 8 + (idx >> 2)) * 65536 + wm * 16384 + (idx & 3) * 64;
        const unsigned uo = (lane >> 2) * 256 + (lane & 3) * 16;
        const char* cs = ((wm & 2) ? a.cim : a.cre) + (size_t)(tn * 64 + (wm & 1) * 2) * 2048;
        unsigned co[4];
#pragma unroll
        for (int i = 0; i < 4; ++i) {
            const int pair = i * 4 + g4, v = r ^ pair, row = pair * 2 + (v >> 3);
            co[i] = ((row & 15) * 4 + (row >> 4)) * 2048 + (v & 7) * 16 - i * 1024;
        }
        bool kv_ok = false;
#pragma unroll
        for (int it = 0; it < 4; ++it) asm volatile("" : "=v"(uu[it]));
        auto cst = [&](int place, int slice) { const char* s = cs + (slice << 7); DMA(lb + place + wm * 4096, co[0], s); dmao_<1024>(co[1], s); dmao_<2048>(co[2], s); dmao_<3072>(co[3], s); };
        auto items = [&](auto pc) {
            constexpr int p = decltype(pc)::value;
            if constexpr (PU == p) {
                WAIT_V(it_cnt(PU - 1, 5, PU - 1)); TIE(fl);
                kv_ok = __all(fl[0] == tagA && fl[1] == tagB);
                const char* us = kv_ok ? usrc : a.x;
                for (int it = 0; it < 4; ++it) GLD_B128_SC1(uu[it], uo, us + it * 4096, 0);
            }
            if constexpr (PC0 == p) cst(g2_place(0), SLICE(0));
            if constexpr (PC1 == p) cst(g2_place(1), SLICE(1));
            if constexpr (PU == p + 1) GLD_B64_SC1(fl, fpoll, flg + FL_KV, 0);
            if constexpr (PC2 == p) cst(g2_place(5), SLICE(5));
            if constexpr (PC3 == p) cst(g2_place(6), SLICE(6));
        };
#define ITEMS(p) items(std::integral_constant<int, p>{})
        static_assert(KFB > KAT + 1 + __builtin_popcount(KV3) && KV3 < (1 << 23) && KFB <= 13 && KFB <= PC0 && KFB < PU - 1 && nib(PLT, 0) >= KAT && nib(PLT, 3) < PU - 1, "schedule");
        static_for<KAT + 2 + __builtin_popcount(KV3), 15>([&](auto nc) { hb(nc); if constexpr (decltype(nc)::value < 14) items(nc); });
        const unsigned ud = lb + (lane & 3) * CHSZ + UOFF + ((lane >> 2) & 1) * UPL + (16 + wm * 32 + (lane >> 3)) * 16;
        auto zero = [&]() {
            const u32x4 z4 = {0, 0, 0, 0};
            if (lane < 24) *LDSP(u32x4, lb + L_SY + l16) = z4;
            if (lane < 56) *LDSP(u32x4, lb + L_AGG + wm * 896 + l16) = z4;
            *LDSP(u32x4, lane < 32 ? cb + ((lane >> 3) & 1) * CST + (lane & 7) * 16 : cb + UOFF + (g4 & 1) * UPL + r * 16) = z4;
        };
        auto fill = [&]() {
#pragma unroll
            for (int it = 0; it < 4; ++it) TIE(sw[it]);
#pragma unroll
            for (int it = 0; it < 4; ++it) {
                const unsigned q = cb + 128 + it * 1024 + l16;
                const unsigned d0 = sw[it][0], d1 = sw[it][1], d2 = sw[it][2], d3 = sw[it][3];
                const unsigned o1 = it ? __builtin_amdgcn_readlane(sw[it ? it - 1 : 0][3], 63) : 0u;
                const unsigned x1 = __builtin_amdgcn_update_dpp(o1, d3, 0x138, 0xf, 0xf, false);
                *LDSP(u32x4, q) = sw[it];
                *LDSP(u32x4, q + CST) = u32x4{__builtin_amdgcn_alignbyte(d0, x1, 2), __builtin_amdgcn_alignbyte(d1, d0, 2), __builtin_amdgcn_alignbyte(d2, d1, 2), __builtin_amdgcn_alignbyte(d3, d2, 2)};
            }
        };
        auto uput = [&]() {
            WAIT_V(it_cnt(PU, 1, UE ? 15 : 16));
#pragma unroll
            for (int it = 0; it < 4; ++it) TIE(uu[it]);
            if (!kv_ok) {
                TSE(23);
                for (unsigned spin = 0; spin < SPIN_CAP; ++spin) {
                    GLD_B64_SC1(fl, fpoll, flg + FL_KV, 0);
                    WAIT_V(0); TIE(fl);
                    if (__all(fl[0] == tagA && fl[1] == tagB)) break;
                    __builtin_amdgcn_s_sleep(8);
                }
#pragma unroll
                for (int it = 0; it < 4; ++it) GLD_B128_SC1(uu[it], uo, usrc + it * 4096, 0);
                WAIT_V(0);
#pragma unroll
                for (int it = 0; it < 4; ++it) TIE(uu[it]);
            }
#pragma unroll
            for (int it = 0; it < 4; ++it) *LDSP(u32x4, ud + it * 128) = uu[it];
        };
        static_assert(PU <= (UE ? 15 : 16), "u");
        ITEMS(14);
        if (UE) __builtin_amdgcn_s_setprio(UPRIO);
        if (UE >= 2) zero();
        if (UE == 2) fill();
        BAR();
        ITEMS(15);
        TS(4);
        if (UE < 2) zero();
        if (UE & 1) fill();
        if (UE) uput();
        WAIT_L(0);
        TS(5);
        BAR();
        __builtin_amdgcn_s_setprio(FPRIO);
        ITEMS(16);
        TS(11);
        const unsigned pmine = lb + L_SY + 32 + wm * 4, pall = lb + L_SY + 32 + (lane & 3) * 4;
        if (!UE) { fill(); TS(13); uput(); sy_post<0>(pmine); sy_wait<0>(pall); }
        if (!CPOST) { WAIT_V(0); sy_post<1>(pmine); }
        __builtin_amdgcn_s_setprio(CVPRIO);
        TS(6);
        f32x4 ca[8];
        {
            unsigned kb[4];
#pragma unroll
            for (int i = 0; i < 4; ++i) { kb[i] = cb + (~r & 1) * CST + 2 * (64 + 16 + r - 8 * g4 - 7 + (~r & 1)) + i * 1024; TIE(kb[i]); }
            unsigned ub = cb + UOFF + (g4 & 1) * UPL + (15 + r + (g4 >> 1)) * 16 - 64;
#pragma unroll
            for (int i = 0; i < 8; ++i) { ca[i] = f32x4{0, 0, 0, 0}; TIE(ca[i]); }
            s16x4 FA[4][2]; bf16x8 FB[2][8];
#define CV_A(s, o, e) dsr2_<16 * ((s) + ((o) >> 3)) + 128 * (cv_m((o) & 7) & 1) + 2 * (e)>(FA[(o) & 3][e], kb[cv_m((o) & 7) >> 1])
#define CV_B(s, jb) DSR128(FB[(s) & 1][jb], ub, 64 + 256 * (jb) - 32 * (s))
            static_for<0, 8>([&](auto ic) { constexpr int n = decltype(ic)::value; CV_B(0, n); CV_A(0, n >> 1, n & 1); });
            WAIT_L(0);
            auto body = [&]() {
                static_for<0, 72>([&](auto ic) {
                    constexpr int s = decltype(ic)::value / 36, p = decltype(ic)::value % 36, o = cv_grp(p), m = cv_m(o), jb = p - cv_first(o);
                    if constexpr (jb == 0) { WAIT_LB(cv_wait(o)); if constexpr (!(AMF & 1)) { TIE(FA[o & 3][0]); TIE(FA[o & 3][1]); } }
                    if constexpr (p == 0 && !(AMF & 1)) { for (int i = 0; i < 8; ++i) TIE(FB[s][i]); }
                    const bf16x8 fa = __builtin_shufflevector(FA[o & 3][0], FA[o & 3][1], 0, 1, 2, 3, 4, 5, 6, 7);
                    if constexpr (AMF & 1) mfma_(ca[jb + m], fa, FB[s][jb]); else
                    ca[jb + m] = __builtin_amdgcn_mfma_f32_16x16x32_bf16(fa, FB[s][jb], ca[jb + m], 0, 0, 0);
                    SB();
                    constexpr int rd = cv_read(p);
                    if constexpr (rd >= 32) CV_B(s + 1, rd - 32);
                    else if constexpr (rd >= 0) CV_A(s, rd >> 1, rd & 1);
                    SB();
                });
                for (int i = 0; i < 4; ++i) { kb[i] += 128; TIE(kb[i]); }
                ub -= 64;
            };
#pragma unroll 1
            for (int it = 0; it < QIT; ++it) body();
            {
                TS(19);
                if (CPOST) { WAIT_V(0); sy_post<1>(pmine); WAIT_L(0); }
                if (CVLATE) sy_wait<1>(lb + L_SY + (CVLATE == 2 ? wm + (lane & 1) * 4 : lane & 7) * 4);
                TS(20);
            }
#pragma unroll 1
            for (int it = QIT; it < 4; ++it) body();
            WAIT_L(0);
            if (AMF & 1) asm volatile("s_nop 7\n\ts_nop 7" : "+v"(ca[0]), "+v"(ca[1]), "+v"(ca[2]), "+v"(ca[3]), "+v"(ca[4]), "+v"(ca[5]), "+v"(ca[6]), "+v"(ca[7]));
#pragma unroll
            for (int i = 0; i < 4; ++i) { TIE(FA[i][0]); TIE(FA[i][1]); }
#pragma unroll
            for (int i = 0; i < 8; ++i) TIE(FB[0][i]);
#undef CV_A
#undef CV_B
        }
        TS(7);
        __builtin_amdgcn_s_setprio(3);
        constexpr int NBAR = __builtin_popcount(BARM & ~VBM);
        unsigned zpub = (xcd * 32 + l31) * 256 + idx * 8, zpoll = ZOWN ? (xcd * 32 + idx) * 256 + l31 * 8 : fpoll;
        {
            const float Dd = bf2f(dskl);
            const char* zd = ws + WS_ZT + (size_t)(xcd * 128 + wm * 32) * 4096 + idx * 128;
            u32x2 kvr[8];
#pragma unroll
            for (int jt = 0; jt < 8; ++jt) kvr[jt] = *LDSP(u32x2, cb + UOFF + (g4 >> 1) * UPL + (16 + 16 * jt + r) * 16 + (1 - (g4 & 1)) * 8);
#pragma unroll
            for (int jt = 0; jt < 8; ++jt) {
                const f32x4 y = ca[jt];
                const u32x2 kq = kvr[jt];
                *LDSP(u32x2, cb + (jt * 4 + (r >> 2)) * 144 + (r & 3) * 32 + g4 * 8) =
                    u32x2{pack2(rb1(y[0]) + rb1(bfhi(kq[1]) * Dd), rb1(y[1]) + rb1(bflo(kq[1]) * Dd)), pack2(rb1(y[2]) + rb1(bfhi(kq[0]) * Dd), rb1(y[3]) + rb1(bflo(kq[0]) * Dd))};
            }
            unsigned za = lb + (r >> 2) * CHSZ + (wm * 8 + (g4 >> 1)) * 144 + ((g4 & 1) * 2 + ((r >> 1) & 1)) * 32 + (r & 1) * 8, zo = (lane >> 3) * 4096 + (lane & 7) * 16;
            TIE(za); TIE(zo); TIE(zpub); TIE(zpoll);
            sy_post<3>(pmine); sy_wait<3, 0>(pall);
            v4i16 t[4][2];
#pragma unroll
            for (int it = 0; it < 4; ++it)
#pragma unroll
                for (int e = 0; e < 2; ++e) t[it][e] = __builtin_amdgcn_ds_read_tr16_b64_v4i16(LDSP(v4i16, za + it * 288 + e * 16));
#pragma unroll
            for (int it = 0; it < 4; ++it) { TIE(t[it][0]); TIE(t[it][1]); }
            if (ZSB) BAR();
#pragma unroll
            for (int it = 0; it < 4; ++it) { const bf16x8 f = __builtin_shufflevector(t[it][0], t[it][1], 0, 1, 2, 3, 4, 5, 6, 7); GST_B128_SC1(zo, f, zd + it * 32768, 0); }
        }
#pragma unroll
        for (int i = ZSB; i < ZFB; ++i) BAR();
        WAIT_V(0);
        sy_post<2>(pmine);
        if (wm == 0) {
            sy_wait<2>(pall);
            if (ZOWN) { if (lane < 32) GST_B64_SC1(zpub, tag, flg + FL_Z); }
            else if (lane == 0) GST_B64_SC1(fpub, tag, flg + FL_Z);
        }
        TS(8);
        unsigned qm[8][4];
        if (QM) {
#pragma unroll
            for (int n = 0; n < 8; ++n) {
                TIE(qv[n]);
                qm[n][0] = qv[n][0] & 0xffffu; qm[n][1] = qv[n][0] & 0xffff0000u; qm[n][2] = qv[n][1] & 0xffffu; qm[n][3] = qv[n][1] & 0xffff0000u;
                TIE(qm[n][0]); TIE(qm[n][1]); TIE(qm[n][2]); TIE(qm[n][3]);
            }
        }
#pragma unroll
        for (int i = ZFB; i < NBAR - 1; ++i) { BAR();
        }
        if (ZSL) __builtin_amdgcn_s_sleep(ZSL);
        asm volatile("s_cmp_lg_u32 %3, 0\n\ts_cbranch_scc1 1f\n\tglobal_load_dwordx2 %0, %1, %2 sc1\n1:" : "=&v"(fl) : "v"(zpoll), "s"(flg + FL_Z), "s"(wm) : "scc", "memory");
        if (NBAR > ZFB) BAR();
        if (wm == 0) {
            unsigned left = SPIN_CAP;
            asm volatile("s_branch 2f\n1:\n\ts_sleep %5\n\tglobal_load_dwordx2 %0, %2, %3 sc1\n2:\n\ts_waitcnt vmcnt(0)\n\tv_cmp_ne_u64 vcc, %4, %0\n\ts_cbranch_vccz 3f\n\ts_sub_u32 %1, %1, 1\n\ts_cbranch_scc0 1b\n3:"
                         : "+v"(fl), "+s"(left) : "v"(zpoll), "s"(flg + FL_Z), "s"(tag), "n"(ZPS) : "vcc", "scc", "memory");
            sy_post<4>(pmine);
        } else sy_wait<4, 2>(lb + L_SY + 32);
        u32x4 zv[4];
        {
            ITEMS(18);
            const char* zsrc = ws + WS_ZT + (size_t)(xcd * 128 + idx * 4 + wm) * 4096;
            static_for<0, 4>([&](auto ic) { constexpr int it = decltype(ic)::value; GLD_B128_SC1(zv[it], (l31 >> 2) * 512 + lh * 64 + (lane & 3) * 16, zsrc, it * 128); });
        }
        WAIT_V(0);
        TS(9);
#pragma unroll
        for (int it = 0; it < 4; ++it) TIE(zv[it]);
        if (!(XAB & 1)) {
        quad_swap<0>(zv[0], zv[1]); quad_swap<0>(zv[2], zv[3]);
        quad_swap<1>(zv[0], zv[2]); quad_swap<1>(zv[1], zv[3]); }
#pragma unroll
        for (int it = 0; it < 4; ++it) TIE(zv[it]);
        char* const oa = a.out + (size_t)(idx * 64 + wm * 16) * 8192 + xcd * 512;
#pragma unroll
        for (int n = 0; n < 8; ++n) {
            if (!QM) TIE(qv[n]);
            const unsigned z0 = zv[n & 3][2 * (n >> 2)], z1 = zv[n & 3][2 * (n >> 2) + 1];
            const f32x4 o = (XAB & 2) ? __builtin_bit_cast(f32x4, zv[n & 3]) : QM ? mul4bf(u32x2{z0, z1}, qm[n]) :
                f32x4{rb1(bflo(qv[n][0]) * bflo(z0)), rb1(bfhi(qv[n][0]) * bfhi(z0)), rb1(bflo(qv[n][1]) * bflo(z1)), rb1(bfhi(qv[n][1]) * bfhi(z1))};
            if (n == 0 && OSG) for (int i = 0; i < (idx & 3); ++i) __builtin_amdgcn_s_sleep(OSG);
            if (!(XAB & 4))
            GST_B128_NT(lh * 32768 + l31 * 16, o, oa + ((n >> 2) * 8 + (n & 3)) * 8192, 0);
            if (OSL) __builtin_amdgcn_s_sleep(OSL);
        }
        asm volatile(".Lfk_code_a:");
        TSE(10);
        TS_FLUSH();
        return;
    }

    u32x2 fl;
    const unsigned* dv = kvv;
#define G1_ISSUE(st, i) do { if constexpr ((st) < DFN) { if constexpr ((i) > 0) dmaof_<(i) * 1024>(dv[i], g1src + ((((st) << 7) + rot7) & 1920)); else dmaf_(g1dst + g1_slot(st), dv[i], g1src + ((((st) << 7) + rot7) & 1920)); } else if constexpr (M1 && (i) > 0) dmao_<(i) * 1024>(dv[i], g1src + ((((st) << 7) + rot7) & 1920)); \
        else DMA(g1dst + g1_slot(st) + (i) * 1024, dv[i], g1src + ((((st) << 7) + rot7) & 1920)); } while (0)
    unsigned k0 = trow + wm * 32768 + g4 * 2048 + ((l15 ^ (2 * g4)) << 4), k1 = k0 + 7168, k2 = (k0 ^ 128) + 14336, k3 = k2 + 7168;
    auto pre = [&](auto ic) {
        constexpr int n = decltype(ic)::value;
        if constexpr (n == KVP) { if (!HKV) kv_dma(g1dst + L_KS, k0, k1, k2, k3, wn ? a.v : a.k); }
        else static_for<0, 4>([&](auto jc) { G1_ISSUE(n - (n > KVP), decltype(jc)::value); });
    };
    static_for<1, AP0 / 4>(pre);
    TS(14);
    SLICE_TABLE();
    const int j0 = 4 * run - (32 - 2 * tm);
    const int pneg = EALL ? 0 : 16 * (wm >> 1) + 15 - (32 - 2 * tm);
    unsigned eo[2], pes, plr, est, hw[2], ta[2], po, pmine, pall, pown, pzero, g2v[2], cad[2][2], to[2][2], xo, dso, fst, fld, cwa;
    unsigned cta = lb + L_TS + st * 16, ctb = lb + L_TR + run * 1024 + st * 16;
    TIE(cta); TIE(ctb);
    f32x4 ct[5]; f32x2 V1;
    const int gm = PAIR ? wid >> 1 : wm, gk = PAIR ? wid & 1 : wn, gs = gk * 4 + (gm & 2);
    auto addrs = [&]() {
        int ln = lane;
        TIE(ln);
        const int l15 = ln & 15, g4 = ln >> 4, st = sq * 16 + l15, run = (wm >> 1) * 4 + g4, j0 = 4 * run - (32 - 2 * tm);
        pes = lb + L_ES + wid * 256 + l15 * 16;
        plr = lb + L_AGG + st * 8 + run * 512; est = ((tm * 2 + (wm >> 1)) * 1024 + tn * 64 + st) * 16;
#pragma unroll
        for (int i = 0; i < 2; ++i) { eo[i] = (j0 + 2 * i < 0 ? 0 : j0 + 2 * i) * 16384 + (tn * 64 + st) * 16; TIE(eo[i]); }
        TIE(plr); TIE(pes); TIE(est);
        const unsigned ht = lb + L_HT + wid * 4096;
#pragma unroll
        for (int j = 0; j < 2; ++j) {
            const int pr = (8 * (g4 & 1) + 4 * j + (l15 >> 2)) ^ ((g4 & 1) * 4), pw = l15 ^ ((l15 >> 3) * 4);
            ta[j] = ht + (g4 >> 1) * 512 + pr * 32 + ((((l15 >> 1) ^ (pr >> 2)) & 1) << 4) + 8 * (ln & 1);
            hw[j] = ht + g4 * 1024 + pw * 32 + (((j ^ (pw >> 2)) & 1) << 4);
            TIE(ta[j]); TIE(hw[j]);
        }
        po = pes + ((wm & 2) ? -512 : 512);
        pmine = lb + L_SY + wid * 4; pall = lb + L_SY + (ln & 7) * 4; pown = lb + L_SY + (ln < 4 ? 32 + ln * 4 : gs * 4 + (ln & 4)); pzero = lb + L_SY;
        xo = (tm * 128 + gm * 32 + gk * 16 + g4 * 4) * 2048 + tn * 128 + l15 * 8 + 4096; dso = xo & 2047;
        fst = (tm * 16 + l15) * 128 + tn * 8; fld = (tm * 16 + tn) * 128 + l15 * 8;
#pragma unroll
        for (int i = 0; i < 2; ++i) {
            const int pair = i * 4 + g4, v = l15 ^ pair ^ ((wm & 1) * 8);
            g2v[i] = (pair * 2 + (v >> 3)) * 8192 + (v & 7) * 16 + (tn * 64 + wm) * 2048 - i * 1024;
            TIE(g2v[i]);
#pragma unroll
            for (int j = 0; j < 2; ++j) {
                cad[i][j] = lb + L_C + (img_off((g4 >> 1) * 64 + l15, 4 * gk + (g4 & 1)) ^ (i * 32) ^ (j * 128));
                to[i][j] = ta[j] + (i ^ gk) * 1024 + (gm & 1) * 2048 + (gs - wid) * 4096;
                TIE(cad[i][j]); TIE(to[i][j]);
            }
        }
        cwa = lb + wid * 2048 + ln * 16;
        TIE(cwa); TIE(po);
        TIE(pmine); TIE(pall); TIE(pown); TIE(pzero); TIE(xo); TIE(dso); TIE(fst); TIE(fld);
    };
    if (!APRE) addrs();
    unsigned aad[2][2], bad[2];
    {
        const int aoff = img_off((wm >> 1) * 64 + l15, g4), boff = img_off(128 + wn * 64 + (wm & 1) * 16 + l15, g4);
#pragma unroll
        for (int ks = 0; ks < 2; ++ks) { aad[ks][0] = lb + (aoff ^ (ks * 64)); aad[ks][1] = lb + (aoff ^ (ks * 64) ^ 128); bad[ks] = lb + (boff ^ (ks * 64)); TIE(aad[ks][0]); TIE(aad[ks][1]); TIE(bad[ks]); }
    }
    if (!HKV && KVP >= AP0 / 4) { TIE(k0); TIE(k1); TIE(k2); TIE(k3); }
    static_for<AP0 / 4, NBK + 1>(pre);
    TS(1);
    float nL1i, nC1r, nC1i;
    unsigned tagA, tagB;
    f32x4 acc[4][2];
    {
#pragma unroll
        for (int i = 0; i < 4; ++i) { acc[i][0] = f32x4{0, 0, 0, 0}; acc[i][1] = f32x4{0, 0, 0, 0}; }
        bf16x8 fa[2][4], fb[2][2];
        if (GPRIO) __builtin_amdgcn_s_setprio(GPRIO);
        WAIT_V(g1_cnt(0)); BAR();
        TS(2);
#define G1_FRAG(g, n) do { constexpr int bo_ = g1_slot((g) >> 1), hi_ = bo_ & ~65535, x_ = (n) < 3 ? (n) >> 1 : (n) - 2; \
        if constexpr ((n) == 0 || (n) == 2) DSR128(fb[(g) & 1][x_], bad[(g) & 1] + hi_, bo_ - hi_ + x_ * 4096); \
        else DSR128(fa[(g) & 1][x_], aad[(g) & 1][x_ & 1] + hi_, bo_ - hi_ + x_ * 2048); } while (0)
        static_for<0, 6>([&](auto ic) { constexpr int n = decltype(ic)::value; G1_FRAG(0, n); });
        static_for<0, 32>([&](auto ic) {
            constexpr int g = decltype(ic)::value, gn = g + 1, n0 = g1_upto(g - 1), nd = g1_upto(g) - n0;
            constexpr bool bar = (gn & 1) == 0 && gn < 32;
            if constexpr (g == 1) {
                DSR128(ct[0], cta, 0); DSR128(ct[1], cta, 1024); DSR128(ct[2], cta, 2048); DSR128(ct[3], ctb, 0); DSR128(ct[4], ctb, 8192); DSR64(V1, ctb, 16384);
            }
            if constexpr (g == 3) {
#pragma unroll
                for (int i = 0; i < 5; ++i) TIE(ct[i]);
                TIE(V1);
                nL1i = -ct[0][1]; nC1r = -ct[0][2]; nC1i = -ct[0][3];
                TIE(nL1i); TIE(nC1r); TIE(nC1i);
                tagA = __builtin_amdgcn_readfirstlane(__float_as_uint(ct[2][2])); tagB = __builtin_amdgcn_readfirstlane(__float_as_uint(ct[2][3]));
                ties_(tagA); ties_(tagB);
            }
            if constexpr (bar) {
                static_assert(g1_cnt(gn >> 1) >= 0, "schedule");
                WAIT_VB(g1_cnt(gn >> 1)); WAIT_LB(0); BAR();
                if constexpr (((gn >> 1) & 3) == 0 || gn >= 26) TS(gn >= 26 ? 23 + (gn >> 1) - 13 : 16 + (gn >> 3));
#pragma unroll
                for (int i = 0; i < 4; ++i) if (!(AMF & 2)) TIE(fa[g & 1][i]);
                if (!(AMF & 2)) { TIE(fb[g & 1][0]); TIE(fb[g & 1][1]); }
            }
            static_for<0, 8>([&](auto mc) {
                constexpr int m = decltype(mc)::value, i = m >> 1, j = m & 1;
                if constexpr (W1 && !bar && g > 0 && g < 31) {
                    if constexpr (m == 0) WAIT_LB(W1 == 1 ? 3 : 2);
                    if constexpr (m == 2 && W1 == 1) WAIT_LB(4);
                    if constexpr (m == 4) WAIT_LB(4);
                } else
                if constexpr (!bar && (i == 0 || j == 0)) {
                    WAIT_LB(5 - (i == 0 ? j + 1 : i + 2) + (gn < 32 ? (m < 6 ? m : 6) : 0));
                    if constexpr (!(AMF & 2) || g == 0) { TIE(fa[g & 1][i]); TIE(fb[g & 1][j]); }
                }
                if constexpr ((AMF & 2) && g > 0) mfma_(acc[i][j], fa[g & 1][i], fb[g & 1][j]); else
                acc[i][j] = __builtin_amdgcn_mfma_f32_16x16x32_bf16(fa[g & 1][i], fb[g & 1][j], acc[i][j], 0, 0, 0);
                SB();
                if constexpr (gn < 32 && m < 6) G1_FRAG(gn, m);
                constexpr int d = nd > 2 ? ((m & 1) ? m >> 1 : -1) : (m == DP0 ? 0 : m == DP1 ? 1 : -1);
                if constexpr (d >= 0 && d < nd) G1_ISSUE((n0 + d) >> 2, (n0 + d) & 3);
                SB();
            });
        });
#undef G1_FRAG
    }
    TS(3);
    const unsigned tagG = tagA ^ tagB ^ 0x9E3779B9u;
    __builtin_amdgcn_s_setprio(MPRIO);
    f32x2 ur[8], ui[8], hr, hi;
    const f32x2 L1 = {ct[0][0], ct[0][1]}, C1 = {ct[0][2], ct[0][3]}, L16 = {ct[1][2], ct[1][3]}, Q2r = {ct[3][0], ct[3][1]}, Q2i = {ct[3][2], ct[3][3]};
    const float L8r = ct[1][0], L8i = ct[1][1], L64r = ct[2][0], L64i = ct[2][1];
    u32x4 eg[4], cr[2 * NR + 1];
    const char* g2src = wn ? a.cim : a.cre;
#define CR_LOAD(n0, n1) static_for<n0, n1>([&](auto ic) { constexpr int n = decltype(ic)::value; GLD_B128(cr[n], g2v[n & 1], g2src + (SLICE(7 + (n >> 1)) << 7), (n & 1) * 1024); })
#define E_LOADS "global_load_dwordx4 v[104:107], %[a0], %[b0] sc1\n\tglobal_load_dwordx4 v[108:111], %[a0], %[b1] sc1\n\t" \
                "global_load_dwordx4 v[112:115], %[a1], %[b0] sc1\n\tglobal_load_dwordx4 v[116:119], %[a1], %[b1] sc1\n"
#define E_POLL() asm volatile("s_cmp_lt_i32 %[on], 0\n\ts_cbranch_scc1 1f\n\t" E_LOADS "1:" : "={v[104:107]}"(eg[0]), "={v[108:111]}"(eg[1]), "={v[112:115]}"(eg[2]), "={v[116:119]}"(eg[3]) \
        : [a0] "v"(eo[0]), [a1] "v"(eo[1]), [b0] "s"(ws + WS_E), [b1] "s"(ws + WS_E + 16384), [on] "s"(pneg) : "scc", "memory")
    {
        asm volatile("s_nop 7\n\ts_nop 3" : "+v"(acc[0][0]), "+v"(acc[0][1]), "+v"(acc[1][0]), "+v"(acc[1][1]), "+v"(acc[2][0]), "+v"(acc[2][1]), "+v"(acc[3][0]), "+v"(acc[3][1]));
#pragma unroll
        for (int p = 0; p < 8; ++p) { ur[p] = f32x2{acc[p >> 1][0][2 * (p & 1)], acc[p >> 1][0][2 * (p & 1) + 1]}; ui[p] = f32x2{acc[p >> 1][1][2 * (p & 1)], acc[p >> 1][1][2 * (p & 1) + 1]}; }
        f32x2 gr = ur[0], gi = ui[0];
#pragma unroll
        for (int p = 1; p < 8; ++p) { f32x2 nr, ni; if (PLN & 1) c2_mad(nr, ni, L1[0], nL1i, L1[0], L1[1], gr, gi, ur[p], ui[p]); else pk_cmad(nr, ni, L1, gr, gi, ur[p], ui[p]); gr = nr; gi = ni; }
        float fr, fi;
        cmad(fr, fi, L8r, L8i, gr[0], gi[0], gr[1], gi[1]);
        {
            const f32x2 w = cmul4(V1, f32x2{fr, fi});
            const float wr = sum4(w[0]), wi = sum4(w[1]);
            const u32x4 gq = {__float_as_uint(wr), tagA, __float_as_uint(wi), tagG};
            if (EREP ? (EALL || g4 >= (tm >> 2)) : g4 == 0) GST_B128_SC1(est, gq, ws + WS_E, 0);
        }
        *LDSP(f32x2, plr + 7 * 512) = f32x2{fr, fi};
        *LDSP(unsigned, pes + 8) = 0;
        TS(21);
        if (EP == 2) E_POLL();
        auto coef = [&]() {
#pragma unroll
            for (int p = 0; p < 8; ++p) { f32x2 nr, ni; if (PLN & 2) c2_mul(nr, ni, C1[0], nC1i, nC1r, nC1i, ur[p], ui[p]); else pk_conj_mul(nr, ni, C1, ur[p], ui[p]); ur[p] = nr; ui[p] = ni; TIE(ur[p]); TIE(ui[p]); }
        };
        if (CFB) coef();
        WAIT_L(0); BAR();
        f32x2 e[7];
#pragma unroll
        for (int j = 0; j < 7; ++j) e[j] = *LDSP(f32x2, plr + j * 512);
        if (EP == 1) E_POLL();
        CR_LOAD(0, CR1);
        if (!CFB) coef();
#pragma unroll
        for (int j = 1; j < 7; ++j) { if (PLN & 4) { float xr = e[j][0], xi = e[j][1]; cmac(xr, xi, L16[0], L16[1], e[j - 1][0], e[j - 1][1]); e[j] = f32x2{xr, xi}; } else cmac2(e[j], L16, e[j - 1]); }
        float mr, mi;
        cmad(mr, mi, L8r, L8i, e[6][0], e[6][1], gr[0], gi[0]);
        if (PLN & 4) c2_mul(hr, hi, C1[0], nC1i, nC1r, nC1i, f32x2{e[6][0], mr}, f32x2{e[6][1], mi}); else pk_conj_mul(hr, hi, C1, f32x2{e[6][0], mr}, f32x2{e[6][1], mi});
        TIE(hr); TIE(hi);
        CR_LOAD(CR1, CR1 + CR2);
        TS(4);
    }
    f32x2 G;
    {
        float pr = 0.f, pi = 0.f;
        if (!EP) E_POLL();
        if (pneg >= 0) {
            unsigned t0, t1, t2, left = SPIN_CAP;
            asm volatile("s_waitcnt vmcnt(%[c])\n\ts_branch 2f\n1:\n\ts_sleep %[sl]\n\t" E_LOADS "\ts_waitcnt vmcnt(0)\n2:\n\t"
                         "v_bitop3_b32 %[t0], v105, v109, %[ta] bitop3:0x7e\n\tv_bitop3_b32 %[t1], v113, v117, %[ta] bitop3:0x7e\n\tv_bitop3_b32 %[t2], v107, v111, %[tg] bitop3:0x7e\n\t"
                         "v_or3_b32 %[t0], %[t0], %[t1], %[t2]\n\tv_bitop3_b32 %[t1], v115, v119, %[tg] bitop3:0x7e\n\tv_or_b32 %[t0], %[t0], %[t1]\n\t"
                         "v_cmp_ne_u32 vcc, 0, %[t0]\n\ts_cbranch_vccz 3f\n\ts_sub_u32 %[n], %[n], 1\n\ts_cbranch_scc0 1b\n3:"
                         : "+{v[104:107]}"(eg[0]), "+{v[108:111]}"(eg[1]), "+{v[112:115]}"(eg[2]), "+{v[116:119]}"(eg[3]), [t0] "=&v"(t0), [t1] "=&v"(t1), [t2] "=&v"(t2), [n] "+s"(left)
                         : [a0] "v"(eo[0]), [a1] "v"(eo[1]), [b0] "s"(ws + WS_E), [b1] "s"(ws + WS_E + 16384), [ta] "s"(tagA), [tg] "s"(tagG), [sl] "n"(ESL), [c] "n"(CR1 + CR2) : "vcc", "scc", "memory");
            float ar = __uint_as_float(eg[1][0]), ai = __uint_as_float(eg[1][2]), br = __uint_as_float(eg[3][0]), bi = __uint_as_float(eg[3][2]);
            cmac(ar, ai, L64r, L64i, __uint_as_float(eg[0][0]), __uint_as_float(eg[0][2]));
            cmac(br, bi, L64r, L64i, __uint_as_float(eg[2][0]), __uint_as_float(eg[2][2]));
            if (EALL) { if (j0 < 0) ar = ai = 0.f; if (j0 + 2 < 0) br = bi = 0.f; }
            const f32x2 pu = cmul4(f32x2{ct[4][2], ct[4][3]}, f32x2{ar, ai});
            pr = pu[0]; pi = pu[1];
            cmac(pr, pi, ct[4][0], ct[4][1], br, bi);
            pr = sum4(pr); pi = sum4(pi);
        }
        TS(5);
        *LDSP(u32x4, pes) = u32x4{__float_as_uint(pr), __float_as_uint(pi), 1u, 0u};
        CR_LOAD(CR1 + CR2, CR1 + CR2 + CR3);
        u32x4 o;
        asm volatile("1:\n\tds_read_b128 %0, %1\n\ts_waitcnt lgkmcnt(0)\n\tv_cmp_eq_u32 vcc, 0, v122\n\ts_cbranch_vccnz 1b" : "=&{v[120:123]}"(o) : "v"(po) : "vcc", "memory");
        G = f32x2{pr + __uint_as_float(o[0]), pi + __uint_as_float(o[1])};
    }
    TS(6);
    {
        if (PLN & 8) c2_mac(hr, hi, G[0], -G[1], -G[0], -G[1], Q2r, Q2i); else pk_conj_mac(hr, hi, G, Q2r, Q2i);
#pragma unroll
        for (int p = 0; p < 8; ++p) { if (PLN & 8) c2_mac(ur[p], ui[p], L1[0], L1[1], L1[0], nL1i, hr, hi); else pk_cmac_conj(ur[p], ui[p], L1, hr, hi); hr = ur[p]; hi = ui[p]; }
        const char* hd = ws + WS_H + (size_t)((tm * 4 + (wm & 2)) * 2 + wn) * HP + (tn * 4 + (wm & 1) * 2) * 1024;
#pragma unroll
        for (int e2 = 0; e2 < 2; ++e2)
#pragma unroll
            for (int c = 0; c < 2; ++c) {
                const f32x2* o = c ? ui : ur;
                *LDSP(u32x4, hw[e2] + c * 512) = u32x4{pack2(o[0][e2], o[1][e2]), pack2(o[2][e2], o[3][e2]), pack2(o[4][e2], o[5][e2]), pack2(o[6][e2], o[7][e2])};
            }
        v4i16 t[4][2];
#pragma unroll
        for (int n = 0; n < 4; ++n)
#pragma unroll
            for (int j = 0; j < 2; ++j) t[n][j] = __builtin_amdgcn_ds_read_tr16_b64_v4i16(LDSP(v4i16, ta[j] + n * 1024));
        sy_post<1>(pmine);
        static_for<0, 4>([&](auto ic) {
            constexpr int n = decltype(ic)::value;
            const bf16x8 f = __builtin_shufflevector(t[n][0], t[n][1], 0, 1, 2, 3, 4, 5, 6, 7);
            GST_B128_SC1(l16, f, hd + (n >> 1) * 2 * HP + ((n & 1) ^ wn) * 1024, 0);
        });
    }
    TS(7);
    bf16x8 fa2[64];
    u32x2 xv[4], dsv;
    const char* Aw = ws + WS_H + (size_t)((tm * 4 + gm) * 2 + gk) * HP;
    const unsigned g2dst = lb + wid * 2048;
#define G2_ISSUE(st, i) do { if constexpr (i) dmao_<1024>(g2v[1], g2src + (SLICE(st) << 7)); else DMA(g2dst + g2_place(st), g2v[0], g2src + (SLICE(st) << 7)); } while (0)
#define G2_A(g) GLD_B128_SC1(fa2[g], l16, Aw + (SLICE((g) >> 2) << 12), ((g) & 3) * 1024)
    {
        {
            sy_wait<1>(pown);
            s16x4 fo[4][2]; bf16x8 c[8];
            static_for<0, 2>([&](auto kc) {
                constexpr int ks = decltype(kc)::value;
                static_for<0, 4>([&](auto ic) { constexpr int i = decltype(ic)::value; dsrtr_<ks * 4096>(fo[2 * ks + (i >> 1)][i & 1], to[i >> 1][i & 1]); });
                static_for<0, 4>([&](auto ic) { constexpr int i = decltype(ic)::value; DSR128(c[4 * ks + i], cad[ks][i & 1], 2 * G2STAGE + i * 2048); });
            });
            static_for<0, 8>([&](auto ic) {
                constexpr int n = decltype(ic)::value, ks = n >> 2, i = n & 3;
                WAIT_L(ks ? 3 - i : 11 - i);
                TIE(c[n]);
                if constexpr (i == 0) { TIE(fo[2 * ks][0]); TIE(fo[2 * ks][1]); TIE(fo[2 * ks + 1][0]); TIE(fo[2 * ks + 1][1]); }
#pragma unroll
                for (int j = 0; j < 2; ++j)
                    acc[i][j] = __builtin_amdgcn_mfma_f32_16x16x32_bf16(__builtin_shufflevector(fo[2 * ks + j][0], fo[2 * ks + j][1], 0, 1, 2, 3, 4, 5, 6, 7), c[n], ks ? acc[i][j] : f32x4{0, 0, 0, 0}, 0, 0, 0);
                SB();
            });
#pragma unroll
            for (int i = 0; i < 4; ++i) { TIE(acc[i][0]); TIE(acc[i][1]); }
        }
        if (CEARLY) sy_post<0>(pmine);
        static_for<0, 4>([&](auto ic) { constexpr int it = decltype(ic)::value; GLD_B64(xv[it], xo, a.x, it * 2048 - 4096); });
        GLD_B64(dsv, dso, a.dskip, 0);
        if (CEARLY) {
            sy_wait<0>(pall);
            static_for<4, 4 + CEARLY>([&](auto ic) { constexpr int n = decltype(ic)::value; G2_ISSUE(n >> 1, n & 1); });
        }
        TS(16);
        WAIT_V(5 + CEARLY);
        sy_post<2>(pmine);
        if (!CEARLY || wid == (NOOWN ? 1 : 0) || (NOOWN == 1 && wid == 0)) sy_wait<2, 0>(pall);
        const u32x2 tagv = {__float_as_uint(ct[2][2]), __float_as_uint(ct[2][3])};
        if (wid == (NOOWN ? 1 : 0)) asm volatile("s_mov_b64 exec, 0xffff\n\tglobal_store_dwordx2 %0, %1, %2 sc1\n\ts_mov_b64 exec, -1" :: "v"(fst), "v"(tagv), "s"(flg + FL_H) : "memory");
        if (PFIRST) asm volatile("s_cmp_lg_u32 %3, 0\n\ts_cbranch_scc1 1f\n\tglobal_load_dwordx2 %0, %1, %2 sc1\n1:" : "=&v"(fl) : "v"(fld), "s"(flg + FL_H), "s"(wid) : "scc", "memory");
        TS(8);
        static_for<4 + CEARLY, 10>([&](auto ic) { constexpr int n = decltype(ic)::value; G2_ISSUE(n >> 1, n & 1); });
        CR_LOAD(2 * NR - CRL, 2 * NR);
        TS(13);
        if (wid == 0) {
            unsigned left = SPIN_CAP;
            if (PFIRST)
            asm volatile("s_waitcnt vmcnt(6)\n\ts_branch 3f\n1:\n\tglobal_load_dwordx2 %0, %2, %3 sc1\n\ts_waitcnt vmcnt(0)\n3:\n\tv_cmp_ne_u64 vcc, %0, %4\n\ts_cbranch_vccz 2f\n\ts_sub_u32 %1, %1, 1\n\ts_cbranch_scc0 1b\n2:"
                         : "+v"(fl), "+s"(left) : "v"(fld), "s"(flg + FL_H), "v"(tagv) : "vcc", "scc", "memory");
            else
            asm volatile("1:\n\tglobal_load_dwordx2 %0, %2, %3 sc1\n\ts_waitcnt vmcnt(0)\n\tv_cmp_ne_u64 vcc, %0, %4\n\ts_cbranch_vccz 2f\n\ts_sub_u32 %1, %1, 1\n\ts_cbranch_scc0 1b\n2:"
                         : "=&v"(fl), "+s"(left) : "v"(fld), "s"(flg + FL_H), "v"(tagv) : "vcc", "scc", "memory");
            sy_post<3>(pmine);
            if (b == 255 && lane == 0) GST_B64_SC1(0u * lane, tagv, flg + FL_EPOCH);
        } else if (g2_bar(3)) sy_wait<3>(pzero);
        else { WAIT_V(CRL); sy_post<3>(pmine); }
        if (!g2_bar(3)) sy_wait<3>(pall);
        TS(20);
        static_for<4, 4 + AW>([&](auto ic) { constexpr int g = decltype(ic)::value; G2_A(g); });
    }
    TS(9);
    __builtin_amdgcn_s_setprio(0);
    unsigned yw, yr, yp, yo;
    static_assert(PAIR, "yr");
    unsigned dm[4];
    auto dmask = [&]() { TIE(dsv); dm[0] = dsv[0] & 0xffffu; dm[1] = dsv[0] & 0xffff0000u; dm[2] = dsv[1] & 0xffffu; dm[3] = dsv[1] & 0xffff0000u; TIE(dm[0]); TIE(dm[1]); TIE(dm[2]); TIE(dm[3]); };
    auto skip = [&](int it) { TIE(xv[it]); const f32x4 y = mul4bf(xv[it], dm); for (int i = 0; i < 4; ++i) acc[i][0][it] = add1(acc[i][0][it], y[i]); if (it & 1) { for (int i = 0; i < 4; ++i) TIE(acc[i][0]); } };
    {
        bf16x8 fb[2][4];
#define G2_FRAG(g, i) do { constexpr int pl_ = g2_place((g) >> 1), b_ = pl_ >= L_C ? L_C : pl_ >= 32768 ? 32768 : 0; DSR128(fb[(g) & 1][i], cad[(g) & 1][(i) & 1] + (b_ - L_C), pl_ - b_ + (i) * 2048); } while (0)
        static_for<0, 4>([&](auto ic) { constexpr int i = decltype(ic)::value; G2_FRAG(2, i); });
        if (YS == 0) { WAIT_V(AW + 6 + CRL); dmask(); skip(0); skip(1); skip(2); skip(3); }
        static_for<2, 32>([&](auto ic) {
            constexpr int g = decltype(ic)::value, gn = g + 1;
            constexpr bool bar = g2_bar(g) && !((VBM >> g) & 1);
            constexpr int n0 = g2_upto(g - 1), n1 = g2_upto(g);
            if constexpr (bar) {
                constexpr int ca = g2_cnt(2 * g, 2 * g), cs = g2_cover(g) >= 7 + NR ? g2_cnt(100 + 2 * g2_cover(g) + 1, 2 * g) : 63;
                static_assert(cs >= 0 && g2_wr(g - 1) >= 2 * (g2_cover(g) < 6 + NR ? g2_cover(g) - 6 : NR), "schedule");
                WAIT_VB(AWB && ca < cs ? ca : cs); WAIT_LB(0); BAR();
                if constexpr (!AWB) WAIT_V(ca);
#pragma unroll
                for (int i = 0; i < 4; ++i) if (!(AMF & 4)) TIE(fb[g & 1][i]);
                if (!(AMF & 4)) TIE(fa2[2 * g]);
            }
            static_for<0, 8>([&](auto mc) {
                constexpr int m = decltype(mc)::value, i = m & 3, j = m >> 2, f = 2 * g + j;
                static_assert(g2_cnt(f, f) >= 0, "schedule");
                constexpr int jj = LSW && g == 31 ? 1 - j : j, ff = 2 * g + jj;
                if constexpr (i == 0 && (j || !bar)) { WAIT_VB(LSW && g == 31 ? 0 : g2_cnt(f, f)); if constexpr (!(AMF & 4) || g == 2 || g == 31) TIE(fa2[ff]); }
                if constexpr (W2 && g > 2 && g < 31) { if constexpr (!bar && j == 0 && (i & W2) == 0) WAIT_LB(3 - W2 + g2_wr(g - 1) - g2_wr(g - 2) + (g - 1 == EGX)); } else
                if constexpr (!bar && j == 0) { WAIT_LB((gn < 32 ? 3 : 3 - i) + g2_wr(g - 1) - g2_wr(g - 2) + (g - 1 == EGX)); if constexpr (!(AMF & 4) || g == 2 || g == 31) TIE(fb[g & 1][i]); }
                if constexpr ((AMF & 4) && g > 2 && g < 31) mfma_(acc[i][jj], fa2[ff], fb[g & 1][i]); else
                acc[i][jj] = __builtin_amdgcn_mfma_f32_16x16x32_bf16(fa2[ff], fb[g & 1][i], acc[i][jj], 0, 0, 0);
                SB();
                if constexpr (LSW && g == 31 && j) *LDSP(f32x4, yw + i * 1024) = acc[i][1];
                if constexpr (g == EGX && m == 4) sy_post<5>(pmine);
                if constexpr (g == 29 && m == 5) { yw = cwa + L_Y + wid * 2048; yr = yw ^ (PAIR ? 4096 : 16384); yp = pmine ^ (PAIR ? 4 : 16); yo = (cwa & 255) + ((cwa & 768) << 7); TIE(yw); TIE(yr); TIE(yp); TIE(yo); }
                if constexpr (gn < 32 && m < 4) G2_FRAG(gn, m);
                if constexpr (m >= 4 && g2_wr(g - 1) + m - 4 < g2_wr(g)) {
                    constexpr int n = g2_wr(g - 1) + m - 4, pl = g2_place(7 + (n >> 1)), pb = pl >= L_C ? L_C : 0;
                    asm volatile("ds_write_b128 %0, %1 offset:%2" :: "v"(cwa + pb), "v"(cr[n]), "n"(pl - pb + (n & 1) * 1024) : "memory");
                }
                if constexpr (i == 1 && n0 + j < n1) G2_ISSUE((n0 + j) >> 1, (n0 + j) & 1);
                if constexpr (i >= 2 && g2_ald(f, i & 1) >= 0) G2_A(g2_ald(f, i & 1));
                SB();
            });
        });
#undef G2_FRAG
    }
    TS(11);
    if (EPRIO) __builtin_amdgcn_s_setprio(EPRIO);
    {
#pragma unroll
        for (int i = 0; i < 4; ++i) if (!LSW) *LDSP(f32x4, yw + i * 1024) = acc[i][1];
        sy_post<4>(pmine);
        sy_wait<4, 0>(yp);
        char* const ob = a.out + (size_t)(tm * 128 + gm * 32 + gk * 16) * 8192 + 4096 + tn * 256;
        f32x4 w[4];
#pragma unroll
        for (int i = 0; i < 4; ++i) w[i] = *LDSP(f32x4, yr + i * 1024);
        f32x4 o[4];
#pragma unroll
        for (int r = 0; r < 4; ++r) { o[r] = f32x4{add1(w[0][r], acc[0][0][r]), add1(w[1][r], acc[1][0][r]), add1(w[2][r], acc[2][0][r]), add1(w[3][r], acc[3][0][r])}; if (EGX) TIE(o[r]); }
        if (EGX && !wn) sy_wait<5, 1>(pall);
#pragma unroll
        for (int r = 0; r < 4; ++r) GST_B128_NT(yo, o[r], ob + r * 8192, 0);
    }
    asm volatile(".Lfk_code_b:");
    TSE(12);
    TS_FLUSH();
}

}

extern "C" void kernel_launch(void* const* d_in, const int* in_sizes, int n_in, void* d_out, int out_size, void* d_ws, size_t ws_size,
                              hipStream_t stream) {
    (void)in_sizes; (void)n_in; (void)out_size; (void)ws_size;
    fk::Args a;
    a.q = (const char*)d_in[0]; a.k = (const char*)d_in[1]; a.v = (const char*)d_in[2]; a.sk = (const char*)d_in[3];
    a.ssmk = (const char*)d_in[4]; a.ssmD = (const char*)d_in[5]; a.x = (const char*)d_in[6]; a.lre = (const char*)d_in[7];
    a.lim = (const char*)d_in[8]; a.bre = (const char*)d_in[9]; a.bim = (const char*)d_in[10]; a.cre = (const char*)d_in[11];
    a.cim = (const char*)d_in[12]; a.logdt = (const char*)d_in[13]; a.dskip = (const char*)d_in[14];
    a.out = (char*)d_out; a.ws = (char*)d_ws;
    hipLaunchKernelGGL(fk::fused, dim3(256), dim3(768), 0, stream, a);
}
```

```cpp
#include <hip/hip_runtime.h>
#include <stdint.h>
#include <type_traits>

namespace fk {

constexpr int S = 2048, D = 1024, P = 1024;

typedef __attribute__((ext_vector_type(8))) short bf16x8;
typedef __attribute__((ext_vector_type(4))) short s16x4;
typedef __attribute__((ext_vector_type(16))) float f32x16;
typedef __attribute__((ext_vector_type(4))) float f32x4;
typedef __attribute__((ext_vector_type(2))) float f32x2;
typedef __attribute__((ext_vector_type(4))) unsigned u32x4;
typedef __attribute__((ext_vector_type(2))) unsigned u32x2;
#define LDSP(T, a) ((__attribute__((address_space(3))) T*)(uintptr_t)(unsigned)(a))

__device__ __forceinline__ float bf2f(unsigned v) { return __uint_as_float(v << 16); }
__device__ __forceinline__ float bflo(unsigned w) { return __uint_as_float(w << 16); }
__device__ __forceinline__ float bfhi(unsigned w) { return __uint_as_float(w & 0xffff0000u); }
__device__ __forceinline__ uint16_t f2bf(float f) { __bf16 b = (__bf16)f; return __builtin_bit_cast(uint16_t, b); }
__device__ __forceinline__ float rbf(float f) { return bf2f(f2bf(f)); }
typedef __attribute__((ext_vector_type(2))) __bf16 bf16x2;
__device__ __forceinline__ unsigned pack2(float lo, float hi) { return __builtin_bit_cast(unsigned, __builtin_convertvector(f32x2{lo, hi}, bf16x2)); }
__device__ __forceinline__ float rb1(float x) { return __uint_as_float(pack2(0.f, x)); }
__device__ __forceinline__ void cmac(float& xr, float& xi, float ar, float ai, float hr, float hi) {
    asm("v_fmac_f32 %0, %2, %4\n\tv_fmac_f32 %1, %2, %5\n\tv_fma_f32 %0, -%3, %5, %0\n\tv_fmac_f32 %1, %3, %4" : "+v"(xr), "+v"(xi) : "v"(ar), "v"(ai), "v"(hr), "v"(hi));
}
__device__ __forceinline__ void cmad(float& o_r, float& o_i, float ar, float ai, float hr, float hi, float er, float ei) {
    asm("v_fma_f32 %0, %2, %4, %6\n\tv_fma_f32 %1, %2, %5, %7\n\tv_fma_f32 %0, -%3, %5, %0\n\tv_fmac_f32 %1, %3, %4" : "=&v"(o_r), "=&v"(o_i) : "v"(ar), "v"(ai), "v"(hr), "v"(hi), "v"(er), "v"(ei));
}
__device__ __forceinline__ f32x2 cmul4(f32x2 a, f32x2 h) {
    float o_r, o_i;
    asm("v_mul_f32 %0, %2, %4\n\tv_mul_f32 %1, %2, %5\n\tv_fma_f32 %0, -%3, %5, %0\n\tv_fmac_f32 %1, %3, %4" : "=&v"(o_r), "=&v"(o_i) : "v"(a[0]), "v"(a[1]), "v"(h[0]), "v"(h[1]));
    return f32x2{o_r, o_i};
}

#define PK_LO " op_sel:[0,0,0] op_sel_hi:[0,1,1]"
#define PK_HI " op_sel:[1,0,0] op_sel_hi:[1,1,1]"
#define PK_NEG " neg_lo:[1,0,0] neg_hi:[1,0,0]"
__device__ __forceinline__ void pk_cmad(f32x2& o_r, f32x2& o_i, f32x2 a, f32x2 hr, f32x2 hi, f32x2 er, f32x2 ei) {
    asm("v_pk_fma_f32 %0, %2, %3, %5" PK_LO "\n\tv_pk_fma_f32 %1, %2, %4, %6" PK_LO "\n\tv_pk_fma_f32 %0, %2, %4, %0" PK_HI PK_NEG "\n\tv_pk_fma_f32 %1, %2, %3, %1" PK_HI
        : "=&v"(o_r), "=&v"(o_i) : "v"(a), "v"(hr), "v"(hi), "v"(er), "v"(ei));
}
__device__ __forceinline__ void pk_cmac_conj(f32x2& xr, f32x2& xi, f32x2 a, f32x2 hr, f32x2 hi) {
    asm("v_pk_fma_f32 %0, %2, %3, %0" PK_LO "\n\tv_pk_fma_f32 %1, %2, %4, %1" PK_LO "\n\tv_pk_fma_f32 %0, %2, %4, %0" PK_HI "\n\tv_pk_fma_f32 %1, %2, %3, %1" PK_HI PK_NEG
        : "+v"(xr), "+v"(xi) : "v"(a), "v"(hr), "v"(hi));
}
__device__ __forceinline__ void pk_conj_mul(f32x2& o_r, f32x2& o_i, f32x2 a, f32x2 hr, f32x2 hi) {
    asm("v_pk_mul_f32 %0, %2, %3 op_sel:[0,0] op_sel_hi:[0,1]\n\tv_pk_mul_f32 %1, %2, %4 op_sel:[0,0] op_sel_hi:[0,1] neg_lo:[1,0] neg_hi:[1,0]\n\t"
        "v_pk_fma_f32 %0, %2, %4, %0" PK_HI PK_NEG "\n\tv_pk_fma_f32 %1, %2, %3, %1" PK_HI PK_NEG : "=&v"(o_r), "=&v"(o_i) : "v"(a), "v"(hr), "v"(hi));
}
__device__ __forceinline__ void pk_conj_mac(f32x2& xr, f32x2& xi, f32x2 a, f32x2 hr, f32x2 hi) {
    asm("v_pk_fma_f32 %0, %2, %3, %0" PK_LO "\n\tv_pk_fma_f32 %1, %2, %4, %1" PK_LO PK_NEG "\n\tv_pk_fma_f32 %0, %2, %4, %0" PK_HI PK_NEG "\n\tv_pk_fma_f32 %1, %2, %3, %1" PK_HI PK_NEG
        : "+v"(xr), "+v"(xi) : "v"(a), "v"(hr), "v"(hi));
}
__device__ __forceinline__ void cmac2(f32x2& x, f32x2 a, f32x2 h) {
    asm("v_pk_fma_f32 %0, %1, %2, %0" PK_LO "\n\tv_pk_fma_f32 %0, %1, %2, %0 op_sel:[1,1,0] op_sel_hi:[1,0,1] neg_lo:[1,0,0]" : "+v"(x) : "v"(a), "v"(h));
}
#define PLN 0
__device__ __forceinline__ void c2_mac(f32x2& xr, f32x2& xi, float p, float q, float r, float s, f32x2 hr, f32x2 hi) {
    float a = xr[0], b = xi[0], c = xr[1], d = xi[1];
    asm("v_fmac_f32 %0, %4, %8\n\tv_fmac_f32 %1, %6, %9\n\tv_fmac_f32 %2, %4, %10\n\tv_fmac_f32 %3, %6, %11\n\t"
        "v_fmac_f32 %0, %5, %9\n\tv_fmac_f32 %1, %7, %8\n\tv_fmac_f32 %2, %5, %11\n\tv_fmac_f32 %3, %7, %10"
        : "+v"(a), "+v"(b), "+v"(c), "+v"(d) : "v"(p), "v"(q), "v"(r), "v"(s), "v"(hr[0]), "v"(hi[0]), "v"(hr[1]), "v"(hi[1]));
    xr = f32x2{a, c}; xi = f32x2{b, d};
}
__device__ __forceinline__ void c2_mad(f32x2& xr, f32x2& xi, float p, float q, float r, float s, f32x2 hr, f32x2 hi, f32x2 er, f32x2 ei) {
    float a, b, c, d;
    asm("v_fma_f32 %0, %4, %8, %12\n\tv_fma_f32 %1, %6, %9, %13\n\tv_fma_f32 %2, %4, %10, %14\n\tv_fma_f32 %3, %6, %11, %15\n\t"
        "v_fmac_f32 %0, %5, %9\n\tv_fmac_f32 %1, %7, %8\n\tv_fmac_f32 %2, %5, %11\n\tv_fmac_f32 %3, %7, %10"
        : "=&v"(a), "=&v"(b), "=&v"(c), "=&v"(d) : "v"(p), "v"(q), "v"(r), "v"(s), "v"(hr[0]), "v"(hi[0]), "v"(hr[1]), "v"(hi[1]), "v"(er[0]), "v"(ei[0]), "v"(er[1]), "v"(ei[1]));
    xr = f32x2{a, c}; xi = f32x2{b, d};
}
__device__ __forceinline__ void c2_mul(f32x2& xr, f32x2& xi, float p, float q, float r, float s, f32x2 hr, f32x2 hi) {
    float a, b, c, d;
    asm("v_mul_f32 %0, %4, %8\n\tv_mul_f32 %1, %6, %9\n\tv_mul_f32 %2, %4, %10\n\tv_mul_f32 %3, %6, %11\n\t"
        "v_fmac_f32 %0, %5, %9\n\tv_fmac_f32 %1, %7, %8\n\tv_fmac_f32 %2, %5, %11\n\tv_fmac_f32 %3, %7, %10"
        : "=&v"(a), "=&v"(b), "=&v"(c), "=&v"(d) : "v"(p), "v"(q), "v"(r), "v"(s), "v"(hr[0]), "v"(hi[0]), "v"(hr[1]), "v"(hi[1]));
    xr = f32x2{a, c}; xi = f32x2{b, d};
}
__device__ __forceinline__ float add1(float a, float b) { float c; asm("v_add_f32 %0, %1, %2" : "=v"(c) : "v"(a), "v"(b)); return c; }
__device__ __forceinline__ void mfma_(f32x4& c, bf16x8 a, bf16x8 b) { asm volatile("v_mfma_f32_16x16x32_bf16 %0, %1, %2, %0" : "+v"(c) : "v"(a), "v"(b)); }
__device__ __forceinline__ float sum4(float x) {
    const auto p = __builtin_amdgcn_permlane32_swap(__float_as_uint(x), __float_as_uint(x), false, false);
    x = __uint_as_float(p[0]) + __uint_as_float(p[1]);
    const auto q = __builtin_amdgcn_permlane16_swap(__float_as_uint(x), __float_as_uint(x), false, false);
    return __uint_as_float(q[0]) + __uint_as_float(q[1]);
}

template <int I, int N, class F>
__device__ __forceinline__ void static_for(F&& f) { if constexpr (I < N) { f(std::integral_constant<int, I>{}); static_for<I + 1, N>(f); } }

#define TS_DECL
#define TS0a()
#define TS0b()
#define TSE(i)
#define TS(i)
#define TS_FLUSH()

#define WAIT_V(n) asm volatile("s_waitcnt vmcnt(%0)" ::"n"((n) > 63 ? 63 : (n)) : "memory")
#define WAIT_L(n) asm volatile("s_waitcnt lgkmcnt(%0)" ::"n"((n) > 15 ? 15 : (n)) : "memory")
#define BW 1
constexpr int wenc(int v, int l) { v = v > 63 ? 63 : v; l = l > 15 ? 15 : l; return (v & 15) | 0x70 | (l << 8) | ((v >> 4) << 14); }
#define WAIT_VB(n) do { if constexpr (BW) __builtin_amdgcn_s_waitcnt(wenc(n, 15)); else WAIT_V(n); } while (0)
#define WAIT_LB(n) do { if constexpr (BW) __builtin_amdgcn_s_waitcnt(wenc(63, n)); else WAIT_L(n); } while (0)
#define BAR() asm volatile("s_barrier" ::: "memory")
#define SB() __builtin_amdgcn_sched_barrier(0)
template <class T> __device__ __forceinline__ void tie_(T& x) { asm volatile("" : "+v"(x)); }
#define TIE(x) tie_(x)
template <class T> __device__ __forceinline__ void ties_(T& x) { asm volatile("" : "+s"(x)); }
__device__ __forceinline__ unsigned take(unsigned x) { unsigned y; asm volatile("v_mov_b32 %0, %1" : "=v"(y) : "v"(x)); return y; }
#define DEF_LD(name, ins) template <int OFF, class T> __device__ __forceinline__ void name(T& dst, unsigned voff, const char* sb) { \
    asm volatile(ins : "=v"(dst) : "v"(voff), "s"(sb), "n"(OFF) : "memory"); }
DEF_LD(gld_u16_, "global_load_ushort %0, %1, %2 offset:%3")
DEF_LD(gld_b32_, "global_load_dword %0, %1, %2 offset:%3")
DEF_LD(gld_b64_, "global_load_dwordx2 %0, %1, %2 offset:%3")
DEF_LD(gld_b128_, "global_load_dwordx4 %0, %1, %2 offset:%3")
#define NTQ " nt"
#define NTK " nt"
DEF_LD(gld_b64_nt_, "global_load_dwordx2 %0, %1, %2 offset:%3" NTQ)
DEF_LD(gld_b128_nt_, "global_load_dwordx4 %0, %1, %2 offset:%3" NTQ)
DEF_LD(gld_b64_sc1_, "global_load_dwordx2 %0, %1, %2 offset:%3 sc1")
DEF_LD(gld_b128_sc1_, "global_load_dwordx4 %0, %1, %2 offset:%3 sc1")
#define DEF_ST(name, ins) template <int OFF, class T> __device__ __forceinline__ void name(unsigned voff, const T& v, const char* sb) { \
    asm volatile(ins :: "v"(voff), "v"(v), "s"(sb), "n"(OFF) : "memory"); }
DEF_ST(gst_b128_, "global_store_dwordx4 %0, %1, %2 offset:%3\n\ts_nop 1")
DEF_ST(gst_b128_nt_, "global_store_dwordx4 %0, %1, %2 offset:%3 nt\n\ts_nop 1")
DEF_ST(gst_b128_sc1_, "global_store_dwordx4 %0, %1, %2 offset:%3 sc1\n\ts_nop 1")
DEF_ST(gst_b64_sc1_, "global_store_dwordx2 %0, %1, %2 offset:%3 sc1")
#define GLD_U16(dst, voff, sb) gld_u16_<0>(dst, voff, sb)
#define GLD_B32(dst, voff, sb, off) gld_b32_<(off)>(dst, voff, sb)
#define GLD_B64(dst, voff, sb, off) gld_b64_<(off)>(dst, voff, sb)
#define GLD_B128(dst, voff, sb, off) gld_b128_<(off)>(dst, voff, sb)
#define GLD_B64_NT(dst, voff, sb, off) gld_b64_nt_<(off)>(dst, voff, sb)
#define GLD_B128_NT(dst, voff, sb, off) gld_b128_nt_<(off)>(dst, voff, sb)
#define GLD_B64_SC1(dst, voff, sb, off) gld_b64_sc1_<(off)>(dst, voff, sb)
#define GLD_B128_SC1(dst, voff, sb, off) gld_b128_sc1_<(off)>(dst, voff, sb)
#define GST_B128(voff, v, sb, off) gst_b128_<(off)>(voff, v, sb)
#define GST_B128_NT(voff, v, sb, off) gst_b128_nt_<(off)>(voff, v, sb)
#define GST_B128_SC1(voff, v, sb, off) gst_b128_sc1_<(off)>(voff, v, sb)
#define GST_B64_SC1(voff, v, sb) gst_b64_sc1_<0>(voff, v, sb)
__device__ __forceinline__ void dma_(unsigned ldsaddr, unsigned voff, const char* sb) {
    asm volatile("s_mov_b32 m0, %0\n\ts_nop 0\n\tglobal_load_lds_dwordx4 %1, %2" :: "s"(ldsaddr), "v"(voff), "s"(sb) : "memory"); }
__device__ __forceinline__ void dma_nt_(unsigned ldsaddr, unsigned voff, const char* sb) {
    asm volatile("s_mov_b32 m0, %0\n\ts_nop 0\n\tglobal_load_lds_dwordx4 %1, %2 nt" :: "s"(ldsaddr), "v"(voff), "s"(sb) : "memory"); }
#define DF 0
#define DFN 4
#define FL_0 ""
#define FL_1 " nt"
#define FL_2 " sc1"
#define FL_3 " sc1 nt"
#define FL_4 " sc0 sc1"
#define FL_5 " sc0 sc1 nt"
#define FL_6 " sc0 nt"
#define FLX_(x) FL_##x
#define FLX(x) FLX_(x)
__device__ __forceinline__ void dmaf_(unsigned ldsaddr, unsigned voff, const char* sb) {
    asm volatile("s_mov_b32 m0, %0\n\ts_nop 0\n\tglobal_load_lds_dwordx4 %1, %2" FLX(DF) :: "s"(ldsaddr), "v"(voff), "s"(sb) : "memory"); }
template <int OFF> __device__ __forceinline__ void dmaof_(unsigned voff, const char* sb) {
    asm volatile("global_load_lds_dwordx4 %0, %1 offset:%2" FLX(DF) :: "v"(voff), "s"(sb), "n"(OFF) : "memory"); }
#define DMA(ldsaddr, voff, sb) dma_(ldsaddr, voff, sb)
template <int OFF> __device__ __forceinline__ void dmao_(unsigned voff, const char* sb) {
    asm volatile("global_load_lds_dwordx4 %0, %1 offset:%2" :: "v"(voff), "s"(sb), "n"(OFF) : "memory"); }
template <int OFF, class T> __device__ __forceinline__ void dsr128_(T& dst, unsigned addr) { asm volatile("ds_read_b128 %0, %1 offset:%2" : "=v"(dst) : "v"(addr), "n"(OFF)); }
template <int OFF, class T> __device__ __forceinline__ void dsr64_(T& dst, unsigned addr) { asm volatile("ds_read_b64 %0, %1 offset:%2" : "=v"(dst) : "v"(addr), "n"(OFF)); }
template <int OFF, class T> __device__ __forceinline__ void dsr2_(T& dst, unsigned addr) { asm volatile("ds_read2_b32 %0, %1 offset0:%2 offset1:%3" : "=v"(dst) : "v"(addr), "n"(OFF), "n"(OFF + 1)); }
#define DSR128(dst, addr, off) dsr128_<(off)>(dst, addr)
#define DSR64(dst, addr, off) dsr64_<(off)>(dst, addr)

__device__ __forceinline__ void kv4(unsigned& o01, unsigned& o23, unsigned ka, unsigned kb, unsigned kc, unsigned kd, const unsigned* w, unsigned v0, unsigned v1) {
    unsigned t1, t3, p0, p1, p2, p3;
    asm(
        "v_dot2_f32_bf16 %0, %8, %19, 0\n\tv_dot2_f32_bf16 %1, %9, %18, 0\n\tv_dot2_f32_bf16 %2, %9, %19, 0\n\tv_dot2_f32_bf16 %3, %10, %18, 0\n\t"
        "v_dot2_f32_bf16 %4, %9, %16, 0\n\tv_dot2_f32_bf16 %5, %9, %17, 0\n\tv_dot2_f32_bf16 %6, %10, %16, 0\n\tv_dot2_f32_bf16 %7, %10, %17, 0\n\t"
        "v_cvt_pk_bf16_f32 %0, %0, %4\n\tv_cvt_pk_bf16_f32 %1, %1, %5\n\tv_cvt_pk_bf16_f32 %2, %2, %6\n\tv_cvt_pk_bf16_f32 %3, %3, %7\n\t"
        "v_dot2_f32_bf16 %4, %9, %15, 0\n\tv_dot2_f32_bf16 %5, %10, %14, 0\n\tv_dot2_f32_bf16 %6, %10, %15, 0\n\tv_dot2_f32_bf16 %7, %11, %14, 0\n\t"
        "v_dot2_f32_bf16 %0, %0, %22, 0\n\tv_dot2_f32_bf16 %1, %1, %22, 0\n\tv_dot2_f32_bf16 %2, %2, %22, 0\n\tv_dot2_f32_bf16 %3, %3, %22, 0\n\t"
        "v_cvt_pk_bf16_f32 %0, %0, %4\n\tv_cvt_pk_bf16_f32 %1, %1, %5\n\tv_cvt_pk_bf16_f32 %2, %2, %6\n\tv_cvt_pk_bf16_f32 %3, %3, %7\n\t"
        "v_dot2_f32_bf16 %4, %10, %12, 0\n\tv_dot2_f32_bf16 %5, %10, %13, 0\n\tv_dot2_f32_bf16 %6, %11, %12, 0\n\tv_dot2_f32_bf16 %7, %11, %13, 0\n\t"
        "v_dot2_f32_bf16 %0, %0, %22, 0\n\tv_dot2_f32_bf16 %1, %1, %22, 0\n\tv_dot2_f32_bf16 %2, %2, %22, 0\n\tv_dot2_f32_bf16 %3, %3, %22, 0\n\t"
        "v_cvt_pk_bf16_f32 %0, %0, %4\n\tv_cvt_pk_bf16_f32 %1, %1, %5\n\tv_cvt_pk_bf16_f32 %2, %2, %6\n\tv_cvt_pk_bf16_f32 %3, %3, %7\n\t"
        "v_dot2_f32_bf16 %0, %0, %22, 0\n\tv_dot2_f32_bf16 %1, %1, %22, 0\n\tv_dot2_f32_bf16 %2, %2, %22, 0\n\tv_dot2_f32_bf16 %3, %3, %22, 0\n\t"
        "v_cvt_pk_bf16_f32 %0, %0, 0\n\tv_cvt_pk_bf16_f32 %1, 0, %1\n\tv_cvt_pk_bf16_f32 %2, %2, 0\n\tv_cvt_pk_bf16_f32 %3, 0, %3\n\t"
        "v_dot2_f32_bf16 %0, %0, %20, 0\n\tv_dot2_f32_bf16 %1, %1, %20, 0\n\tv_dot2_f32_bf16 %2, %2, %21, 0\n\tv_dot2_f32_bf16 %3, %3, %21, 0\n\t"
        "s_nop 1\n\tv_cvt_pk_bf16_f32 %0, %1, %0\n\tv_cvt_pk_bf16_f32 %2, %3, %2\n\t"
        : "=&v"(o01), "=&v"(t1), "=&v"(o23), "=&v"(t3), "=&v"(p0), "=&v"(p1), "=&v"(p2), "=&v"(p3)
        : "v"(ka), "v"(kb), "v"(kc), "v"(kd), "v"(w[0]), "v"(w[1]), "v"(w[2]), "v"(w[3]), "v"(w[4]), "v"(w[5]), "v"(w[6]), "v"(w[7]), "v"(v0), "v"(v1), "s"(0x3f803f80u));
}
__device__ __forceinline__ void kv4a(unsigned* t, unsigned* e, unsigned ka, unsigned kb, unsigned kc, unsigned kd, const unsigned* w) {
    asm volatile(
        "v_and_b32 %8, %21, %15\n\tv_lshlrev_b32 %9, 16, %16\n\tv_and_b32 %10, %21, %16\n\tv_lshlrev_b32 %11, 16, %17\n\t"
        "v_and_b32 %12, %21, %17\n\tv_mul_f32 %0, %20, %8\n\tv_mul_f32 %1, %20, %9\n\tv_mul_f32 %2, %20, %10\n\t"
        "v_mul_f32 %3, %20, %11\n\tv_mul_f32 %4, %19, %9\n\tv_mul_f32 %5, %19, %10\n\tv_mul_f32 %6, %19, %11\n\t"
        "v_mul_f32 %7, %19, %12\n\tv_lshlrev_b32 %13, 16, %18\n\tv_and_b32 %14, %21, %18\n\tv_cvt_pk_bf16_f32 %0, 0, %0\n\t"
        "v_cvt_pk_bf16_f32 %1, 0, %1\n\tv_cvt_pk_bf16_f32 %2, 0, %2\n\tv_cvt_pk_bf16_f32 %3, 0, %3\n\tv_cvt_pk_bf16_f32 %4, 0, %4\n\t"
        "v_cvt_pk_bf16_f32 %5, 0, %5\n\tv_cvt_pk_bf16_f32 %6, 0, %6\n\tv_cvt_pk_bf16_f32 %7, 0, %7\n\t"
        : "=&v"(t[0]), "=&v"(t[1]), "=&v"(t[2]), "=&v"(t[3]), "=&v"(t[4]), "=&v"(t[5]), "=&v"(t[6]), "=&v"(t[7]), "=&v"(e[0]), "=&v"(e[1]), "=&v"(e[2]), "=&v"(e[3]), "=&v"(e[4]), "=&v"(e[5]), "=&v"(e[6])
        : "v"(ka), "v"(kb), "v"(kc), "v"(kd), "v"(w[2]), "v"(w[3]), "s"(0xffff0000u));
}
__device__ __forceinline__ void kv4b(unsigned* t, const unsigned* e, const unsigned* w) {
    asm volatile(
        "v_add_f32 %0, %0, %4\n\tv_add_f32 %1, %1, %5\n\tv_add_f32 %2, %2, %6\n\tv_add_f32 %3, %3, %7\n\t"
        "v_mul_f32 %4, %14, %8\n\tv_mul_f32 %5, %14, %9\n\tv_mul_f32 %6, %14, %10\n\tv_mul_f32 %7, %14, %11\n\t"
        "v_cvt_pk_bf16_f32 %0, 0, %0\n\tv_cvt_pk_bf16_f32 %1, 0, %1\n\tv_cvt_pk_bf16_f32 %2, 0, %2\n\tv_cvt_pk_bf16_f32 %3, 0, %3\n\t"
        "v_cvt_pk_bf16_f32 %4, 0, %4\n\tv_cvt_pk_bf16_f32 %5, 0, %5\n\tv_cvt_pk_bf16_f32 %6, 0, %6\n\tv_cvt_pk_bf16_f32 %7, 0, %7\n\t"
        "v_add_f32 %0, %0, %4\n\tv_add_f32 %1, %1, %5\n\tv_add_f32 %2, %2, %6\n\tv_add_f32 %3, %3, %7\n\t"
        "v_mul_f32 %4, %13, %9\n\tv_mul_f32 %5, %13, %10\n\tv_mul_f32 %6, %13, %11\n\tv_mul_f32 %7, %13, %12\n\t"
        : "+v"(t[0]), "+v"(t[1]), "+v"(t[2]), "+v"(t[3]), "+v"(t[4]), "+v"(t[5]), "+v"(t[6]), "+v"(t[7]) : "v"(e[2]), "v"(e[3]), "v"(e[4]), "v"(e[5]), "v"(e[6]), "v"(w[0]), "v"(w[1]));
}
__device__ __forceinline__ void kv4c(unsigned* t, unsigned v0, unsigned v1) {
    asm volatile(
        "v_cvt_pk_bf16_f32 %0, 0, %0\n\tv_cvt_pk_bf16_f32 %1, 0, %1\n\tv_cvt_pk_bf16_f32 %2, 0, %2\n\tv_cvt_pk_bf16_f32 %3, 0, %3\n\t"
        "v_cvt_pk_bf16_f32 %4, 0, %4\n\tv_cvt_pk_bf16_f32 %5, 0, %5\n\tv_cvt_pk_bf16_f32 %6, 0, %6\n\tv_cvt_pk_bf16_f32 %7, 0, %7\n\t"
        "v_add_f32 %0, %0, %4\n\tv_add_f32 %1, %1, %5\n\tv_add_f32 %2, %2, %6\n\tv_add_f32 %3, %3, %7\n\t"
        "v_lshlrev_b32 %4, 16, %8\n\tv_and_b32 %5, %10, %8\n\tv_lshlrev_b32 %6, 16, %9\n\tv_and_b32 %7, %10, %9\n\t"
        "v_cvt_pk_bf16_f32 %0, 0, %0\n\tv_cvt_pk_bf16_f32 %1, 0, %1\n\tv_cvt_pk_bf16_f32 %2, 0, %2\n\tv_cvt_pk_bf16_f32 %3, 0, %3\n\t"
        "v_mul_f32 %0, %0, %4\n\tv_mul_f32 %1, %1, %5\n\tv_mul_f32 %2, %2, %6\n\tv_mul_f32 %3, %3, %7\n\t"
        "v_cvt_pk_bf16_f32 %0, %1, %0\n\tv_cvt_pk_bf16_f32 %2, %3, %2\n\t"
        : "+v"(t[0]), "+v"(t[1]), "+v"(t[2]), "+v"(t[3]), "+v"(t[4]), "+v"(t[5]), "+v"(t[6]), "+v"(t[7]) : "v"(v0), "v"(v1), "s"(0xffff0000u));
}
__device__ __forceinline__ f32x4 mul4bf(u32x2 x, const unsigned* d) {
    float a, b, c, e;
    asm("v_dot2_f32_bf16 %0, %4, %6, 0\n\tv_dot2_f32_bf16 %1, %4, %7, 0\n\tv_dot2_f32_bf16 %2, %5, %8, 0\n\tv_dot2_f32_bf16 %3, %5, %9, 0\n\t"
        "v_cvt_pk_bf16_f32 %0, 0, %0\n\tv_cvt_pk_bf16_f32 %1, 0, %1\n\tv_cvt_pk_bf16_f32 %2, 0, %2\n\tv_cvt_pk_bf16_f32 %3, 0, %3"
        : "=&v"(a), "=&v"(b), "=&v"(c), "=&v"(e) : "v"(x[0]), "v"(x[1]), "v"(d[0]), "v"(d[1]), "v"(d[2]), "v"(d[3]));
    return f32x4{a, b, c, e};
}
template <int BIT> __device__ __forceinline__ void quad_swap(u32x4& a, u32x4& b) {
    unsigned n0, n1, n2, n3, b0 = b[0], b1 = b[1], b2 = b[2], b3 = b[3];
#define QS_(d, s, k) "v_cndmask_b32_dpp " d ", " s ", " k ", vcc quad_perm:[%14,%15,%16,%17] row_mask:0xf bank_mask:0xf\n\t"
    asm("s_mov_b64 vcc, %12\n\ts_nop 0\n\t" QS_("%0", "%4", "%8") QS_("%1", "%5", "%9") QS_("%2", "%6", "%10") QS_("%3", "%7", "%11")
        "s_mov_b64 vcc, %13\n\t" QS_("%4", "%8", "%4") QS_("%5", "%9", "%5") QS_("%6", "%10", "%6") QS_("%7", "%11", "%7")
        : "=&v"(n0), "=&v"(n1), "=&v"(n2), "=&v"(n3), "+v"(b0), "+v"(b1), "+v"(b2), "+v"(b3)
        : "v"(a[0]), "v"(a[1]), "v"(a[2]), "v"(a[3]), "s"(BIT ? 0x3333333333333333ull : 0x5555555555555555ull), "s"(BIT ? 0xccccccccccccccccull : 0xaaaaaaaaaaaaaaaaull),
          "n"(BIT ? 2 : 1), "n"(BIT ? 3 : 0), "n"(BIT ? 0 : 3), "n"(BIT ? 1 : 2) : "vcc");
#undef QS_
    a = u32x4{n0, n1, n2, n3}; b = u32x4{b0, b1, b2, b3};
}
__device__ __forceinline__ void kv_dma(unsigned ldsaddr, unsigned k0, unsigned k1, unsigned k2, unsigned k3, const char* sb) {
    asm volatile("s_mov_b32 m0, %5\n\ts_nop 0\n\tglobal_load_lds_dwordx4 %0, %4" NTK "\n\tglobal_load_lds_dwordx4 %1, %4 offset:1024" NTK "\n\t"
                 "global_load_lds_dwordx4 %2, %4 offset:2048" NTK "\n\tglobal_load_lds_dwordx4 %3, %4 offset:3072" NTK
                 :: "v"(k0), "v"(k1), "v"(k2), "v"(k3), "s"(sb), "s"(ldsaddr) : "memory");
}
#define IF_(c, C) "s_cmp_lg_u32 " c ", " C "\n\ts_cbranch_scc1 1f\n\t"
template <int C, int N> __device__ __forceinline__ void wait_if(int c) { asm volatile(IF_("%0", "%1") "s_waitcnt vmcnt(%2)\n1:" :: "s"(c), "n"(C), "n"(N) : "scc", "memory"); }
template <int C, int M> __device__ __forceinline__ void flag_if(int c, unsigned voff, u32x2 v, const char* sb) {
    asm volatile(IF_("%0", "%1") "s_mov_b32 exec_lo, %5\n\ts_mov_b32 exec_hi, 0\n\tglobal_store_dwordx2 %2, %3, %4 sc1\n\ts_mov_b64 exec, -1\n1:" :: "s"(c), "n"(C), "v"(voff), "v"(v), "s"(sb), "n"(M) : "scc", "memory"); }
template <int C> __device__ __forceinline__ void st4_if(int c, unsigned voff, const u32x4* v, const char* s0, const char* s1) {
    asm volatile(IF_("%0", "%1") "global_store_dwordx4 %2, %3, %7 sc1\n\tglobal_store_dwordx4 %2, %4, %7 offset:1024 sc1\n\tglobal_store_dwordx4 %2, %5, %8 sc1\n\tglobal_store_dwordx4 %2, %6, %8 offset:1024 sc1\n\ts_nop 1\n1:"
                 :: "s"(c), "n"(C), "v"(voff), "v"(v[0]), "v"(v[1]), "v"(v[2]), "v"(v[3]), "s"(s0), "s"(s1) : "scc", "memory"); }
template <int C> __device__ __forceinline__ void ld4_if(int c, unsigned voff, u32x4* v, const char* s0) {
    asm volatile(IF_("%4", "%5") "global_load_dwordx4 %0, %6, %7 nt\n\tglobal_load_dwordx4 %1, %6, %7 offset:1024 nt\n\tglobal_load_dwordx4 %2, %6, %7 offset:2048 nt\n\tglobal_load_dwordx4 %3, %6, %7 offset:3072 nt\n1:"
                 : "+v"(v[0]), "+v"(v[1]), "+v"(v[2]), "+v"(v[3]) : "s"(c), "n"(C), "v"(voff), "s"(s0) : "scc", "memory"); }
template <int C> __device__ __forceinline__ void ld8_if(int c, unsigned voff, u32x2* v, const char* s0, const char* s1) {
    asm volatile(IF_("%8", "%9") "global_load_dwordx2 %0, %10, %11 offset:-4096 nt\n\tglobal_load_dwordx2 %1, %10, %11 offset:-2048 nt\n\tglobal_load_dwordx2 %2, %10, %11 nt\n\tglobal_load_dwordx2 %3, %10, %11 offset:2048 nt\n\t"
                 "global_load_dwordx2 %4, %10, %12 offset:-4096 nt\n\tglobal_load_dwordx2 %5, %10, %12 offset:-2048 nt\n\tglobal_load_dwordx2 %6, %10, %12 nt\n\tglobal_load_dwordx2 %7, %10, %12 offset:2048 nt\n1:"
                 : "+v"(v[0]), "+v"(v[1]), "+v"(v[2]), "+v"(v[3]), "+v"(v[4]), "+v"(v[5]), "+v"(v[6]), "+v"(v[7]) : "s"(c), "n"(C), "v"(voff), "s"(s0), "s"(s1) : "scc", "memory"); }
template <int E> __device__ __forceinline__ void sy_post(unsigned myaddr) { asm volatile("ds_write_b32 %0, %0 offset:%1" :: "v"(myaddr), "n"(E * 64) : "memory"); }
template <int E, int SL = 1> __device__ __forceinline__ unsigned sy_wait(unsigned addr) {
    unsigned v;
    asm volatile("s_branch 2f\n1:\n\ts_sleep %3\n2:\n\tds_read_b32 %0, %1 offset:%2\n\ts_waitcnt lgkmcnt(0)\n\tv_cmp_eq_u32 vcc, 0, %0\n\ts_cbranch_vccnz 1b"
                 : "=&v"(v) : "v"(addr), "n"(E * 64), "n"(SL) : "vcc", "memory");
    return v;
}
template <int E> __device__ __forceinline__ void sy_pre(unsigned& v, unsigned addr) { asm volatile("ds_read_b32 %0, %1 offset:%2" : "=v"(v) : "v"(addr), "n"(E * 64) : "memory"); }
template <int E> __device__ __forceinline__ void sy_chk(unsigned& v, unsigned addr) {
    asm volatile("s_branch 2f\n1:\n\tds_read_b32 %0, %1 offset:%2\n\ts_waitcnt lgkmcnt(0)\n2:\n\tv_cmp_eq_u32 vcc, 0, %0\n\ts_cbranch_vccnz 1b"
                 : "+v"(v) : "v"(addr), "n"(E * 64) : "vcc", "memory");
}
template <int OFF, class T> __device__ __forceinline__ void dsrtr_(T& dst, unsigned addr) { asm volatile("ds_read_b64_tr_b16 %0, %1 offset:%2" : "=v"(dst) : "v"(addr), "n"(OFF)); }

constexpr size_t WS_H = 0, WS_KVT = (size_t)10 << 20, WS_ZT = (size_t)15 << 20, WS_E = (size_t)20 << 20, WS_FLAG = WS_E + (2048 << 10);
#define HP 65536
#define KP 8192
#define ZP 4096
constexpr int FL_KV = 0, FL_Z = 65536, FL_H = 131072, FL_EPOCH = 196608;
constexpr unsigned SPIN_CAP = 1u << 17;
#define ABL 0
#define CSPLIT 1
#define MERGE 3
#define CB 13
#define PAUSE 0
#define QLATE 0
#define CVPRIO 0
#define STG_SEL (idx & 3)
#define SP0 3
#define SDP 1
#define LP0 3
#define LDP 2
#define KVS 5
#define KVU 3
#define QIT 1
#define ZB 3
#define OS 2
#define MPRIO 2

#define ROT 0x4042
#define R2 0x41
#define CPF 0
#define PRE 4
#define PRO 9
#define G1NT 0
#define G1NTS 16
#define TMIX 0
#define TLBT 0
#define CDEF 0
#define CVLATE 1
#define FPRIO 3
#define YS 0
#define DP0 1
#define DP1 5
#define AWB 1
#define PK 0
#define PU 14
#define PC0 13
#define PC1 14
#define PC2 15
#define PC3 16
#define PQ 0
#define SPF 0
#define SPH 0
#define NB1 4
#define EP 1
#define ESL 0
#define CFB 0
#define P1 1
#define P0 1
#define BKE 0
#define EALL 0
#define EREP 0
#define TOUCH 0
#define FILLB 0
#define CPOST 1
#define CEARLY 0
#define NOOWN 0
#define PFIRST 0
#define ZB0 0
#define CATCH 4
#define PWE 0
#define SWAP 1
#define KVM 0x0FE
#define KFD 3
#define ZW 2
#define ZX 1
#define ZP0 3
#define BARM 0x22220
#define NR 6
#define WPS 2
#define CD0 3
#define AWX 8
#define VBM 0
#define ZOWN 1
#define ZPS 4
#define XAB 0
#define QM 1
#define OSG 0
#define KPL 0xFE
#define KPRIO 1
#define GPRIO 1
#define PST 0x7654
#define PLT 0xB964
#define LC 0
#define PRE2 PRE
#define KVD 0
#define PAIR 1
#define G2P1 0
#define EPRIO 0
#define LSW 1
#define EGX 28
#define CR1 0
#define CR2 0
#define CR3 0
#define CRL (2 * NR - CR1 - CR2 - CR3)
#define UE 1
#define UPRIO 3
#define AMF 7
#define M1 1
#define W2 1
#define W1 0
#define NBK 3
#define XB 4
#define KFB 9
#define APRE 0
#define CB0 1
#define AP0 8
#define HTSE(i)
#define KAT 2
#define SD 2
#define KV3 0x088888
#define KOWN 1
#define HKV 0
#define KVP NBK
#define LZ 1
#define AWN 8
#define CRATE 2
#define ZSB 0
#define ZFB 1
#define ZSL 4
#define OSL 0
constexpr int it_cnt(int p, int k, int last) {
    const int pos[8] = {PK, PU, PC0, PC1, PQ, PU - 1, PC2, PC3}, n[8] = {4, 4, 4, 4, 8, 1, 4, 4};
    int c = 0;
    for (int i = 0; i < 8; ++i) if (pos[i] <= last && (pos[i] > p || (pos[i] == p && i > k))) c += n[i];
    return c;
}
constexpr int nib(int t, int c) { return (t >> (4 * c)) & 15; }
constexpr bool nib_any(int t, int v) { return nib(t, 0) == v || nib(t, 1) == v || nib(t, 2) == v || nib(t, 3) == v; }
constexpr int kv_wait(int late) {
    int t = 0;
    for (int c = 0; c < 4; ++c) t |= ((nib(PLT, c) >= nib(PST, c) && nib(PLT, c) < nib(PST, c) + KFD) == late ? nib(PST, c) + KFD : 15) << (4 * c);
    return t;
}
constexpr int G1STAGE = 32768;
constexpr int g1_slot(int s) { return (NB1 == 5 ? (s + 3) % 5 : s & 3) * G1STAGE; }
constexpr int L_KS = NB1 == 5 ? 65536 : 131072, L_VS = L_KS + 16384;
constexpr int L_TS = 3 * G1STAGE, L_TR = L_TS + 3072;
constexpr int CST = 4288;
constexpr int UOFF = 2 * CST + 128, UPL = 2304;
constexpr int CHSZ = UOFF + 2 * UPL + 32;
constexpr int L_AGG = 4 * CHSZ, L_ES = L_AGG + 15 * 512, L_SY = L_ES + 2048;
constexpr int L_HT = 98304;
constexpr int L_C = 98304, G2STAGE = 16384;
constexpr int L_Y = 16384;
static_assert(L_SY + 384 <= 65536 && L_AGG >= 49152 && UOFF % 16 == 0 && CHSZ % 256 == 32, "lds");

__device__ __forceinline__ int img_off(int r, int c) { return (r >> 1) * 256 + ((((r & 1) * 8 + c) ^ ((r >> 1) & 15)) << 4); }

constexpr int g1_upto(int g) {
    int n = 4 * NBK;
    for (int t = SD; t <= g; ++t) {
        int sb = (t + 1) / 2, sub = (t + 1) % 2, allowed = sb ? 4 * (sb + 3) + 2 * (sub + 1) : 12;
        if (allowed > 64) allowed = 64;
        n = n + CATCH < allowed ? n + CATCH : allowed;
    }
    return n;
}
constexpr int g1_cnt(int s) { return HKV || s >= KVP ? g1_upto(2 * s - 2) - 4 * (s + 1) : s >= KAT ? g1_upto(2 * s - 2) - 4 * KVP : g1_upto(2 * s - 2) - 4 * (s + 1) + 4; }
static_assert(KVP >= 1 && KVP <= NBK && AP0 % 4 == 0, "schedule");
static_assert(g1_upto(28) == 64, "schedule");
constexpr int AW = AWN;
constexpr int g2_place(int s) { return s < 5 ? L_C + ((s + 2) & 3) * G2STAGE : s < 7 ? (s - 1) * G2STAGE : s < 10 ? (9 - s) * G2STAGE : g2_place(s - 9); }
constexpr bool g2_bar(int g) { return (BARM >> g) & 1; }
struct G2Plan { int upto[33], wr[33], ald[64][2], seq[164], use[64]; };
constexpr G2Plan g2_plan() {
    G2Plan p = {};
    int nc = 14 + 2 * NR, nw = 0, nx = 4, lastb = 0, n = 0;
    for (int i = 0; i < 164; ++i) p.seq[i] = 1000;
    for (; nx < 4 + AW; ++nx) p.seq[nx] = ++n;
    for (int t = 0; t < 32; ++t) {
        p.upto[t] = nc; p.wr[t] = nw;
        if (t < 2) continue;
        if (g2_bar(t)) lastb = t;
        const int w = AW + nw < AWX ? AW + nw : AWX;
        for (int f = 2 * t; f < 2 * t + 2; ++f) {
            p.use[f] = n;
            if (nc < 32 && nc == p.upto[t] + (f & 1) && (f & 1) < CRATE && t >= CD0 && lastb >= (nc < 20 ? 1 : (nc | 1) - 18)) p.seq[100 + nc++] = ++n;
            for (int k = 0; k < 2; ++k) {
                p.ald[f][k] = -1;
                if (nx < 64 && nx - f - k < w) { p.ald[f][k] = nx; p.seq[nx++] = ++n; }
            }
        }
        for (int k = 0; k < WPS; ++k) if (nw < 2 * NR && lastb >= (nw < 6 ? 1 : (nw | 1) - 4)) ++nw;
    }
    p.upto[32] = nc; p.wr[32] = nw;
    return p;
}
constexpr G2Plan G2P = g2_plan();
constexpr int g2_upto(int g) { return G2P.upto[g + 1]; }
constexpr int g2_wr(int g) { return G2P.wr[g + 1]; }
constexpr int g2_ald(int f, int k) { return G2P.ald[f][k]; }
constexpr int g2_cnt(int id, int at) { return G2P.use[at] - G2P.seq[id]; }
constexpr int LASTB = 31 - __builtin_clz(BARM);
constexpr int lz_post() { int g = LASTB; while (G2P.seq[2 * g] < G2P.seq[131]) ++g; return g; }
constexpr int lz_chk() { int s = 7 + NR; while (G2P.seq[100 + 2 * s + 1] < G2P.seq[2 * LASTB]) ++s; return 2 * s - 1; }
static_assert(!LZ || (AWB && lz_post() + 1 < lz_chk() && lz_chk() < EGX), "schedule");
constexpr int g2_cover(int g) { int s = (g + 1) / 2; while (s < 15 && !g2_bar(2 * s + 1)) ++s; return s; }
static_assert(BARM >> 17 && g2_cover(3) < 7 && CRATE <= 2, "the epilogue needs stage 8 behind a barrier");

constexpr int cv_m(int o) { return (o & 1) ? 7 - (o >> 1) : o >> 1; }
constexpr int cv_first(int o) { int p = 0; for (int i = 0; i < o; ++i) p += 8 - cv_m(i); return p; }
constexpr int cv_grp(int p) { int o = 0; while (cv_first(o + 1) <= p) ++o; return o; }
constexpr int cv_read(int p) {
    int slot[40] = {};
    for (int i = 0; i < 40; ++i) slot[i] = -1;
    slot[0] = 2 * 3 + 1;
    for (int o = 0; o < 8; ++o) {
        int q = cv_first(o + 1) - 1;
        for (int h = 0; h < 2; ++h) { while (slot[q] >= 0) ++q; slot[q] = 2 * (o + 4) + h; }
    }
    const int bs[8] = {1, 3, 5, 11, 12, 13, 19, 21};
    for (int jb = 0; jb < 8; ++jb) slot[bs[jb]] = 32 + jb;
    return slot[p];
}
static_assert(cv_read(35) == 22 && cv_read(36) == 23 && cv_read(37) == -1 && cv_read(10) == 11 && cv_read(15) == 12 && cv_read(18) == 15 && cv_read(24) == 17, "conv schedule");
constexpr int cv_wait(int o) {
    int n = 0;
    for (int q = cv_first(o) - 1;; --q) {
        const int rd = cv_read(q < 0 ? q + 36 : q);
        if (rd == 2 * (o + (q < 0 ? 8 : 0)) + 1) return n;
        if (rd >= 0) ++n;
    }
}

struct Args {
    const char *q, *k, *v, *sk, *ssmk, *ssmD, *x, *lre, *lim, *bre, *bim, *cre, *cim, *logdt, *dskip;
    char *out, *ws;
};

typedef __attribute__((ext_vector_type(8))) unsigned long long u64x8;
__global__ __launch_bounds__(768, 3) void fused(Args) {
    __shared__ __attribute__((aligned(1024))) char shm[163840];
    TS0a();
    u64x8 ka0, ka1; unsigned long long ka2;
    const char* pc0;
    int code_n;
    asm volatile("s_load_dwordx16 %0, %5, 0x0\n\ts_load_dwordx16 %1, %5, 0x40\n\ts_load_dwordx2 %2, %5, 0x80\n\t"
                 "s_getpc_b64 %3\n.Lfk_code_0:\n\ts_mov_b32 %4, .Lfk_code_a-.Lfk_code_0-4\n\ts_max_i32 %4, %4, .Lfk_code_b-.Lfk_code_0-4"
                 : "=&s"(ka0), "=&s"(ka1), "=&s"(ka2), "=&s"(pc0), "=&s"(code_n) : "s"(__builtin_amdgcn_kernarg_segment_ptr()) : "scc");
    const int tid = threadIdx.x, lane = tid & 63, wraw = __builtin_amdgcn_readfirstlane(tid >> 6);
    const int wid = !SWAP ? wraw : wraw < 4 ? wraw + 8 : wraw - 4;
    const int b = blockIdx.x, xcd = b & 7, idx = b >> 3;
    asm volatile("s_cmp_lt_u32 %0, 8\n\ts_cbranch_scc1 1f\n\ts_setprio 3\n\ts_cmp_lg_u32 %0, 8\n\ts_cbranch_scc1 1f\n\t"
                 "v_lshl_add_u32 v161, %1, 7, %2\n\tv_min_i32 v161, %3, v161\n\ts_mov_b64 exec, 0xffff\n\tglobal_load_dword v161, v161, %4\n\ts_mov_b64 exec, -1\n1:"
                 :: "s"(wid), "v"(lane), "s"(idx * 2048), "s"(code_n), "s"(pc0) : "scc", "memory", "v161");
    TS_DECL
    const int tm = (xcd >> 1) * 4 + (idx >> 3), tn = (xcd & 1) * 8 + (idx & 7);
    const int wm = wid & 3, wn = (wid >> 2) & 1, l31 = lane & 31, lh = lane >> 5;
    const unsigned lb = (unsigned)(uintptr_t)shm;
    const unsigned l16 = lane * 16;
    const int l15 = lane & 15, g4 = lane >> 4, sq = 2 * wn + (wm & 1), st = sq * 16 + l15, run = (wm >> 1) * 4 + g4;
    unsigned kvv[4];
    const int trow = idx * 64 * 2048 + xcd * 256;
    if (wid < 8) {
        const int v = l15 ^ g4, hi = v >> 3;
        const int r0 = wn ? tn * 64 + (wm >> 1) * 32 : tm * 128 + (wm >> 1) * 64 + (wm & 1) * 4;
        const unsigned d0 = (r0 + ((g4 * (wn ? 2 : 9)) & (wn ? 6 : 17))) * 2048 + (v & 7) * 16, odd = wn ? 11 : 14;
        kvv[0] = d0 + (hi << odd); kvv[2] = d0 + ((hi ^ 1) << odd) + (wn ? 32768 - 2048 : 4096 - 2048);
        kvv[1] = (kvv[0] ^ 64) + (wn ? 16384 - 1024 : 65536 - 1024); kvv[3] = (kvv[2] ^ 64) + (wn ? 16384 - 1024 : 65536 - 1024);
    } else {
        kvv[0] = trow + (idx ? -4 * 2048 : 0) + g4 * 2048 + wm * 64 + l15 * 4;
        asm volatile("v_writelane_b32 %0, 0, 0\n\tv_writelane_b32 %0, 4, 1\n\tv_writelane_b32 %0, %1, 2" : "+v"(kvv[0]) : "s"((xcd * 64 + idx * 2 + (wm >> 1)) * 4));
        kvv[1] = (xcd * 128 + wm * 32 + l31) * 8;
        kvv[2] = kvv[3] = (tn * 64 + lane) * 2;
    }
    unsigned g1dst = lb + wid * 4096;
    int rot7 = ((((ROT >> 12) & 15) * (idx & 7) + ((ROT >> 8) & 15) * (idx >> 3) + ((ROT >> 4) & 15) * (xcd >> 1) + (ROT & 15) * (xcd & 1)) & 15) << 7;
    asm volatile("s_waitcnt lgkmcnt(0)" : "+s"(ka0), "+s"(ka1), "+s"(ka2), "+s"(g1dst), "+s"(rot7), "+v"(kvv[0]), "+v"(kvv[1]), "+v"(kvv[2]), "+v"(kvv[3]));
    Args a;
    a.q = (const char*)ka0[0]; a.k = (const char*)ka0[1]; a.v = (const char*)ka0[2]; a.sk = (const char*)ka0[3]; a.ssmk = (const char*)ka0[4]; a.ssmD = (const char*)ka0[5];
    a.x = (const char*)ka0[6]; a.lre = (const char*)ka0[7]; a.lim = (const char*)ka1[0]; a.bre = (const char*)ka1[1]; a.bim = (const char*)ka1[2]; a.cre = (const char*)ka1[3];
    a.cim = (const char*)ka1[4]; a.logdt = (const char*)ka1[5]; a.dskip = (const char*)ka1[6]; a.out = (char*)ka1[7]; a.ws = (char*)ka2;
    char* const ws = a.ws;
    const char* const flg = ws + WS_FLAG;
    TS(26);
    const char* g1src = wn ? ((wm & 1) ? a.bim : a.bre) : a.x;
    asm volatile("s_mov_b32 m0, %[m]\n\ts_cmp_gt_u32 %[w], 7\n\ts_cbranch_scc1 1f\n\t"
                 "global_load_lds_dwordx4 %[k0], %[g]" FLX(DF) "\n\tglobal_load_lds_dwordx4 %[k1], %[g] offset:1024" FLX(DF) "\n\t"
                 "global_load_lds_dwordx4 %[k2], %[g] offset:2048" FLX(DF) "\n\tglobal_load_lds_dwordx4 %[k3], %[g] offset:3072" FLX(DF) "\n\t"
                 "s_branch 2f\n1:\n\tglobal_load_ushort v164, %[k2], %[c1]\n\tglobal_load_ushort v165, %[k2], %[c2]\n\tglobal_load_ushort v166, %[k2], %[c3]\n\t"
                 "v_mov_b64 v[162:163], %[kv]\n\ts_mov_b64 exec, 7\n\tv_mov_b64 v[162:163], %[d]\n\ts_mov_b64 exec, 3\n\tv_mov_b64 v[162:163], %[e]\n\ts_mov_b64 exec, -1\n\t"
                 "v_mad_u64_u32 v[162:163], vcc, %[k0], 1, v[162:163]\n\tglobal_load_dword v160, v[162:163], off sc1\n\tglobal_load_dwordx2 v[162:163], %[k1], %[sk]\n2:"
                 :
                 : [w] "s"(wid), [c1] "s"(a.logdt), [c2] "s"(a.lre), [c3] "s"(a.lim), [d] "s"(a.ssmD), [e] "s"(flg + FL_EPOCH), [sk] "s"(a.sk),
                   [k0] "v"(kvv[0]), [k1] "v"(kvv[1]), [k2] "v"(kvv[2]), [k3] "v"(kvv[3]), [kv] "s"(a.k), [g] "s"(g1src + rot7), [m] "s"(g1dst)
                 : "scc", "vcc", "memory", "v160", "v161", "v162", "v163", "v164", "v165", "v166");
    TS0b();
    TS(27);
    if (P1) BAR();
#define SLICE_TABLE() unsigned slo, shi; { int ix = idx; asm volatile("" : "+s"(ix)); \
        const int rot2 = (((R2 >> 4) & 15) * (ix >> 3) + (R2 & 15) * (xcd >> 1)) & 15, es = (tn - rot2) & 15; \
        const unsigned long long id = 0xFEDCBA9876543210ull, seq = rot2 ? (id >> (4 * rot2)) | (id << (64 - 4 * rot2)) : id; \
        const unsigned long long lo = (1ull << (4 * es)) - 1, tab = ((seq & lo) << 4) | (seq & ~((lo << 4) | 15)) | tn; \
        slo = (unsigned)tab; shi = (unsigned)(tab >> 32); asm volatile("" : "+s"(slo), "+s"(shi)); }
#define SLICE(st) ((((st) < 8 ? slo : shi) >> (4 * ((st) & 7))) & 15)
    typedef __attribute__((ext_vector_type(4))) short v4i16;
    int role = wid;
    asm volatile("" : "+s"(role));

    if (role >= 8) {
        int lane;
        asm volatile("v_mbcnt_lo_u32_b32 %0, -1, 0\n\tv_mbcnt_hi_u32_b32 %0, -1, %0" : "=v"(lane));
        __builtin_assume(lane >= 0 && lane < 64);
        const int l31 = lane & 31, lh = lane >> 5, l15 = lane & 15, g4 = lane >> 4;
        const unsigned l16 = lane * 16;
        const int chn = xcd * 128 + idx * 4 + wm;
        const unsigned cb = lb + wm * CHSZ;
        const int r = l15;
        const unsigned fpub = KOWN ? (xcd * 32 + l31) * 256 + idx * 8 : xcd * 256 + idx * 8, fpoll = KOWN ? (xcd * 32 + idx) * 256 + l31 * 8 : xcd * 256 + l31 * 8;
        u32x2 fl; u32x4 sw[4], uu[4];
        unsigned tch = 0;
        if (TOUCH) {
            const int ln = wm * 4 + (lane & 3);
            const unsigned fo = (unsigned)(WS_FLAG - WS_E) + (wm == 0 ? FL_H + (tm * 16 + tn) * 128 : (wm == 1 ? FL_KV : FL_Z) + xcd * 256 + (lane & 1) * 128);
            const unsigned o = (lane & 4) && (TOUCH & 2) ? fo : ((tm * 2 + (ln >> 3)) * 1024 + tn * 64) * 16 + (ln & 7) * 128;
            asm volatile("s_mov_b64 exec, 0xff\n\tglobal_load_dword %0, %1, %2\n\ts_mov_b64 exec, -1" : "=&v"(tch) : "v"(o), "s"(ws + WS_E) : "memory");
        }
        SLICE_TABLE();
        TS(1);
        unsigned sm, cl[3]; u32x2 kw0;
        asm volatile("s_waitcnt vmcnt(0)" : "={v160}"(sm), "={v[162:163]}"(kw0), "={v164}"(cl[0]), "={v165}"(cl[1]), "={v166}"(cl[2]) :: "memory", "v161");
        TS(22);
        const unsigned hal = take(sm);
        const unsigned tagA = __builtin_amdgcn_readlane(hal, 0) + 1, tagB = __builtin_amdgcn_readlane(hal, 1) + (tagA == 0 ? 1u : 0u);
        const u32x2 tag = {tagA, tagB};
        f32x2 L1, C1;
        {
            const float dt = rbf(expf(bf2f(take(cl[0]))));
            const float lr = bf2f(take(cl[1])), li = bf2f(take(cl[2]));
            const float e = expf(dt * lr);
            float sn, cs;
            sincosf(dt * li, &sn, &cs);
            L1 = f32x2{e * cs, e * sn};
            const float nr = L1[0] - 1.f, ni = L1[1], den = lr * lr + li * li;
            C1 = f32x2{2.f * ((nr * lr + ni * li) / den), 2.f * ((ni * lr - nr * li) / den)};
            TIE(L1); TIE(C1);
        }

        f32x2 p[11];
        p[0] = L1;
        if (PWE) {
#pragma unroll
            for (int i = 1; i <= 10; ++i) { p[i] = cmul4(p[i - 1], p[i - 1]); TIE(p[i]); }
        }
        {
            if (!PWE) {
#pragma unroll
                for (int i = 1; i <= 10; ++i) p[i] = cmul4(p[i - 1], p[i - 1]);
            }
            const f32x2 one = {1.f, 0.f}, zero = {0.f, 0.f};
            const unsigned ts = lb + L_TS + l16, tr = lb + L_TR + wm * 2048 + l16;
            if (wm == 0) {
                *LDSP(f32x4, ts) = f32x4{L1[0], L1[1], C1[0], C1[1]};
                *LDSP(f32x4, ts + 1024) = f32x4{p[3][0], p[3][1], p[4][0], p[4][1]};
                *LDSP(f32x4, ts + 2048) = f32x4{p[6][0], p[6][1], __uint_as_float(tagA), __uint_as_float(tagB)};
            }
            f32x2 Q[2], V[2], W[2];
            Q[0] = cmul4(cmul4(C1, (wm & 1) ? p[5] : one), (wm & 2) ? p[6] : one); Q[1] = cmul4(Q[0], p[4]);
            V[1] = (wm & 1) ? one : p[5]; V[0] = cmul4(V[1], p[4]);
            W[1] = cmul4((wm & 1) ? one : p[9], (wm & 2) ? one : p[10]); W[0] = cmul4(W[1], p[8]);
#pragma unroll
            for (int i = 0; i < 2; ++i) {
                const f32x2 Q8 = cmul4(Q[i], p[3]), U = 4 * wm + 2 * i >= 16 - tm ? cmul4(W[i], p[7]) : zero, Wz = 4 * wm + 2 * i + 1 >= 16 - tm ? W[i] : zero;
                *LDSP(f32x4, tr + i * 1024) = f32x4{Q[i][0], Q8[0], Q[i][1], Q8[1]};
                *LDSP(f32x4, tr + i * 1024 + 8192) = f32x4{Wz[0], Wz[1], U[0], U[1]};
                *LDSP(f32x2, tr + i * 1024 + 16384) = V[i];
            }
        }
        auto b0 = [&]() {
            unsigned k0 = trow + wm * 32768 + g4 * 2048 + ((l15 ^ (2 * g4)) << 4), k1 = k0 + 7168, k2 = (k0 ^ 128) + 14336, k3 = k2 + 7168;
            if (HKV) { TIE(k0); TIE(k1); TIE(k2); TIE(k3); }
            if (HKV == 1) { kv_dma(lb + L_KS + wm * 4096, k0, k1, k2, k3, a.k); kv_dma(lb + L_VS + wm * 4096, k0, k1, k2, k3, a.v); }
            WAIT_L(0); BAR(); HTSE(1);
            if (HKV == 2) { kv_dma(lb + L_KS + wm * 4096, k0, k1, k2, k3, a.k); kv_dma(lb + L_VS + wm * 4096, k0, k1, k2, k3, a.v); }
        };
        if (CB0 == 0) b0();
        const unsigned dskl = (__builtin_amdgcn_readlane(hal, 2) >> ((wm & 1) * 16)) & 0xffffu;
        u32x4 kvo[4];
        u32x2 qv[8];
        const char* krow = a.ssmk + (size_t)chn * 4096;
        const char* qs = a.q + (size_t)(idx * 64 + wm * 16) * 2048 + xcd * 256;
        const char* kvd = ws + WS_KVT + (size_t)(xcd * 8 + wm * 2) * 65536 + idx * 2048;
        const int pl = (PLT >> ((STG_SEL) * 4)) & 15;
        const unsigned qo = lh * 8192 + l31 * 8 + 4096;
        auto hb = [&](auto nc) {
            constexpr int nb = decltype(nc)::value;
            if constexpr (nb == KAT && HKV) WAIT_V(0);
            if constexpr (nb == KFB) { WAIT_V(12); int lm = (1 << KFB) - (2 << (KAT + __builtin_popcount(KV3))); ties_(lm); wait_if<0, 0>((lm >> pl) & 1); }
            BAR();
            if constexpr (nb == KFB) flag_if<0, KOWN ? -1 : 1>(wm, fpub, tag, flg + FL_KV);
            if constexpr (nib_any(PLT, nb)) { ld4_if<nb>(pl, l16, sw, krow); ld8_if<nb>(pl, qo, qv, qs, qs + 16384); }
        };
#define HB(n) hb(std::integral_constant<int, (n) + NBK>{})
        {
            unsigned kw[2][4], ta[2], th[2];
            u32x2 hm[2];
#pragma unroll
            for (int j = 0; j < 2; ++j) {
                const unsigned x = take(kw0[j]);
                const auto y = __builtin_amdgcn_permlane16_swap(x, x, false, false);
#pragma unroll
                for (int p = 0; p < 2; ++p) { kw[p][2 * j] = y[p] << 16; kw[p][2 * j + 1] = y[p] & 0xffff0000u; }
            }
#pragma unroll
            for (int p = 0; p < 2; ++p) {
                unsigned h[3];
#pragma unroll
                for (int j = 0; j < 3; ++j) h[j] = (__shfl(hal, p * 8 + (r >> 1) + 16 * (j + 1)) >> ((r & 1) * 16)) & 0xffffu;
                hm[p] = idx ? u32x2{h[0] << 16, h[1] | (h[2] << 16)} : u32x2{0u, 0u};
                const int q = r >> 2, pp = r & 3;
                ta[p] = lb + L_KS + (8 * g4 + q) * 256 + (((wm * 4 + p * 2 + (pp >> 1)) ^ (2 * (q | ((g4 & 1) << 2)))) << 4) + (pp & 1) * 8;
                th[p] = (ta[p] ^ 128) - 1024;
                TIE(hm[p]); TIE(ta[p]); TIE(th[p]);
#pragma unroll
                for (int j = 0; j < 4; ++j) TIE(kw[p][j]);
            }
            TS(21);
            if (CB0 == 1) b0();
#pragma unroll
            for (int i = 0; i < 4; ++i) asm volatile("" : "=v"(sw[i]), "=v"(qv[2 * i]), "=v"(qv[2 * i + 1]));
            static_for<1, KAT + 1>(hb);
            TS(2);
            u32x2 kt[4][3], vt[4][2];
#pragma unroll
            for (int u = 0; u < 4; ++u) {
                const int p = u >> 1, o = (u & 1) * 8192;
                auto tr = [&](unsigned ad) -> u32x2 { return __builtin_bit_cast(u32x2, __builtin_amdgcn_ds_read_tr16_b64_v4i16(LDSP(v4i16, ad))); };
                kt[u][0] = tr(th[p] + o); kt[u][1] = tr(ta[p] + o); vt[u][0] = tr(ta[p] + o + 16384); kt[u][2] = tr(ta[p] + o + 1024); vt[u][1] = tr(ta[p] + o + 17408);
                SB();
            }
            TS(14);
            __builtin_amdgcn_s_setprio(KPRIO);
            static_for<0, 4>([&](auto uc) {
                constexpr int u = decltype(uc)::value, p = u >> 1, oh = u & 1;
                const u32x2 k0 = oh == 0 && g4 == 0 ? hm[p] : kt[u][0];
                unsigned t0[8], t1[8], e[7];
#define K3(n) do { if constexpr ((KV3 >> (6 * u + n)) & 1) hb(std::integral_constant<int, KAT + 1 + __builtin_popcount(KV3 & ((1 << (6 * u + n)) - 1))>{}); } while (0)
                kv4a(t0, e, k0[0], k0[1], kt[u][1][0], kt[u][1][1], kw[p]); K3(0); kv4b(t0, e, kw[p]); K3(1); kv4c(t0, vt[u][0][0], vt[u][0][1]); K3(2);
                kv4a(t1, e, kt[u][1][0], kt[u][1][1], kt[u][2][0], kt[u][2][1], kw[p]); K3(3); kv4b(t1, e, kw[p]); K3(4); kv4c(t1, vt[u][1][0], vt[u][1][1]);
                kvo[u] = u32x4{t1[2], t1[0], t0[2], t0[0]};
                GST_B128_SC1(l16, kvo[u], kvd + (u >> 1) * 65536, (u & 1) * 1024);
                K3(5);
#undef K3
            });
            hb(std::integral_constant<int, KAT + 1 + __builtin_popcount(KV3)>{});
            __builtin_amdgcn_s_setprio(3);
        }
        TS(3);
        const char* usrc = ws + WS_KVT + (size_t)(xcd * 8 + (idx >> 2)) * 65536 + wm * 16384 + (idx & 3) * 64;
        const unsigned uo = (lane >> 2) * 256 + (lane & 3) * 16;
        const char* cs = ((wm & 2) ? a.cim : a.cre) + (size_t)(tn * 64 + (wm & 1) * 2) * 2048;
        unsigned co[4];
#pragma unroll
        for (int i = 0; i < 4; ++i) {
            const int pair = i * 4 + g4, v = r ^ pair, row = pair * 2 + (v >> 3);
            co[i] = ((row & 15) * 4 + (row >> 4)) * 2048 + (v & 7) * 16 - i * 1024;
        }
        bool kv_ok = false;
#pragma unroll
        for (int it = 0; it < 4; ++it) asm volatile("" : "=v"(uu[it]));
        auto cst = [&](int place, int slice) { const char* s = cs + (slice << 7); DMA(lb + place + wm * 4096, co[0], s); dmao_<1024>(co[1], s); dmao_<2048>(co[2], s); dmao_<3072>(co[3], s); };
        auto items = [&](auto pc) {
            constexpr int p = decltype(pc)::value;
            if constexpr (PU == p) {
                WAIT_V(it_cnt(PU - 1, 5, PU - 1)); TIE(fl);
                kv_ok = __all(fl[0] == tagA && fl[1] == tagB);
                const char* us = kv_ok ? usrc : a.x;
                for (int it = 0; it < 4; ++it) GLD_B128_SC1(uu[it], uo, us + it * 4096, 0);
            }
            if constexpr (PC0 == p) cst(g2_place(0), SLICE(0));
            if constexpr (PC1 == p) cst(g2_place(1), SLICE(1));
            if constexpr (PU == p + 1) GLD_B64_SC1(fl, fpoll, flg + FL_KV, 0);
            if constexpr (PC2 == p) cst(g2_place(5), SLICE(5));
            if constexpr (PC3 == p) cst(g2_place(6), SLICE(6));
        };
#define ITEMS(p) items(std::integral_constant<int, p>{})
        static_assert(KFB > KAT + 1 + __builtin_popcount(KV3) && KV3 < (1 << 23) && KFB <= 13 && KFB <= PC0 && KFB < PU - 1 && nib(PLT, 0) >= KAT && nib(PLT, 3) < PU - 1, "schedule");
        static_for<KAT + 2 + __builtin_popcount(KV3), 15>([&](auto nc) { hb(nc); if constexpr (decltype(nc)::value < 14) items(nc); });
        const unsigned ud = lb + (lane & 3) * CHSZ + UOFF + ((lane >> 2) & 1) * UPL + (16 + wm * 32 + (lane >> 3)) * 16;
        auto zero = [&]() {
            const u32x4 z4 = {0, 0, 0, 0};
            if (lane < 24) *LDSP(u32x4, lb + L_SY + l16) = z4;
            if (lane < 56) *LDSP(u32x4, lb + L_AGG + wm * 896 + l16) = z4;
            *LDSP(u32x4, lane < 32 ? cb + ((lane >> 3) & 1) * CST + (lane & 7) * 16 : cb + UOFF + (g4 & 1) * UPL + r * 16) = z4;
        };
        auto fill = [&]() {
#pragma unroll
            for (int it = 0; it < 4; ++it) TIE(sw[it]);
#pragma unroll
            for (int it = 0; it < 4; ++it) {
                const unsigned q = cb + 128 + it * 1024 + l16;
                const unsigned d0 = sw[it][0], d1 = sw[it][1], d2 = sw[it][2], d3 = sw[it][3];
                const unsigned o1 = it ? __builtin_amdgcn_readlane(sw[it ? it - 1 : 0][3], 63) : 0u;
                const unsigned x1 = __builtin_amdgcn_update_dpp(o1, d3, 0x138, 0xf, 0xf, false);
                *LDSP(u32x4, q) = sw[it];
                *LDSP(u32x4, q + CST) = u32x4{__builtin_amdgcn_alignbyte(d0, x1, 2), __builtin_amdgcn_alignbyte(d1, d0, 2), __builtin_amdgcn_alignbyte(d2, d1, 2), __builtin_amdgcn_alignbyte(d3, d2, 2)};
            }
        };
        auto uput = [&]() {
            WAIT_V(it_cnt(PU, 1, UE ? 15 : 16));
#pragma unroll
            for (int it = 0; it < 4; ++it) TIE(uu[it]);
            if (!kv_ok) {
                TSE(23);
                for (unsigned spin = 0; spin < SPIN_CAP; ++spin) {
                    GLD_B64_SC1(fl, fpoll, flg + FL_KV, 0);
                    WAIT_V(0); TIE(fl);
                    if (__all(fl[0] == tagA && fl[1] == tagB)) break;
                    __builtin_amdgcn_s_sleep(8);
                }
#pragma unroll
                for (int it = 0; it < 4; ++it) GLD_B128_SC1(uu[it], uo, usrc + it * 4096, 0);
                WAIT_V(0);
#pragma unroll
                for (int it = 0; it < 4; ++it) TIE(uu[it]);
            }
#pragma unroll
            for (int it = 0; it < 4; ++it) *LDSP(u32x4, ud + it * 128) = uu[it];
        };
        static_assert(PU <= (UE ? 15 : 16), "u");
        ITEMS(14);
        if (UE) __builtin_amdgcn_s_setprio(UPRIO);
        if (UE >= 2) zero();
        if (UE == 2) fill();
        BAR();
        ITEMS(15);
        TS(4);
        if (UE < 2) zero();
        if (UE & 1) fill();
        if (UE) uput();
        WAIT_L(0);
        TS(5);
        BAR();
        __builtin_amdgcn_s_setprio(FPRIO);
        ITEMS(16);
        TS(11);
        const unsigned pmine = lb + L_SY + 32 + wm * 4, pall = lb + L_SY + 32 + (lane & 3) * 4;
        if (!UE) { fill(); TS(13); uput(); sy_post<0>(pmine); sy_wait<0>(pall); }
        if (!CPOST) { WAIT_V(0); sy_post<1>(pmine); }
        __builtin_amdgcn_s_setprio(CVPRIO);
        TS(6);
        f32x4 ca[8];
        {
            unsigned kb[4];
#pragma unroll
            for (int i = 0; i < 4; ++i) { kb[i] = cb + (~r & 1) * CST + 2 * (64 + 16 + r - 8 * g4 - 7 + (~r & 1)) + i * 1024; TIE(kb[i]); }
            unsigned ub = cb + UOFF + (g4 & 1) * UPL + (15 + r + (g4 >> 1)) * 16 - 64;
#pragma unroll
            for (int i = 0; i < 8; ++i) { ca[i] = f32x4{0, 0, 0, 0}; TIE(ca[i]); }
            s16x4 FA[4][2]; bf16x8 FB[2][8];
#define CV_A(s, o, e) dsr2_<16 * ((s) + ((o) >> 3)) + 128 * (cv_m((o) & 7) & 1) + 2 * (e)>(FA[(o) & 3][e], kb[cv_m((o) & 7) >> 1])
#define CV_B(s, jb) DSR128(FB[(s) & 1][jb], ub, 64 + 256 * (jb) - 32 * (s))
            static_for<0, 8>([&](auto ic) { constexpr int n = decltype(ic)::value; CV_B(0, n); CV_A(0, n >> 1, n & 1); });
            WAIT_L(0);
            auto body = [&]() {
                static_for<0, 72>([&](auto ic) {
                    constexpr int s = decltype(ic)::value / 36, p = decltype(ic)::value % 36, o = cv_grp(p), m = cv_m(o), jb = p - cv_first(o);
                    if constexpr (jb == 0) { WAIT_LB(cv_wait(o)); if constexpr (!(AMF & 1)) { TIE(FA[o & 3][0]); TIE(FA[o & 3][1]); } }
                    if constexpr (p == 0 && !(AMF & 1)) { for (int i = 0; i < 8; ++i) TIE(FB[s][i]); }
                    const bf16x8 fa = __builtin_shufflevector(FA[o & 3][0], FA[o & 3][1], 0, 1, 2, 3, 4, 5, 6, 7);
                    if constexpr (AMF & 1) mfma_(ca[jb + m], fa, FB[s][jb]); else
                    ca[jb + m] = __builtin_amdgcn_mfma_f32_16x16x32_bf16(fa, FB[s][jb], ca[jb + m], 0, 0, 0);
                    SB();
                    constexpr int rd = cv_read(p);
                    if constexpr (rd >= 32) CV_B(s + 1, rd - 32);
                    else if constexpr (rd >= 0) CV_A(s, rd >> 1, rd & 1);
                    SB();
                });
                for (int i = 0; i < 4; ++i) { kb[i] += 128; TIE(kb[i]); }
                ub -= 64;
            };
#pragma unroll 1
            for (int it = 0; it < QIT; ++it) body();
            {
                TS(19);
                if (CPOST) { WAIT_V(0); sy_post<1>(pmine); WAIT_L(0); }
                if (CVLATE) sy_wait<1>(lb + L_SY + (CVLATE == 2 ? wm + (lane & 1) * 4 : lane & 7) * 4);
                TS(20);
            }
#pragma unroll 1
            for (int it = QIT; it < 4; ++it) body();
            WAIT_L(0);
            if (AMF & 1) asm volatile("s_nop 7\n\ts_nop 7" : "+v"(ca[0]), "+v"(ca[1]), "+v"(ca[2]), "+v"(ca[3]), "+v"(ca[4]), "+v"(ca[5]), "+v"(ca[6]), "+v"(ca[7]));
#pragma unroll
            for (int i = 0; i < 4; ++i) { TIE(FA[i][0]); TIE(FA[i][1]); }
#pragma unroll
            for (int i = 0; i < 8; ++i) TIE(FB[0][i]);
#undef CV_A
#undef CV_B
        }
        TS(7);
        __builtin_amdgcn_s_setprio(3);
        constexpr int NBAR = __builtin_popcount(BARM & ~VBM);
        unsigned zpub = (xcd * 32 + l31) * 256 + idx * 8, zpoll = ZOWN ? (xcd * 32 + idx) * 256 + l31 * 8 : fpoll;
        {
            const float Dd = bf2f(dskl);
            const char* zd = ws + WS_ZT + (size_t)(xcd * 128 + wm * 32) * 4096 + idx * 128;
            u32x2 kvr[8];
#pragma unroll
            for (int jt = 0; jt < 8; ++jt) kvr[jt] = *LDSP(u32x2, cb + UOFF + (g4 >> 1) * UPL + (16 + 16 * jt + r) * 16 + (1 - (g4 & 1)) * 8);
#pragma unroll
            for (int jt = 0; jt < 8; ++jt) {
                const f32x4 y = ca[jt];
                const u32x2 kq = kvr[jt];
                *LDSP(u32x2, cb + (jt * 4 + (r >> 2)) * 144 + (r & 3) * 32 + g4 * 8) =
                    u32x2{pack2(rb1(y[0]) + rb1(bfhi(kq[1]) * Dd), rb1(y[1]) + rb1(bflo(kq[1]) * Dd)), pack2(rb1(y[2]) + rb1(bfhi(kq[0]) * Dd), rb1(y[3]) + rb1(bflo(kq[0]) * Dd))};
            }
            unsigned za = lb + (r >> 2) * CHSZ + (wm * 8 + (g4 >> 1)) * 144 + ((g4 & 1) * 2 + ((r >> 1) & 1)) * 32 + (r & 1) * 8, zo = (lane >> 3) * 4096 + (lane & 7) * 16;
            TIE(za); TIE(zo); TIE(zpub); TIE(zpoll);
            sy_post<3>(pmine); sy_wait<3, 0>(pall);
            v4i16 t[4][2];
#pragma unroll
            for (int it = 0; it < 4; ++it)
#pragma unroll
                for (int e = 0; e < 2; ++e) t[it][e] = __builtin_amdgcn_ds_read_tr16_b64_v4i16(LDSP(v4i16, za + it * 288 + e * 16));
#pragma unroll
            for (int it = 0; it < 4; ++it) { TIE(t[it][0]); TIE(t[it][1]); }
            if (ZSB) BAR();
#pragma unroll
            for (int it = 0; it < 4; ++it) { const bf16x8 f = __builtin_shufflevector(t[it][0], t[it][1], 0, 1, 2, 3, 4, 5, 6, 7); GST_B128_SC1(zo, f, zd + it * 32768, 0); }
        }
#pragma unroll
        for (int i = ZSB; i < ZFB; ++i) BAR();
        WAIT_V(0);
        sy_post<2>(pmine);
        if (wm == 0) {
            sy_wait<2>(pall);
            if (ZOWN) { if (lane < 32) GST_B64_SC1(zpub, tag, flg + FL_Z); }
            else if (lane == 0) GST_B64_SC1(fpub, tag, flg + FL_Z);
        }
        TS(8);
        unsigned qm[8][4];
        if (QM) {
#pragma unroll
            for (int n = 0; n < 8; ++n) {
                TIE(qv[n]);
                qm[n][0] = qv[n][0] & 0xffffu; qm[n][1] = qv[n][0] & 0xffff0000u; qm[n][2] = qv[n][1] & 0xffffu; qm[n][3] = qv[n][1] & 0xffff0000u;
                TIE(qm[n][0]); TIE(qm[n][1]); TIE(qm[n][2]); TIE(qm[n][3]);
            }
        }
#pragma unroll
        for (int i = ZFB; i < NBAR - 1; ++i) { BAR();
        }
        if (ZSL) __builtin_amdgcn_s_sleep(ZSL);
        asm volatile("s_cmp_lg_u32 %3, 0\n\ts_cbranch_scc1 1f\n\tglobal_load_dwordx2 %0, %1, %2 sc1\n1:" : "=&v"(fl) : "v"(zpoll), "s"(flg + FL_Z), "s"(wm) : "scc", "memory");
        if (NBAR > ZFB) BAR();
        if (wm == 0) {
            unsigned left = SPIN_CAP;
            asm volatile("s_branch 2f\n1:\n\ts_sleep %5\n\tglobal_load_dwordx2 %0, %2, %3 sc1\n2:\n\ts_waitcnt vmcnt(0)\n\tv_cmp_ne_u64 vcc, %4, %0\n\ts_cbranch_vccz 3f\n\ts_sub_u32 %1, %1, 1\n\ts_cbranch_scc0 1b\n3:"
                         : "+v"(fl), "+s"(left) : "v"(zpoll), "s"(flg + FL_Z), "s"(tag), "n"(ZPS) : "vcc", "scc", "memory");
            sy_post<4>(pmine);
        } else sy_wait<4, 2>(lb + L_SY + 32);
        u32x4 zv[4];
        {
            ITEMS(18);
            const char* zsrc = ws + WS_ZT + (size_t)(xcd * 128 + idx * 4 + wm) * 4096;
            static_for<0, 4>([&](auto ic) { constexpr int it = decltype(ic)::value; GLD_B128_SC1(zv[it], (l31 >> 2) * 512 + lh * 64 + (lane & 3) * 16, zsrc, it * 128); });
        }
        WAIT_V(0);
        TS(9);
#pragma unroll
        for (int it = 0; it < 4; ++it) TIE(zv[it]);
        if (!(XAB & 1)) {
        quad_swap<0>(zv[0], zv[1]); quad_swap<0>(zv[2], zv[3]);
        quad_swap<1>(zv[0], zv[2]); quad_swap<1>(zv[1], zv[3]); }
#pragma unroll
        for (int it = 0; it < 4; ++it) TIE(zv[it]);
        char* const oa = a.out + (size_t)(idx * 64 + wm * 16) * 8192 + xcd * 512;
#pragma unroll
        for (int n = 0; n < 8; ++n) {
            if (!QM) TIE(qv[n]);
            const unsigned z0 = zv[n & 3][2 * (n >> 2)], z1 = zv[n & 3][2 * (n >> 2) + 1];
            const f32x4 o = (XAB & 2) ? __builtin_bit_cast(f32x4, zv[n & 3]) : QM ? mul4bf(u32x2{z0, z1}, qm[n]) :
                f32x4{rb1(bflo(qv[n][0]) * bflo(z0)), rb1(bfhi(qv[n][0]) * bfhi(z0)), rb1(bflo(qv[n][1]) * bflo(z1)), rb1(bfhi(qv[n][1]) * bfhi(z1))};
            if (n == 0 && OSG) for (int i = 0; i < (idx & 3); ++i) __builtin_amdgcn_s_sleep(OSG);
            if (!(XAB & 4))
            GST_B128_NT(lh * 32768 + l31 * 16, o, oa + ((n >> 2) * 8 + (n & 3)) * 8192, 0);
            if (OSL) __builtin_amdgcn_s_sleep(OSL);
        }
        asm volatile(".Lfk_code_a:");
        TSE(10);
        TS_FLUSH();
        return;
    }

    u32x2 fl;
    const unsigned* dv = kvv;
#define G1_ISSUE(st, i) do { if constexpr ((st) < DFN) { if constexpr ((i) > 0) dmaof_<(i) * 1024>(dv[i], g1src + ((((st) << 7) + rot7) & 1920)); else dmaf_(g1dst + g1_slot(st), dv[i], g1src + ((((st) << 7) + rot7) & 1920)); } else if constexpr (M1 && (i) > 0) dmao_<(i) * 1024>(dv[i], g1src + ((((st) << 7) + rot7) & 1920)); \
        else DMA(g1dst + g1_slot(st) + (i) * 1024, dv[i], g1src + ((((st) << 7) + rot7) & 1920)); } while (0)
    unsigned k0 = trow + wm * 32768 + g4 * 2048 + ((l15 ^ (2 * g4)) << 4), k1 = k0 + 7168, k2 = (k0 ^ 128) + 14336, k3 = k2 + 7168;
    auto pre = [&](auto ic) {
        constexpr int n = decltype(ic)::value;
        if constexpr (n == KVP) { if (!HKV) kv_dma(g1dst + L_KS, k0, k1, k2, k3, wn ? a.v : a.k); }
        else static_for<0, 4>([&](auto jc) { G1_ISSUE(n - (n > KVP), decltype(jc)::value); });
    };
    static_for<1, AP0 / 4>(pre);
    TS(14);
    SLICE_TABLE();
    const int j0 = 4 * run - (32 - 2 * tm);
    const int pneg = EALL ? 0 : 16 * (wm >> 1) + 15 - (32 - 2 * tm);
    unsigned eo[2], pes, plr, est, hw[2], ta[2], po, pmine, pall, pown, pzero, g2v[2], cad[2][2], to[2][2], xo, dso, fst, fld, cwa;
    unsigned cta = lb + L_TS + st * 16, ctb = lb + L_TR + run * 1024 + st * 16;
    TIE(cta); TIE(ctb);
    f32x4 ct[5]; f32x2 V1;
    const int gm = PAIR ? wid >> 1 : wm, gk = PAIR ? wid & 1 : wn, gs = gk * 4 + (gm & 2);
    auto addrs = [&]() {
        int ln = lane;
        TIE(ln);
        const int l15 = ln & 15, g4 = ln >> 4, st = sq * 16 + l15, run = (wm >> 1) * 4 + g4, j0 = 4 * run - (32 - 2 * tm);
        pes = lb + L_ES + wid * 256 + l15 * 16;
        plr = lb + L_AGG + st * 8 + run * 512; est = ((tm * 2 + (wm >> 1)) * 1024 + tn * 64 + st) * 16;
#pragma unroll
        for (int i = 0; i < 2; ++i) { eo[i] = (j0 + 2 * i < 0 ? 0 : j0 + 2 * i) * 16384 + (tn * 64 + st) * 16; TIE(eo[i]); }
        TIE(plr); TIE(pes); TIE(est);
        const unsigned ht = lb + L_HT + wid * 4096;
#pragma unroll
        for (int j = 0; j < 2; ++j) {
            const int pr = (8 * (g4 & 1) + 4 * j + (l15 >> 2)) ^ ((g4 & 1) * 4), pw = l15 ^ ((l15 >> 3) * 4);
            ta[j] = ht + (g4 >> 1) * 512 + pr * 32 + ((((l15 >> 1) ^ (pr >> 2)) & 1) << 4) + 8 * (ln & 1);
            hw[j] = ht + g4 * 1024 + pw * 32 + (((j ^ (pw >> 2)) & 1) << 4);
            TIE(ta[j]); TIE(hw[j]);
        }
        po = pes + ((wm & 2) ? -512 : 512);
        pmine = lb + L_SY + wid * 4; pall = lb + L_SY + (ln & 7) * 4; pown = lb + L_SY + (ln < 4 ? 32 + ln * 4 : gs * 4 + (ln & 4)); pzero = lb + L_SY;
        xo = (tm * 128 + gm * 32 + gk * 16 + g4 * 4) * 2048 + tn * 128 + l15 * 8 + 4096; dso = xo & 2047;
        fst = (tm * 16 + l15) * 128 + tn * 8; fld = (tm * 16 + tn) * 128 + l15 * 8;
#pragma unroll
        for (int i = 0; i < 2; ++i) {
            const int pair = i * 4 + g4, v = l15 ^ pair ^ ((wm & 1) * 8);
            g2v[i] = (pair * 2 + (v >> 3)) * 8192 + (v & 7) * 16 + (tn * 64 + wm) * 2048 - i * 1024;
            TIE(g2v[i]);
#pragma unroll
            for (int j = 0; j < 2; ++j) {
                cad[i][j] = lb + L_C + (img_off((g4 >> 1) * 64 + l15, 4 * gk + (g4 & 1)) ^ (i * 32) ^ (j * 128));
                to[i][j] = ta[j] + (i ^ gk) * 1024 + (gm & 1) * 2048 + (gs - wid) * 4096;
                TIE(cad[i][j]); TIE(to[i][j]);
            }
        }
        cwa = lb + wid * 2048 + ln * 16;
        TIE(cwa); TIE(po);
        TIE(pmine); TIE(pall); TIE(pown); TIE(pzero); TIE(xo); TIE(dso); TIE(fst); TIE(fld);
    };
    if (!APRE) addrs();
    unsigned aad[2][2], bad[2];
    {
        const int aoff = img_off((wm >> 1) * 64 + l15, g4), boff = img_off(128 + wn * 64 + (wm & 1) * 16 + l15, g4);
#pragma unroll
        for (int ks = 0; ks < 2; ++ks) { aad[ks][0] = lb + (aoff ^ (ks * 64)); aad[ks][1] = lb + (aoff ^ (ks * 64) ^ 128); bad[ks] = lb + (boff ^ (ks * 64)); TIE(aad[ks][0]); TIE(aad[ks][1]); TIE(bad[ks]); }
    }
    if (!HKV && KVP >= AP0 / 4) { TIE(k0); TIE(k1); TIE(k2); TIE(k3); }
    static_for<AP0 / 4, NBK + 1>(pre);
    TS(1);
    float nL1i, nC1r, nC1i;
    unsigned tagA, tagB;
    f32x4 acc[4][2];
    {
#pragma unroll
        for (int i = 0; i < 4; ++i) { acc[i][0] = f32x4{0, 0, 0, 0}; acc[i][1] = f32x4{0, 0, 0, 0}; }
        bf16x8 fa[2][4], fb[2][2];
        if (GPRIO) __builtin_amdgcn_s_setprio(GPRIO);
        WAIT_V(g1_cnt(0)); BAR();
        TS(2);
#define G1_FRAG(g, n) do { constexpr int bo_ = g1_slot((g) >> 1), hi_ = bo_ & ~65535, x_ = (n) < 3 ? (n) >> 1 : (n) - 2; \
        if constexpr ((n) == 0 || (n) == 2) DSR128(fb[(g) & 1][x_], bad[(g) & 1] + hi_, bo_ - hi_ + x_ * 4096); \
        else DSR128(fa[(g) & 1][x_], aad[(g) & 1][x_ & 1] + hi_, bo_ - hi_ + x_ * 2048); } while (0)
        static_for<0, 6>([&](auto ic) { constexpr int n = decltype(ic)::value; G1_FRAG(0, n); });
        static_for<0, 32>([&](auto ic) {
            constexpr int g = decltype(ic)::value, gn = g + 1, n0 = g1_upto(g - 1), nd = g1_upto(g) - n0;
            constexpr bool bar = (gn & 1) == 0 && gn < 32;
            if constexpr (g == 1) {
                DSR128(ct[0], cta, 0); DSR128(ct[1], cta, 1024); DSR128(ct[2], cta, 2048); DSR128(ct[3], ctb, 0); DSR128(ct[4], ctb, 8192); DSR64(V1, ctb, 16384);
            }
            if constexpr (g == 3) {
#pragma unroll
                for (int i = 0; i < 5; ++i) TIE(ct[i]);
                TIE(V1);
                nL1i = -ct[0][1]; nC1r = -ct[0][2]; nC1i = -ct[0][3];
                TIE(nL1i); TIE(nC1r); TIE(nC1i);
                tagA = __builtin_amdgcn_readfirstlane(__float_as_uint(ct[2][2])); tagB = __builtin_amdgcn_readfirstlane(__float_as_uint(ct[2][3]));
                ties_(tagA); ties_(tagB);
            }
            if constexpr (bar) {
                static_assert(g1_cnt(gn >> 1) >= 0, "schedule");
                WAIT_VB(g1_cnt(gn >> 1)); WAIT_LB(0); BAR();
                if constexpr (((gn >> 1) & 3) == 0 || gn >= 26) TS(gn >= 26 ? 23 + (gn >> 1) - 13 : 16 + (gn >> 3));
#pragma unroll
                for (int i = 0; i < 4; ++i) if (!(AMF & 2)) TIE(fa[g & 1][i]);
                if (!(AMF & 2)) { TIE(fb[g & 1][0]); TIE(fb[g & 1][1]); }
            }
            static_for<0, 8>([&](auto mc) {
                constexpr int m = decltype(mc)::value, i = m >> 1, j = m & 1;
                if constexpr (W1 && !bar && g > 0 && g < 31) {
                    if constexpr (m == 0) WAIT_LB(W1 == 1 ? 3 : 2);
                    if constexpr (m == 2 && W1 == 1) WAIT_LB(4);
                    if constexpr (m == 4) WAIT_LB(4);
                } else
                if constexpr (!bar && (i == 0 || j == 0)) {
                    WAIT_LB(5 - (i == 0 ? j + 1 : i + 2) + (gn < 32 ? (m < 6 ? m : 6) : 0));
                    if constexpr (!(AMF & 2) || g == 0) { TIE(fa[g & 1][i]); TIE(fb[g & 1][j]); }
                }
                if constexpr ((AMF & 2) && g > 0) mfma_(acc[i][j], fa[g & 1][i], fb[g & 1][j]); else
                acc[i][j] = __builtin_amdgcn_mfma_f32_16x16x32_bf16(fa[g & 1][i], fb[g & 1][j], acc[i][j], 0, 0, 0);
                SB();
                if constexpr (gn < 32 && m < 6) G1_FRAG(gn, m);
                constexpr int d = nd > 2 ? ((m & 1) ? m >> 1 : -1) : (m == DP0 ? 0 : m == DP1 ? 1 : -1);
                if constexpr (d >= 0 && d < nd) G1_ISSUE((n0 + d) >> 2, (n0 + d) & 3);
                SB();
            });
        });
#undef G1_FRAG
    }
    TS(3);
    const unsigned tagG = tagA ^ tagB ^ 0x9E3779B9u;
    __builtin_amdgcn_s_setprio(MPRIO);
    f32x2 ur[8], ui[8], hr, hi;
    const f32x2 L1 = {ct[0][0], ct[0][1]}, C1 = {ct[0][2], ct[0][3]}, L16 = {ct[1][2], ct[1][3]}, Q2r = {ct[3][0], ct[3][1]}, Q2i = {ct[3][2], ct[3][3]};
    const float L8r = ct[1][0], L8i = ct[1][1], L64r = ct[2][0], L64i = ct[2][1];
    u32x4 eg[4], cr[2 * NR + 1];
    const char* g2src = wn ? a.cim : a.cre;
#define CR_LOAD(n0, n1) static_for<n0, n1>([&](auto ic) { constexpr int n = decltype(ic)::value; GLD_B128(cr[n], g2v[n & 1], g2src + (SLICE(7 + (n >> 1)) << 7), (n & 1) * 1024); })
#define E_LOADS "global_load_dwordx4 v[104:107], %[a0], %[b0] sc1\n\tglobal_load_dwordx4 v[108:111], %[a0], %[b1] sc1\n\t" \
                "global_load_dwordx4 v[112:115], %[a1], %[b0] sc1\n\tglobal_load_dwordx4 v[116:119], %[a1], %[b1] sc1\n"
#define E_POLL() asm volatile("s_cmp_lt_i32 %[on], 0\n\ts_cbranch_scc1 1f\n\t" E_LOADS "1:" : "={v[104:107]}"(eg[0]), "={v[108:111]}"(eg[1]), "={v[112:115]}"(eg[2]), "={v[116:119]}"(eg[3]) \
        : [a0] "v"(eo[0]), [a1] "v"(eo[1]), [b0] "s"(ws + WS_E), [b1] "s"(ws + WS_E + 16384), [on] "s"(pneg) : "scc", "memory")
    {
        asm volatile("s_nop 7\n\ts_nop 3" : "+v"(acc[0][0]), "+v"(acc[0][1]), "+v"(acc[1][0]), "+v"(acc[1][1]), "+v"(acc[2][0]), "+v"(acc[2][1]), "+v"(acc[3][0]), "+v"(acc[3][1]));
#pragma unroll
        for (int p = 0; p < 8; ++p) { ur[p] = f32x2{acc[p >> 1][0][2 * (p & 1)], acc[p >> 1][0][2 * (p & 1) + 1]}; ui[p] = f32x2{acc[p >> 1][1][2 * (p & 1)], acc[p >> 1][1][2 * (p & 1) + 1]}; }
        f32x2 gr = ur[0], gi = ui[0];
#pragma unroll
        for (int p = 1; p < 8; ++p) { f32x2 nr, ni; if (PLN & 1) c2_mad(nr, ni, L1[0], nL1i, L1[0], L1[1], gr, gi, ur[p], ui[p]); else pk_cmad(nr, ni, L1, gr, gi, ur[p], ui[p]); gr = nr; gi = ni; }
        float fr, fi;
        cmad(fr, fi, L8r, L8i, gr[0], gi[0], gr[1], gi[1]);
        {
            const f32x2 w = cmul4(V1, f32x2{fr, fi});
            const float wr = sum4(w[0]), wi = sum4(w[1]);
            const u32x4 gq = {__float_as_uint(wr), tagA, __float_as_uint(wi), tagG};
            if (EREP ? (EALL || g4 >= (tm >> 2)) : g4 == 0) GST_B128_SC1(est, gq, ws + WS_E, 0);
        }
        *LDSP(f32x2, plr + 7 * 512) = f32x2{fr, fi};
        *LDSP(unsigned, pes + 8) = 0;
        TS(21);
        if (EP == 2) E_POLL();
        auto coef = [&]() {
#pragma unroll
            for (int p = 0; p < 8; ++p) { f32x2 nr, ni; if (PLN & 2) c2_mul(nr, ni, C1[0], nC1i, nC1r, nC1i, ur[p], ui[p]); else pk_conj_mul(nr, ni, C1, ur[p], ui[p]); ur[p] = nr; ui[p] = ni; TIE(ur[p]); TIE(ui[p]); }
        };
        if (CFB) coef();
        WAIT_L(0); BAR();
        f32x2 e[7];
#pragma unroll
        for (int j = 0; j < 7; ++j) e[j] = *LDSP(f32x2, plr + j * 512);
        if (EP == 1) E_POLL();
        CR_LOAD(0, CR1);
        if (!CFB) coef();
#pragma unroll
        for (int j = 1; j < 7; ++j) { if (PLN & 4) { float xr = e[j][0], xi = e[j][1]; cmac(xr, xi, L16[0], L16[1], e[j - 1][0], e[j - 1][1]); e[j] = f32x2{xr, xi}; } else cmac2(e[j], L16, e[j - 1]); }
        float mr, mi;
        cmad(mr, mi, L8r, L8i, e[6][0], e[6][1], gr[0], gi[0]);
        if (PLN & 4) c2_mul(hr, hi, C1[0], nC1i, nC1r, nC1i, f32x2{e[6][0], mr}, f32x2{e[6][1], mi}); else pk_conj_mul(hr, hi, C1, f32x2{e[6][0], mr}, f32x2{e[6][1], mi});
        TIE(hr); TIE(hi);
        CR_LOAD(CR1, CR1 + CR2);
        TS(4);
    }
    f32x2 G;
    {
        float pr = 0.f, pi = 0.f;
        if (!EP) E_POLL();
        if (pneg >= 0) {
            unsigned t0, t1, t2, left = SPIN_CAP;
            asm volatile("s_waitcnt vmcnt(%[c])\n\ts_branch 2f\n1:\n\ts_sleep %[sl]\n\t" E_LOADS "\ts_waitcnt vmcnt(0)\n2:\n\t"
                         "v_bitop3_b32 %[t0], v105, v109, %[ta] bitop3:0x7e\n\tv_bitop3_b32 %[t1], v113, v117, %[ta] bitop3:0x7e\n\tv_bitop3_b32 %[t2], v107, v111, %[tg] bitop3:0x7e\n\t"
                         "v_or3_b32 %[t0], %[t0], %[t1], %[t2]\n\tv_bitop3_b32 %[t1], v115, v119, %[tg] bitop3:0x7e\n\tv_or_b32 %[t0], %[t0], %[t1]\n\t"
                         "v_cmp_ne_u32 vcc, 0, %[t0]\n\ts_cbranch_vccz 3f\n\ts_sub_u32 %[n], %[n], 1\n\ts_cbranch_scc0 1b\n3:"
                         : "+{v[104:107]}"(eg[0]), "+{v[108:111]}"(eg[1]), "+{v[112:115]}"(eg[2]), "+{v[116:119]}"(eg[3]), [t0] "=&v"(t0), [t1] "=&v"(t1), [t2] "=&v"(t2), [n] "+s"(left)
                         : [a0] "v"(eo[0]), [a1] "v"(eo[1]), [b0] "s"(ws + WS_E), [b1] "s"(ws + WS_E + 16384), [ta] "s"(tagA), [tg] "s"(tagG), [sl] "n"(ESL), [c] "n"(CR1 + CR2) : "vcc", "scc", "memory");
            float ar = __uint_as_float(eg[1][0]), ai = __uint_as_float(eg[1][2]), br = __uint_as_float(eg[3][0]), bi = __uint_as_float(eg[3][2]);
            cmac(ar, ai, L64r, L64i, __uint_as_float(eg[0][0]), __uint_as_float(eg[0][2]));
            cmac(br, bi, L64r, L64i, __uint_as_float(eg[2][0]), __uint_as_float(eg[2][2]));
            if (EALL) { if (j0 < 0) ar = ai = 0.f; if (j0 + 2 < 0) br = bi = 0.f; }
            const f32x2 pu = cmul4(f32x2{ct[4][2], ct[4][3]}, f32x2{ar, ai});
            pr = pu[0]; pi = pu[1];
            cmac(pr, pi, ct[4][0], ct[4][1], br, bi);
            pr = sum4(pr); pi = sum4(pi);
        }
        TS(5);
        *LDSP(u32x4, pes) = u32x4{__float_as_uint(pr), __float_as_uint(pi), 1u, 0u};
        CR_LOAD(CR1 + CR2, CR1 + CR2 + CR3);
        u32x4 o;
        asm volatile("1:\n\tds_read_b128 %0, %1\n\ts_waitcnt lgkmcnt(0)\n\tv_cmp_eq_u32 vcc, 0, v122\n\ts_cbranch_vccnz 1b" : "=&{v[120:123]}"(o) : "v"(po) : "vcc", "memory");
        G = f32x2{pr + __uint_as_float(o[0]), pi + __uint_as_float(o[1])};
    }
    TS(6);
    {
        if (PLN & 8) c2_mac(hr, hi, G[0], -G[1], -G[0], -G[1], Q2r, Q2i); else pk_conj_mac(hr, hi, G, Q2r, Q2i);
#pragma unroll
        for (int p = 0; p < 8; ++p) { if (PLN & 8) c2_mac(ur[p], ui[p], L1[0], L1[1], L1[0], nL1i, hr, hi); else pk_cmac_conj(ur[p], ui[p], L1, hr, hi); hr = ur[p]; hi = ui[p]; }
        const char* hd = ws + WS_H + (size_t)((tm * 4 + (wm & 2)) * 2 + wn) * HP + (tn * 4 + (wm & 1) * 2) * 1024;
#pragma unroll
        for (int e2 = 0; e2 < 2; ++e2)
#pragma unroll
            for (int c = 0; c < 2; ++c) {
                const f32x2* o = c ? ui : ur;
                *LDSP(u32x4, hw[e2] + c * 512) = u32x4{pack2(o[0][e2], o[1][e2]), pack2(o[2][e2], o[3][e2]), pack2(o[4][e2], o[5][e2]), pack2(o[6][e2], o[7][e2])};
            }
        v4i16 t[4][2];
#pragma unroll
        for (int n = 0; n < 4; ++n)
#pragma unroll
            for (int j = 0; j < 2; ++j) t[n][j] = __builtin_amdgcn_ds_read_tr16_b64_v4i16(LDSP(v4i16, ta[j] + n * 1024));
        sy_post<1>(pmine);
        static_for<0, 4>([&](auto ic) {
            constexpr int n = decltype(ic)::value;
            const bf16x8 f = __builtin_shufflevector(t[n][0], t[n][1], 0, 1, 2, 3, 4, 5, 6, 7);
            GST_B128_SC1(l16, f, hd + (n >> 1) * 2 * HP + ((n & 1) ^ wn) * 1024, 0);
        });
    }
    TS(7);
    bf16x8 fa2[64];
    u32x2 xv[4], dsv;
    const char* Aw = ws + WS_H + (size_t)((tm * 4 + gm) * 2 + gk) * HP;
    const unsigned g2dst = lb + wid * 2048;
#define G2_ISSUE(st, i) do { if constexpr (i) dmao_<1024>(g2v[1], g2src + (SLICE(st) << 7)); else DMA(g2dst + g2_place(st), g2v[0], g2src + (SLICE(st) << 7)); } while (0)
#define G2_A(g) GLD_B128_SC1(fa2[g], l16, Aw + (SLICE((g) >> 2) << 12), ((g) & 3) * 1024)
    {
        {
            sy_wait<1>(pown);
            s16x4 fo[4][2]; bf16x8 c[8];
            static_for<0, 2>([&](auto kc) {
                constexpr int ks = decltype(kc)::value;
                static_for<0, 4>([&](auto ic) { constexpr int i = decltype(ic)::value; dsrtr_<ks * 4096>(fo[2 * ks + (i >> 1)][i & 1], to[i >> 1][i & 1]); });
                static_for<0, 4>([&](auto ic) { constexpr int i = decltype(ic)::value; DSR128(c[4 * ks + i], cad[ks][i & 1], 2 * G2STAGE + i * 2048); });
            });
            static_for<0, 8>([&](auto ic) {
                constexpr int n = decltype(ic)::value, ks = n >> 2, i = n & 3;
                WAIT_L(ks ? 3 - i : 11 - i);
                TIE(c[n]);
                if constexpr (i == 0) { TIE(fo[2 * ks][0]); TIE(fo[2 * ks][1]); TIE(fo[2 * ks + 1][0]); TIE(fo[2 * ks + 1][1]); }
#pragma unroll
                for (int j = 0; j < 2; ++j)
                    acc[i][j] = __builtin_amdgcn_mfma_f32_16x16x32_bf16(__builtin_shufflevector(fo[2 * ks + j][0], fo[2 * ks + j][1], 0, 1, 2, 3, 4, 5, 6, 7), c[n], ks ? acc[i][j] : f32x4{0, 0, 0, 0}, 0, 0, 0);
                SB();
            });
#pragma unroll
            for (int i = 0; i < 4; ++i) { TIE(acc[i][0]); TIE(acc[i][1]); }
        }
        if (CEARLY) sy_post<0>(pmine);
        static_for<0, 4>([&](auto ic) { constexpr int it = decltype(ic)::value; GLD_B64(xv[it], xo, a.x, it * 2048 - 4096); });
        GLD_B64(dsv, dso, a.dskip, 0);
        if (CEARLY) {
            sy_wait<0>(pall);
            static_for<4, 4 + CEARLY>([&](auto ic) { constexpr int n = decltype(ic)::value; G2_ISSUE(n >> 1, n & 1); });
        }
        TS(16);
        WAIT_V(5 + CEARLY);
        sy_post<2>(pmine);
        if (!CEARLY || wid == (NOOWN ? 1 : 0) || (NOOWN == 1 && wid == 0)) sy_wait<2, 0>(pall);
        const u32x2 tagv = {__float_as_uint(ct[2][2]), __float_as_uint(ct[2][3])};
        if (wid == (NOOWN ? 1 : 0)) asm volatile("s_mov_b64 exec, 0xffff\n\tglobal_store_dwordx2 %0, %1, %2 sc1\n\ts_mov_b64 exec, -1" :: "v"(fst), "v"(tagv), "s"(flg + FL_H) : "memory");
        if (PFIRST) asm volatile("s_cmp_lg_u32 %3, 0\n\ts_cbranch_scc1 1f\n\tglobal_load_dwordx2 %0, %1, %2 sc1\n1:" : "=&v"(fl) : "v"(fld), "s"(flg + FL_H), "s"(wid) : "scc", "memory");
        TS(8);
        static_for<4 + CEARLY, 10>([&](auto ic) { constexpr int n = decltype(ic)::value; G2_ISSUE(n >> 1, n & 1); });
        CR_LOAD(2 * NR - CRL, 2 * NR);
        TS(13);
        if (wid == 0) {
            unsigned left = SPIN_CAP;
            if (PFIRST)
            asm volatile("s_waitcnt vmcnt(6)\n\ts_branch 3f\n1:\n\tglobal_load_dwordx2 %0, %2, %3 sc1\n\ts_waitcnt vmcnt(0)\n3:\n\tv_cmp_ne_u64 vcc, %0, %4\n\ts_cbranch_vccz 2f\n\ts_sub_u32 %1, %1, 1\n\ts_cbranch_scc0 1b\n2:"
                         : "+v"(fl), "+s"(left) : "v"(fld), "s"(flg + FL_H), "v"(tagv) : "vcc", "scc", "memory");
            else
            asm volatile("1:\n\tglobal_load_dwordx2 %0, %2, %3 sc1\n\ts_waitcnt vmcnt(0)\n\tv_cmp_ne_u64 vcc, %0, %4\n\ts_cbranch_vccz 2f\n\ts_sub_u32 %1, %1, 1\n\ts_cbranch_scc0 1b\n2:"
                         : "=&v"(fl), "+s"(left) : "v"(fld), "s"(flg + FL_H), "v"(tagv) : "vcc", "scc", "memory");
            sy_post<3>(pmine);
            if (b == 255 && lane == 0) GST_B64_SC1(0u * lane, tagv, flg + FL_EPOCH);
        } else if (g2_bar(3)) sy_wait<3>(pzero);
        else { WAIT_V(CRL); sy_post<3>(pmine); }
        if (!g2_bar(3)) sy_wait<3>(pall);
        TS(20);
        static_for<4, 4 + AW>([&](auto ic) { constexpr int g = decltype(ic)::value; G2_A(g); });
    }
    TS(9);
    __builtin_amdgcn_s_setprio(0);
    unsigned yw, yr, yp, yo, lzw;
    static_assert(PAIR, "yr");
    unsigned dm[4];
    auto dmask = [&]() { TIE(dsv); dm[0] = dsv[0] & 0xffffu; dm[1] = dsv[0] & 0xffff0000u; dm[2] = dsv[1] & 0xffffu; dm[3] = dsv[1] & 0xffff0000u; TIE(dm[0]); TIE(dm[1]); TIE(dm[2]); TIE(dm[3]); };
    auto skip = [&](int it) { TIE(xv[it]); const f32x4 y = mul4bf(xv[it], dm); for (int i = 0; i < 4; ++i) acc[i][0][it] = add1(acc[i][0][it], y[i]); if (it & 1) { for (int i = 0; i < 4; ++i) TIE(acc[i][0]); } };
    {
        bf16x8 fb[2][4];
#define G2_FRAG(g, i) do { constexpr int pl_ = g2_place((g) >> 1), b_ = pl_ >= L_C ? L_C : pl_ >= 32768 ? 32768 : 0; DSR128(fb[(g) & 1][i], cad[(g) & 1][(i) & 1] + (b_ - L_C), pl_ - b_ + (i) * 2048); } while (0)
        static_for<0, 4>([&](auto ic) { constexpr int i = decltype(ic)::value; G2_FRAG(2, i); });
        if (YS == 0) { WAIT_V(AW + 6 + CRL); dmask(); skip(0); skip(1); skip(2); skip(3); }
        static_for<2, 32>([&](auto ic) {
            constexpr int g = decltype(ic)::value, gn = g + 1;
            constexpr bool bar = g2_bar(g) && !((VBM >> g) & 1);
            constexpr int n0 = g2_upto(g - 1), n1 = g2_upto(g);
            if constexpr (bar) {
                constexpr int ca = g2_cnt(2 * g, 2 * g), cs = g2_cover(g) >= 7 + NR && !(LZ && g == LASTB) ? g2_cnt(100 + 2 * g2_cover(g) + 1, 2 * g) : 63;
                static_assert(cs >= 0 && g2_wr(g - 1) >= 2 * (g2_cover(g) < 6 + NR ? g2_cover(g) - 6 : NR), "schedule");
                WAIT_VB(AWB && ca < cs ? ca : cs); WAIT_LB(0); BAR();
                if constexpr (!AWB) WAIT_V(ca);
#pragma unroll
                for (int i = 0; i < 4; ++i) if (!(AMF & 4)) TIE(fb[g & 1][i]);
                if (!(AMF & 4)) TIE(fa2[2 * g]);
            }
            if constexpr (LZ && g == lz_chk()) { WAIT_LB(0); sy_chk<0>(lzw, pall); }
            constexpr int xl = g2_wr(g - 1) - g2_wr(g - 2) + (g - 1 == EGX) + (LZ && g - 1 == lz_post());
            static_for<0, 8>([&](auto mc) {
                constexpr int m = decltype(mc)::value, i = m & 3, j = m >> 2, f = 2 * g + j;
                static_assert(g2_cnt(f, f) >= 0, "schedule");
                constexpr int jj = LSW && g == 31 ? 1 - j : j, ff = 2 * g + jj;
                if constexpr (i == 0 && (j || !bar)) { WAIT_VB(LSW && g == 31 ? 0 : g2_cnt(f, f)); if constexpr (!(AMF & 4) || g == 2 || g == 31) TIE(fa2[ff]); }
                if constexpr (W2 && g > 2 && g < 31) { if constexpr (!bar && j == 0 && (i & W2) == 0) WAIT_LB(3 - W2 + xl); } else
                if constexpr (!bar && j == 0) { WAIT_LB((gn < 32 ? 3 : 3 - i) + xl); if constexpr (!(AMF & 4) || g == 2 || g == 31) TIE(fb[g & 1][i]); }
                if constexpr ((AMF & 4) && g > 2 && g < 31) mfma_(acc[i][jj], fa2[ff], fb[g & 1][i]); else
                acc[i][jj] = __builtin_amdgcn_mfma_f32_16x16x32_bf16(fa2[ff], fb[g & 1][i], acc[i][jj], 0, 0, 0);
                SB();
                if constexpr (LSW && g == 31 && j) *LDSP(f32x4, yw + i * 1024) = acc[i][1];
                if constexpr (g == EGX && m == 4) sy_post<5>(pmine);
                if constexpr (LZ && g == lz_post() && m == 4) sy_post<0>(pmine);
                if constexpr (LZ && g + 1 == lz_chk() && m == 4) sy_pre<0>(lzw, pall);
                if constexpr (g == 29 && m == 5) { yw = cwa + L_Y + wid * 2048; yr = yw ^ (PAIR ? 4096 : 16384); yp = pmine ^ (PAIR ? 4 : 16); yo = (cwa & 255) + ((cwa & 768) << 7); TIE(yw); TIE(yr); TIE(yp); TIE(yo); }
                if constexpr (gn < 32 && m < 4) G2_FRAG(gn, m);
                if constexpr (m >= 4 && g2_wr(g - 1) + m - 4 < g2_wr(g)) {
                    constexpr int n = g2_wr(g - 1) + m - 4, pl = g2_place(7 + (n >> 1)), pb = pl >= L_C ? L_C : 0;
                    asm volatile("ds_write_b128 %0, %1 offset:%2" :: "v"(cwa + pb), "v"(cr[n]), "n"(pl - pb + (n & 1) * 1024) : "memory");
                }
                if constexpr (i == 1 && n0 + j < n1) G2_ISSUE((n0 + j) >> 1, (n0 + j) & 1);
                if constexpr (i >= 2 && g2_ald(f, i & 1) >= 0) G2_A(g2_ald(f, i & 1));
                SB();
            });
        });
#undef G2_FRAG
    }
    TS(11);
    if (EPRIO) __builtin_amdgcn_s_setprio(EPRIO);
    {
#pragma unroll
        for (int i = 0; i < 4; ++i) if (!LSW) *LDSP(f32x4, yw + i * 1024) = acc[i][1];
        sy_post<4>(pmine);
        sy_wait<4, 0>(yp);
        char* const ob = a.out + (size_t)(tm * 128 + gm * 32 + gk * 16) * 8192 + 4096 + tn * 256;
        f32x4 w[4];
#pragma unroll
        for (int i = 0; i < 4; ++i) w[i] = *LDSP(f32x4, yr + i * 1024);
        f32x4 o[4];
#pragma unroll
        for (int r = 0; r < 4; ++r) { o[r] = f32x4{add1(w[0][r], acc[0][0][r]), add1(w[1][r], acc[1][0][r]), add1(w[2][r], acc[2][0][r]), add1(w[3][r], acc[3][0][r])}; if (EGX) TIE(o[r]); }
        if (EGX && !wn) sy_wait<5, 1>(pall);
#pragma unroll
        for (int r = 0; r < 4; ++r) GST_B128_NT(yo, o[r], ob + r * 8192, 0);
    }
    asm volatile(".Lfk_code_b:");
    TSE(12);
    TS_FLUSH();
}

}

extern "C" void kernel_launch(void* const* d_in, const int* in_sizes, int n_in, void* d_out, int out_size, void* d_ws, size_t ws_size,
                              hipStream_t stream) {
    (void)in_sizes; (void)n_in; (void)out_size; (void)ws_size;
    fk::Args a;
    a.q = (const char*)d_in[0]; a.k = (const char*)d_in[1]; a.v = (const char*)d_in[2]; a.sk = (const char*)d_in[3];
    a.ssmk = (const char*)d_in[4]; a.ssmD = (const char*)d_in[5]; a.x = (const char*)d_in[6]; a.lre = (const char*)d_in[7];
    a.lim = (const char*)d_in[8]; a.bre = (const char*)d_in[9]; a.bim = (const char*)d_in[10]; a.cre = (const char*)d_in[11];
    a.cim = (const char*)d_in[12]; a.logdt = (const char*)d_in[13]; a.dskip = (const char*)d_in[14];
    a.out = (char*)d_out; a.ws = (char*)d_ws;
    hipLaunchKernelGGL(fk::fused, dim3(256), dim3(768), 0, stream, a);
}
```

```cpp
#include <hip/hip_runtime.h>
#include <stdint.h>
#include <type_traits>

#define ZM 0
#define RX 3
#define P19M 7
#define SNK 0
namespace fk {

constexpr int S = 2048, D = 1024, P = 1024;

typedef __attribute__((ext_vector_type(8))) short bf16x8;
typedef __attribute__((ext_vector_type(4))) short s16x4;
typedef __attribute__((ext_vector_type(16))) float f32x16;
typedef __attribute__((ext_vector_type(4))) float f32x4;
typedef __attribute__((ext_vector_type(2))) float f32x2;
typedef __attribute__((ext_vector_type(4))) unsigned u32x4;
typedef __attribute__((ext_vector_type(2))) unsigned u32x2;
#define LDSP(T, a) ((__attribute__((address_space(3))) T*)(uintptr_t)(unsigned)(a))

__device__ __forceinline__ float bf2f(unsigned v) { return __uint_as_float(v << 16); }
__device__ __forceinline__ float bflo(unsigned w) { return __uint_as_float(w << 16); }
__device__ __forceinline__ float bfhi(unsigned w) { return __uint_as_float(w & 0xffff0000u); }
__device__ __forceinline__ uint16_t f2bf(float f) { __bf16 b = (__bf16)f; return __builtin_bit_cast(uint16_t, b); }
__device__ __forceinline__ float rbf(float f) { return bf2f(f2bf(f)); }
typedef __attribute__((ext_vector_type(2))) __bf16 bf16x2;
__device__ __forceinline__ unsigned pack2(float lo, float hi) { return __builtin_bit_cast(unsigned, __builtin_convertvector(f32x2{lo, hi}, bf16x2)); }
__device__ __forceinline__ float rb1(float x) { return __uint_as_float(pack2(0.f, x)); }
__device__ __forceinline__ void cmac(float& xr, float& xi, float ar, float ai, float hr, float hi) {
    asm("v_fmac_f32 %0, %2, %4\n\tv_fmac_f32 %1, %2, %5\n\tv_fma_f32 %0, -%3, %5, %0\n\tv_fmac_f32 %1, %3, %4" : "+v"(xr), "+v"(xi) : "v"(ar), "v"(ai), "v"(hr), "v"(hi));
}
__device__ __forceinline__ void cmad(float& o_r, float& o_i, float ar, float ai, float hr, float hi, float er, float ei) {
    asm("v_fma_f32 %0, %2, %4, %6\n\tv_fma_f32 %1, %2, %5, %7\n\tv_fma_f32 %0, -%3, %5, %0\n\tv_fmac_f32 %1, %3, %4" : "=&v"(o_r), "=&v"(o_i) : "v"(ar), "v"(ai), "v"(hr), "v"(hi), "v"(er), "v"(ei));
}
__device__ __forceinline__ f32x2 cmul4(f32x2 a, f32x2 h) {
    float o_r, o_i;
    asm("v_mul_f32 %0, %2, %4\n\tv_mul_f32 %1, %2, %5\n\tv_fma_f32 %0, -%3, %5, %0\n\tv_fmac_f32 %1, %3, %4" : "=&v"(o_r), "=&v"(o_i) : "v"(a[0]), "v"(a[1]), "v"(h[0]), "v"(h[1]));
    return f32x2{o_r, o_i};
}

#define PK_LO " op_sel:[0,0,0] op_sel_hi:[0,1,1]"
#define PK_HI " op_sel:[1,0,0] op_sel_hi:[1,1,1]"
#define PK_NEG " neg_lo:[1,0,0] neg_hi:[1,0,0]"
__device__ __forceinline__ void pk_cmad(f32x2& o_r, f32x2& o_i, f32x2 a, f32x2 hr, f32x2 hi, f32x2 er, f32x2 ei) {
    asm("v_pk_fma_f32 %0, %2, %3, %5" PK_LO "\n\tv_pk_fma_f32 %1, %2, %4, %6" PK_LO "\n\tv_pk_fma_f32 %0, %2, %4, %0" PK_HI PK_NEG "\n\tv_pk_fma_f32 %1, %2, %3, %1" PK_HI
        : "=&v"(o_r), "=&v"(o_i) : "v"(a), "v"(hr), "v"(hi), "v"(er), "v"(ei));
}
__device__ __forceinline__ void pk_cmac_conj(f32x2& xr, f32x2& xi, f32x2 a, f32x2 hr, f32x2 hi) {
    asm("v_pk_fma_f32 %0, %2, %3, %0" PK_LO "\n\tv_pk_fma_f32 %1, %2, %4, %1" PK_LO "\n\tv_pk_fma_f32 %0, %2, %4, %0" PK_HI "\n\tv_pk_fma_f32 %1, %2, %3, %1" PK_HI PK_NEG
        : "+v"(xr), "+v"(xi) : "v"(a), "v"(hr), "v"(hi));
}
__device__ __forceinline__ void pk_conj_mul(f32x2& o_r, f32x2& o_i, f32x2 a, f32x2 hr, f32x2 hi) {
    asm("v_pk_mul_f32 %0, %2, %3 op_sel:[0,0] op_sel_hi:[0,1]\n\tv_pk_mul_f32 %1, %2, %4 op_sel:[0,0] op_sel_hi:[0,1] neg_lo:[1,0] neg_hi:[1,0]\n\t"
        "v_pk_fma_f32 %0, %2, %4, %0" PK_HI PK_NEG "\n\tv_pk_fma_f32 %1, %2, %3, %1" PK_HI PK_NEG : "=&v"(o_r), "=&v"(o_i) : "v"(a), "v"(hr), "v"(hi));
}
__device__ __forceinline__ void pk_conj_mac(f32x2& xr, f32x2& xi, f32x2 a, f32x2 hr, f32x2 hi) {
    asm("v_pk_fma_f32 %0, %2, %3, %0" PK_LO "\n\tv_pk_fma_f32 %1, %2, %4, %1" PK_LO PK_NEG "\n\tv_pk_fma_f32 %0, %2, %4, %0" PK_HI PK_NEG "\n\tv_pk_fma_f32 %1, %2, %3, %1" PK_HI PK_NEG
        : "+v"(xr), "+v"(xi) : "v"(a), "v"(hr), "v"(hi));
}
__device__ __forceinline__ void cmac2(f32x2& x, f32x2 a, f32x2 h) {
    asm("v_pk_fma_f32 %0, %1, %2, %0" PK_LO "\n\tv_pk_fma_f32 %0, %1, %2, %0 op_sel:[1,1,0] op_sel_hi:[1,0,1] neg_lo:[1,0,0]" : "+v"(x) : "v"(a), "v"(h));
}
#define PLN 0
__device__ __forceinline__ void c2_mac(f32x2& xr, f32x2& xi, float p, float q, float r, float s, f32x2 hr, f32x2 hi) {
    float a = xr[0], b = xi[0], c = xr[1], d = xi[1];
    asm("v_fmac_f32 %0, %4, %8\n\tv_fmac_f32 %1, %6, %9\n\tv_fmac_f32 %2, %4, %10\n\tv_fmac_f32 %3, %6, %11\n\t"
        "v_fmac_f32 %0, %5, %9\n\tv_fmac_f32 %1, %7, %8\n\tv_fmac_f32 %2, %5, %11\n\tv_fmac_f32 %3, %7, %10"
        : "+v"(a), "+v"(b), "+v"(c), "+v"(d) : "v"(p), "v"(q), "v"(r), "v"(s), "v"(hr[0]), "v"(hi[0]), "v"(hr[1]), "v"(hi[1]));
    xr = f32x2{a, c}; xi = f32x2{b, d};
}
__device__ __forceinline__ void c2_mad(f32x2& xr, f32x2& xi, float p, float q, float r, float s, f32x2 hr, f32x2 hi, f32x2 er, f32x2 ei) {
    float a, b, c, d;
    asm("v_fma_f32 %0, %4, %8, %12\n\tv_fma_f32 %1, %6, %9, %13\n\tv_fma_f32 %2, %4, %10, %14\n\tv_fma_f32 %3, %6, %11, %15\n\t"
        "v_fmac_f32 %0, %5, %9\n\tv_fmac_f32 %1, %7, %8\n\tv_fmac_f32 %2, %5, %11\n\tv_fmac_f32 %3, %7, %10"
        : "=&v"(a), "=&v"(b), "=&v"(c), "=&v"(d) : "v"(p), "v"(q), "v"(r), "v"(s), "v"(hr[0]), "v"(hi[0]), "v"(hr[1]), "v"(hi[1]), "v"(er[0]), "v"(ei[0]), "v"(er[1]), "v"(ei[1]));
    xr = f32x2{a, c}; xi = f32x2{b, d};
}
__device__ __forceinline__ void c2_mul(f32x2& xr, f32x2& xi, float p, float q, float r, float s, f32x2 hr, f32x2 hi) {
    float a, b, c, d;
    asm("v_mul_f32 %0, %4, %8\n\tv_mul_f32 %1, %6, %9\n\tv_mul_f32 %2, %4, %10\n\tv_mul_f32 %3, %6, %11\n\t"
        "v_fmac_f32 %0, %5, %9\n\tv_fmac_f32 %1, %7, %8\n\tv_fmac_f32 %2, %5, %11\n\tv_fmac_f32 %3, %7, %10"
        : "=&v"(a), "=&v"(b), "=&v"(c), "=&v"(d) : "v"(p), "v"(q), "v"(r), "v"(s), "v"(hr[0]), "v"(hi[0]), "v"(hr[1]), "v"(hi[1]));
    xr = f32x2{a, c}; xi = f32x2{b, d};
}
__device__ __forceinline__ float mul1(float a, float b) { float c; asm("v_mul_f32 %0, %1, %2" : "=v"(c) : "v"(a), "v"(b)); return c; }
__device__ __forceinline__ float add1(float a, float b) { float c; asm("v_add_f32 %0, %1, %2" : "=v"(c) : "v"(a), "v"(b)); return c; }
__device__ __forceinline__ void mfma_(f32x4& c, bf16x8 a, bf16x8 b) { asm volatile("v_mfma_f32_16x16x32_bf16 %0, %1, %2, %0" : "+v"(c) : "v"(a), "v"(b)); }
__device__ __forceinline__ float sum4(float x) {
    const auto p = __builtin_amdgcn_permlane32_swap(__float_as_uint(x), __float_as_uint(x), false, false);
    x = __uint_as_float(p[0]) + __uint_as_float(p[1]);
    const auto q = __builtin_amdgcn_permlane16_swap(__float_as_uint(x), __float_as_uint(x), false, false);
    return __uint_as_float(q[0]) + __uint_as_float(q[1]);
}

template <int I, int N, class F>
__device__ __forceinline__ void static_for(F&& f) { if constexpr (I < N) { f(std::integral_constant<int, I>{}); static_for<I + 1, N>(f); } }

#define TS_DECL
#define TS0a()
#define TS0b()
#define TSE(i)
#define TS(i)
#define TS_FLUSH()

#define WAIT_V(n) asm volatile("s_waitcnt vmcnt(%0)" ::"n"((n) > 63 ? 63 : (n)) : "memory")
#define WAIT_L(n) asm volatile("s_waitcnt lgkmcnt(%0)" ::"n"((n) > 15 ? 15 : (n)) : "memory")
#define BW 1
constexpr int wenc(int v, int l) { v = v > 63 ? 63 : v; l = l > 15 ? 15 : l; return (v & 15) | 0x70 | (l << 8) | ((v >> 4) << 14); }
#define WAIT_VB(n) do { if constexpr (BW) __builtin_amdgcn_s_waitcnt(wenc(n, 15)); else WAIT_V(n); } while (0)
#define WAIT_LB(n) do { if constexpr (BW) __builtin_amdgcn_s_waitcnt(wenc(63, n)); else WAIT_L(n); } while (0)
#define BAR() asm volatile("s_barrier" ::: "memory")
#define SB() __builtin_amdgcn_sched_barrier(0)
template <class T> __device__ __forceinline__ void tie_(T& x) { asm volatile("" : "+v"(x)); }
#define TIE(x) tie_(x)
template <class T> __device__ __forceinline__ void ties_(T& x) { asm volatile("" : "+s"(x)); }
__device__ __forceinline__ unsigned take(unsigned x) { unsigned y; asm volatile("v_mov_b32 %0, %1" : "=v"(y) : "v"(x)); return y; }
#define DEF_LD(name, ins) template <int OFF, class T> __device__ __forceinline__ void name(T& dst, unsigned voff, const char* sb) { \
    asm volatile(ins : "=v"(dst) : "v"(voff), "s"(sb), "n"(OFF) : "memory"); }
DEF_LD(gld_u16_, "global_load_ushort %0, %1, %2 offset:%3")
DEF_LD(gld_b32_, "global_load_dword %0, %1, %2 offset:%3")
DEF_LD(gld_b64_, "global_load_dwordx2 %0, %1, %2 offset:%3")
DEF_LD(gld_b128_, "global_load_dwordx4 %0, %1, %2 offset:%3")
#define NTQ " nt"
#define NTK " nt"
DEF_LD(gld_b64_nt_, "global_load_dwordx2 %0, %1, %2 offset:%3" NTQ)
DEF_LD(gld_b128_nt_, "global_load_dwordx4 %0, %1, %2 offset:%3" NTQ)
DEF_LD(gld_b64_sc1_, "global_load_dwordx2 %0, %1, %2 offset:%3 sc1")
DEF_LD(gld_b128_sc1_, "global_load_dwordx4 %0, %1, %2 offset:%3 sc1")
#define DEF_ST(name, ins) template <int OFF, class T> __device__ __forceinline__ void name(unsigned voff, const T& v, const char* sb) { \
    asm volatile(ins :: "v"(voff), "v"(v), "s"(sb), "n"(OFF) : "memory"); }
DEF_ST(gst_b128_, "global_store_dwordx4 %0, %1, %2 offset:%3\n\ts_nop 1")
DEF_ST(gst_b128_nt_, "global_store_dwordx4 %0, %1, %2 offset:%3 nt\n\ts_nop 1")
DEF_ST(gst_b128_sc1_, "global_store_dwordx4 %0, %1, %2 offset:%3 sc1\n\ts_nop 1")
DEF_ST(gst_b64_sc1_, "global_store_dwordx2 %0, %1, %2 offset:%3 sc1")
#define GLD_U16(dst, voff, sb) gld_u16_<0>(dst, voff, sb)
#define GLD_B32(dst, voff, sb, off) gld_b32_<(off)>(dst, voff, sb)
#define GLD_B64(dst, voff, sb, off) gld_b64_<(off)>(dst, voff, sb)
#define GLD_B128(dst, voff, sb, off) gld_b128_<(off)>(dst, voff, sb)
#define GLD_B64_NT(dst, voff, sb, off) gld_b64_nt_<(off)>(dst, voff, sb)
#define GLD_B128_NT(dst, voff, sb, off) gld_b128_nt_<(off)>(dst, voff, sb)
#define GLD_B64_SC1(dst, voff, sb, off) gld_b64_sc1_<(off)>(dst, voff, sb)
#define GLD_B128_SC1(dst, voff, sb, off) gld_b128_sc1_<(off)>(dst, voff, sb)
#define GST_B128(voff, v, sb, off) gst_b128_<(off)>(voff, v, sb)
#define GST_B128_NT(voff, v, sb, off) gst_b128_nt_<(off)>(voff, v, sb)
#define GST_B128_SC1(voff, v, sb, off) gst_b128_sc1_<(off)>(voff, v, sb)
#define GST_B64_SC1(voff, v, sb) gst_b64_sc1_<0>(voff, v, sb)
__device__ __forceinline__ void dma_(unsigned ldsaddr, unsigned voff, const char* sb) {
    asm volatile("s_mov_b32 m0, %0\n\ts_nop 0\n\tglobal_load_lds_dwordx4 %1, %2" :: "s"(ldsaddr), "v"(voff), "s"(sb) : "memory"); }
__device__ __forceinline__ void dma_nt_(unsigned ldsaddr, unsigned voff, const char* sb) {
    asm volatile("s_mov_b32 m0, %0\n\ts_nop 0\n\tglobal_load_lds_dwordx4 %1, %2 nt" :: "s"(ldsaddr), "v"(voff), "s"(sb) : "memory"); }
#define DF 0
#define DFN 4
#define FL_0 ""
#define FL_1 " nt"
#define FL_2 " sc1"
#define FL_3 " sc1 nt"
#define FL_4 " sc0 sc1"
#define FL_5 " sc0 sc1 nt"
#define FL_6 " sc0 nt"
#define FLX_(x) FL_##x
#define FLX(x) FLX_(x)
__device__ __forceinline__ void dmaf_(unsigned ldsaddr, unsigned voff, const char* sb) {
    asm volatile("s_mov_b32 m0, %0\n\ts_nop 0\n\tglobal_load_lds_dwordx4 %1, %2" FLX(DF) :: "s"(ldsaddr), "v"(voff), "s"(sb) : "memory"); }
template <int OFF> __device__ __forceinline__ void dmaof_(unsigned voff, const char* sb) {
    asm volatile("global_load_lds_dwordx4 %0, %1 offset:%2" FLX(DF) :: "v"(voff), "s"(sb), "n"(OFF) : "memory"); }
#define DMA(ldsaddr, voff, sb) dma_(ldsaddr, voff, sb)
template <int OFF> __device__ __forceinline__ void dmao_(unsigned voff, const char* sb) {
    asm volatile("global_load_lds_dwordx4 %0, %1 offset:%2" :: "v"(voff), "s"(sb), "n"(OFF) : "memory"); }
template <int OFF, class T> __device__ __forceinline__ void dsr128_(T& dst, unsigned addr) { asm volatile("ds_read_b128 %0, %1 offset:%2" : "=v"(dst) : "v"(addr), "n"(OFF)); }
template <int OFF, class T> __device__ __forceinline__ void dsr64_(T& dst, unsigned addr) { asm volatile("ds_read_b64 %0, %1 offset:%2" : "=v"(dst) : "v"(addr), "n"(OFF)); }
template <int OFF, class T> __device__ __forceinline__ void dsr2_(T& dst, unsigned addr) { asm volatile("ds_read2_b32 %0, %1 offset0:%2 offset1:%3" : "=v"(dst) : "v"(addr), "n"(OFF), "n"(OFF + 1)); }
#define DSR128(dst, addr, off) dsr128_<(off)>(dst, addr)
#define DSR64(dst, addr, off) dsr64_<(off)>(dst, addr)

__device__ __forceinline__ void kv4(unsigned& o01, unsigned& o23, unsigned ka, unsigned kb, unsigned kc, unsigned kd, const unsigned* w, unsigned v0, unsigned v1) {
    unsigned t1, t3, p0, p1, p2, p3;
    asm(
        "v_dot2_f32_bf16 %0, %8, %19, 0\n\tv_dot2_f32_bf16 %1, %9, %18, 0\n\tv_dot2_f32_bf16 %2, %9, %19, 0\n\tv_dot2_f32_bf16 %3, %10, %18, 0\n\t"
        "v_dot2_f32_bf16 %4, %9, %16, 0\n\tv_dot2_f32_bf16 %5, %9, %17, 0\n\tv_dot2_f32_bf16 %6, %10, %16, 0\n\tv_dot2_f32_bf16 %7, %10, %17, 0\n\t"
        "v_cvt_pk_bf16_f32 %0, %0, %4\n\tv_cvt_pk_bf16_f32 %1, %1, %5\n\tv_cvt_pk_bf16_f32 %2, %2, %6\n\tv_cvt_pk_bf16_f32 %3, %3, %7\n\t"
        "v_dot2_f32_bf16 %4, %9, %15, 0\n\tv_dot2_f32_bf16 %5, %10, %14, 0\n\tv_dot2_f32_bf16 %6, %10, %15, 0\n\tv_dot2_f32_bf16 %7, %11, %14, 0\n\t"
        "v_dot2_f32_bf16 %0, %0, %22, 0\n\tv_dot2_f32_bf16 %1, %1, %22, 0\n\tv_dot2_f32_bf16 %2, %2, %22, 0\n\tv_dot2_f32_bf16 %3, %3, %22, 0\n\t"
        "v_cvt_pk_bf16_f32 %0, %0, %4\n\tv_cvt_pk_bf16_f32 %1, %1, %5\n\tv_cvt_pk_bf16_f32 %2, %2, %6\n\tv_cvt_pk_bf16_f32 %3, %3, %7\n\t"
        "v_dot2_f32_bf16 %4, %10, %12, 0\n\tv_dot2_f32_bf16 %5, %10, %13, 0\n\tv_dot2_f32_bf16 %6, %11, %12, 0\n\tv_dot2_f32_bf16 %7, %11, %13, 0\n\t"
        "v_dot2_f32_bf16 %0, %0, %22, 0\n\tv_dot2_f32_bf16 %1, %1, %22, 0\n\tv_dot2_f32_bf16 %2, %2, %22, 0\n\tv_dot2_f32_bf16 %3, %3, %22, 0\n\t"
        "v_cvt_pk_bf16_f32 %0, %0, %4\n\tv_cvt_pk_bf16_f32 %1, %1, %5\n\tv_cvt_pk_bf16_f32 %2, %2, %6\n\tv_cvt_pk_bf16_f32 %3, %3, %7\n\t"
        "v_dot2_f32_bf16 %0, %0, %22, 0\n\tv_dot2_f32_bf16 %1, %1, %22, 0\n\tv_dot2_f32_bf16 %2, %2, %22, 0\n\tv_dot2_f32_bf16 %3, %3, %22, 0\n\t"
        "v_cvt_pk_bf16_f32 %0, %0, 0\n\tv_cvt_pk_bf16_f32 %1, 0, %1\n\tv_cvt_pk_bf16_f32 %2, %2, 0\n\tv_cvt_pk_bf16_f32 %3, 0, %3\n\t"
        "v_dot2_f32_bf16 %0, %0, %20, 0\n\tv_dot2_f32_bf16 %1, %1, %20, 0\n\tv_dot2_f32_bf16 %2, %2, %21, 0\n\tv_dot2_f32_bf16 %3, %3, %21, 0\n\t"
        "s_nop 1\n\tv_cvt_pk_bf16_f32 %0, %1, %0\n\tv_cvt_pk_bf16_f32 %2, %3, %2\n\t"
        : "=&v"(o01), "=&v"(t1), "=&v"(o23), "=&v"(t3), "=&v"(p0), "=&v"(p1), "=&v"(p2), "=&v"(p3)
        : "v"(ka), "v"(kb), "v"(kc), "v"(kd), "v"(w[0]), "v"(w[1]), "v"(w[2]), "v"(w[3]), "v"(w[4]), "v"(w[5]), "v"(w[6]), "v"(w[7]), "v"(v0), "v"(v1), "s"(0x3f803f80u));
}
__device__ __forceinline__ void kv4a(unsigned* t, unsigned* e, unsigned ka, unsigned kb, unsigned kc, unsigned kd, const unsigned* w) {
    asm volatile(
        "v_and_b32 %8, %21, %15\n\tv_lshlrev_b32 %9, 16, %16\n\tv_and_b32 %10, %21, %16\n\tv_lshlrev_b32 %11, 16, %17\n\t"
        "v_and_b32 %12, %21, %17\n\tv_mul_f32 %0, %20, %8\n\tv_mul_f32 %1, %20, %9\n\tv_mul_f32 %2, %20, %10\n\t"
        "v_mul_f32 %3, %20, %11\n\tv_mul_f32 %4, %19, %9\n\tv_mul_f32 %5, %19, %10\n\tv_mul_f32 %6, %19, %11\n\t"
        "v_mul_f32 %7, %19, %12\n\tv_lshlrev_b32 %13, 16, %18\n\tv_and_b32 %14, %21, %18\n\tv_cvt_pk_bf16_f32 %0, 0, %0\n\t"
        "v_cvt_pk_bf16_f32 %1, 0, %1\n\tv_cvt_pk_bf16_f32 %2, 0, %2\n\tv_cvt_pk_bf16_f32 %3, 0, %3\n\tv_cvt_pk_bf16_f32 %4, 0, %4\n\t"
        "v_cvt_pk_bf16_f32 %5, 0, %5\n\tv_cvt_pk_bf16_f32 %6, 0, %6\n\tv_cvt_pk_bf16_f32 %7, 0, %7\n\t"
        : "=&v"(t[0]), "=&v"(t[1]), "=&v"(t[2]), "=&v"(t[3]), "=&v"(t[4]), "=&v"(t[5]), "=&v"(t[6]), "=&v"(t[7]), "=&v"(e[0]), "=&v"(e[1]), "=&v"(e[2]), "=&v"(e[3]), "=&v"(e[4]), "=&v"(e[5]), "=&v"(e[6])
        : "v"(ka), "v"(kb), "v"(kc), "v"(kd), "v"(w[2]), "v"(w[3]), "s"(0xffff0000u));
}
__device__ __forceinline__ void kv4b(unsigned* t, const unsigned* e, const unsigned* w) {
    asm volatile(
        "v_add_f32 %0, %0, %4\n\tv_add_f32 %1, %1, %5\n\tv_add_f32 %2, %2, %6\n\tv_add_f32 %3, %3, %7\n\t"
        "v_mul_f32 %4, %14, %8\n\tv_mul_f32 %5, %14, %9\n\tv_mul_f32 %6, %14, %10\n\tv_mul_f32 %7, %14, %11\n\t"
        "v_cvt_pk_bf16_f32 %0, 0, %0\n\tv_cvt_pk_bf16_f32 %1, 0, %1\n\tv_cvt_pk_bf16_f32 %2, 0, %2\n\tv_cvt_pk_bf16_f32 %3, 0, %3\n\t"
        "v_cvt_pk_bf16_f32 %4, 0, %4\n\tv_cvt_pk_bf16_f32 %5, 0, %5\n\tv_cvt_pk_bf16_f32 %6, 0, %6\n\tv_cvt_pk_bf16_f32 %7, 0, %7\n\t"
        "v_add_f32 %0, %0, %4\n\tv_add_f32 %1, %1, %5\n\tv_add_f32 %2, %2, %6\n\tv_add_f32 %3, %3, %7\n\t"
        "v_mul_f32 %4, %13, %9\n\tv_mul_f32 %5, %13, %10\n\tv_mul_f32 %6, %13, %11\n\tv_mul_f32 %7, %13, %12\n\t"
        : "+v"(t[0]), "+v"(t[1]), "+v"(t[2]), "+v"(t[3]), "+v"(t[4]), "+v"(t[5]), "+v"(t[6]), "+v"(t[7]) : "v"(e[2]), "v"(e[3]), "v"(e[4]), "v"(e[5]), "v"(e[6]), "v"(w[0]), "v"(w[1]));
}
__device__ __forceinline__ void kv4c(unsigned* t, unsigned v0, unsigned v1) {
    asm volatile(
        "v_cvt_pk_bf16_f32 %0, 0, %0\n\tv_cvt_pk_bf16_f32 %1, 0, %1\n\tv_cvt_pk_bf16_f32 %2, 0, %2\n\tv_cvt_pk_bf16_f32 %3, 0, %3\n\t"
        "v_cvt_pk_bf16_f32 %4, 0, %4\n\tv_cvt_pk_bf16_f32 %5, 0, %5\n\tv_cvt_pk_bf16_f32 %6, 0, %6\n\tv_cvt_pk_bf16_f32 %7, 0, %7\n\t"
        "v_add_f32 %0, %0, %4\n\tv_add_f32 %1, %1, %5\n\tv_add_f32 %2, %2, %6\n\tv_add_f32 %3, %3, %7\n\t"
        "v_lshlrev_b32 %4, 16, %8\n\tv_and_b32 %5, %10, %8\n\tv_lshlrev_b32 %6, 16, %9\n\tv_and_b32 %7, %10, %9\n\t"
        "v_cvt_pk_bf16_f32 %0, 0, %0\n\tv_cvt_pk_bf16_f32 %1, 0, %1\n\tv_cvt_pk_bf16_f32 %2, 0, %2\n\tv_cvt_pk_bf16_f32 %3, 0, %3\n\t"
        "v_mul_f32 %0, %0, %4\n\tv_mul_f32 %1, %1, %5\n\tv_mul_f32 %2, %2, %6\n\tv_mul_f32 %3, %3, %7\n\t"
        "v_cvt_pk_bf16_f32 %0, %1, %0\n\tv_cvt_pk_bf16_f32 %2, %3, %2\n\t"
        : "+v"(t[0]), "+v"(t[1]), "+v"(t[2]), "+v"(t[3]), "+v"(t[4]), "+v"(t[5]), "+v"(t[6]), "+v"(t[7]) : "v"(v0), "v"(v1), "s"(0xffff0000u));
}
__device__ __forceinline__ void kf4a(unsigned* t, unsigned* e, unsigned ka, unsigned kb, unsigned kc, unsigned kd, const unsigned* w) {
    asm volatile(
        "v_and_b32 %4, %16, %11\n\tv_lshlrev_b32 %5, 16, %12\n\tv_and_b32 %6, %16, %12\n\tv_lshlrev_b32 %7, 16, %13\n\t"
        "v_and_b32 %8, %16, %13\n\tv_mul_f32 %0, %15, %4\n\tv_mul_f32 %1, %15, %5\n\tv_mul_f32 %2, %15, %6\n\t"
        "v_mul_f32 %3, %15, %7\n\tv_lshlrev_b32 %9, 16, %14\n\tv_and_b32 %10, %16, %14\n\t"
        : "=&v"(t[0]), "=&v"(t[1]), "=&v"(t[2]), "=&v"(t[3]), "=&v"(e[0]), "=&v"(e[1]), "=&v"(e[2]), "=&v"(e[3]), "=&v"(e[4]), "=&v"(e[5]), "=&v"(e[6])
        : "v"(ka), "v"(kb), "v"(kc), "v"(kd), "v"(w[3]), "s"(0xffff0000u));
}
__device__ __forceinline__ void kf4b(unsigned* t, const unsigned* e, const unsigned* w) {
    asm volatile(
        "v_fmac_f32 %0, %12, %4\n\tv_fmac_f32 %1, %12, %5\n\tv_fmac_f32 %2, %12, %6\n\tv_fmac_f32 %3, %12, %7\n\t"
        "v_fmac_f32 %0, %11, %5\n\tv_fmac_f32 %1, %11, %6\n\tv_fmac_f32 %2, %11, %7\n\tv_fmac_f32 %3, %11, %8\n\t"
        "v_fmac_f32 %0, %10, %6\n\tv_fmac_f32 %1, %10, %7\n\tv_fmac_f32 %2, %10, %8\n\tv_fmac_f32 %3, %10, %9\n\t"
        : "+v"(t[0]), "+v"(t[1]), "+v"(t[2]), "+v"(t[3]) : "v"(e[1]), "v"(e[2]), "v"(e[3]), "v"(e[4]), "v"(e[5]), "v"(e[6]), "v"(w[0]), "v"(w[1]), "v"(w[2]));
}
__device__ __forceinline__ void kf4c(unsigned* t, unsigned v0, unsigned v1) {
    asm volatile(
        "v_lshlrev_b32 %4, 16, %8\n\tv_and_b32 %5, %10, %8\n\tv_lshlrev_b32 %6, 16, %9\n\tv_and_b32 %7, %10, %9\n\t"
        "v_mul_f32 %0, %0, %4\n\tv_mul_f32 %1, %1, %5\n\tv_mul_f32 %2, %2, %6\n\tv_mul_f32 %3, %3, %7\n\t"
        "v_cvt_pk_bf16_f32 %0, %1, %0\n\tv_cvt_pk_bf16_f32 %2, %3, %2\n\t"
        : "+v"(t[0]), "+v"(t[1]), "+v"(t[2]), "+v"(t[3]), "=&v"(t[4]), "=&v"(t[5]), "=&v"(t[6]), "=&v"(t[7]) : "v"(v0), "v"(v1), "s"(0xffff0000u));
}
__device__ __forceinline__ f32x4 mul4bf(u32x2 x, const unsigned* d) {
    float a, b, c, e;
    asm("v_dot2_f32_bf16 %0, %4, %6, 0\n\tv_dot2_f32_bf16 %1, %4, %7, 0\n\tv_dot2_f32_bf16 %2, %5, %8, 0\n\tv_dot2_f32_bf16 %3, %5, %9, 0\n\t"
        "v_cvt_pk_bf16_f32 %0, 0, %0\n\tv_cvt_pk_bf16_f32 %1, 0, %1\n\tv_cvt_pk_bf16_f32 %2, 0, %2\n\tv_cvt_pk_bf16_f32 %3, 0, %3"
        : "=&v"(a), "=&v"(b), "=&v"(c), "=&v"(e) : "v"(x[0]), "v"(x[1]), "v"(d[0]), "v"(d[1]), "v"(d[2]), "v"(d[3]));
    return f32x4{a, b, c, e};
}
template <int BIT> __device__ __forceinline__ void quad_swap(u32x4& a, u32x4& b) {
    unsigned n0, n1, n2, n3, b0 = b[0], b1 = b[1], b2 = b[2], b3 = b[3];
#define QS_(d, s, k) "v_cndmask_b32_dpp " d ", " s ", " k ", vcc quad_perm:[%14,%15,%16,%17] row_mask:0xf bank_mask:0xf\n\t"
    asm("s_mov_b64 vcc, %12\n\ts_nop 0\n\t" QS_("%0", "%4", "%8") QS_("%1", "%5", "%9") QS_("%2", "%6", "%10") QS_("%3", "%7", "%11")
        "s_mov_b64 vcc, %13\n\t" QS_("%4", "%8", "%4") QS_("%5", "%9", "%5") QS_("%6", "%10", "%6") QS_("%7", "%11", "%7")
        : "=&v"(n0), "=&v"(n1), "=&v"(n2), "=&v"(n3), "+v"(b0), "+v"(b1), "+v"(b2), "+v"(b3)
        : "v"(a[0]), "v"(a[1]), "v"(a[2]), "v"(a[3]), "s"(BIT ? 0x3333333333333333ull : 0x5555555555555555ull), "s"(BIT ? 0xccccccccccccccccull : 0xaaaaaaaaaaaaaaaaull),
          "n"(BIT ? 2 : 1), "n"(BIT ? 3 : 0), "n"(BIT ? 0 : 3), "n"(BIT ? 1 : 2) : "vcc");
#undef QS_
    a = u32x4{n0, n1, n2, n3}; b = u32x4{b0, b1, b2, b3};
}
__device__ __forceinline__ void kv_dma(unsigned ldsaddr, unsigned k0, unsigned k1, unsigned k2, unsigned k3, const char* sb) {
    asm volatile("s_mov_b32 m0, %5\n\ts_nop 0\n\tglobal_load_lds_dwordx4 %0, %4" NTK "\n\tglobal_load_lds_dwordx4 %1, %4 offset:1024" NTK "\n\t"
                 "global_load_lds_dwordx4 %2, %4 offset:2048" NTK "\n\tglobal_load_lds_dwordx4 %3, %4 offset:3072" NTK
                 :: "v"(k0), "v"(k1), "v"(k2), "v"(k3), "s"(sb), "s"(ldsaddr) : "memory");
}
#define IF_(c, C) "s_cmp_lg_u32 " c ", " C "\n\ts_cbranch_scc1 1f\n\t"
template <int C, int N> __device__ __forceinline__ void wait_if(int c) { asm volatile(IF_("%0", "%1") "s_waitcnt vmcnt(%2)\n1:" :: "s"(c), "n"(C), "n"(N) : "scc", "memory"); }
template <int C, int M> __device__ __forceinline__ void flag_if(int c, unsigned voff, u32x2 v, const char* sb) {
    asm volatile(IF_("%0", "%1") "s_mov_b32 exec_lo, %5\n\ts_mov_b32 exec_hi, 0\n\tglobal_store_dwordx2 %2, %3, %4 sc1\n\ts_mov_b64 exec, -1\n1:" :: "s"(c), "n"(C), "v"(voff), "v"(v), "s"(sb), "n"(M) : "scc", "memory"); }
template <int C> __device__ __forceinline__ void st4_if(int c, unsigned voff, const u32x4* v, const char* s0, const char* s1) {
    asm volatile(IF_("%0", "%1") "global_store_dwordx4 %2, %3, %7 sc1\n\tglobal_store_dwordx4 %2, %4, %7 offset:1024 sc1\n\tglobal_store_dwordx4 %2, %5, %8 sc1\n\tglobal_store_dwordx4 %2, %6, %8 offset:1024 sc1\n\ts_nop 1\n1:"
                 :: "s"(c), "n"(C), "v"(voff), "v"(v[0]), "v"(v[1]), "v"(v[2]), "v"(v[3]), "s"(s0), "s"(s1) : "scc", "memory"); }
template <int C> __device__ __forceinline__ void ld4_if(int c, unsigned voff, u32x4* v, const char* s0) {
    asm volatile(IF_("%4", "%5") "global_load_dwordx4 %0, %6, %7 nt\n\tglobal_load_dwordx4 %1, %6, %7 offset:1024 nt\n\tglobal_load_dwordx4 %2, %6, %7 offset:2048 nt\n\tglobal_load_dwordx4 %3, %6, %7 offset:3072 nt\n1:"
                 : "+v"(v[0]), "+v"(v[1]), "+v"(v[2]), "+v"(v[3]) : "s"(c), "n"(C), "v"(voff), "s"(s0) : "scc", "memory"); }
template <int C> __device__ __forceinline__ void ld8_if(int c, unsigned voff, u32x2* v, const char* s0, const char* s1) {
    asm volatile(IF_("%8", "%9") "global_load_dwordx2 %0, %10, %11 offset:-4096 nt\n\tglobal_load_dwordx2 %1, %10, %11 offset:-2048 nt\n\tglobal_load_dwordx2 %2, %10, %11 nt\n\tglobal_load_dwordx2 %3, %10, %11 offset:2048 nt\n\t"
                 "global_load_dwordx2 %4, %10, %12 offset:-4096 nt\n\tglobal_load_dwordx2 %5, %10, %12 offset:-2048 nt\n\tglobal_load_dwordx2 %6, %10, %12 nt\n\tglobal_load_dwordx2 %7, %10, %12 offset:2048 nt\n1:"
                 : "+v"(v[0]), "+v"(v[1]), "+v"(v[2]), "+v"(v[3]), "+v"(v[4]), "+v"(v[5]), "+v"(v[6]), "+v"(v[7]) : "s"(c), "n"(C), "v"(voff), "s"(s0), "s"(s1) : "scc", "memory"); }
template <int E> __device__ __forceinline__ void sy_post(unsigned myaddr) { asm volatile("ds_write_b32 %0, %0 offset:%1" :: "v"(myaddr), "n"(E * 64) : "memory"); }
template <int E, int SL = 1> __device__ __forceinline__ unsigned sy_wait(unsigned addr) {
    unsigned v;
    asm volatile("s_branch 2f\n1:\n\ts_sleep %3\n2:\n\tds_read_b32 %0, %1 offset:%2\n\ts_waitcnt lgkmcnt(0)\n\tv_cmp_eq_u32 vcc, 0, %0\n\ts_cbranch_vccnz 1b"
                 : "=&v"(v) : "v"(addr), "n"(E * 64), "n"(SL) : "vcc", "memory");
    return v;
}
template <int E> __device__ __forceinline__ void sy_pre(unsigned& v, unsigned addr) { asm volatile("ds_read_b32 %0, %1 offset:%2" : "=v"(v) : "v"(addr), "n"(E * 64) : "memory"); }
template <int E> __device__ __forceinline__ void sy_chk(unsigned& v, unsigned addr) {
    asm volatile("s_branch 2f\n1:\n\tds_read_b32 %0, %1 offset:%2\n\ts_waitcnt lgkmcnt(0)\n2:\n\tv_cmp_eq_u32 vcc, 0, %0\n\ts_cbranch_vccnz 1b"
                 : "+v"(v) : "v"(addr), "n"(E * 64) : "vcc", "memory");
}
template <int OFF, class T> __device__ __forceinline__ void dsrtr_(T& dst, unsigned addr) { asm volatile("ds_read_b64_tr_b16 %0, %1 offset:%2" : "=v"(dst) : "v"(addr), "n"(OFF)); }

constexpr size_t WS_H = 0, WS_KVT = (size_t)10 << 20, WS_ZT = (size_t)15 << 20, WS_E = (size_t)20 << 20, WS_FLAG = WS_E + (2048 << 10);
#define HP 65536
#define KP 8192
#define ZP 4096
constexpr int FL_KV = 0, FL_Z = 65536, FL_H = 131072, FL_EPOCH = 196608;
constexpr unsigned SPIN_CAP = 1u << 17;
#define ABL 0
#define CSPLIT 1
#define MERGE 3
#define CB 13
#define PAUSE 0
#define QLATE 0
#define CVPRIO 0
#define STG_SEL (idx & 3)
#define SP0 3
#define SDP 1
#define LP0 3
#define LDP 2
#define KVS 5
#define KVU 3
#define QIT 0
#define ZB 3
#define OS 2
#define MPRIO 2

#define ROT 0x4042
#define R2 0x41
#define CPF 0
#define PRE 4
#define PRO 9
#define G1NT 0
#define G1NTS 16
#define TMIX 0
#define TLBT 0
#define CDEF 0
#define CVLATE 1
#define FPRIO 3
#define YS 0
#define DP0 1
#define DP1 5
#define AWB 1
#define PK 0
#define PU 15
#define PC0 13
#define PC1 14
#define PC2 16
#define PC3 16
#define PQ 0
#define SPF 0
#define SPH 0
#define NB1 4
#define EP 1
#define ESL 0
#define CFB 0
#define P1 1
#define P0 1
#define BKE 0
#define EALL 0
#define EREP 0
#define TOUCH 0
#define FILLB 0
#define CPOST 1
#define CEARLY 0
#define NOOWN 0
#define PFIRST 0
#define ZB0 0
#define CATCH 4
#define PWE 0
#define SWAP 1
#define KVM 0x0FE
#define KFD 3
#define ZW 2
#define ZX 1
#define ZP0 3
#define BARM 0x22220
#define NR 6
#define WPS 2
#define CD0 3
#define AWX 8
#define VBM 0
#define ZOWN 1
#define ZPS 4
#define XAB 0
#define QM 0
#define OSG 0
#define KPL 0xFE
#define KPRIO 1
#define GPRIO 1
#define PST 0x7654
#define PLT 0xB964
#define LC 0
#define PRE2 PRE
#define KVD 0
#define PAIR 1
#define G2P1 0
#define EPRIO 0
#define LSW 1
#define EGX 28
#define CR1 0
#define CR2 0
#define CR3 0
#define CRL (2 * NR - CR1 - CR2 - CR3)
#define UE 0
#define UPRIO 3
#define AMF 7
#define M1 1
#define W2 1
#define W1 0
#define NBK 3
#define XB 4
#define KFB 9
#define APRE 0
#define CB0 1
#define AP0 8
#define HTSE(i)
#define KAT 2
#define SD 2
#define KV3 0x088888
#define KOWN 1
#define HKV 0
#define KVP NBK
#define LZ 1
#define AWN 8
#define CRATE 2
#define ZSB 0
#define ZFB 1
#define ZSL 4
#define OSL 0
constexpr int it_cnt(int p, int k, int last) {
    const int pos[8] = {PK, PU, PC0, PC1, PQ, PU - 1, PC2, PC3}, n[8] = {4, 4, 4, 4, 8, 1, 4, 4};
    int c = 0;
    for (int i = 0; i < 8; ++i) if (pos[i] <= last && (pos[i] > p || (pos[i] == p && i > k))) c += n[i];
    return c;
}
constexpr int nib(int t, int c) { return (t >> (4 * c)) & 15; }
constexpr bool nib_any(int t, int v) { return nib(t, 0) == v || nib(t, 1) == v || nib(t, 2) == v || nib(t, 3) == v; }
constexpr int kv_wait(int late) {
    int t = 0;
    for (int c = 0; c < 4; ++c) t |= ((nib(PLT, c) >= nib(PST, c) && nib(PLT, c) < nib(PST, c) + KFD) == late ? nib(PST, c) + KFD : 15) << (4 * c);
    return t;
}
constexpr int G1STAGE = 32768;
constexpr int g1_slot(int s) { return (NB1 == 5 ? (s + 3) % 5 : s & 3) * G1STAGE; }
constexpr int L_KS = NB1 == 5 ? 65536 : 131072, L_VS = L_KS + 16384;
constexpr int L_TS = 3 * G1STAGE, L_TR = L_TS + 3072;
constexpr int CST = 4288;
constexpr int UOFF = 2 * CST + 128, UPL = 2304;
constexpr int CHSZ = UOFF + 2 * UPL + 32;
constexpr int L_AGG = 4 * CHSZ, L_ES = L_AGG + 15 * 512, L_SY = L_ES + 2048;
constexpr int L_HT = 98304;
constexpr int L_C = 98304, G2STAGE = 16384;
constexpr int L_Y = 16384;
static_assert(L_SY + 384 <= 65536 && L_AGG >= 49152 && UOFF % 16 == 0 && CHSZ % 256 == 32, "lds");

__device__ __forceinline__ int img_off(int r, int c) { return (r >> 1) * 256 + ((((r & 1) * 8 + c) ^ ((r >> 1) & 15)) << 4); }

constexpr int g1_upto(int g) {
    int n = 4 * NBK;
    for (int t = SD; t <= g; ++t) {
        int sb = (t + 1) / 2, sub = (t + 1) % 2, allowed = sb ? 4 * (sb + 3) + 2 * (sub + 1) : 12;
        if (allowed > 64) allowed = 64;
        n = n + CATCH < allowed ? n + CATCH : allowed;
    }
    return n;
}
constexpr int g1_cnt(int s) { return HKV || s >= KVP ? g1_upto(2 * s - 2) - 4 * (s + 1) : s >= KAT ? g1_upto(2 * s - 2) - 4 * KVP : g1_upto(2 * s - 2) - 4 * (s + 1) + 4; }
static_assert(KVP >= 1 && KVP <= NBK && AP0 % 4 == 0, "schedule");
static_assert(g1_upto(28) == 64, "schedule");
constexpr int AW = AWN;
constexpr int g2_place(int s) { return s < 5 ? L_C + ((s + 2) & 3) * G2STAGE : s < 7 ? (s - 1) * G2STAGE : s < 10 ? (9 - s) * G2STAGE : g2_place(s - 9); }
constexpr bool g2_bar(int g) { return (BARM >> g) & 1; }
struct G2Plan { int upto[33], wr[33], ald[64][2], seq[164], use[64]; };
constexpr G2Plan g2_plan() {
    G2Plan p = {};
    int nc = 14 + 2 * NR, nw = 0, nx = 4, lastb = 0, n = 0;
    for (int i = 0; i < 164; ++i) p.seq[i] = 1000;
    for (; nx < 4 + AW; ++nx) p.seq[nx] = ++n;
    for (int t = 0; t < 32; ++t) {
        p.upto[t] = nc; p.wr[t] = nw;
        if (t < 2) continue;
        if (g2_bar(t)) lastb = t;
        const int w = AW + nw < AWX ? AW + nw : AWX;
        for (int f = 2 * t; f < 2 * t + 2; ++f) {
            p.use[f] = n;
            if (nc < 32 && nc == p.upto[t] + (f & 1) && (f & 1) < CRATE && t >= CD0 && lastb >= (nc < 20 ? 1 : (nc | 1) - 18)) p.seq[100 + nc++] = ++n;
            for (int k = 0; k < 2; ++k) {
                p.ald[f][k] = -1;
                if (nx < 64 && nx - f - k < w) { p.ald[f][k] = nx; p.seq[nx++] = ++n; }
            }
        }
        for (int k = 0; k < WPS; ++k) if (nw < 2 * NR && lastb >= (nw < 6 ? 1 : (nw | 1) - 4)) ++nw;
    }
    p.upto[32] = nc; p.wr[32] = nw;
    return p;
}
constexpr G2Plan G2P = g2_plan();
constexpr int g2_upto(int g) { return G2P.upto[g + 1]; }
constexpr int g2_wr(int g) { return G2P.wr[g + 1]; }
constexpr int g2_ald(int f, int k) { return G2P.ald[f][k]; }
constexpr int g2_cnt(int id, int at) { return G2P.use[at] - G2P.seq[id]; }
constexpr int LASTB = 31 - __builtin_clz(BARM);
constexpr int lz_post() { int g = LASTB; while (G2P.seq[2 * g] < G2P.seq[131]) ++g; return g; }
constexpr int lz_chk() { int s = 7 + NR; while (G2P.seq[100 + 2 * s + 1] < G2P.seq[2 * LASTB]) ++s; return 2 * s - 1; }
static_assert(!LZ || (AWB && lz_post() + 1 < lz_chk() && lz_chk() < EGX), "schedule");
constexpr int g2_cover(int g) { int s = (g + 1) / 2; while (s < 15 && !g2_bar(2 * s + 1)) ++s; return s; }
static_assert(BARM >> 17 && g2_cover(3) < 7 && CRATE <= 2, "the epilogue needs stage 8 behind a barrier");

constexpr int cv_m(int o) { return (o & 1) ? 7 - (o >> 1) : o >> 1; }
constexpr int cv_first(int o) { int p = 0; for (int i = 0; i < o; ++i) p += 8 - cv_m(i); return p; }
constexpr int cv_grp(int p) { int o = 0; while (cv_first(o + 1) <= p) ++o; return o; }
constexpr int cv_read(int p) {
    int slot[40] = {};
    for (int i = 0; i < 40; ++i) slot[i] = -1;
    slot[0] = 2 * 3 + 1;
    for (int o = 0; o < 8; ++o) {
        int q = cv_first(o + 1) - 1;
        for (int h = 0; h < 2; ++h) { while (slot[q] >= 0) ++q; slot[q] = 2 * (o + 4) + h; }
    }
    const int bs[8] = {1, 3, 5, 11, 12, 13, 19, 21};
    for (int jb = 0; jb < 8; ++jb) slot[bs[jb]] = 32 + jb;
    return slot[p];
}
static_assert(cv_read(35) == 22 && cv_read(36) == 23 && cv_read(37) == -1 && cv_read(10) == 11 && cv_read(15) == 12 && cv_read(18) == 15 && cv_read(24) == 17, "conv schedule");
constexpr int cv_wait(int o) {
    int n = 0;
    for (int q = cv_first(o) - 1;; --q) {
        const int rd = cv_read(q < 0 ? q + 36 : q);
        if (rd == 2 * (o + (q < 0 ? 8 : 0)) + 1) return n;
        if (rd >= 0) ++n;
    }
}

struct Args {
    const char *q, *k, *v, *sk, *ssmk, *ssmD, *x, *lre, *lim, *bre, *bim, *cre, *cim, *logdt, *dskip;
    char *out, *ws;
};

typedef __attribute__((ext_vector_type(8))) unsigned long long u64x8;
__global__ __launch_bounds__(768, 3) void fused(Args) {
    __shared__ __attribute__((aligned(1024))) char shm[163840];
    TS0a();
    u64x8 ka0, ka1; unsigned long long ka2;
    const char* pc0;
    int code_n;
    asm volatile("s_load_dwordx16 %0, %5, 0x0\n\ts_load_dwordx16 %1, %5, 0x40\n\ts_load_dwordx2 %2, %5, 0x80\n\t"
                 "s_getpc_b64 %3\n.Lfk_code_0:\n\ts_mov_b32 %4, .Lfk_code_a-.Lfk_code_0-4\n\ts_max_i32 %4, %4, .Lfk_code_b-.Lfk_code_0-4"
                 : "=&s"(ka0), "=&s"(ka1), "=&s"(ka2), "=&s"(pc0), "=&s"(code_n) : "s"(__builtin_amdgcn_kernarg_segment_ptr()) : "scc");
    const int tid = threadIdx.x, lane = tid & 63, wraw = __builtin_amdgcn_readfirstlane(tid >> 6);
    const int wid = !SWAP ? wraw : wraw < 4 ? wraw + 8 : wraw - 4;
    const int b = blockIdx.x, xcd = b & 7, idx = b >> 3;
    asm volatile("s_cmp_lt_u32 %0, 8\n\ts_cbranch_scc1 1f\n\ts_setprio 3\n\ts_cmp_lg_u32 %0, 8\n\ts_cbranch_scc1 1f\n\t"
                 "v_lshl_add_u32 v161, %1, 7, %2\n\tv_min_i32 v161, %3, v161\n\ts_mov_b64 exec, 0xffff\n\tglobal_load_dword v161, v161, %4\n\ts_mov_b64 exec, -1\n1:"
                 :: "s"(wid), "v"(lane), "s"(idx * 2048), "s"(code_n), "s"(pc0) : "scc", "memory", "v161");
    TS_DECL
    const int tm = (xcd >> 1) * 4 + (idx >> 3), tn = (xcd & 1) * 8 + (idx & 7);
    const int wm = wid & 3, wn = (wid >> 2) & 1, l31 = lane & 31, lh = lane >> 5;
    const unsigned lb = (unsigned)(uintptr_t)shm;
    const unsigned l16 = lane * 16;
    const int l15 = lane & 15, g4 = lane >> 4, sq = 2 * wn + (wm & 1), st = sq * 16 + l15, run = (wm >> 1) * 4 + g4;
    unsigned kvv[4];
    const int trow = idx * 64 * 2048 + xcd * 256;
    if (wid < 8) {
        const int v = l15 ^ g4, hi = v >> 3;
        const int r0 = wn ? tn * 64 + (wm >> 1) * 32 : tm * 128 + (wm >> 1) * 64 + (wm & 1) * 4;
        const unsigned d0 = (r0 + ((g4 * (wn ? 2 : 9)) & (wn ? 6 : 17))) * 2048 + (v & 7) * 16, odd = wn ? 11 : 14;
        kvv[0] = d0 + (hi << odd); kvv[2] = d0 + ((hi ^ 1) << odd) + (wn ? 32768 - 2048 : 4096 - 2048);
        kvv[1] = (kvv[0] ^ 64) + (wn ? 16384 - 1024 : 65536 - 1024); kvv[3] = (kvv[2] ^ 64) + (wn ? 16384 - 1024 : 65536 - 1024);
    } else {
        kvv[0] = trow + (idx ? -4 * 2048 : 0) + g4 * 2048 + wm * 64 + l15 * 4;
        asm volatile("v_writelane_b32 %0, 0, 0\n\tv_writelane_b32 %0, 4, 1\n\tv_writelane_b32 %0, %1, 2" : "+v"(kvv[0]) : "s"((xcd * 64 + idx * 2 + (wm >> 1)) * 4));
        kvv[1] = (xcd * 128 + wm * 32 + l31) * 8;
        kvv[2] = kvv[3] = (tn * 64 + lane) * 2;
    }
    unsigned g1dst = lb + wid * 4096;
    int rot7 = ((((ROT >> 12) & 15) * (idx & 7) + ((ROT >> 8) & 15) * (idx >> 3) + ((ROT >> 4) & 15) * (xcd >> 1) + (ROT & 15) * (xcd & 1)) & 15) << 7;
    asm volatile("s_waitcnt lgkmcnt(0)" : "+s"(ka0), "+s"(ka1), "+s"(ka2), "+s"(g1dst), "+s"(rot7), "+v"(kvv[0]), "+v"(kvv[1]), "+v"(kvv[2]), "+v"(kvv[3]));
    Args a;
    a.q = (const char*)ka0[0]; a.k = (const char*)ka0[1]; a.v = (const char*)ka0[2]; a.sk = (const char*)ka0[3]; a.ssmk = (const char*)ka0[4]; a.ssmD = (const char*)ka0[5];
    a.x = (const char*)ka0[6]; a.lre = (const char*)ka0[7]; a.lim = (const char*)ka1[0]; a.bre = (const char*)ka1[1]; a.bim = (const char*)ka1[2]; a.cre = (const char*)ka1[3];
    a.cim = (const char*)ka1[4]; a.logdt = (const char*)ka1[5]; a.dskip = (const char*)ka1[6]; a.out = (char*)ka1[7]; a.ws = (char*)ka2;
    char* const ws = a.ws;
    const char* const flg = ws + WS_FLAG;
    TS(26);
    const char* g1src = wn ? ((wm & 1) ? a.bim : a.bre) : a.x;
    asm volatile("s_mov_b32 m0, %[m]\n\ts_cmp_gt_u32 %[w], 7\n\ts_cbranch_scc1 1f\n\t"
                 "global_load_lds_dwordx4 %[k0], %[g]" FLX(DF) "\n\tglobal_load_lds_dwordx4 %[k1], %[g] offset:1024" FLX(DF) "\n\t"
                 "global_load_lds_dwordx4 %[k2], %[g] offset:2048" FLX(DF) "\n\tglobal_load_lds_dwordx4 %[k3], %[g] offset:3072" FLX(DF) "\n\t"
                 "s_branch 2f\n1:\n\tglobal_load_ushort v164, %[k2], %[c1]\n\tglobal_load_ushort v165, %[k2], %[c2]\n\tglobal_load_ushort v166, %[k2], %[c3]\n\t"
                 "v_mov_b64 v[162:163], %[kv]\n\ts_mov_b64 exec, 7\n\tv_mov_b64 v[162:163], %[d]\n\ts_mov_b64 exec, 3\n\tv_mov_b64 v[162:163], %[e]\n\ts_mov_b64 exec, -1\n\t"
                 "v_mad_u64_u32 v[162:163], vcc, %[k0], 1, v[162:163]\n\tglobal_load_dword v160, v[162:163], off sc1\n\tglobal_load_dwordx2 v[162:163], %[k1], %[sk]\n2:"
                 :
                 : [w] "s"(wid), [c1] "s"(a.logdt), [c2] "s"(a.lre), [c3] "s"(a.lim), [d] "s"(a.ssmD), [e] "s"(flg + FL_EPOCH), [sk] "s"(a.sk),
                   [k0] "v"(kvv[0]), [k1] "v"(kvv[1]), [k2] "v"(kvv[2]), [k3] "v"(kvv[3]), [kv] "s"(a.k), [g] "s"(g1src + rot7), [m] "s"(g1dst)
                 : "scc", "vcc", "memory", "v160", "v161", "v162", "v163", "v164", "v165", "v166");
    TS0b();
    TS(27);
    if (P1) BAR();
#define SLICE_TABLE() unsigned slo, shi; { int ix = idx; asm volatile("" : "+s"(ix)); \
        const int rot2 = (((R2 >> 4) & 15) * (ix >> 3) + (R2 & 15) * (xcd >> 1)) & 15, es = (tn - rot2) & 15; \
        const unsigned long long id = 0xFEDCBA9876543210ull, seq = rot2 ? (id >> (4 * rot2)) | (id << (64 - 4 * rot2)) : id; \
        const unsigned long long lo = (1ull << (4 * es)) - 1, tab = ((seq & lo) << 4) | (seq & ~((lo << 4) | 15)) | tn; \
        slo = (unsigned)tab; shi = (unsigned)(tab >> 32); asm volatile("" : "+s"(slo), "+s"(shi)); }
#define SLICE(st) ((((st) < 8 ? slo : shi) >> (4 * ((st) & 7))) & 15)
    typedef __attribute__((ext_vector_type(4))) short v4i16;
    int role = wid;
    asm volatile("" : "+s"(role));

    if (role >= 8) {
        int lane;
        asm volatile("v_mbcnt_lo_u32_b32 %0, -1, 0\n\tv_mbcnt_hi_u32_b32 %0, -1, %0" : "=v"(lane));
        __builtin_assume(lane >= 0 && lane < 64);
        const int l31 = lane & 31, lh = lane >> 5, l15 = lane & 15, g4 = lane >> 4;
        const unsigned l16 = lane * 16;
        const int chn = xcd * 128 + idx * 4 + wm;
        const unsigned cb = lb + wm * CHSZ;
        const int r = l15;
        const unsigned fpub = KOWN ? (xcd * 32 + l31) * 256 + idx * 8 : xcd * 256 + idx * 8, fpoll = KOWN ? (xcd * 32 + idx) * 256 + l31 * 8 : xcd * 256 + l31 * 8;
        u32x2 fl; u32x4 sw[4], uu[4];
        unsigned tch = 0;
        if (TOUCH) {
            const int ln = wm * 4 + (lane & 3);
            const unsigned fo = (unsigned)(WS_FLAG - WS_E) + (wm == 0 ? FL_H + (tm * 16 + tn) * 128 : (wm == 1 ? FL_KV : FL_Z) + xcd * 256 + (lane & 1) * 128);
            const unsigned o = (lane & 4) && (TOUCH & 2) ? fo : ((tm * 2 + (ln >> 3)) * 1024 + tn * 64) * 16 + (ln & 7) * 128;
            asm volatile("s_mov_b64 exec, 0xff\n\tglobal_load_dword %0, %1, %2\n\ts_mov_b64 exec, -1" : "=&v"(tch) : "v"(o), "s"(ws + WS_E) : "memory");
        }
        SLICE_TABLE();
        TS(1);
        unsigned sm, cl[3]; u32x2 kw0;
        asm volatile("s_waitcnt vmcnt(0)" : "={v160}"(sm), "={v[162:163]}"(kw0), "={v164}"(cl[0]), "={v165}"(cl[1]), "={v166}"(cl[2]) :: "memory", "v161");
        TS(22);
        const unsigned hal = take(sm);
        const unsigned tagA = __builtin_amdgcn_readlane(hal, 0) + 1, tagB = __builtin_amdgcn_readlane(hal, 1) + (tagA == 0 ? 1u : 0u);
        const u32x2 tag = {tagA, tagB};
        f32x2 L1, C1;
        {
            const float dt = rbf(expf(bf2f(take(cl[0]))));
            const float lr = bf2f(take(cl[1])), li = bf2f(take(cl[2]));
            const float e = expf(dt * lr);
            float sn, cs;
            sincosf(dt * li, &sn, &cs);
            L1 = f32x2{e * cs, e * sn};
            const float nr = L1[0] - 1.f, ni = L1[1], den = lr * lr + li * li;
            C1 = f32x2{2.f * ((nr * lr + ni * li) / den), 2.f * ((ni * lr - nr * li) / den)};
            TIE(L1); TIE(C1);
        }

        f32x2 p[11];
        p[0] = L1;
        if (PWE) {
#pragma unroll
            for (int i = 1; i <= 10; ++i) { p[i] = cmul4(p[i - 1], p[i - 1]); TIE(p[i]); }
        }
        {
            if (!PWE) {
#pragma unroll
                for (int i = 1; i <= 10; ++i) p[i] = cmul4(p[i - 1], p[i - 1]);
            }
            const f32x2 one = {1.f, 0.f}, zero = {0.f, 0.f};
            const unsigned ts = lb + L_TS + l16, tr = lb + L_TR + wm * 2048 + l16;
            if (wm == 0) {
                *LDSP(f32x4, ts) = f32x4{L1[0], L1[1], C1[0], C1[1]};
                *LDSP(f32x4, ts + 1024) = f32x4{p[3][0], p[3][1], p[4][0], p[4][1]};
                *LDSP(f32x4, ts + 2048) = f32x4{p[6][0], p[6][1], __uint_as_float(tagA), __uint_as_float(tagB)};
            }
            f32x2 Q[2], V[2], W[2];
            Q[0] = cmul4(cmul4(C1, (wm & 1) ? p[5] : one), (wm & 2) ? p[6] : one); Q[1] = cmul4(Q[0], p[4]);
            V[1] = (wm & 1) ? one : p[5]; V[0] = cmul4(V[1], p[4]);
            W[1] = cmul4((wm & 1) ? one : p[9], (wm & 2) ? one : p[10]); W[0] = cmul4(W[1], p[8]);
#pragma unroll
            for (int i = 0; i < 2; ++i) {
                const f32x2 Q8 = cmul4(Q[i], p[3]), U = 4 * wm + 2 * i >= 16 - tm ? cmul4(W[i], p[7]) : zero, Wz = 4 * wm + 2 * i + 1 >= 16 - tm ? W[i] : zero;
                *LDSP(f32x4, tr + i * 1024) = f32x4{Q[i][0], Q8[0], Q[i][1], Q8[1]};
                *LDSP(f32x4, tr + i * 1024 + 8192) = f32x4{Wz[0], Wz[1], U[0], U[1]};
                *LDSP(f32x2, tr + i * 1024 + 16384) = V[i];
            }
        }
        auto b0 = [&]() {
            unsigned k0 = trow + wm * 32768 + g4 * 2048 + ((l15 ^ (2 * g4)) << 4), k1 = k0 + 7168, k2 = (k0 ^ 128) + 14336, k3 = k2 + 7168;
            if (HKV) { TIE(k0); TIE(k1); TIE(k2); TIE(k3); }
            if (HKV == 1) { kv_dma(lb + L_KS + wm * 4096, k0, k1, k2, k3, a.k); kv_dma(lb + L_VS + wm * 4096, k0, k1, k2, k3, a.v); }
            WAIT_L(0); BAR(); HTSE(1);
            if (HKV == 2) { kv_dma(lb + L_KS + wm * 4096, k0, k1, k2, k3, a.k); kv_dma(lb + L_VS + wm * 4096, k0, k1, k2, k3, a.v); }
        };
        if (CB0 == 0) b0();
        const unsigned dskl = (__builtin_amdgcn_readlane(hal, 2) >> ((wm & 1) * 16)) & 0xffffu;
        u32x4 kvo[4];
        u32x2 qv[8];
        const char* krow = a.ssmk + (size_t)chn * 4096;
        const char* qs = a.q + (size_t)(idx * 64 + wm * 16) * 2048 + xcd * 256;
        const char* kvd = ws + WS_KVT + (size_t)(xcd * 8 + wm * 2) * 65536 + idx * 2048;
        const int pl = (PLT >> ((STG_SEL) * 4)) & 15;
        const unsigned qo = lh * 8192 + l31 * 8 + 4096;
        auto hb = [&](auto nc) {
            constexpr int nb = decltype(nc)::value;
            if constexpr (nb == KAT && HKV) WAIT_V(0);
            if constexpr (nb == KFB) { WAIT_V(12); int lm = (1 << KFB) - (2 << (KAT + __builtin_popcount(KV3))); ties_(lm); wait_if<0, 0>((lm >> pl) & 1); }
            BAR();
            if constexpr (nb == KFB) flag_if<0, KOWN ? -1 : 1>(wm, fpub, tag, flg + FL_KV);
            if constexpr (nib_any(PLT, nb)) { ld4_if<nb>(pl, l16, sw, krow); ld8_if<nb>(pl, qo, qv, qs, qs + 16384); }
        };
#define HB(n) hb(std::integral_constant<int, (n) + NBK>{})
        {
            unsigned kw[2][4], ta[2], th[2];
            u32x2 hm[2];
#pragma unroll
            for (int j = 0; j < 2; ++j) {
                const unsigned x = take(kw0[j]);
                const auto y = __builtin_amdgcn_permlane16_swap(x, x, false, false);
#pragma unroll
                for (int p = 0; p < 2; ++p) { kw[p][2 * j] = y[p] << 16; kw[p][2 * j + 1] = y[p] & 0xffff0000u; }
            }
#pragma unroll
            for (int p = 0; p < 2; ++p) {
                unsigned h[3];
#pragma unroll
                for (int j = 0; j < 3; ++j) h[j] = (__shfl(hal, p * 8 + (r >> 1) + 16 * (j + 1)) >> ((r & 1) * 16)) & 0xffffu;
                hm[p] = idx ? u32x2{h[0] << 16, h[1] | (h[2] << 16)} : u32x2{0u, 0u};
                const int q = r >> 2, pp = r & 3;
                ta[p] = lb + L_KS + (8 * g4 + q) * 256 + (((wm * 4 + p * 2 + (pp >> 1)) ^ (2 * (q | ((g4 & 1) << 2)))) << 4) + (pp & 1) * 8;
                th[p] = (ta[p] ^ 128) - 1024;
                TIE(hm[p]); TIE(ta[p]); TIE(th[p]);
#pragma unroll
                for (int j = 0; j < 4; ++j) TIE(kw[p][j]);
            }
            TS(21);
            if (CB0 == 1) b0();
#pragma unroll
            for (int i = 0; i < 4; ++i) asm volatile("" : "=v"(sw[i]), "=v"(qv[2 * i]), "=v"(qv[2 * i + 1]));
            static_for<1, KAT + 1>(hb);
            TS(2);
            u32x2 kt[4][3], vt[4][2];
#pragma unroll
            for (int u = 0; u < 4; ++u) {
                const int p = u >> 1, o = (u & 1) * 8192;
                auto tr = [&](unsigned ad) -> u32x2 { return __builtin_bit_cast(u32x2, __builtin_amdgcn_ds_read_tr16_b64_v4i16(LDSP(v4i16, ad))); };
                kt[u][0] = tr(th[p] + o); kt[u][1] = tr(ta[p] + o); vt[u][0] = tr(ta[p] + o + 16384); kt[u][2] = tr(ta[p] + o + 1024); vt[u][1] = tr(ta[p] + o + 17408);
                SB();
            }
            TS(14);
            __builtin_amdgcn_s_setprio(KPRIO);
            static_for<0, 4>([&](auto uc) {
                constexpr int u = decltype(uc)::value, p = u >> 1, oh = u & 1;
                const u32x2 k0 = oh == 0 && g4 == 0 ? hm[p] : kt[u][0];
                unsigned t0[8], t1[8], e[7];
#define K3(n) do { if constexpr ((KV3 >> (6 * u + n)) & 1) hb(std::integral_constant<int, KAT + 1 + __builtin_popcount(KV3 & ((1 << (6 * u + n)) - 1))>{}); } while (0)
                if constexpr (RX & 1) {
                kf4a(t0, e, k0[0], k0[1], kt[u][1][0], kt[u][1][1], kw[p]); K3(0); kf4b(t0, e, kw[p]); K3(1); kf4c(t0, vt[u][0][0], vt[u][0][1]); K3(2);
                kf4a(t1, e, kt[u][1][0], kt[u][1][1], kt[u][2][0], kt[u][2][1], kw[p]); K3(3); kf4b(t1, e, kw[p]); K3(4); kf4c(t1, vt[u][1][0], vt[u][1][1]);
                } else {
                kv4a(t0, e, k0[0], k0[1], kt[u][1][0], kt[u][1][1], kw[p]); K3(0); kv4b(t0, e, kw[p]); K3(1); kv4c(t0, vt[u][0][0], vt[u][0][1]); K3(2);
                kv4a(t1, e, kt[u][1][0], kt[u][1][1], kt[u][2][0], kt[u][2][1], kw[p]); K3(3); kv4b(t1, e, kw[p]); K3(4); kv4c(t1, vt[u][1][0], vt[u][1][1]);
                }
                kvo[u] = u32x4{t1[2], t1[0], t0[2], t0[0]};
                GST_B128_SC1(l16, kvo[u], kvd + (u >> 1) * 65536, (u & 1) * 1024);
                K3(5);
#undef K3
            });
            hb(std::integral_constant<int, KAT + 1 + __builtin_popcount(KV3)>{});
            __builtin_amdgcn_s_setprio(3);
        }
        TS(3);
        const char* usrc = ws + WS_KVT + (size_t)(xcd * 8 + (idx >> 2)) * 65536 + wm * 16384 + (idx & 3) * 64;
        const unsigned uo = (lane >> 2) * 256 + (lane & 3) * 16;
        const char* cs = ((wm & 2) ? a.cim : a.cre) + (size_t)(tn * 64 + (wm & 1) * 2) * 2048;
        unsigned co[4];
#pragma unroll
        for (int i = 0; i < 4; ++i) {
            const int pair = i * 4 + g4, v = r ^ pair, row = pair * 2 + (v >> 3);
            co[i] = ((row & 15) * 4 + (row >> 4)) * 2048 + (v & 7) * 16 - i * 1024;
        }
        bool kv_ok = false;
#pragma unroll
        for (int it = 0; it < 4; ++it) asm volatile("" : "=v"(uu[it]));
        auto cst = [&](int place, int slice) { const char* s = cs + (slice << 7); DMA(lb + place + wm * 4096, co[0], s); dmao_<1024>(co[1], s); dmao_<2048>(co[2], s); dmao_<3072>(co[3], s); };
        auto items = [&](auto pc) {
            constexpr int p = decltype(pc)::value;
            if constexpr (PU == p) {
                WAIT_V(it_cnt(PU - 1, 5, PU - 1)); TIE(fl);
                kv_ok = __all(fl[0] == tagA && fl[1] == tagB);
                const char* us = kv_ok ? usrc : a.x;
                for (int it = 0; it < 4; ++it) GLD_B128_SC1(uu[it], uo, us + it * 4096, 0);
            }
            if constexpr (PC0 == p) cst(g2_place(0), SLICE(0));
            if constexpr (PC1 == p) cst(g2_place(1), SLICE(1));
            if constexpr (PU == p + 1) GLD_B64_SC1(fl, fpoll, flg + FL_KV, 0);
            if constexpr (PC2 == p) cst(g2_place(5), SLICE(5));
            if constexpr (PC3 == p) cst(g2_place(6), SLICE(6));
        };
#define ITEMS(p) items(std::integral_constant<int, p>{})
        static_assert(KFB > KAT + 1 + __builtin_popcount(KV3) && KV3 < (1 << 23) && KFB <= 13 && KFB <= PC0 && KFB < PU - 1 && nib(PLT, 0) >= KAT && nib(PLT, 3) < PU - 1, "schedule");
        static_for<KAT + 2 + __builtin_popcount(KV3), 15>([&](auto nc) { hb(nc); if constexpr (decltype(nc)::value < 14) items(nc); });
        const unsigned ud = lb + (lane & 3) * CHSZ + UOFF + ((lane >> 2) & 1) * UPL + (16 + wm * 32 + (lane >> 3)) * 16;
        float dz = lane == 0 ? bf2f(dskl) : 0.f;
        if (RX & 2) TIE(dz);
        auto zero = [&]() {
            const u32x4 z4 = {0, 0, 0, 0};
            if (lane < 24) *LDSP(u32x4, lb + L_SY + l16) = z4;
            if (lane < 56) *LDSP(u32x4, lb + L_AGG + wm * 896 + l16) = z4;
            *LDSP(u32x4, lane < 32 ? cb + ((lane >> 3) & 1) * CST + (lane & 7) * 16 : cb + UOFF + (g4 & 1) * UPL + r * 16) = z4;
        };
        auto fill = [&]() {
#pragma unroll
            for (int it = 0; it < 4; ++it) TIE(sw[it]);
            if (RX & 2) sw[0][0] = pack2(bflo(sw[0][0]) + dz, bfhi(sw[0][0]));
#pragma unroll
            for (int it = 0; it < 4; ++it) {
                const unsigned q = cb + 128 + it * 1024 + l16;
                const unsigned d0 = sw[it][0], d1 = sw[it][1], d2 = sw[it][2], d3 = sw[it][3];
                const unsigned o1 = it ? __builtin_amdgcn_readlane(sw[it ? it - 1 : 0][3], 63) : 0u;
                const unsigned x1 = __builtin_amdgcn_update_dpp(o1, d3, 0x138, 0xf, 0xf, false);
                *LDSP(u32x4, q) = sw[it];
                *LDSP(u32x4, q + CST) = u32x4{__builtin_amdgcn_alignbyte(d0, x1, 2), __builtin_amdgcn_alignbyte(d1, d0, 2), __builtin_amdgcn_alignbyte(d2, d1, 2), __builtin_amdgcn_alignbyte(d3, d2, 2)};
            }
        };
        auto uput = [&]() {
            WAIT_V(it_cnt(PU, 1, UE ? 15 : 16));
#pragma unroll
            for (int it = 0; it < 4; ++it) TIE(uu[it]);
            if (!kv_ok) {
                TSE(23);
                for (unsigned spin = 0; spin < SPIN_CAP; ++spin) {
                    GLD_B64_SC1(fl, fpoll, flg + FL_KV, 0);
                    WAIT_V(0); TIE(fl);
                    if (__all(fl[0] == tagA && fl[1] == tagB)) break;
                    __builtin_amdgcn_s_sleep(8);
                }
#pragma unroll
                for (int it = 0; it < 4; ++it) GLD_B128_SC1(uu[it], uo, usrc + it * 4096, 0);
                WAIT_V(0);
#pragma unroll
                for (int it = 0; it < 4; ++it) TIE(uu[it]);
            }
#pragma unroll
            for (int it = 0; it < 4; ++it) *LDSP(u32x4, ud + it * 128) = uu[it];
        };
        static_assert(PU <= (UE ? 15 : 16), "u");
        ITEMS(14);
        if (UE) __builtin_amdgcn_s_setprio(UPRIO);
        if (UE >= 2) zero();
        if (UE == 2) fill();
        BAR();
        ITEMS(15);
        TS(4);
        if (UE < 2) zero();
        if (UE & 1) fill();
        if (UE) uput();
        WAIT_L(0);
        TS(5);
        BAR();
        __builtin_amdgcn_s_setprio(FPRIO);
        ITEMS(16);
        TS(11);
        const unsigned pmine = lb + L_SY + 32 + wm * 4, pall = lb + L_SY + 32 + (lane & 3) * 4;
        if (!UE) { fill(); TS(13); uput(); sy_post<0>(pmine); ITEMS(17); sy_wait<0>(pall); }
        if (!CPOST) { WAIT_V(0); sy_post<1>(pmine); }
        __builtin_amdgcn_s_setprio(CVPRIO);
        TS(6);
        f32x4 ca[8];
        {
            unsigned kb[4];
#pragma unroll
            for (int i = 0; i < 4; ++i) { kb[i] = cb + (~r & 1) * CST + 2 * (64 + 16 + r - 8 * g4 - 7 + (~r & 1)) + i * 1024; TIE(kb[i]); }
            unsigned ub = cb + UOFF + (g4 & 1) * UPL + (15 + r + (g4 >> 1)) * 16 - 64;
#pragma unroll
            for (int i = 0; i < 8; ++i) { ca[i] = f32x4{0, 0, 0, 0}; TIE(ca[i]); }
            s16x4 FA[4][2]; bf16x8 FB[2][8];
#define CV_A(s, o, e) dsr2_<16 * ((s) + ((o) >> 3)) + 128 * (cv_m((o) & 7) & 1) + 2 * (e)>(FA[(o) & 3][e], kb[cv_m((o) & 7) >> 1])
#define CV_B(s, jb) DSR128(FB[(s) & 1][jb], ub, 64 + 256 * (jb) - 32 * (s))
            static_for<0, 8>([&](auto ic) { constexpr int n = decltype(ic)::value; CV_B(0, n); CV_A(0, n >> 1, n & 1); });
            WAIT_L(0);
            auto body = [&]() {
                static_for<0, 72>([&](auto ic) {
                    constexpr int s = decltype(ic)::value / 36, p = decltype(ic)::value % 36, o = cv_grp(p), m = cv_m(o), jb = p - cv_first(o);
                    if constexpr (jb == 0) { WAIT_LB(cv_wait(o)); if constexpr (!(AMF & 1)) { TIE(FA[o & 3][0]); TIE(FA[o & 3][1]); } }
                    if constexpr (p == 0 && !(AMF & 1)) { for (int i = 0; i < 8; ++i) TIE(FB[s][i]); }
                    const bf16x8 fa = __builtin_shufflevector(FA[o & 3][0], FA[o & 3][1], 0, 1, 2, 3, 4, 5, 6, 7);
                    if constexpr (AMF & 1) mfma_(ca[jb + m], fa, FB[s][jb]); else
                    ca[jb + m] = __builtin_amdgcn_mfma_f32_16x16x32_bf16(fa, FB[s][jb], ca[jb + m], 0, 0, 0);
                    SB();
                    constexpr int rd = cv_read(p);
                    if constexpr (rd >= 32) CV_B(s + 1, rd - 32);
                    else if constexpr (rd >= 0) CV_A(s, rd >> 1, rd & 1);
                    SB();
                });
                for (int i = 0; i < 4; ++i) { kb[i] += 128; TIE(kb[i]); }
                ub -= 64;
            };
#pragma unroll 1
            for (int it = 0; it < QIT; ++it) body();
            {
                TS(19);
                if (CPOST) { WAIT_V(0); sy_post<1>(pmine); WAIT_L(0); }
                static_assert(PC3 != 19 || (ZFB == 1 && (BARM & 0x3ff) == 0x220), "stages 5, 6 are covered by the second barrier");
                if (PC3 == 19) { sy_wait<0>(lb + L_ES + (lane & P19M) * 256 + 8); ITEMS(19); }
                if (CVLATE) sy_wait<1>(lb + L_SY + (CVLATE == 2 ? wm + (lane & 1) * 4 : lane & 7) * 4);
                TS(20);
            }
#pragma unroll 1
            for (int it = QIT; it < 4; ++it) body();
            WAIT_L(0);
            if (AMF & 1) asm volatile("s_nop 7\n\ts_nop 7" : "+v"(ca[0]), "+v"(ca[1]), "+v"(ca[2]), "+v"(ca[3]), "+v"(ca[4]), "+v"(ca[5]), "+v"(ca[6]), "+v"(ca[7]));
#pragma unroll
            for (int i = 0; i < 4; ++i) { TIE(FA[i][0]); TIE(FA[i][1]); }
#pragma unroll
            for (int i = 0; i < 8; ++i) TIE(FB[0][i]);
#undef CV_A
#undef CV_B
        }
        TS(7);
        __builtin_amdgcn_s_setprio(3);
        constexpr int NBAR = __builtin_popcount(BARM & ~VBM);
        unsigned zpub = (xcd * 32 + l31) * 256 + idx * 8, zpoll = ZOWN ? (xcd * 32 + idx) * 256 + l31 * 8 : fpoll;
        {
            const float Dd = bf2f(dskl);
            const char* zd = ws + WS_ZT + (size_t)(xcd * 128 + wm * 32) * 4096 + idx * 128;
            u32x2 kvr[8];
#pragma unroll
            for (int jt = 0; jt < 8; ++jt) if (!(RX & 2)) kvr[jt] = *LDSP(u32x2, cb + UOFF + (g4 >> 1) * UPL + (16 + 16 * jt + r) * 16 + (1 - (g4 & 1)) * 8);
#pragma unroll
            for (int jt = 0; jt < 8; ++jt) {
                const f32x4 y = ca[jt];
                const u32x2 kq = kvr[jt];
                *LDSP(u32x2, cb + (jt * 4 + (r >> 2)) * 144 + (r & 3) * 32 + g4 * 8) = (RX & 2) ? u32x2{pack2(y[0], y[1]), pack2(y[2], y[3])} : ZM ?
                    u32x2{pack2(add1(rb1(y[0]), rb1(mul1(bfhi(kq[1]), Dd))), add1(rb1(y[1]), rb1(mul1(bflo(kq[1]), Dd)))), pack2(add1(rb1(y[2]), rb1(mul1(bfhi(kq[0]), Dd))), add1(rb1(y[3]), rb1(mul1(bflo(kq[0]), Dd))))} :
                    u32x2{pack2(rb1(y[0]) + rb1(bfhi(kq[1]) * Dd), rb1(y[1]) + rb1(bflo(kq[1]) * Dd)), pack2(rb1(y[2]) + rb1(bfhi(kq[0]) * Dd), rb1(y[3]) + rb1(bflo(kq[0]) * Dd))};
            }
            unsigned za = lb + (r >> 2) * CHSZ + (wm * 8 + (g4 >> 1)) * 144 + ((g4 & 1) * 2 + ((r >> 1) & 1)) * 32 + (r & 1) * 8, zo = (lane >> 3) * 4096 + (lane & 7) * 16;
            TIE(za); TIE(zo); TIE(zpub); TIE(zpoll);
            sy_post<3>(pmine); sy_wait<3, 0>(pall);
            v4i16 t[4][2];
#pragma unroll
            for (int it = 0; it < 4; ++it)
#pragma unroll
                for (int e = 0; e < 2; ++e) t[it][e] = __builtin_amdgcn_ds_read_tr16_b64_v4i16(LDSP(v4i16, za + it * 288 + e * 16));
#pragma unroll
            for (int it = 0; it < 4; ++it) { TIE(t[it][0]); TIE(t[it][1]); }
            if (ZSB) BAR();
#pragma unroll
            for (int it = 0; it < 4; ++it) { const bf16x8 f = __builtin_shufflevector(t[it][0], t[it][1], 0, 1, 2, 3, 4, 5, 6, 7); GST_B128_SC1(zo, f, zd + it * 32768, 0); }
        }
#pragma unroll
        for (int i = ZSB; i < ZFB; ++i) BAR();
        WAIT_V(0);
        sy_post<2>(pmine);
        if (wm == 0) {
            sy_wait<2>(pall);
            if (ZOWN) { if (lane < 32) GST_B64_SC1(zpub, tag, flg + FL_Z); }
            else if (lane == 0) GST_B64_SC1(fpub, tag, flg + FL_Z);
        }
        TS(8);
        unsigned qm[8][4];
        if (QM) {
#pragma unroll
            for (int n = 0; n < 8; ++n) {
                TIE(qv[n]);
                if (QM == 2) { qm[n][0] = qv[n][0] << 16; qm[n][2] = qv[n][1] << 16; } else { qm[n][0] = qv[n][0] & 0xffffu; qm[n][2] = qv[n][1] & 0xffffu; }
                qm[n][1] = qv[n][0] & 0xffff0000u; qm[n][3] = qv[n][1] & 0xffff0000u;
                TIE(qm[n][0]); TIE(qm[n][1]); TIE(qm[n][2]); TIE(qm[n][3]);
            }
        }
#pragma unroll
        for (int i = ZFB; i < NBAR - 1; ++i) { BAR();
        }
        if (ZSL) __builtin_amdgcn_s_sleep(ZSL);
        asm volatile("s_cmp_lg_u32 %3, 0\n\ts_cbranch_scc1 1f\n\tglobal_load_dwordx2 %0, %1, %2 sc1\n1:" : "=&v"(fl) : "v"(zpoll), "s"(flg + FL_Z), "s"(wm) : "scc", "memory");
        if (NBAR > ZFB) BAR();
        if (wm == 0) {
            unsigned left = SPIN_CAP;
            asm volatile("s_branch 2f\n1:\n\ts_sleep %5\n\tglobal_load_dwordx2 %0, %2, %3 sc1\n2:\n\ts_waitcnt vmcnt(0)\n\tv_cmp_ne_u64 vcc, %4, %0\n\ts_cbranch_vccz 3f\n\ts_sub_u32 %1, %1, 1\n\ts_cbranch_scc0 1b\n3:"
                         : "+v"(fl), "+s"(left) : "v"(zpoll), "s"(flg + FL_Z), "s"(tag), "n"(ZPS) : "vcc", "scc", "memory");
            sy_post<4>(pmine);
        } else sy_wait<4, 2>(lb + L_SY + 32);
        u32x4 zv[4];
        {
            ITEMS(18);
            const char* zsrc = ws + WS_ZT + (size_t)(xcd * 128 + idx * 4 + wm) * 4096;
            static_for<0, 4>([&](auto ic) { constexpr int it = decltype(ic)::value; GLD_B128_SC1(zv[it], (l31 >> 2) * 512 + lh * 64 + (lane & 3) * 16, zsrc, it * 128); });
        }
        WAIT_V(0);
        TS(9);
#pragma unroll
        for (int it = 0; it < 4; ++it) TIE(zv[it]);
        if (!(XAB & 1)) {
        quad_swap<0>(zv[0], zv[1]); quad_swap<0>(zv[2], zv[3]);
        quad_swap<1>(zv[0], zv[2]); quad_swap<1>(zv[1], zv[3]); }
#pragma unroll
        for (int it = 0; it < 4; ++it) TIE(zv[it]);
        char* const oa = a.out + (size_t)(idx * 64 + wm * 16) * 8192 + xcd * 512;
#pragma unroll
        for (int n = 0; n < 8; ++n) {
            if (!QM) TIE(qv[n]);
            const unsigned z0 = zv[n & 3][2 * (n >> 2)], z1 = zv[n & 3][2 * (n >> 2) + 1];
            const f32x4 o = (XAB & 2) ? __builtin_bit_cast(f32x4, zv[n & 3]) : QM == 1 ? mul4bf(u32x2{z0, z1}, qm[n]) :
                QM == 2 && (RX & 4) ? f32x4{mul1(__uint_as_float(qm[n][0]), bflo(z0)), mul1(__uint_as_float(qm[n][1]), bfhi(z0)), mul1(__uint_as_float(qm[n][2]), bflo(z1)), mul1(__uint_as_float(qm[n][3]), bfhi(z1))} :
                QM == 2 ? f32x4{rb1(mul1(__uint_as_float(qm[n][0]), bflo(z0))), rb1(mul1(__uint_as_float(qm[n][1]), bfhi(z0))), rb1(mul1(__uint_as_float(qm[n][2]), bflo(z1))), rb1(mul1(__uint_as_float(qm[n][3]), bfhi(z1)))} :
                f32x4{rb1(bflo(qv[n][0]) * bflo(z0)), rb1(bfhi(qv[n][0]) * bfhi(z0)), rb1(bflo(qv[n][1]) * bflo(z1)), rb1(bfhi(qv[n][1]) * bfhi(z1))};
            if (n == 0 && OSG) for (int i = 0; i < (idx & 3); ++i) __builtin_amdgcn_s_sleep(OSG);
            if (!(XAB & 4))
            GST_B128_NT(lh * 32768 + l31 * 16, o, oa + ((n >> 2) * 8 + (n & 3)) * 8192, 0);
            if (OSL) __builtin_amdgcn_s_sleep(OSL);
        }
        asm volatile(".Lfk_code_a:");
        TSE(10);
        TS_FLUSH();
        return;
    }

    u32x2 fl;
    const unsigned* dv = kvv;
#define G1_ISSUE(st, i) do { if constexpr ((st) < DFN) { if constexpr ((i) > 0) dmaof_<(i) * 1024>(dv[i], g1src + ((((st) << 7) + rot7) & 1920)); else dmaf_(g1dst + g1_slot(st), dv[i], g1src + ((((st) << 7) + rot7) & 1920)); } else if constexpr (M1 && (i) > 0) dmao_<(i) * 1024>(dv[i], g1src + ((((st) << 7) + rot7) & 1920)); \
        else DMA(g1dst + g1_slot(st) + (i) * 1024, dv[i], g1src + ((((st) << 7) + rot7) & 1920)); } while (0)
    unsigned k0 = trow + wm * 32768 + g4 * 2048 + ((l15 ^ (2 * g4)) << 4), k1 = k0 + 7168, k2 = (k0 ^ 128) + 14336, k3 = k2 + 7168;
    auto pre = [&](auto ic) {
        constexpr int n = decltype(ic)::value;
        if constexpr (n == KVP) { if (!HKV) kv_dma(g1dst + L_KS, k0, k1, k2, k3, wn ? a.v : a.k); }
        else static_for<0, 4>([&](auto jc) { G1_ISSUE(n - (n > KVP), decltype(jc)::value); });
    };
    static_for<1, AP0 / 4>(pre);
    TS(14);
    SLICE_TABLE();
    const int j0 = 4 * run - (32 - 2 * tm);
    const int pneg = EALL ? 0 : 16 * (wm >> 1) + 15 - (32 - 2 * tm);
    unsigned eo[2], pes, plr, est, hw[2], ta[2], po, pmine, pall, pown, pzero, g2v[2], cad[2][2], to[2][2], xo, dso, fst, fld, cwa;
    unsigned cta = lb + L_TS + st * 16, ctb = lb + L_TR + run * 1024 + st * 16;
    TIE(cta); TIE(ctb);
    f32x4 ct[5]; f32x2 V1;
    const int gm = PAIR ? wid >> 1 : wm, gk = PAIR ? wid & 1 : wn, gs = gk * 4 + (gm & 2);
    auto addrs = [&]() {
        int ln = lane;
        TIE(ln);
        const int l15 = ln & 15, g4 = ln >> 4, st = sq * 16 + l15, run = (wm >> 1) * 4 + g4, j0 = 4 * run - (32 - 2 * tm);
        pes = lb + L_ES + wid * 256 + l15 * 16;
        plr = lb + L_AGG + st * 8 + run * 512; est = ((tm * 2 + (wm >> 1)) * 1024 + tn * 64 + st) * 16;
#pragma unroll
        for (int i = 0; i < 2; ++i) { eo[i] = (j0 + 2 * i < 0 ? 0 : j0 + 2 * i) * 16384 + (tn * 64 + st) * 16; TIE(eo[i]); }
        TIE(plr); TIE(pes); TIE(est);
        const unsigned ht = lb + L_HT + wid * 4096;
#pragma unroll
        for (int j = 0; j < 2; ++j) {
            const int pr = (8 * (g4 & 1) + 4 * j + (l15 >> 2)) ^ ((g4 & 1) * 4), pw = l15 ^ ((l15 >> 3) * 4);
            ta[j] = ht + (g4 >> 1) * 512 + pr * 32 + ((((l15 >> 1) ^ (pr >> 2)) & 1) << 4) + 8 * (ln & 1);
            hw[j] = ht + g4 * 1024 + pw * 32 + (((j ^ (pw >> 2)) & 1) << 4);
            TIE(ta[j]); TIE(hw[j]);
        }
        po = pes + ((wm & 2) ? -512 : 512);
        pmine = lb + L_SY + wid * 4; pall = lb + L_SY + (ln & 7) * 4; pown = lb + L_SY + (ln < 4 ? 32 + ln * 4 : gs * 4 + (ln & 4)); pzero = lb + L_SY;
        xo = (tm * 128 + gm * 32 + gk * 16 + g4 * 4) * 2048 + tn * 128 + l15 * 8 + 4096; dso = xo & 2047;
        fst = (tm * 16 + l15) * 128 + tn * 8; fld = (tm * 16 + tn) * 128 + l15 * 8;
#pragma unroll
        for (int i = 0; i < 2; ++i) {
            const int pair = i * 4 + g4, v = l15 ^ pair ^ ((wm & 1) * 8);
            g2v[i] = (pair * 2 + (v >> 3)) * 8192 + (v & 7) * 16 + (tn * 64 + wm) * 2048 - i * 1024;
            TIE(g2v[i]);
#pragma unroll
            for (int j = 0; j < 2; ++j) {
                cad[i][j] = lb + L_C + (img_off((g4 >> 1) * 64 + l15, 4 * gk + (g4 & 1)) ^ (i * 32) ^ (j * 128));
                to[i][j] = ta[j] + (i ^ gk) * 1024 + (gm & 1) * 2048 + (gs - wid) * 4096;
                TIE(cad[i][j]); TIE(to[i][j]);
            }
        }
        cwa = lb + wid * 2048 + ln * 16;
        TIE(cwa); TIE(po);
        TIE(pmine); TIE(pall); TIE(pown); TIE(pzero); TIE(xo); TIE(dso); TIE(fst); TIE(fld);
    };
    if (!APRE) addrs();
    unsigned aad[2][2], bad[2];
    {
        const int aoff = img_off((wm >> 1) * 64 + l15, g4), boff = img_off(128 + wn * 64 + (wm & 1) * 16 + l15, g4);
#pragma unroll
        for (int ks = 0; ks < 2; ++ks) { aad[ks][0] = lb + (aoff ^ (ks * 64)); aad[ks][1] = lb + (aoff ^ (ks * 64) ^ 128); bad[ks] = lb + (boff ^ (ks * 64)); TIE(aad[ks][0]); TIE(aad[ks][1]); TIE(bad[ks]); }
    }
    if (!HKV && KVP >= AP0 / 4) { TIE(k0); TIE(k1); TIE(k2); TIE(k3); }
    static_for<AP0 / 4, NBK + 1>(pre);
    TS(1);
    float nL1i, nC1r, nC1i;
    unsigned tagA, tagB;
    f32x4 acc[4][2];
    {
#pragma unroll
        for (int i = 0; i < 4; ++i) { acc[i][0] = f32x4{0, 0, 0, 0}; acc[i][1] = f32x4{0, 0, 0, 0}; }
        bf16x8 fa[2][4], fb[2][2];
        if (GPRIO) __builtin_amdgcn_s_setprio(GPRIO);
        WAIT_V(g1_cnt(0)); BAR();
        TS(2);
#define G1_FRAG(g, n) do { constexpr int bo_ = g1_slot((g) >> 1), hi_ = bo_ & ~65535, x_ = (n) < 3 ? (n) >> 1 : (n) - 2; \
        if constexpr ((n) == 0 || (n) == 2) DSR128(fb[(g) & 1][x_], bad[(g) & 1] + hi_, bo_ - hi_ + x_ * 4096); \
        else DSR128(fa[(g) & 1][x_], aad[(g) & 1][x_ & 1] + hi_, bo_ - hi_ + x_ * 2048); } while (0)
        static_for<0, 6>([&](auto ic) { constexpr int n = decltype(ic)::value; G1_FRAG(0, n); });
        static_for<0, 32>([&](auto ic) {
            constexpr int g = decltype(ic)::value, gn = g + 1, n0 = g1_upto(g - 1), nd = g1_upto(g) - n0;
            constexpr bool bar = (gn & 1) == 0 && gn < 32;
            if constexpr (g == 1) {
                DSR128(ct[0], cta, 0); DSR128(ct[1], cta, 1024); DSR128(ct[2], cta, 2048); DSR128(ct[3], ctb, 0); DSR128(ct[4], ctb, 8192); DSR64(V1, ctb, 16384);
            }
            if constexpr (g == 3) {
#pragma unroll
                for (int i = 0; i < 5; ++i) TIE(ct[i]);
                TIE(V1);
                nL1i = -ct[0][1]; nC1r = -ct[0][2]; nC1i = -ct[0][3];
                TIE(nL1i); TIE(nC1r); TIE(nC1i);
                tagA = __builtin_amdgcn_readfirstlane(__float_as_uint(ct[2][2])); tagB = __builtin_amdgcn_readfirstlane(__float_as_uint(ct[2][3]));
                ties_(tagA); ties_(tagB);
            }
            if constexpr (bar) {
                static_assert(g1_cnt(gn >> 1) >= 0, "schedule");
                WAIT_VB(g1_cnt(gn >> 1)); WAIT_LB(0); BAR();
                if constexpr (((gn >> 1) & 3) == 0 || gn >= 26) TS(gn >= 26 ? 23 + (gn >> 1) - 13 : 16 + (gn >> 3));
#pragma unroll
                for (int i = 0; i < 4; ++i) if (!(AMF & 2)) TIE(fa[g & 1][i]);
                if (!(AMF & 2)) { TIE(fb[g & 1][0]); TIE(fb[g & 1][1]); }
            }
            static_for<0, 8>([&](auto mc) {
                constexpr int m = decltype(mc)::value, i = m >> 1, j = m & 1, js = (SNK & 1) && (i & 1) ? 1 - j : j;
                if constexpr (W1 && !bar && g > 0 && g < 31) {
                    if constexpr (m == 0) WAIT_LB(W1 == 1 ? 3 : 2);
                    if constexpr (m == 2 && W1 == 1) WAIT_LB(4);
                    if constexpr (m == 4) WAIT_LB(4);
                } else
                if constexpr (!bar && (i == 0 || j == 0)) {
                    WAIT_LB(5 - (i == 0 ? j + 1 : i + 2) + (gn < 32 ? (m < 6 ? m : 6) : 0));
                    if constexpr (!(AMF & 2) || g == 0) { TIE(fa[g & 1][i]); TIE(fb[g & 1][j]); }
                }
                if constexpr ((AMF & 2) && g > 0) mfma_(acc[i][js], fa[g & 1][i], fb[g & 1][js]); else
                acc[i][j] = __builtin_amdgcn_mfma_f32_16x16x32_bf16(fa[g & 1][i], fb[g & 1][j], acc[i][j], 0, 0, 0);
                SB();
                if constexpr (gn < 32 && m < 6) G1_FRAG(gn, m);
                constexpr int d = nd > 2 ? ((m & 1) ? m >> 1 : -1) : (m == DP0 ? 0 : m == DP1 ? 1 : -1);
                if constexpr (d >= 0 && d < nd) G1_ISSUE((n0 + d) >> 2, (n0 + d) & 3);
                SB();
            });
        });
#undef G1_FRAG
    }
    TS(3);
    const unsigned tagG = tagA ^ tagB ^ 0x9E3779B9u;
    __builtin_amdgcn_s_setprio(MPRIO);
    f32x2 ur[8], ui[8], hr, hi;
    const f32x2 L1 = {ct[0][0], ct[0][1]}, C1 = {ct[0][2], ct[0][3]}, L16 = {ct[1][2], ct[1][3]}, Q2r = {ct[3][0], ct[3][1]}, Q2i = {ct[3][2], ct[3][3]};
    const float L8r = ct[1][0], L8i = ct[1][1], L64r = ct[2][0], L64i = ct[2][1];
    u32x4 eg[4], cr[2 * NR + 1];
    const char* g2src = wn ? a.cim : a.cre;
#define CR_LOAD(n0, n1) static_for<n0, n1>([&](auto ic) { constexpr int n = decltype(ic)::value; GLD_B128(cr[n], g2v[n & 1], g2src + (SLICE(7 + (n >> 1)) << 7), (n & 1) * 1024); })
#define E_LOADS "global_load_dwordx4 v[104:107], %[a0], %[b0] sc1\n\tglobal_load_dwordx4 v[108:111], %[a0], %[b1] sc1\n\t" \
                "global_load_dwordx4 v[112:115], %[a1], %[b0] sc1\n\tglobal_load_dwordx4 v[116:119], %[a1], %[b1] sc1\n"
#define E_POLL() asm volatile("s_cmp_lt_i32 %[on], 0\n\ts_cbranch_scc1 1f\n\t" E_LOADS "1:" : "={v[104:107]}"(eg[0]), "={v[108:111]}"(eg[1]), "={v[112:115]}"(eg[2]), "={v[116:119]}"(eg[3]) \
        : [a0] "v"(eo[0]), [a1] "v"(eo[1]), [b0] "s"(ws + WS_E), [b1] "s"(ws + WS_E + 16384), [on] "s"(pneg) : "scc", "memory")
    {
        asm volatile("s_nop 7\n\ts_nop 3" : "+v"(acc[0][0]), "+v"(acc[0][1]), "+v"(acc[1][0]), "+v"(acc[1][1]), "+v"(acc[2][0]), "+v"(acc[2][1]), "+v"(acc[3][0]), "+v"(acc[3][1]));
#pragma unroll
        for (int p = 0; p < 8; ++p) { ur[p] = f32x2{acc[p >> 1][0][2 * (p & 1)], acc[p >> 1][0][2 * (p & 1) + 1]}; ui[p] = f32x2{acc[p >> 1][1][2 * (p & 1)], acc[p >> 1][1][2 * (p & 1) + 1]}; }
        f32x2 gr = ur[0], gi = ui[0];
#pragma unroll
        for (int p = 1; p < 8; ++p) { f32x2 nr, ni; if (PLN & 1) c2_mad(nr, ni, L1[0], nL1i, L1[0], L1[1], gr, gi, ur[p], ui[p]); else pk_cmad(nr, ni, L1, gr, gi, ur[p], ui[p]); gr = nr; gi = ni; }
        float fr, fi;
        cmad(fr, fi, L8r, L8i, gr[0], gi[0], gr[1], gi[1]);
        {
            const f32x2 w = cmul4(V1, f32x2{fr, fi});
            const float wr = sum4(w[0]), wi = sum4(w[1]);
            const u32x4 gq = {__float_as_uint(wr), tagA, __float_as_uint(wi), tagG};
            if (EREP ? (EALL || g4 >= (tm >> 2)) : g4 == 0) GST_B128_SC1(est, gq, ws + WS_E, 0);
        }
        *LDSP(f32x2, plr + 7 * 512) = f32x2{fr, fi};
        *LDSP(unsigned, pes + 8) = 0;
        TS(21);
        if (EP == 2) E_POLL();
        auto coef = [&]() {
#pragma unroll
            for (int p = 0; p < 8; ++p) { f32x2 nr, ni; if (PLN & 2) c2_mul(nr, ni, C1[0], nC1i, nC1r, nC1i, ur[p], ui[p]); else pk_conj_mul(nr, ni, C1, ur[p], ui[p]); ur[p] = nr; ui[p] = ni; TIE(ur[p]); TIE(ui[p]); }
        };
        if (CFB) coef();
        WAIT_L(0); BAR();
        f32x2 e[7];
#pragma unroll
        for (int j = 0; j < 7; ++j) e[j] = *LDSP(f32x2, plr + j * 512);
        if (EP == 1) E_POLL();
        CR_LOAD(0, CR1);
        if (!CFB) coef();
#pragma unroll
        for (int j = 1; j < 7; ++j) { if (PLN & 4) { float xr = e[j][0], xi = e[j][1]; cmac(xr, xi, L16[0], L16[1], e[j - 1][0], e[j - 1][1]); e[j] = f32x2{xr, xi}; } else cmac2(e[j], L16, e[j - 1]); }
        float mr, mi;
        cmad(mr, mi, L8r, L8i, e[6][0], e[6][1], gr[0], gi[0]);
        if (PLN & 4) c2_mul(hr, hi, C1[0], nC1i, nC1r, nC1i, f32x2{e[6][0], mr}, f32x2{e[6][1], mi}); else pk_conj_mul(hr, hi, C1, f32x2{e[6][0], mr}, f32x2{e[6][1], mi});
        TIE(hr); TIE(hi);
        CR_LOAD(CR1, CR1 + CR2);
        TS(4);
    }
    f32x2 G;
    {
        float pr = 0.f, pi = 0.f;
        if (!EP) E_POLL();
        if (pneg >= 0) {
            unsigned t0, t1, t2, left = SPIN_CAP;
            asm volatile("s_waitcnt vmcnt(%[c])\n\ts_branch 2f\n1:\n\ts_sleep %[sl]\n\t" E_LOADS "\ts_waitcnt vmcnt(0)\n2:\n\t"
                         "v_bitop3_b32 %[t0], v105, v109, %[ta] bitop3:0x7e\n\tv_bitop3_b32 %[t1], v113, v117, %[ta] bitop3:0x7e\n\tv_bitop3_b32 %[t2], v107, v111, %[tg] bitop3:0x7e\n\t"
                         "v_or3_b32 %[t0], %[t0], %[t1], %[t2]\n\tv_bitop3_b32 %[t1], v115, v119, %[tg] bitop3:0x7e\n\tv_or_b32 %[t0], %[t0], %[t1]\n\t"
                         "v_cmp_ne_u32 vcc, 0, %[t0]\n\ts_cbranch_vccz 3f\n\ts_sub_u32 %[n], %[n], 1\n\ts_cbranch_scc0 1b\n3:"
                         : "+{v[104:107]}"(eg[0]), "+{v[108:111]}"(eg[1]), "+{v[112:115]}"(eg[2]), "+{v[116:119]}"(eg[3]), [t0] "=&v"(t0), [t1] "=&v"(t1), [t2] "=&v"(t2), [n] "+s"(left)
                         : [a0] "v"(eo[0]), [a1] "v"(eo[1]), [b0] "s"(ws + WS_E), [b1] "s"(ws + WS_E + 16384), [ta] "s"(tagA), [tg] "s"(tagG), [sl] "n"(ESL), [c] "n"(CR1 + CR2) : "vcc", "scc", "memory");
            float ar = __uint_as_float(eg[1][0]), ai = __uint_as_float(eg[1][2]), br = __uint_as_float(eg[3][0]), bi = __uint_as_float(eg[3][2]);
            cmac(ar, ai, L64r, L64i, __uint_as_float(eg[0][0]), __uint_as_float(eg[0][2]));
            cmac(br, bi, L64r, L64i, __uint_as_float(eg[2][0]), __uint_as_float(eg[2][2]));
            if (EALL) { if (j0 < 0) ar = ai = 0.f; if (j0 + 2 < 0) br = bi = 0.f; }
            const f32x2 pu = cmul4(f32x2{ct[4][2], ct[4][3]}, f32x2{ar, ai});
            pr = pu[0]; pi = pu[1];
            cmac(pr, pi, ct[4][0], ct[4][1], br, bi);
            pr = sum4(pr); pi = sum4(pi);
        }
        TS(5);
        *LDSP(u32x4, pes) = u32x4{__float_as_uint(pr), __float_as_uint(pi), 1u, 0u};
        CR_LOAD(CR1 + CR2, CR1 + CR2 + CR3);
        u32x4 o;
        asm volatile("1:\n\tds_read_b128 %0, %1\n\ts_waitcnt lgkmcnt(0)\n\tv_cmp_eq_u32 vcc, 0, v122\n\ts_cbranch_vccnz 1b" : "=&{v[120:123]}"(o) : "v"(po) : "vcc", "memory");
        G = f32x2{pr + __uint_as_float(o[0]), pi + __uint_as_float(o[1])};
    }
    TS(6);
    {
        if (PLN & 8) c2_mac(hr, hi, G[0], -G[1], -G[0], -G[1], Q2r, Q2i); else pk_conj_mac(hr, hi, G, Q2r, Q2i);
#pragma unroll
        for (int p = 0; p < 8; ++p) { if (PLN & 8) c2_mac(ur[p], ui[p], L1[0], L1[1], L1[0], nL1i, hr, hi); else pk_cmac_conj(ur[p], ui[p], L1, hr, hi); hr = ur[p]; hi = ui[p]; }
        const char* hd = ws + WS_H + (size_t)((tm * 4 + (wm & 2)) * 2 + wn) * HP + (tn * 4 + (wm & 1) * 2) * 1024;
#pragma unroll
        for (int e2 = 0; e2 < 2; ++e2)
#pragma unroll
            for (int c = 0; c < 2; ++c) {
                const f32x2* o = c ? ui : ur;
                *LDSP(u32x4, hw[e2] + c * 512) = u32x4{pack2(o[0][e2], o[1][e2]), pack2(o[2][e2], o[3][e2]), pack2(o[4][e2], o[5][e2]), pack2(o[6][e2], o[7][e2])};
            }
        v4i16 t[4][2];
#pragma unroll
        for (int n = 0; n < 4; ++n)
#pragma unroll
            for (int j = 0; j < 2; ++j) t[n][j] = __builtin_amdgcn_ds_read_tr16_b64_v4i16(LDSP(v4i16, ta[j] + n * 1024));
        sy_post<1>(pmine);
        static_for<0, 4>([&](auto ic) {
            constexpr int n = decltype(ic)::value;
            const bf16x8 f = __builtin_shufflevector(t[n][0], t[n][1], 0, 1, 2, 3, 4, 5, 6, 7);
            GST_B128_SC1(l16, f, hd + (n >> 1) * 2 * HP + ((n & 1) ^ wn) * 1024, 0);
        });
    }
    TS(7);
    bf16x8 fa2[64];
    u32x2 xv[4], dsv;
    const char* Aw = ws + WS_H + (size_t)((tm * 4 + gm) * 2 + gk) * HP;
    const unsigned g2dst = lb + wid * 2048;
#define G2_ISSUE(st, i) do { if constexpr (i) dmao_<1024>(g2v[1], g2src + (SLICE(st) << 7)); else DMA(g2dst + g2_place(st), g2v[0], g2src + (SLICE(st) << 7)); } while (0)
#define G2_A(g) GLD_B128_SC1(fa2[g], l16, Aw + (SLICE((g) >> 2) << 12), ((g) & 3) * 1024)
    {
        {
            sy_wait<1>(pown);
            s16x4 fo[4][2]; bf16x8 c[8];
            static_for<0, 2>([&](auto kc) {
                constexpr int ks = decltype(kc)::value;
                static_for<0, 4>([&](auto ic) { constexpr int i = decltype(ic)::value; dsrtr_<ks * 4096>(fo[2 * ks + (i >> 1)][i & 1], to[i >> 1][i & 1]); });
                static_for<0, 4>([&](auto ic) { constexpr int i = decltype(ic)::value; DSR128(c[4 * ks + i], cad[ks][i & 1], 2 * G2STAGE + i * 2048); });
            });
            static_for<0, 8>([&](auto ic) {
                constexpr int n = decltype(ic)::value, ks = n >> 2, i = n & 3;
                WAIT_L(ks ? 3 - i : 11 - i);
                TIE(c[n]);
                if constexpr (i == 0) { TIE(fo[2 * ks][0]); TIE(fo[2 * ks][1]); TIE(fo[2 * ks + 1][0]); TIE(fo[2 * ks + 1][1]); }
#pragma unroll
                for (int j = 0; j < 2; ++j)
                    acc[i][j] = __builtin_amdgcn_mfma_f32_16x16x32_bf16(__builtin_shufflevector(fo[2 * ks + j][0], fo[2 * ks + j][1], 0, 1, 2, 3, 4, 5, 6, 7), c[n], ks ? acc[i][j] : f32x4{0, 0, 0, 0}, 0, 0, 0);
                SB();
            });
#pragma unroll
            for (int i = 0; i < 4; ++i) { TIE(acc[i][0]); TIE(acc[i][1]); }
        }
        if (CEARLY) sy_post<0>(pmine);
        static_for<0, 4>([&](auto ic) { constexpr int it = decltype(ic)::value; GLD_B64(xv[it], xo, a.x, it * 2048 - 4096); });
        GLD_B64(dsv, dso, a.dskip, 0);
        if (CEARLY) {
            sy_wait<0>(pall);
            static_for<4, 4 + CEARLY>([&](auto ic) { constexpr int n = decltype(ic)::value; G2_ISSUE(n >> 1, n & 1); });
        }
        TS(16);
        WAIT_V(5 + CEARLY);
        sy_post<2>(pmine);
        if (!CEARLY || wid == (NOOWN ? 1 : 0) || (NOOWN == 1 && wid == 0)) sy_wait<2, 0>(pall);
        const u32x2 tagv = {__float_as_uint(ct[2][2]), __float_as_uint(ct[2][3])};
        if (wid == (NOOWN ? 1 : 0)) asm volatile("s_mov_b64 exec, 0xffff\n\tglobal_store_dwordx2 %0, %1, %2 sc1\n\ts_mov_b64 exec, -1" :: "v"(fst), "v"(tagv), "s"(flg + FL_H) : "memory");
        if (PFIRST) asm volatile("s_cmp_lg_u32 %3, 0\n\ts_cbranch_scc1 1f\n\tglobal_load_dwordx2 %0, %1, %2 sc1\n1:" : "=&v"(fl) : "v"(fld), "s"(flg + FL_H), "s"(wid) : "scc", "memory");
        TS(8);
        static_for<4 + CEARLY, 10>([&](auto ic) { constexpr int n = decltype(ic)::value; G2_ISSUE(n >> 1, n & 1); });
        CR_LOAD(2 * NR - CRL, 2 * NR);
        TS(13);
        if (wid == 0) {
            unsigned left = SPIN_CAP;
            if (PFIRST)
            asm volatile("s_waitcnt vmcnt(6)\n\ts_branch 3f\n1:\n\tglobal_load_dwordx2 %0, %2, %3 sc1\n\ts_waitcnt vmcnt(0)\n3:\n\tv_cmp_ne_u64 vcc, %0, %4\n\ts_cbranch_vccz 2f\n\ts_sub_u32 %1, %1, 1\n\ts_cbranch_scc0 1b\n2:"
                         : "+v"(fl), "+s"(left) : "v"(fld), "s"(flg + FL_H), "v"(tagv) : "vcc", "scc", "memory");
            else
            asm volatile("1:\n\tglobal_load_dwordx2 %0, %2, %3 sc1\n\ts_waitcnt vmcnt(0)\n\tv_cmp_ne_u64 vcc, %0, %4\n\ts_cbranch_vccz 2f\n\ts_sub_u32 %1, %1, 1\n\ts_cbranch_scc0 1b\n2:"
                         : "=&v"(fl), "+s"(left) : "v"(fld), "s"(flg + FL_H), "v"(tagv) : "vcc", "scc", "memory");
            sy_post<3>(pmine);
            if (b == 255 && lane == 0) GST_B64_SC1(0u * lane, tagv, flg + FL_EPOCH);
        } else if (g2_bar(3)) sy_wait<3>(pzero);
        else { WAIT_V(CRL); sy_post<3>(pmine); }
        if (!g2_bar(3)) sy_wait<3>(pall);
        TS(20);
        static_for<4, 4 + AW>([&](auto ic) { constexpr int g = decltype(ic)::value; G2_A(g); });
    }
    TS(9);
    __builtin_amdgcn_s_setprio(0);
    unsigned yw, yr, yp, yo, lzw;
    static_assert(PAIR, "yr");
    unsigned dm[4];
    auto dmask = [&]() { TIE(dsv); dm[0] = dsv[0] & 0xffffu; dm[1] = dsv[0] & 0xffff0000u; dm[2] = dsv[1] & 0xffffu; dm[3] = dsv[1] & 0xffff0000u; TIE(dm[0]); TIE(dm[1]); TIE(dm[2]); TIE(dm[3]); };
    auto skip = [&](int it) { TIE(xv[it]); const f32x4 y = mul4bf(xv[it], dm); for (int i = 0; i < 4; ++i) acc[i][0][it] = add1(acc[i][0][it], y[i]); if (it & 1) { for (int i = 0; i < 4; ++i) TIE(acc[i][0]); } };
    {
        bf16x8 fb[2][4];
#define G2_FRAG(g, i) do { constexpr int pl_ = g2_place((g) >> 1), b_ = pl_ >= L_C ? L_C : pl_ >= 32768 ? 32768 : 0; DSR128(fb[(g) & 1][i], cad[(g) & 1][(i) & 1] + (b_ - L_C), pl_ - b_ + (i) * 2048); } while (0)
        static_for<0, 4>([&](auto ic) { constexpr int i = decltype(ic)::value; G2_FRAG(2, i); });
        if (YS == 0) { WAIT_V(AW + 6 + CRL); dmask(); skip(0); skip(1); skip(2); skip(3); }
        static_for<2, 32>([&](auto ic) {
            constexpr int g = decltype(ic)::value, gn = g + 1;
            constexpr bool bar = g2_bar(g) && !((VBM >> g) & 1);
            constexpr int n0 = g2_upto(g - 1), n1 = g2_upto(g);
            if constexpr (bar) {
                constexpr int ca = g2_cnt(2 * g, 2 * g), cs = g2_cover(g) >= 7 + NR && !(LZ && g == LASTB) ? g2_cnt(100 + 2 * g2_cover(g) + 1, 2 * g) : 63;
                static_assert(cs >= 0 && g2_wr(g - 1) >= 2 * (g2_cover(g) < 6 + NR ? g2_cover(g) - 6 : NR), "schedule");
                WAIT_VB(AWB && ca < cs ? ca : cs); WAIT_LB(0); BAR();
                if constexpr (!AWB) WAIT_V(ca);
#pragma unroll
                for (int i = 0; i < 4; ++i) if (!(AMF & 4)) TIE(fb[g & 1][i]);
                if (!(AMF & 4)) TIE(fa2[2 * g]);
            }
            if constexpr (LZ && g == lz_chk()) { WAIT_LB(0); sy_chk<0>(lzw, pall); }
            constexpr int xl = g2_wr(g - 1) - g2_wr(g - 2) + (g - 1 == EGX) + (LZ && g - 1 == lz_post());
            static_for<0, 8>([&](auto mc) {
                constexpr int m = decltype(mc)::value, i = m & 3, j = m >> 2, f = 2 * g + j;
                static_assert(g2_cnt(f, f) >= 0, "schedule");
                constexpr int jj = LSW && g == 31 ? 1 - j : j, ff = 2 * g + jj;
                if constexpr (i == 0 && (j || !bar)) { WAIT_VB(LSW && g == 31 ? 0 : g2_cnt(f, f)); if constexpr (!(AMF & 4) || g == 2 || g == 31) TIE(fa2[ff]); }
                if constexpr (W2 && g > 2 && g < 31) { if constexpr (!bar && j == 0 && (i & W2) == 0) WAIT_LB(3 - W2 + xl); } else
                if constexpr (!bar && j == 0) { WAIT_LB((gn < 32 ? 3 : 3 - i) + xl); if constexpr (!(AMF & 4) || g == 2 || g == 31) TIE(fb[g & 1][i]); }
                constexpr int is = (SNK & 2) && j ? 3 - i : i;
                if constexpr ((AMF & 4) && g > 2 && g < 31) mfma_(acc[is][jj], fa2[ff], fb[g & 1][is]); else
                acc[i][jj] = __builtin_amdgcn_mfma_f32_16x16x32_bf16(fa2[ff], fb[g & 1][i], acc[i][jj], 0, 0, 0);
                SB();
                if constexpr (LSW && g == 31 && j) *LDSP(f32x4, yw + i * 1024) = acc[i][1];
                if constexpr (g == EGX && m == 4) sy_post<5>(pmine);
                if constexpr (LZ && g == lz_post() && m == 4) sy_post<0>(pmine);
                if constexpr (LZ && g + 1 == lz_chk() && m == 4) sy_pre<0>(lzw, pall);
                if constexpr (g == 29 && m == 5) { yw = cwa + L_Y + wid * 2048; yr = yw ^ (PAIR ? 4096 : 16384); yp = pmine ^ (PAIR ? 4 : 16); yo = (cwa & 255) + ((cwa & 768) << 7); TIE(yw); TIE(yr); TIE(yp); TIE(yo); }
                if constexpr (gn < 32 && m < 4) G2_FRAG(gn, m);
                if constexpr (m >= 4 && g2_wr(g - 1) + m - 4 < g2_wr(g)) {
                    constexpr int n = g2_wr(g - 1) + m - 4, pl = g2_place(7 + (n >> 1)), pb = pl >= L_C ? L_C : 0;
                    asm volatile("ds_write_b128 %0, %1 offset:%2" :: "v"(cwa + pb), "v"(cr[n]), "n"(pl - pb + (n & 1) * 1024) : "memory");
                }
                if constexpr (i == 1 && n0 + j < n1) G2_ISSUE((n0 + j) >> 1, (n0 + j) & 1);
                if constexpr (i >= 2 && g2_ald(f, i & 1) >= 0) G2_A(g2_ald(f, i & 1));
                SB();
            });
        });
#undef G2_FRAG
    }
    TS(11);
    if (EPRIO) __builtin_amdgcn_s_setprio(EPRIO);
    {
#pragma unroll
        for (int i = 0; i < 4; ++i) if (!LSW) *LDSP(f32x4, yw + i * 1024) = acc[i][1];
        sy_post<4>(pmine);
        sy_wait<4, 0>(yp);
        char* const ob = a.out + (size_t)(tm * 128 + gm * 32 + gk * 16) * 8192 + 4096 + tn * 256;
        f32x4 w[4];
#pragma unroll
        for (int i = 0; i < 4; ++i) w[i] = *LDSP(f32x4, yr + i * 1024);
        f32x4 o[4];
#pragma unroll
        for (int r = 0; r < 4; ++r) { o[r] = f32x4{add1(w[0][r], acc[0][0][r]), add1(w[1][r], acc[1][0][r]), add1(w[2][r], acc[2][0][r]), add1(w[3][r], acc[3][0][r])}; if (EGX) TIE(o[r]); }
        if (EGX && !wn) sy_wait<5, 1>(pall);
#pragma unroll
        for (int r = 0; r < 4; ++r) GST_B128_NT(yo, o[r], ob + r * 8192, 0);
    }
    asm volatile(".Lfk_code_b:");
    TSE(12);
    TS_FLUSH();
}

}

extern "C" void kernel_launch(void* const* d_in, const int* in_sizes, int n_in, void* d_out, int out_size, void* d_ws, size_t ws_size,
                              hipStream_t stream) {
    (void)in_sizes; (void)n_in; (void)out_size; (void)ws_size;
    fk::Args a;
    a.q = (const char*)d_in[0]; a.k = (const char*)d_in[1]; a.v = (const char*)d_in[2]; a.sk = (const char*)d_in[3];
    a.ssmk = (const char*)d_in[4]; a.ssmD = (const char*)d_in[5]; a.x = (const char*)d_in[6]; a.lre = (const char*)d_in[7];
    a.lim = (const char*)d_in[8]; a.bre = (const char*)d_in[9]; a.bim = (const char*)d_in[10]; a.cre = (const char*)d_in[11];
    a.cim = (const char*)d_in[12]; a.logdt = (const char*)d_in[13]; a.dskip = (const char*)d_in[14];
    a.out = (char*)d_out; a.ws = (char*)d_ws;
    hipLaunchKernelGGL(fk::fused, dim3(256), dim3(768), 0, stream, a);
}
```

```cpp
#include <hip/hip_runtime.h>
#include <stdint.h>
#include <type_traits>

#define ZM 0
#define RX 3
#define P19M 7
#define QSL 2
#define ZP_ 15
#define FP_ 16
#define P14 3
#define P15 3
#define P16 3
#define S16 0
#define OPRIO 3
#define OS0 0
#define OSW 0
#define ZE -1
#define EM 2
#define EM_ON "s_mov_b64 exec, vcc\n\t"
#define EM_OFF "\ts_mov_b64 exec, -1\n"
#define EMD 0
#define EMOD "sc1"
#define TCH 0
#define TCB 10
#define SQ 1
#define SN 2
#define SNK 0
namespace fk {

constexpr int S = 2048, D = 1024, P = 1024;

typedef __attribute__((ext_vector_type(8))) short bf16x8;
typedef __attribute__((ext_vector_type(4))) short s16x4;
typedef __attribute__((ext_vector_type(16))) float f32x16;
typedef __attribute__((ext_vector_type(4))) float f32x4;
typedef __attribute__((ext_vector_type(2))) float f32x2;
typedef __attribute__((ext_vector_type(4))) unsigned u32x4;
typedef __attribute__((ext_vector_type(2))) unsigned u32x2;
#define LDSP(T, a) ((__attribute__((address_space(3))) T*)(uintptr_t)(unsigned)(a))

__device__ __forceinline__ float bf2f(unsigned v) { return __uint_as_float(v << 16); }
__device__ __forceinline__ float bflo(unsigned w) { return __uint_as_float(w << 16); }
__device__ __forceinline__ float bfhi(unsigned w) { return __uint_as_float(w & 0xffff0000u); }
__device__ __forceinline__ uint16_t f2bf(float f) { __bf16 b = (__bf16)f; return __builtin_bit_cast(uint16_t, b); }
__device__ __forceinline__ float rbf(float f) { return bf2f(f2bf(f)); }
typedef __attribute__((ext_vector_type(2))) __bf16 bf16x2;
__device__ __forceinline__ unsigned pack2(float lo, float hi) { return __builtin_bit_cast(unsigned, __builtin_convertvector(f32x2{lo, hi}, bf16x2)); }
__device__ __forceinline__ float rb1(float x) { return __uint_as_float(pack2(0.f, x)); }
__device__ __forceinline__ void cmac(float& xr, float& xi, float ar, float ai, float hr, float hi) {
    asm("v_fmac_f32 %0, %2, %4\n\tv_fmac_f32 %1, %2, %5\n\tv_fma_f32 %0, -%3, %5, %0\n\tv_fmac_f32 %1, %3, %4" : "+v"(xr), "+v"(xi) : "v"(ar), "v"(ai), "v"(hr), "v"(hi));
}
__device__ __forceinline__ void cmad(float& o_r, float& o_i, float ar, float ai, float hr, float hi, float er, float ei) {
    asm("v_fma_f32 %0, %2, %4, %6\n\tv_fma_f32 %1, %2, %5, %7\n\tv_fma_f32 %0, -%3, %5, %0\n\tv_fmac_f32 %1, %3, %4" : "=&v"(o_r), "=&v"(o_i) : "v"(ar), "v"(ai), "v"(hr), "v"(hi), "v"(er), "v"(ei));
}
__device__ __forceinline__ f32x2 cmul4(f32x2 a, f32x2 h) {
    float o_r, o_i;
    asm("v_mul_f32 %0, %2, %4\n\tv_mul_f32 %1, %2, %5\n\tv_fma_f32 %0, -%3, %5, %0\n\tv_fmac_f32 %1, %3, %4" : "=&v"(o_r), "=&v"(o_i) : "v"(a[0]), "v"(a[1]), "v"(h[0]), "v"(h[1]));
    return f32x2{o_r, o_i};
}

#define PK_LO " op_sel:[0,0,0] op_sel_hi:[0,1,1]"
#define PK_HI " op_sel:[1,0,0] op_sel_hi:[1,1,1]"
#define PK_NEG " neg_lo:[1,0,0] neg_hi:[1,0,0]"
__device__ __forceinline__ void pk_cmad(f32x2& o_r, f32x2& o_i, f32x2 a, f32x2 hr, f32x2 hi, f32x2 er, f32x2 ei) {
    asm("v_pk_fma_f32 %0, %2, %3, %5" PK_LO "\n\tv_pk_fma_f32 %1, %2, %4, %6" PK_LO "\n\tv_pk_fma_f32 %0, %2, %4, %0" PK_HI PK_NEG "\n\tv_pk_fma_f32 %1, %2, %3, %1" PK_HI
        : "=&v"(o_r), "=&v"(o_i) : "v"(a), "v"(hr), "v"(hi), "v"(er), "v"(ei));
}
__device__ __forceinline__ void pk_cmac_conj(f32x2& xr, f32x2& xi, f32x2 a, f32x2 hr, f32x2 hi) {
    asm("v_pk_fma_f32 %0, %2, %3, %0" PK_LO "\n\tv_pk_fma_f32 %1, %2, %4, %1" PK_LO "\n\tv_pk_fma_f32 %0, %2, %4, %0" PK_HI "\n\tv_pk_fma_f32 %1, %2, %3, %1" PK_HI PK_NEG
        : "+v"(xr), "+v"(xi) : "v"(a), "v"(hr), "v"(hi));
}
__device__ __forceinline__ void pk_conj_mul(f32x2& o_r, f32x2& o_i, f32x2 a, f32x2 hr, f32x2 hi) {
    asm("v_pk_mul_f32 %0, %2, %3 op_sel:[0,0] op_sel_hi:[0,1]\n\tv_pk_mul_f32 %1, %2, %4 op_sel:[0,0] op_sel_hi:[0,1] neg_lo:[1,0] neg_hi:[1,0]\n\t"
        "v_pk_fma_f32 %0, %2, %4, %0" PK_HI PK_NEG "\n\tv_pk_fma_f32 %1, %2, %3, %1" PK_HI PK_NEG : "=&v"(o_r), "=&v"(o_i) : "v"(a), "v"(hr), "v"(hi));
}
__device__ __forceinline__ void pk_conj_mac(f32x2& xr, f32x2& xi, f32x2 a, f32x2 hr, f32x2 hi) {
    asm("v_pk_fma_f32 %0, %2, %3, %0" PK_LO "\n\tv_pk_fma_f32 %1, %2, %4, %1" PK_LO PK_NEG "\n\tv_pk_fma_f32 %0, %2, %4, %0" PK_HI PK_NEG "\n\tv_pk_fma_f32 %1, %2, %3, %1" PK_HI PK_NEG
        : "+v"(xr), "+v"(xi) : "v"(a), "v"(hr), "v"(hi));
}
__device__ __forceinline__ void cmac2(f32x2& x, f32x2 a, f32x2 h) {
    asm("v_pk_fma_f32 %0, %1, %2, %0" PK_LO "\n\tv_pk_fma_f32 %0, %1, %2, %0 op_sel:[1,1,0] op_sel_hi:[1,0,1] neg_lo:[1,0,0]" : "+v"(x) : "v"(a), "v"(h));
}
#define PLN 0
__device__ __forceinline__ void c2_mac(f32x2& xr, f32x2& xi, float p, float q, float r, float s, f32x2 hr, f32x2 hi) {
    float a = xr[0], b = xi[0], c = xr[1], d = xi[1];
    asm("v_fmac_f32 %0, %4, %8\n\tv_fmac_f32 %1, %6, %9\n\tv_fmac_f32 %2, %4, %10\n\tv_fmac_f32 %3, %6, %11\n\t"
        "v_fmac_f32 %0, %5, %9\n\tv_fmac_f32 %1, %7, %8\n\tv_fmac_f32 %2, %5, %11\n\tv_fmac_f32 %3, %7, %10"
        : "+v"(a), "+v"(b), "+v"(c), "+v"(d) : "v"(p), "v"(q), "v"(r), "v"(s), "v"(hr[0]), "v"(hi[0]), "v"(hr[1]), "v"(hi[1]));
    xr = f32x2{a, c}; xi = f32x2{b, d};
}
__device__ __forceinline__ void c2_mad(f32x2& xr, f32x2& xi, float p, float q, float r, float s, f32x2 hr, f32x2 hi, f32x2 er, f32x2 ei) {
    float a, b, c, d;
    asm("v_fma_f32 %0, %4, %8, %12\n\tv_fma_f32 %1, %6, %9, %13\n\tv_fma_f32 %2, %4, %10, %14\n\tv_fma_f32 %3, %6, %11, %15\n\t"
        "v_fmac_f32 %0, %5, %9\n\tv_fmac_f32 %1, %7, %8\n\tv_fmac_f32 %2, %5, %11\n\tv_fmac_f32 %3, %7, %10"
        : "=&v"(a), "=&v"(b), "=&v"(c), "=&v"(d) : "v"(p), "v"(q), "v"(r), "v"(s), "v"(hr[0]), "v"(hi[0]), "v"(hr[1]), "v"(hi[1]), "v"(er[0]), "v"(ei[0]), "v"(er[1]), "v"(ei[1]));
    xr = f32x2{a, c}; xi = f32x2{b, d};
}
__device__ __forceinline__ void c2_mul(f32x2& xr, f32x2& xi, float p, float q, float r, float s, f32x2 hr, f32x2 hi) {
    float a, b, c, d;
    asm("v_mul_f32 %0, %4, %8\n\tv_mul_f32 %1, %6, %9\n\tv_mul_f32 %2, %4, %10\n\tv_mul_f32 %3, %6, %11\n\t"
        "v_fmac_f32 %0, %5, %9\n\tv_fmac_f32 %1, %7, %8\n\tv_fmac_f32 %2, %5, %11\n\tv_fmac_f32 %3, %7, %10"
        : "=&v"(a), "=&v"(b), "=&v"(c), "=&v"(d) : "v"(p), "v"(q), "v"(r), "v"(s), "v"(hr[0]), "v"(hi[0]), "v"(hr[1]), "v"(hi[1]));
    xr = f32x2{a, c}; xi = f32x2{b, d};
}
__device__ __forceinline__ float mul1(float a, float b) { float c; asm("v_mul_f32 %0, %1, %2" : "=v"(c) : "v"(a), "v"(b)); return c; }
__device__ __forceinline__ float add1(float a, float b) { float c; asm("v_add_f32 %0, %1, %2" : "=v"(c) : "v"(a), "v"(b)); return c; }
__device__ __forceinline__ void mfma_(f32x4& c, bf16x8 a, bf16x8 b) { asm volatile("v_mfma_f32_16x16x32_bf16 %0, %1, %2, %0" : "+v"(c) : "v"(a), "v"(b)); }
__device__ __forceinline__ float sum4(float x) {
    const auto p = __builtin_amdgcn_permlane32_swap(__float_as_uint(x), __float_as_uint(x), false, false);
    x = __uint_as_float(p[0]) + __uint_as_float(p[1]);
    const auto q = __builtin_amdgcn_permlane16_swap(__float_as_uint(x), __float_as_uint(x), false, false);
    return __uint_as_float(q[0]) + __uint_as_float(q[1]);
}

template <int I, int N, class F>
__device__ __forceinline__ void static_for(F&& f) { if constexpr (I < N) { f(std::integral_constant<int, I>{}); static_for<I + 1, N>(f); } }

#define TS_DECL
#define TS0a()
#define TS0b()
#define TSE(i)
#define TS(i)
#define TS_FLUSH()

#define WAIT_V(n) asm volatile("s_waitcnt vmcnt(%0)" ::"n"((n) > 63 ? 63 : (n)) : "memory")
#define WAIT_L(n) asm volatile("s_waitcnt lgkmcnt(%0)" ::"n"((n) > 15 ? 15 : (n)) : "memory")
#define BW 1
constexpr int wenc(int v, int l) { v = v > 63 ? 63 : v; l = l > 15 ? 15 : l; return (v & 15) | 0x70 | (l << 8) | ((v >> 4) << 14); }
#define WAIT_VB(n) do { if constexpr (BW) __builtin_amdgcn_s_waitcnt(wenc(n, 15)); else WAIT_V(n); } while (0)
#define WAIT_LB(n) do { if constexpr (BW) __builtin_amdgcn_s_waitcnt(wenc(63, n)); else WAIT_L(n); } while (0)
#define BAR() asm volatile("s_barrier" ::: "memory")
#define SB() __builtin_amdgcn_sched_barrier(0)
template <class T> __device__ __forceinline__ void tie_(T& x) { asm volatile("" : "+v"(x)); }
#define TIE(x) tie_(x)
template <class T> __device__ __forceinline__ void ties_(T& x) { asm volatile("" : "+s"(x)); }
__device__ __forceinline__ unsigned take(unsigned x) { unsigned y; asm volatile("v_mov_b32 %0, %1" : "=v"(y) : "v"(x)); return y; }
#define DEF_LD(name, ins) template <int OFF, class T> __device__ __forceinline__ void name(T& dst, unsigned voff, const char* sb) { \
    asm volatile(ins : "=v"(dst) : "v"(voff), "s"(sb), "n"(OFF) : "memory"); }
DEF_LD(gld_u16_, "global_load_ushort %0, %1, %2 offset:%3")
DEF_LD(gld_b32_, "global_load_dword %0, %1, %2 offset:%3")
DEF_LD(gld_b64_, "global_load_dwordx2 %0, %1, %2 offset:%3")
DEF_LD(gld_b128_, "global_load_dwordx4 %0, %1, %2 offset:%3")
#define NTQ " nt"
#define NTK " nt"
DEF_LD(gld_b64_nt_, "global_load_dwordx2 %0, %1, %2 offset:%3" NTQ)
DEF_LD(gld_b128_nt_, "global_load_dwordx4 %0, %1, %2 offset:%3" NTQ)
DEF_LD(gld_b64_sc1_, "global_load_dwordx2 %0, %1, %2 offset:%3 sc1")
DEF_LD(gld_b128_sc1_, "global_load_dwordx4 %0, %1, %2 offset:%3 sc1")
#define DEF_ST(name, ins) template <int OFF, class T> __device__ __forceinline__ void name(unsigned voff, const T& v, const char* sb) { \
    asm volatile(ins :: "v"(voff), "v"(v), "s"(sb), "n"(OFF) : "memory"); }
DEF_ST(gst_b128_, "global_store_dwordx4 %0, %1, %2 offset:%3\n\ts_nop 1")
DEF_ST(gst_b128_nt_, "global_store_dwordx4 %0, %1, %2 offset:%3 nt\n\ts_nop 1")
DEF_ST(gst_b128_sc1_, "global_store_dwordx4 %0, %1, %2 offset:%3 sc1\n\ts_nop 1")
DEF_ST(gst_b64_sc1_, "global_store_dwordx2 %0, %1, %2 offset:%3 sc1")
#define GLD_U16(dst, voff, sb) gld_u16_<0>(dst, voff, sb)
#define GLD_B32(dst, voff, sb, off) gld_b32_<(off)>(dst, voff, sb)
#define GLD_B64(dst, voff, sb, off) gld_b64_<(off)>(dst, voff, sb)
#define GLD_B128(dst, voff, sb, off) gld_b128_<(off)>(dst, voff, sb)
#define GLD_B64_NT(dst, voff, sb, off) gld_b64_nt_<(off)>(dst, voff, sb)
#define GLD_B128_NT(dst, voff, sb, off) gld_b128_nt_<(off)>(dst, voff, sb)
#define GLD_B64_SC1(dst, voff, sb, off) gld_b64_sc1_<(off)>(dst, voff, sb)
#define GLD_B128_SC1(dst, voff, sb, off) gld_b128_sc1_<(off)>(dst, voff, sb)
#define GST_B128(voff, v, sb, off) gst_b128_<(off)>(voff, v, sb)
#define GST_B128_NT(voff, v, sb, off) gst_b128_nt_<(off)>(voff, v, sb)
#define GST_B128_SC1(voff, v, sb, off) gst_b128_sc1_<(off)>(voff, v, sb)
#define GST_B64_SC1(voff, v, sb) gst_b64_sc1_<0>(voff, v, sb)
__device__ __forceinline__ void dma_(unsigned ldsaddr, unsigned voff, const char* sb) {
    asm volatile("s_mov_b32 m0, %0\n\ts_nop 0\n\tglobal_load_lds_dwordx4 %1, %2" :: "s"(ldsaddr), "v"(voff), "s"(sb) : "memory"); }
__device__ __forceinline__ void dma_nt_(unsigned ldsaddr, unsigned voff, const char* sb) {
    asm volatile("s_mov_b32 m0, %0\n\ts_nop 0\n\tglobal_load_lds_dwordx4 %1, %2 nt" :: "s"(ldsaddr), "v"(voff), "s"(sb) : "memory"); }
#define DF 0
#define DFN 4
#define FL_0 ""
#define FL_1 " nt"
#define FL_2 " sc1"
#define FL_3 " sc1 nt"
#define FL_4 " sc0 sc1"
#define FL_5 " sc0 sc1 nt"
#define FL_6 " sc0 nt"
#define FLX_(x) FL_##x
#define FLX(x) FLX_(x)
__device__ __forceinline__ void dmaf_(unsigned ldsaddr, unsigned voff, const char* sb) {
    asm volatile("s_mov_b32 m0, %0\n\ts_nop 0\n\tglobal_load_lds_dwordx4 %1, %2" FLX(DF) :: "s"(ldsaddr), "v"(voff), "s"(sb) : "memory"); }
template <int OFF> __device__ __forceinline__ void dmaof_(unsigned voff, const char* sb) {
    asm volatile("global_load_lds_dwordx4 %0, %1 offset:%2" FLX(DF) :: "v"(voff), "s"(sb), "n"(OFF) : "memory"); }
#define DMA(ldsaddr, voff, sb) dma_(ldsaddr, voff, sb)
template <int OFF> __device__ __forceinline__ void dmao_(unsigned voff, const char* sb) {
    asm volatile("global_load_lds_dwordx4 %0, %1 offset:%2" :: "v"(voff), "s"(sb), "n"(OFF) : "memory"); }
template <int OFF, class T> __device__ __forceinline__ void dsr128_(T& dst, unsigned addr) { asm volatile("ds_read_b128 %0, %1 offset:%2" : "=v"(dst) : "v"(addr), "n"(OFF)); }
template <int OFF, class T> __device__ __forceinline__ void dsr64_(T& dst, unsigned addr) { asm volatile("ds_read_b64 %0, %1 offset:%2" : "=v"(dst) : "v"(addr), "n"(OFF)); }
template <int OFF, class T> __device__ __forceinline__ void dsr2_(T& dst, unsigned addr) { asm volatile("ds_read2_b32 %0, %1 offset0:%2 offset1:%3" : "=v"(dst) : "v"(addr), "n"(OFF), "n"(OFF + 1)); }
#define DSR128(dst, addr, off) dsr128_<(off)>(dst, addr)
#define DSR64(dst, addr, off) dsr64_<(off)>(dst, addr)

__device__ __forceinline__ void kv4(unsigned& o01, unsigned& o23, unsigned ka, unsigned kb, unsigned kc, unsigned kd, const unsigned* w, unsigned v0, unsigned v1) {
    unsigned t1, t3, p0, p1, p2, p3;
    asm(
        "v_dot2_f32_bf16 %0, %8, %19, 0\n\tv_dot2_f32_bf16 %1, %9, %18, 0\n\tv_dot2_f32_bf16 %2, %9, %19, 0\n\tv_dot2_f32_bf16 %3, %10, %18, 0\n\t"
        "v_dot2_f32_bf16 %4, %9, %16, 0\n\tv_dot2_f32_bf16 %5, %9, %17, 0\n\tv_dot2_f32_bf16 %6, %10, %16, 0\n\tv_dot2_f32_bf16 %7, %10, %17, 0\n\t"
        "v_cvt_pk_bf16_f32 %0, %0, %4\n\tv_cvt_pk_bf16_f32 %1, %1, %5\n\tv_cvt_pk_bf16_f32 %2, %2, %6\n\tv_cvt_pk_bf16_f32 %3, %3, %7\n\t"
        "v_dot2_f32_bf16 %4, %9, %15, 0\n\tv_dot2_f32_bf16 %5, %10, %14, 0\n\tv_dot2_f32_bf16 %6, %10, %15, 0\n\tv_dot2_f32_bf16 %7, %11, %14, 0\n\t"
        "v_dot2_f32_bf16 %0, %0, %22, 0\n\tv_dot2_f32_bf16 %1, %1, %22, 0\n\tv_dot2_f32_bf16 %2, %2, %22, 0\n\tv_dot2_f32_bf16 %3, %3, %22, 0\n\t"
        "v_cvt_pk_bf16_f32 %0, %0, %4\n\tv_cvt_pk_bf16_f32 %1, %1, %5\n\tv_cvt_pk_bf16_f32 %2, %2, %6\n\tv_cvt_pk_bf16_f32 %3, %3, %7\n\t"
        "v_dot2_f32_bf16 %4, %10, %12, 0\n\tv_dot2_f32_bf16 %5, %10, %13, 0\n\tv_dot2_f32_bf16 %6, %11, %12, 0\n\tv_dot2_f32_bf16 %7, %11, %13, 0\n\t"
        "v_dot2_f32_bf16 %0, %0, %22, 0\n\tv_dot2_f32_bf16 %1, %1, %22, 0\n\tv_dot2_f32_bf16 %2, %2, %22, 0\n\tv_dot2_f32_bf16 %3, %3, %22, 0\n\t"
        "v_cvt_pk_bf16_f32 %0, %0, %4\n\tv_cvt_pk_bf16_f32 %1, %1, %5\n\tv_cvt_pk_bf16_f32 %2, %2, %6\n\tv_cvt_pk_bf16_f32 %3, %3, %7\n\t"
        "v_dot2_f32_bf16 %0, %0, %22, 0\n\tv_dot2_f32_bf16 %1, %1, %22, 0\n\tv_dot2_f32_bf16 %2, %2, %22, 0\n\tv_dot2_f32_bf16 %3, %3, %22, 0\n\t"
        "v_cvt_pk_bf16_f32 %0, %0, 0\n\tv_cvt_pk_bf16_f32 %1, 0, %1\n\tv_cvt_pk_bf16_f32 %2, %2, 0\n\tv_cvt_pk_bf16_f32 %3, 0, %3\n\t"
        "v_dot2_f32_bf16 %0, %0, %20, 0\n\tv_dot2_f32_bf16 %1, %1, %20, 0\n\tv_dot2_f32_bf16 %2, %2, %21, 0\n\tv_dot2_f32_bf16 %3, %3, %21, 0\n\t"
        "s_nop 1\n\tv_cvt_pk_bf16_f32 %0, %1, %0\n\tv_cvt_pk_bf16_f32 %2, %3, %2\n\t"
        : "=&v"(o01), "=&v"(t1), "=&v"(o23), "=&v"(t3), "=&v"(p0), "=&v"(p1), "=&v"(p2), "=&v"(p3)
        : "v"(ka), "v"(kb), "v"(kc), "v"(kd), "v"(w[0]), "v"(w[1]), "v"(w[2]), "v"(w[3]), "v"(w[4]), "v"(w[5]), "v"(w[6]), "v"(w[7]), "v"(v0), "v"(v1), "s"(0x3f803f80u));
}
__device__ __forceinline__ void kv4a(unsigned* t, unsigned* e, unsigned ka, unsigned kb, unsigned kc, unsigned kd, const unsigned* w) {
    asm volatile(
        "v_and_b32 %8, %21, %15\n\tv_lshlrev_b32 %9, 16, %16\n\tv_and_b32 %10, %21, %16\n\tv_lshlrev_b32 %11, 16, %17\n\t"
        "v_and_b32 %12, %21, %17\n\tv_mul_f32 %0, %20, %8\n\tv_mul_f32 %1, %20, %9\n\tv_mul_f32 %2, %20, %10\n\t"
        "v_mul_f32 %3, %20, %11\n\tv_mul_f32 %4, %19, %9\n\tv_mul_f32 %5, %19, %10\n\tv_mul_f32 %6, %19, %11\n\t"
        "v_mul_f32 %7, %19, %12\n\tv_lshlrev_b32 %13, 16, %18\n\tv_and_b32 %14, %21, %18\n\tv_cvt_pk_bf16_f32 %0, 0, %0\n\t"
        "v_cvt_pk_bf16_f32 %1, 0, %1\n\tv_cvt_pk_bf16_f32 %2, 0, %2\n\tv_cvt_pk_bf16_f32 %3, 0, %3\n\tv_cvt_pk_bf16_f32 %4, 0, %4\n\t"
        "v_cvt_pk_bf16_f32 %5, 0, %5\n\tv_cvt_pk_bf16_f32 %6, 0, %6\n\tv_cvt_pk_bf16_f32 %7, 0, %7\n\t"
        : "=&v"(t[0]), "=&v"(t[1]), "=&v"(t[2]), "=&v"(t[3]), "=&v"(t[4]), "=&v"(t[5]), "=&v"(t[6]), "=&v"(t[7]), "=&v"(e[0]), "=&v"(e[1]), "=&v"(e[2]), "=&v"(e[3]), "=&v"(e[4]), "=&v"(e[5]), "=&v"(e[6])
        : "v"(ka), "v"(kb), "v"(kc), "v"(kd), "v"(w[2]), "v"(w[3]), "s"(0xffff0000u));
}
__device__ __forceinline__ void kv4b(unsigned* t, const unsigned* e, const unsigned* w) {
    asm volatile(
        "v_add_f32 %0, %0, %4\n\tv_add_f32 %1, %1, %5\n\tv_add_f32 %2, %2, %6\n\tv_add_f32 %3, %3, %7\n\t"
        "v_mul_f32 %4, %14, %8\n\tv_mul_f32 %5, %14, %9\n\tv_mul_f32 %6, %14, %10\n\tv_mul_f32 %7, %14, %11\n\t"
        "v_cvt_pk_bf16_f32 %0, 0, %0\n\tv_cvt_pk_bf16_f32 %1, 0, %1\n\tv_cvt_pk_bf16_f32 %2, 0, %2\n\tv_cvt_pk_bf16_f32 %3, 0, %3\n\t"
        "v_cvt_pk_bf16_f32 %4, 0, %4\n\tv_cvt_pk_bf16_f32 %5, 0, %5\n\tv_cvt_pk_bf16_f32 %6, 0, %6\n\tv_cvt_pk_bf16_f32 %7, 0, %7\n\t"
        "v_add_f32 %0, %0, %4\n\tv_add_f32 %1, %1, %5\n\tv_add_f32 %2, %2, %6\n\tv_add_f32 %3, %3, %7\n\t"
        "v_mul_f32 %4, %13, %9\n\tv_mul_f32 %5, %13, %10\n\tv_mul_f32 %6, %13, %11\n\tv_mul_f32 %7, %13, %12\n\t"
        : "+v"(t[0]), "+v"(t[1]), "+v"(t[2]), "+v"(t[3]), "+v"(t[4]), "+v"(t[5]), "+v"(t[6]), "+v"(t[7]) : "v"(e[2]), "v"(e[3]), "v"(e[4]), "v"(e[5]), "v"(e[6]), "v"(w[0]), "v"(w[1]));
}
__device__ __forceinline__ void kv4c(unsigned* t, unsigned v0, unsigned v1) {
    asm volatile(
        "v_cvt_pk_bf16_f32 %0, 0, %0\n\tv_cvt_pk_bf16_f32 %1, 0, %1\n\tv_cvt_pk_bf16_f32 %2, 0, %2\n\tv_cvt_pk_bf16_f32 %3, 0, %3\n\t"
        "v_cvt_pk_bf16_f32 %4, 0, %4\n\tv_cvt_pk_bf16_f32 %5, 0, %5\n\tv_cvt_pk_bf16_f32 %6, 0, %6\n\tv_cvt_pk_bf16_f32 %7, 0, %7\n\t"
        "v_add_f32 %0, %0, %4\n\tv_add_f32 %1, %1, %5\n\tv_add_f32 %2, %2, %6\n\tv_add_f32 %3, %3, %7\n\t"
        "v_lshlrev_b32 %4, 16, %8\n\tv_and_b32 %5, %10, %8\n\tv_lshlrev_b32 %6, 16, %9\n\tv_and_b32 %7, %10, %9\n\t"
        "v_cvt_pk_bf16_f32 %0, 0, %0\n\tv_cvt_pk_bf16_f32 %1, 0, %1\n\tv_cvt_pk_bf16_f32 %2, 0, %2\n\tv_cvt_pk_bf16_f32 %3, 0, %3\n\t"
        "v_mul_f32 %0, %0, %4\n\tv_mul_f32 %1, %1, %5\n\tv_mul_f32 %2, %2, %6\n\tv_mul_f32 %3, %3, %7\n\t"
        "v_cvt_pk_bf16_f32 %0, %1, %0\n\tv_cvt_pk_bf16_f32 %2, %3, %2\n\t"
        : "+v"(t[0]), "+v"(t[1]), "+v"(t[2]), "+v"(t[3]), "+v"(t[4]), "+v"(t[5]), "+v"(t[6]), "+v"(t[7]) : "v"(v0), "v"(v1), "s"(0xffff0000u));
}
__device__ __forceinline__ void kf4a(unsigned* t, unsigned* e, unsigned ka, unsigned kb, unsigned kc, unsigned kd, const unsigned* w) {
    asm volatile(
        "v_and_b32 %4, %16, %11\n\tv_lshlrev_b32 %5, 16, %12\n\tv_and_b32 %6, %16, %12\n\tv_lshlrev_b32 %7, 16, %13\n\t"
        "v_and_b32 %8, %16, %13\n\tv_mul_f32 %0, %15, %4\n\tv_mul_f32 %1, %15, %5\n\tv_mul_f32 %2, %15, %6\n\t"
        "v_mul_f32 %3, %15, %7\n\tv_lshlrev_b32 %9, 16, %14\n\tv_and_b32 %10, %16, %14\n\t"
        : "=&v"(t[0]), "=&v"(t[1]), "=&v"(t[2]), "=&v"(t[3]), "=&v"(e[0]), "=&v"(e[1]), "=&v"(e[2]), "=&v"(e[3]), "=&v"(e[4]), "=&v"(e[5]), "=&v"(e[6])
        : "v"(ka), "v"(kb), "v"(kc), "v"(kd), "v"(w[3]), "s"(0xffff0000u));
}
__device__ __forceinline__ void kf4b(unsigned* t, const unsigned* e, const unsigned* w) {
    asm volatile(
        "v_fmac_f32 %0, %12, %4\n\tv_fmac_f32 %1, %12, %5\n\tv_fmac_f32 %2, %12, %6\n\tv_fmac_f32 %3, %12, %7\n\t"
        "v_fmac_f32 %0, %11, %5\n\tv_fmac_f32 %1, %11, %6\n\tv_fmac_f32 %2, %11, %7\n\tv_fmac_f32 %3, %11, %8\n\t"
        "v_fmac_f32 %0, %10, %6\n\tv_fmac_f32 %1, %10, %7\n\tv_fmac_f32 %2, %10, %8\n\tv_fmac_f32 %3, %10, %9\n\t"
        : "+v"(t[0]), "+v"(t[1]), "+v"(t[2]), "+v"(t[3]) : "v"(e[1]), "v"(e[2]), "v"(e[3]), "v"(e[4]), "v"(e[5]), "v"(e[6]), "v"(w[0]), "v"(w[1]), "v"(w[2]));
}
__device__ __forceinline__ void kf4c(unsigned* t, unsigned v0, unsigned v1) {
    asm volatile(
        "v_lshlrev_b32 %4, 16, %8\n\tv_and_b32 %5, %10, %8\n\tv_lshlrev_b32 %6, 16, %9\n\tv_and_b32 %7, %10, %9\n\t"
        "v_mul_f32 %0, %0, %4\n\tv_mul_f32 %1, %1, %5\n\tv_mul_f32 %2, %2, %6\n\tv_mul_f32 %3, %3, %7\n\t"
        "v_cvt_pk_bf16_f32 %0, %1, %0\n\tv_cvt_pk_bf16_f32 %2, %3, %2\n\t"
        : "+v"(t[0]), "+v"(t[1]), "+v"(t[2]), "+v"(t[3]), "=&v"(t[4]), "=&v"(t[5]), "=&v"(t[6]), "=&v"(t[7]) : "v"(v0), "v"(v1), "s"(0xffff0000u));
}
__device__ __forceinline__ f32x4 mul4bf(u32x2 x, const unsigned* d) {
    float a, b, c, e;
    asm("v_dot2_f32_bf16 %0, %4, %6, 0\n\tv_dot2_f32_bf16 %1, %4, %7, 0\n\tv_dot2_f32_bf16 %2, %5, %8, 0\n\tv_dot2_f32_bf16 %3, %5, %9, 0\n\t"
        "v_cvt_pk_bf16_f32 %0, 0, %0\n\tv_cvt_pk_bf16_f32 %1, 0, %1\n\tv_cvt_pk_bf16_f32 %2, 0, %2\n\tv_cvt_pk_bf16_f32 %3, 0, %3"
        : "=&v"(a), "=&v"(b), "=&v"(c), "=&v"(e) : "v"(x[0]), "v"(x[1]), "v"(d[0]), "v"(d[1]), "v"(d[2]), "v"(d[3]));
    return f32x4{a, b, c, e};
}
template <int BIT> __device__ __forceinline__ void quad_swap(u32x4& a, u32x4& b) {
    unsigned n0, n1, n2, n3, b0 = b[0], b1 = b[1], b2 = b[2], b3 = b[3];
#define QS_(d, s, k) "v_cndmask_b32_dpp " d ", " s ", " k ", vcc quad_perm:[%14,%15,%16,%17] row_mask:0xf bank_mask:0xf\n\t"
    asm("s_mov_b64 vcc, %12\n\ts_nop 0\n\t" QS_("%0", "%4", "%8") QS_("%1", "%5", "%9") QS_("%2", "%6", "%10") QS_("%3", "%7", "%11")
        "s_mov_b64 vcc, %13\n\t" QS_("%4", "%8", "%4") QS_("%5", "%9", "%5") QS_("%6", "%10", "%6") QS_("%7", "%11", "%7")
        : "=&v"(n0), "=&v"(n1), "=&v"(n2), "=&v"(n3), "+v"(b0), "+v"(b1), "+v"(b2), "+v"(b3)
        : "v"(a[0]), "v"(a[1]), "v"(a[2]), "v"(a[3]), "s"(BIT ? 0x3333333333333333ull : 0x5555555555555555ull), "s"(BIT ? 0xccccccccccccccccull : 0xaaaaaaaaaaaaaaaaull),
          "n"(BIT ? 2 : 1), "n"(BIT ? 3 : 0), "n"(BIT ? 0 : 3), "n"(BIT ? 1 : 2) : "vcc");
#undef QS_
    a = u32x4{n0, n1, n2, n3}; b = u32x4{b0, b1, b2, b3};
}
__device__ __forceinline__ void kv_dma(unsigned ldsaddr, unsigned k0, unsigned k1, unsigned k2, unsigned k3, const char* sb) {
    asm volatile("s_mov_b32 m0, %5\n\ts_nop 0\n\tglobal_load_lds_dwordx4 %0, %4" NTK "\n\tglobal_load_lds_dwordx4 %1, %4 offset:1024" NTK "\n\t"
                 "global_load_lds_dwordx4 %2, %4 offset:2048" NTK "\n\tglobal_load_lds_dwordx4 %3, %4 offset:3072" NTK
                 :: "v"(k0), "v"(k1), "v"(k2), "v"(k3), "s"(sb), "s"(ldsaddr) : "memory");
}
#define IF_(c, C) "s_cmp_lg_u32 " c ", " C "\n\ts_cbranch_scc1 1f\n\t"
template <int C, int N> __device__ __forceinline__ void wait_if(int c) { asm volatile(IF_("%0", "%1") "s_waitcnt vmcnt(%2)\n1:" :: "s"(c), "n"(C), "n"(N) : "scc", "memory"); }
template <int C, int M> __device__ __forceinline__ void flag_if(int c, unsigned voff, u32x2 v, const char* sb) {
    asm volatile(IF_("%0", "%1") "s_mov_b32 exec_lo, %5\n\ts_mov_b32 exec_hi, 0\n\tglobal_store_dwordx2 %2, %3, %4 sc1\n\ts_mov_b64 exec, -1\n1:" :: "s"(c), "n"(C), "v"(voff), "v"(v), "s"(sb), "n"(M) : "scc", "memory"); }
template <int C> __device__ __forceinline__ void st4_if(int c, unsigned voff, const u32x4* v, const char* s0, const char* s1) {
    asm volatile(IF_("%0", "%1") "global_store_dwordx4 %2, %3, %7 sc1\n\tglobal_store_dwordx4 %2, %4, %7 offset:1024 sc1\n\tglobal_store_dwordx4 %2, %5, %8 sc1\n\tglobal_store_dwordx4 %2, %6, %8 offset:1024 sc1\n\ts_nop 1\n1:"
                 :: "s"(c), "n"(C), "v"(voff), "v"(v[0]), "v"(v[1]), "v"(v[2]), "v"(v[3]), "s"(s0), "s"(s1) : "scc", "memory"); }
template <int C> __device__ __forceinline__ void ld4_if(int c, unsigned voff, u32x4* v, const char* s0) {
    asm volatile(IF_("%4", "%5") "global_load_dwordx4 %0, %6, %7 nt\n\tglobal_load_dwordx4 %1, %6, %7 offset:1024 nt\n\tglobal_load_dwordx4 %2, %6, %7 offset:2048 nt\n\tglobal_load_dwordx4 %3, %6, %7 offset:3072 nt\n1:"
                 : "+v"(v[0]), "+v"(v[1]), "+v"(v[2]), "+v"(v[3]) : "s"(c), "n"(C), "v"(voff), "s"(s0) : "scc", "memory"); }
template <int C> __device__ __forceinline__ void ld8_if(int c, unsigned voff, u32x2* v, const char* s0, const char* s1) {
    asm volatile(IF_("%8", "%9") "global_load_dwordx2 %0, %10, %11 offset:-4096 nt\n\tglobal_load_dwordx2 %1, %10, %11 offset:-2048 nt\n\tglobal_load_dwordx2 %2, %10, %11 nt\n\tglobal_load_dwordx2 %3, %10, %11 offset:2048 nt\n\t"
                 "global_load_dwordx2 %4, %10, %12 offset:-4096 nt\n\tglobal_load_dwordx2 %5, %10, %12 offset:-2048 nt\n\tglobal_load_dwordx2 %6, %10, %12 nt\n\tglobal_load_dwordx2 %7, %10, %12 offset:2048 nt\n1:"
                 : "+v"(v[0]), "+v"(v[1]), "+v"(v[2]), "+v"(v[3]), "+v"(v[4]), "+v"(v[5]), "+v"(v[6]), "+v"(v[7]) : "s"(c), "n"(C), "v"(voff), "s"(s0), "s"(s1) : "scc", "memory"); }
template <int E> __device__ __forceinline__ void sy_post(unsigned myaddr) { asm volatile("ds_write_b32 %0, %0 offset:%1" :: "v"(myaddr), "n"(E * 64) : "memory"); }
template <int E, int SL = 1> __device__ __forceinline__ unsigned sy_wait(unsigned addr) {
    unsigned v;
    asm volatile("s_branch 2f\n1:\n\ts_sleep %3\n2:\n\tds_read_b32 %0, %1 offset:%2\n\ts_waitcnt lgkmcnt(0)\n\tv_cmp_eq_u32 vcc, 0, %0\n\ts_cbranch_vccnz 1b"
                 : "=&v"(v) : "v"(addr), "n"(E * 64), "n"(SL) : "vcc", "memory");
    return v;
}
template <int E> __device__ __forceinline__ void sy_pre(unsigned& v, unsigned addr) { asm volatile("ds_read_b32 %0, %1 offset:%2" : "=v"(v) : "v"(addr), "n"(E * 64) : "memory"); }
template <int E> __device__ __forceinline__ void sy_chk(unsigned& v, unsigned addr) {
    asm volatile("s_branch 2f\n1:\n\tds_read_b32 %0, %1 offset:%2\n\ts_waitcnt lgkmcnt(0)\n2:\n\tv_cmp_eq_u32 vcc, 0, %0\n\ts_cbranch_vccnz 1b"
                 : "+v"(v) : "v"(addr), "n"(E * 64) : "vcc", "memory");
}
template <int OFF, class T> __device__ __forceinline__ void dsrtr_(T& dst, unsigned addr) { asm volatile("ds_read_b64_tr_b16 %0, %1 offset:%2" : "=v"(dst) : "v"(addr), "n"(OFF)); }

constexpr size_t WS_H = 0, WS_KVT = (size_t)10 << 20, WS_ZT = (size_t)15 << 20, WS_E = (size_t)20 << 20, WS_FLAG = WS_E + (2048 << 10);
#define HP 65536
#define KP 8192
#define ZP 4096
constexpr int FL_KV = 0, FL_Z = 65536, FL_H = 131072, FL_EPOCH = 196608;
constexpr unsigned SPIN_CAP = 1u << 17;
#define ABL 0
#define CSPLIT 1
#define MERGE 3
#define CB 13
#define PAUSE 0
#define QLATE 0
#define CVPRIO 0
#define STG_SEL (idx & 3)
#define SP0 3
#define SDP 1
#define LP0 3
#define LDP 2
#define KVS 5
#define KVU 3
#define QIT 0
#define ZB 3
#define OS 2
#define MPRIO 2

#define ROT 0x4042
#define R2 0x14
#define CPF 0
#define PRE 4
#define PRO 9
#define G1NT 0
#define G1NTS 16
#define TMIX 0
#define TLBT 0
#define CDEF 0
#define CVLATE 1
#define FPRIO 3
#define YS 0
#define DP0 1
#define DP1 5
#define AWB 1
#define PK 0
#define PU 15
#define PC0 13
#define PC1 14
#define PC2 16
#define PC3 16
#define PQ 0
#define SPF 0
#define SPH 0
#define NB1 4
#define EP 0
#define ESL 0
#define CFB 0
#define P1 1
#define P0 1
#define BKE 0
#define EALL 0
#define EREP 0
#define TOUCH 0
#define FILLB 0
#define CPOST 1
#define CEARLY 0
#define NOOWN 0
#define PFIRST 0
#define ZB0 0
#define CATCH 4
#define PWE 0
#define SWAP 1
#define KVM 0x0FE
#define KFD 3
#define ZW 2
#define ZX 1
#define ZP0 3
#define BARM 0x22220
#define NR 6
#define WPS 2
#define CD0 3
#define AWX 8
#define VBM 0
#define ZOWN 1
#define ZPS 4
#define XAB 0
#define QM 0
#define OSG 0
#define KPL 0xFE
#define KPRIO 1
#define GPRIO 1
#define PST 0x7654
#define PLT 0xB964
#define LC 0
#define PRE2 PRE
#define KVD 0
#define PAIR 1
#define G2P1 0
#define EPRIO 0
#define LSW 1
#define EGX 28
#define CR1 0
#define CR2 0
#define CR3 0
#define CRL (2 * NR - CR1 - CR2 - CR3)
#define UE 0
#define UPRIO 3
#define AMF 7
#define M1 1
#define W2 1
#define W1 0
#define NBK 3
#define XB 4
#define KFB 9
#define APRE 0
#define CB0 1
#define AP0 8
#define HTSE(i)
#define KAT 2
#define SD 2
#define KV3 0x088888
#define KOWN 1
#define HKV 0
#define KVP NBK
#define LZ 1
#define AWN 8
#define CRATE 2
#define ZSB 0
#define ZFB 1
#define ZSL 4
#define OSL 0
constexpr int it_cnt(int p, int k, int last) {
    const int pos[8] = {PU, PK, PC0, PC1, PQ, PU - 1, PC2, PC3}, n[8] = {4, 4, 4, 4, 8, 1, 4, 4};
    int c = 0;
    for (int i = 0; i < 8; ++i) if (pos[i] <= last && (pos[i] > p || (pos[i] == p && i > k))) c += n[i];
    return c;
}
constexpr int nib(int t, int c) { return (t >> (4 * c)) & 15; }
constexpr bool nib_any(int t, int v) { return nib(t, 0) == v || nib(t, 1) == v || nib(t, 2) == v || nib(t, 3) == v; }
constexpr int kv_wait(int late) {
    int t = 0;
    for (int c = 0; c < 4; ++c) t |= ((nib(PLT, c) >= nib(PST, c) && nib(PLT, c) < nib(PST, c) + KFD) == late ? nib(PST, c) + KFD : 15) << (4 * c);
    return t;
}
constexpr unsigned long long sl_seq(int i, int inv) {
    int q[16] = {};
    const int near[4] = {1, 5, 6, SN}, far[13] = {2, 3, 4, 7, 8, 9, 10, 11, 12, 13, 14, 15, 0};
    for (int k = 0; k < 4; ++k) q[near[k]] = (i + k) & 3;
    for (int k = 0, n = 0; k < 13; ++k) if (far[k] != SN) q[far[k]] = 4 + (n++ + SQ * i) % 12;
    unsigned long long t = 0;
    for (int k = 0; k < 16; ++k) t |= inv ? (unsigned long long)k << (4 * q[k]) : (unsigned long long)q[k] << (4 * k);
    return t;
}
constexpr int G1STAGE = 32768;
constexpr int g1_slot(int s) { return (NB1 == 5 ? (s + 3) % 5 : s & 3) * G1STAGE; }
constexpr int L_KS = NB1 == 5 ? 65536 : 131072, L_VS = L_KS + 16384;
constexpr int L_TS = 3 * G1STAGE, L_TR = L_TS + 3072;
constexpr int CST = 4288;
constexpr int UOFF = 2 * CST + 128, UPL = 2304;
constexpr int CHSZ = UOFF + 2 * UPL + 32;
constexpr int L_AGG = 4 * CHSZ, L_ES = L_AGG + 15 * 512, L_SY = L_ES + 2048;
constexpr int L_HT = 98304;
constexpr int L_C = 98304, G2STAGE = 16384;
constexpr int L_Y = 16384;
static_assert(L_SY + 384 <= 65536 && L_AGG >= 49152 && UOFF % 16 == 0 && CHSZ % 256 == 32, "lds");

__device__ __forceinline__ int img_off(int r, int c) { return (r >> 1) * 256 + ((((r & 1) * 8 + c) ^ ((r >> 1) & 15)) << 4); }

constexpr int g1_upto(int g) {
    int n = 4 * NBK;
    for (int t = SD; t <= g; ++t) {
        int sb = (t + 1) / 2, sub = (t + 1) % 2, allowed = sb ? 4 * (sb + 3) + 2 * (sub + 1) : 12;
        if (allowed > 64) allowed = 64;
        n = n + CATCH < allowed ? n + CATCH : allowed;
    }
    return n;
}
constexpr int g1_cnt(int s) { return HKV || s >= KVP ? g1_upto(2 * s - 2) - 4 * (s + 1) : s >= KAT ? g1_upto(2 * s - 2) - 4 * KVP : g1_upto(2 * s - 2) - 4 * (s + 1) + 4; }
static_assert(KVP >= 1 && KVP <= NBK && AP0 % 4 == 0, "schedule");
static_assert(g1_upto(28) == 64, "schedule");
constexpr int AW = AWN;
constexpr int g2_place(int s) { return s < 5 ? L_C + ((s + 2) & 3) * G2STAGE : s < 7 ? (s - 1) * G2STAGE : s < 10 ? (9 - s) * G2STAGE : g2_place(s - 9); }
constexpr bool g2_bar(int g) { return (BARM >> g) & 1; }
struct G2Plan { int upto[33], wr[33], ald[64][2], seq[164], use[64]; };
constexpr G2Plan g2_plan() {
    G2Plan p = {};
    int nc = 14 + 2 * NR, nw = 0, nx = 4, lastb = 0, n = 0;
    for (int i = 0; i < 164; ++i) p.seq[i] = 1000;
    for (; nx < 4 + AW; ++nx) p.seq[nx] = ++n;
    for (int t = 0; t < 32; ++t) {
        p.upto[t] = nc; p.wr[t] = nw;
        if (t < 2) continue;
        if (g2_bar(t)) lastb = t;
        const int w = AW + nw < AWX ? AW + nw : AWX;
        for (int f = 2 * t; f < 2 * t + 2; ++f) {
            p.use[f] = n;
            if (nc < 32 && nc == p.upto[t] + (f & 1) && (f & 1) < CRATE && t >= CD0 && lastb >= (nc < 20 ? 1 : (nc | 1) - 18)) p.seq[100 + nc++] = ++n;
            for (int k = 0; k < 2; ++k) {
                p.ald[f][k] = -1;
                if (nx < 64 && nx - f - k < w) { p.ald[f][k] = nx; p.seq[nx++] = ++n; }
            }
        }
        for (int k = 0; k < WPS; ++k) if (nw < 2 * NR && lastb >= (nw < 6 ? 1 : (nw | 1) - 4)) ++nw;
    }
    p.upto[32] = nc; p.wr[32] = nw;
    return p;
}
constexpr G2Plan G2P = g2_plan();
constexpr int g2_upto(int g) { return G2P.upto[g + 1]; }
constexpr int g2_wr(int g) { return G2P.wr[g + 1]; }
constexpr int g2_ald(int f, int k) { return G2P.ald[f][k]; }
constexpr int g2_cnt(int id, int at) { return G2P.use[at] - G2P.seq[id]; }
constexpr int LASTB = 31 - __builtin_clz(BARM);
constexpr int lz_post() { int g = LASTB; while (G2P.seq[2 * g] < G2P.seq[131]) ++g; return g; }
constexpr int lz_chk() { int s = 7 + NR; while (G2P.seq[100 + 2 * s + 1] < G2P.seq[2 * LASTB]) ++s; return 2 * s - 1; }
static_assert(!LZ || (AWB && lz_post() + 1 < lz_chk() && lz_chk() < EGX), "schedule");
constexpr int g2_cover(int g) { int s = (g + 1) / 2; while (s < 15 && !g2_bar(2 * s + 1)) ++s; return s; }
static_assert(BARM >> 17 && g2_cover(3) < 7 && CRATE <= 2, "the epilogue needs stage 8 behind a barrier");

constexpr int cv_m(int o) { return (o & 1) ? 7 - (o >> 1) : o >> 1; }
constexpr int cv_first(int o) { int p = 0; for (int i = 0; i < o; ++i) p += 8 - cv_m(i); return p; }
constexpr int cv_grp(int p) { int o = 0; while (cv_first(o + 1) <= p) ++o; return o; }
constexpr int cv_read(int p) {
    int slot[40] = {};
    for (int i = 0; i < 40; ++i) slot[i] = -1;
    slot[0] = 2 * 3 + 1;
    for (int o = 0; o < 8; ++o) {
        int q = cv_first(o + 1) - 1;
        for (int h = 0; h < 2; ++h) { while (slot[q] >= 0) ++q; slot[q] = 2 * (o + 4) + h; }
    }
    const int bs[8] = {1, 3, 5, 11, 12, 13, 19, 21};
    for (int jb = 0; jb < 8; ++jb) slot[bs[jb]] = 32 + jb;
    return slot[p];
}
static_assert(cv_read(35) == 22 && cv_read(36) == 23 && cv_read(37) == -1 && cv_read(10) == 11 && cv_read(15) == 12 && cv_read(18) == 15 && cv_read(24) == 17, "conv schedule");
constexpr int cv_wait(int o) {
    int n = 0;
    for (int q = cv_first(o) - 1;; --q) {
        const int rd = cv_read(q < 0 ? q + 36 : q);
        if (rd == 2 * (o + (q < 0 ? 8 : 0)) + 1) return n;
        if (rd >= 0) ++n;
    }
}

struct Args {
    const char *q, *k, *v, *sk, *ssmk, *ssmD, *x, *lre, *lim, *bre, *bim, *cre, *cim, *logdt, *dskip;
    char *out, *ws;
};

typedef __attribute__((ext_vector_type(8))) unsigned long long u64x8;
__global__ __launch_bounds__(768, 3) void fused(Args) {
    __shared__ __attribute__((aligned(1024))) char shm[163840];
    TS0a();
    u64x8 ka0, ka1; unsigned long long ka2;
    const char* pc0;
    int code_n;
    asm volatile("s_load_dwordx16 %0, %5, 0x0\n\ts_load_dwordx16 %1, %5, 0x40\n\ts_load_dwordx2 %2, %5, 0x80\n\t"
                 "s_getpc_b64 %3\n.Lfk_code_0:\n\ts_mov_b32 %4, .Lfk_code_a-.Lfk_code_0-4\n\ts_max_i32 %4, %4, .Lfk_code_b-.Lfk_code_0-4"
                 : "=&s"(ka0), "=&s"(ka1), "=&s"(ka2), "=&s"(pc0), "=&s"(code_n) : "s"(__builtin_amdgcn_kernarg_segment_ptr()) : "scc");
    const int tid = threadIdx.x, lane = tid & 63, wraw = __builtin_amdgcn_readfirstlane(tid >> 6);
    const int wid = !SWAP ? wraw : wraw < 4 ? wraw + 8 : wraw - 4;
    const int b = blockIdx.x, xcd = b & 7, idx = b >> 3;
    asm volatile("s_cmp_lt_u32 %0, 8\n\ts_cbranch_scc1 1f\n\ts_setprio 3\n\ts_cmp_lg_u32 %0, 8\n\ts_cbranch_scc1 1f\n\t"
                 "v_lshl_add_u32 v161, %1, 7, %2\n\tv_min_i32 v161, %3, v161\n\ts_mov_b64 exec, 0xffff\n\tglobal_load_dword v161, v161, %4\n\ts_mov_b64 exec, -1\n1:"
                 :: "s"(wid), "v"(lane), "s"(idx * 2048), "s"(code_n), "s"(pc0) : "scc", "memory", "v161");
    TS_DECL
    const int tm = (xcd >> 1) * 4 + (idx >> 3), tn = (xcd & 1) * 8 + (idx & 7);
    const int wm = wid & 3, wn = (wid >> 2) & 1, l31 = lane & 31, lh = lane >> 5;
    const unsigned lb = (unsigned)(uintptr_t)shm;
    const unsigned l16 = lane * 16;
    const int l15 = lane & 15, g4 = lane >> 4, sq = 2 * wn + (wm & 1), st = sq * 16 + l15, run = (wm >> 1) * 4 + g4;
    unsigned kvv[4];
    const int trow = idx * 64 * 2048 + xcd * 256;
    if (wid < 8) {
        const int v = l15 ^ g4, hi = v >> 3;
        const int r0 = wn ? tn * 64 + (wm >> 1) * 32 : tm * 128 + (wm >> 1) * 64 + (wm & 1) * 4;
        const unsigned d0 = (r0 + ((g4 * (wn ? 2 : 9)) & (wn ? 6 : 17))) * 2048 + (v & 7) * 16, odd = wn ? 11 : 14;
        kvv[0] = d0 + (hi << odd); kvv[2] = d0 + ((hi ^ 1) << odd) + (wn ? 32768 - 2048 : 4096 - 2048);
        kvv[1] = (kvv[0] ^ 64) + (wn ? 16384 - 1024 : 65536 - 1024); kvv[3] = (kvv[2] ^ 64) + (wn ? 16384 - 1024 : 65536 - 1024);
    } else {
        kvv[0] = trow + (idx ? -4 * 2048 : 0) + g4 * 2048 + wm * 64 + l15 * 4;
        asm volatile("v_writelane_b32 %0, 0, 0\n\tv_writelane_b32 %0, 4, 1\n\tv_writelane_b32 %0, %1, 2" : "+v"(kvv[0]) : "s"((xcd * 64 + idx * 2 + (wm >> 1)) * 4));
        kvv[1] = (xcd * 128 + wm * 32 + l31) * 8;
        kvv[2] = kvv[3] = (tn * 64 + lane) * 2;
    }
    unsigned g1dst = lb + wid * 4096;
    int rot7 = ((((ROT >> 12) & 15) * (idx & 7) + ((ROT >> 8) & 15) * (idx >> 3) + ((ROT >> 4) & 15) * (xcd >> 1) + (ROT & 15) * (xcd & 1)) & 15) << 7;
    asm volatile("s_waitcnt lgkmcnt(0)" : "+s"(ka0), "+s"(ka1), "+s"(ka2), "+s"(g1dst), "+s"(rot7), "+v"(kvv[0]), "+v"(kvv[1]), "+v"(kvv[2]), "+v"(kvv[3]));
    Args a;
    a.q = (const char*)ka0[0]; a.k = (const char*)ka0[1]; a.v = (const char*)ka0[2]; a.sk = (const char*)ka0[3]; a.ssmk = (const char*)ka0[4]; a.ssmD = (const char*)ka0[5];
    a.x = (const char*)ka0[6]; a.lre = (const char*)ka0[7]; a.lim = (const char*)ka1[0]; a.bre = (const char*)ka1[1]; a.bim = (const char*)ka1[2]; a.cre = (const char*)ka1[3];
    a.cim = (const char*)ka1[4]; a.logdt = (const char*)ka1[5]; a.dskip = (const char*)ka1[6]; a.out = (char*)ka1[7]; a.ws = (char*)ka2;
    char* const ws = a.ws;
    const char* const flg = ws + WS_FLAG;
    TS(26);
    const char* g1src = wn ? ((wm & 1) ? a.bim : a.bre) : a.x;
    asm volatile("s_mov_b32 m0, %[m]\n\ts_cmp_gt_u32 %[w], 7\n\ts_cbranch_scc1 1f\n\t"
                 "global_load_lds_dwordx4 %[k0], %[g]" FLX(DF) "\n\tglobal_load_lds_dwordx4 %[k1], %[g] offset:1024" FLX(DF) "\n\t"
                 "global_load_lds_dwordx4 %[k2], %[g] offset:2048" FLX(DF) "\n\tglobal_load_lds_dwordx4 %[k3], %[g] offset:3072" FLX(DF) "\n\t"
                 "s_branch 2f\n1:\n\tglobal_load_ushort v164, %[k2], %[c1]\n\tglobal_load_ushort v165, %[k2], %[c2]\n\tglobal_load_ushort v166, %[k2], %[c3]\n\t"
                 "v_mov_b64 v[162:163], %[kv]\n\ts_mov_b64 exec, 7\n\tv_mov_b64 v[162:163], %[d]\n\ts_mov_b64 exec, 3\n\tv_mov_b64 v[162:163], %[e]\n\ts_mov_b64 exec, -1\n\t"
                 "v_mad_u64_u32 v[162:163], vcc, %[k0], 1, v[162:163]\n\tglobal_load_dword v160, v[162:163], off sc1\n\tglobal_load_dwordx2 v[162:163], %[k1], %[sk]\n2:"
                 :
                 : [w] "s"(wid), [c1] "s"(a.logdt), [c2] "s"(a.lre), [c3] "s"(a.lim), [d] "s"(a.ssmD), [e] "s"(flg + FL_EPOCH), [sk] "s"(a.sk),
                   [k0] "v"(kvv[0]), [k1] "v"(kvv[1]), [k2] "v"(kvv[2]), [k3] "v"(kvv[3]), [kv] "s"(a.k), [g] "s"(g1src + rot7), [m] "s"(g1dst)
                 : "scc", "vcc", "memory", "v160", "v161", "v162", "v163", "v164", "v165", "v166");
    TS0b();
    TS(27);
    if (P1) BAR();
#define SLICE_TABLE() unsigned slo, shi; { int ix = idx; asm volatile("" : "+s"(ix)); \
        const unsigned long long t0 = (ix & 16) ? ((ix & 8) ? sl_seq(3, 0) : sl_seq(2, 0)) : ((ix & 8) ? sl_seq(1, 0) : sl_seq(0, 0)), iv = (ix & 16) ? ((ix & 8) ? sl_seq(3, 1) : sl_seq(2, 1)) : ((ix & 8) ? sl_seq(1, 1) : sl_seq(0, 1)); \
        const unsigned x4 = (xcd >> 1) * 0x44444444u, own = tn ^ (x4 & 15), es = (unsigned)(iv >> (4 * own)) & 15; \
        const unsigned long long df = ((unsigned)t0 & 15) ^ own, tab = t0 ^ (df << (4 * es)) ^ df; \
        slo = (unsigned)tab ^ x4; shi = (unsigned)(tab >> 32) ^ x4; asm volatile("" : "+s"(slo), "+s"(shi)); }
#define SLICE(st) ((((st) < 8 ? slo : shi) >> (4 * ((st) & 7))) & 15)
    typedef __attribute__((ext_vector_type(4))) short v4i16;
    int role = wid;
    asm volatile("" : "+s"(role));

    if (role >= 8) {
        int lane;
        asm volatile("v_mbcnt_lo_u32_b32 %0, -1, 0\n\tv_mbcnt_hi_u32_b32 %0, -1, %0" : "=v"(lane));
        __builtin_assume(lane >= 0 && lane < 64);
        const int l31 = lane & 31, lh = lane >> 5, l15 = lane & 15, g4 = lane >> 4;
        const unsigned l16 = lane * 16;
        const int chn = xcd * 128 + idx * 4 + wm;
        const unsigned cb = lb + wm * CHSZ;
        const int r = l15;
        const unsigned fpub = KOWN ? (xcd * 32 + l31) * 256 + idx * 8 : xcd * 256 + idx * 8, fpoll = KOWN ? (xcd * 32 + idx) * 256 + l31 * 8 : xcd * 256 + l31 * 8;
        u32x2 fl; u32x4 sw[4], uu[4];
        unsigned tch = 0, tch2 = 0;
        if (TOUCH) {
            const int ln = wm * 4 + (lane & 3);
            const unsigned fo = (unsigned)(WS_FLAG - WS_E) + (wm == 0 ? FL_H + (tm * 16 + tn) * 128 : (wm == 1 ? FL_KV : FL_Z) + xcd * 256 + (lane & 1) * 128);
            const unsigned o = (lane & 4) && (TOUCH & 2) ? fo : ((tm * 2 + (ln >> 3)) * 1024 + tn * 64) * 16 + (ln & 7) * 128;
            asm volatile("s_mov_b64 exec, 0xff\n\tglobal_load_dword %0, %1, %2\n\ts_mov_b64 exec, -1" : "=&v"(tch) : "v"(o), "s"(ws + WS_E) : "memory");
        }
        SLICE_TABLE();
        TS(1);
        unsigned sm, cl[3]; u32x2 kw0;
        asm volatile("s_waitcnt vmcnt(0)" : "={v160}"(sm), "={v[162:163]}"(kw0), "={v164}"(cl[0]), "={v165}"(cl[1]), "={v166}"(cl[2]) :: "memory", "v161");
        TS(22);
        const unsigned hal = take(sm);
        const unsigned tagA = __builtin_amdgcn_readlane(hal, 0) + 1, tagB = __builtin_amdgcn_readlane(hal, 1) + (tagA == 0 ? 1u : 0u);
        const u32x2 tag = {tagA, tagB};
        f32x2 L1, C1;
        {
            const float dt = rbf(expf(bf2f(take(cl[0]))));
            const float lr = bf2f(take(cl[1])), li = bf2f(take(cl[2]));
            const float e = expf(dt * lr);
            float sn, cs;
            sincosf(dt * li, &sn, &cs);
            L1 = f32x2{e * cs, e * sn};
            const float nr = L1[0] - 1.f, ni = L1[1], den = lr * lr + li * li;
            C1 = f32x2{2.f * ((nr * lr + ni * li) / den), 2.f * ((ni * lr - nr * li) / den)};
            TIE(L1); TIE(C1);
        }

        f32x2 p[11];
        p[0] = L1;
        if (PWE) {
#pragma unroll
            for (int i = 1; i <= 10; ++i) { p[i] = cmul4(p[i - 1], p[i - 1]); TIE(p[i]); }
        }
        {
            if (!PWE) {
#pragma unroll
                for (int i = 1; i <= 10; ++i) p[i] = cmul4(p[i - 1], p[i - 1]);
            }
            const f32x2 one = {1.f, 0.f}, zero = {0.f, 0.f};
            const unsigned ts = lb + L_TS + l16, tr = lb + L_TR + wm * 2048 + l16;
            if (wm == 0) {
                *LDSP(f32x4, ts) = f32x4{L1[0], L1[1], C1[0], C1[1]};
                *LDSP(f32x4, ts + 1024) = f32x4{p[3][0], p[3][1], p[4][0], p[4][1]};
                *LDSP(f32x4, ts + 2048) = f32x4{p[6][0], p[6][1], __uint_as_float(tagA), __uint_as_float(tagB)};
            }
            f32x2 Q[2], V[2], W[2];
            Q[0] = cmul4(cmul4(C1, (wm & 1) ? p[5] : one), (wm & 2) ? p[6] : one); Q[1] = cmul4(Q[0], p[4]);
            V[1] = (wm & 1) ? one : p[5]; V[0] = cmul4(V[1], p[4]);
            W[1] = cmul4((wm & 1) ? one : p[9], (wm & 2) ? one : p[10]); W[0] = cmul4(W[1], p[8]);
#pragma unroll
            for (int i = 0; i < 2; ++i) {
                const f32x2 Q8 = cmul4(Q[i], p[3]), U = 4 * wm + 2 * i >= 16 - tm ? cmul4(W[i], p[7]) : zero, Wz = 4 * wm + 2 * i + 1 >= 16 - tm ? W[i] : zero;
                *LDSP(f32x4, tr + i * 1024) = f32x4{Q[i][0], Q8[0], Q[i][1], Q8[1]};
                *LDSP(f32x4, tr + i * 1024 + 8192) = f32x4{Wz[0], Wz[1], U[0], U[1]};
                *LDSP(f32x2, tr + i * 1024 + 16384) = V[i];
            }
        }
        auto b0 = [&]() {
            unsigned k0 = trow + wm * 32768 + g4 * 2048 + ((l15 ^ (2 * g4)) << 4), k1 = k0 + 7168, k2 = (k0 ^ 128) + 14336, k3 = k2 + 7168;
            if (HKV) { TIE(k0); TIE(k1); TIE(k2); TIE(k3); }
            if (HKV == 1) { kv_dma(lb + L_KS + wm * 4096, k0, k1, k2, k3, a.k); kv_dma(lb + L_VS + wm * 4096, k0, k1, k2, k3, a.v); }
            WAIT_L(0); BAR(); HTSE(1);
            if (HKV == 2) { kv_dma(lb + L_KS + wm * 4096, k0, k1, k2, k3, a.k); kv_dma(lb + L_VS + wm * 4096, k0, k1, k2, k3, a.v); }
        };
        if (CB0 == 0) b0();
        const unsigned dskl = (__builtin_amdgcn_readlane(hal, 2) >> ((wm & 1) * 16)) & 0xffffu;
        u32x4 kvo[4];
        u32x2 qv[8];
        const char* krow = a.ssmk + (size_t)chn * 4096;
        const char* qs = a.q + (size_t)(idx * 64 + wm * 16) * 2048 + xcd * 256;
        const char* kvd = ws + WS_KVT + (size_t)(xcd * 8 + wm * 2) * 65536 + idx * 2048;
        const int pl = (PLT >> ((STG_SEL) * 4)) & 15;
        const unsigned qo = lh * 8192 + l31 * 8 + 4096;
        auto hb = [&](auto nc) {
            constexpr int nb = decltype(nc)::value;
            if constexpr (nb == KAT && HKV) WAIT_V(0);
            if constexpr (nb == KFB) { WAIT_V((PK ? 0 : 4) + (PQ ? 0 : 8)); int lm = (1 << KFB) - (2 << (KAT + __builtin_popcount(KV3))); ties_(lm); wait_if<0, 0>((lm >> pl) & 1); }
            BAR();
            if constexpr (nb == KFB) flag_if<0, KOWN ? -1 : 1>(wm, fpub, tag, flg + FL_KV);
            if constexpr (TCH && nb == TCB) {
                if (TCH == 1) { const u32x4 nz = {~tagA, ~tagA, ~tagA, ~tagA}; if (lane < 32) GST_B128_SC1(((tm * 2 + (wm >> 1)) * 1024 + tn * 64 + (wm & 1) * 32) * 16 + l16, nz, ws + WS_E, 0); }
                else if (TCH == 3) {
                    const unsigned o = (tn * 64 + lane) * 2048 + ((tm & 3) * 4 + wm) * 128;
                    asm volatile("global_load_dword %0, %2, %3\n\tglobal_load_dword %1, %2, %4" : "=&v"(tch), "=&v"(tch2) : "v"(o), "s"(a.cre), "s"(a.cim) : "memory");
                } else if (TCH == 4) {
                    asm volatile("global_load_dword %0, %1, %2" : "=&v"(tch) : "v"((tn * 64 + lane) * 2048), "s"(((wm & 1) ? a.cim : a.cre) + (((wm & 2) ? SLICE(6) : SLICE(5)) << 7)) : "memory");
                }
                else asm volatile("global_load_dword %0, %1, %2 sc1" : "=&v"(tch) : "v"((lane >> 1) * 16384 + tn * 1024 + wm * 256 + (lane & 1) * 128), "s"(ws + WS_E) : "memory");
            }
            if constexpr (nib_any(PLT, nb)) { if (!PK) ld4_if<nb>(pl, l16, sw, krow); if (!PQ) ld8_if<nb>(pl, qo, qv, qs, qs + 16384); }
        };
#define HB(n) hb(std::integral_constant<int, (n) + NBK>{})
        {
            unsigned kw[2][4], ta[2], th[2];
            u32x2 hm[2];
#pragma unroll
            for (int j = 0; j < 2; ++j) {
                const unsigned x = take(kw0[j]);
                const auto y = __builtin_amdgcn_permlane16_swap(x, x, false, false);
#pragma unroll
                for (int p = 0; p < 2; ++p) { kw[p][2 * j] = y[p] << 16; kw[p][2 * j + 1] = y[p] & 0xffff0000u; }
            }
#pragma unroll
            for (int p = 0; p < 2; ++p) {
                unsigned h[3];
#pragma unroll
                for (int j = 0; j < 3; ++j) h[j] = (__shfl(hal, p * 8 + (r >> 1) + 16 * (j + 1)) >> ((r & 1) * 16)) & 0xffffu;
                hm[p] = idx ? u32x2{h[0] << 16, h[1] | (h[2] << 16)} : u32x2{0u, 0u};
                const int q = r >> 2, pp = r & 3;
                ta[p] = lb + L_KS + (8 * g4 + q) * 256 + (((wm * 4 + p * 2 + (pp >> 1)) ^ (2 * (q | ((g4 & 1) << 2)))) << 4) + (pp & 1) * 8;
                th[p] = (ta[p] ^ 128) - 1024;
                TIE(hm[p]); TIE(ta[p]); TIE(th[p]);
#pragma unroll
                for (int j = 0; j < 4; ++j) TIE(kw[p][j]);
            }
            TS(21);
            if (CB0 == 1) b0();
#pragma unroll
            for (int i = 0; i < 4; ++i) asm volatile("" : "=v"(sw[i]), "=v"(qv[2 * i]), "=v"(qv[2 * i + 1]));
            static_for<1, KAT + 1>(hb);
            TS(2);
            u32x2 kt[4][3], vt[4][2];
#pragma unroll
            for (int u = 0; u < 4; ++u) {
                const int p = u >> 1, o = (u & 1) * 8192;
                auto tr = [&](unsigned ad) -> u32x2 { return __builtin_bit_cast(u32x2, __builtin_amdgcn_ds_read_tr16_b64_v4i16(LDSP(v4i16, ad))); };
                kt[u][0] = tr(th[p] + o); kt[u][1] = tr(ta[p] + o); vt[u][0] = tr(ta[p] + o + 16384); kt[u][2] = tr(ta[p] + o + 1024); vt[u][1] = tr(ta[p] + o + 17408);
                SB();
            }
            TS(14);
            __builtin_amdgcn_s_setprio(KPRIO);
            static_for<0, 4>([&](auto uc) {
                constexpr int u = decltype(uc)::value, p = u >> 1, oh = u & 1;
                const u32x2 k0 = oh == 0 && g4 == 0 ? hm[p] : kt[u][0];
                unsigned t0[8], t1[8], e[7];
#define K3(n) do { if constexpr ((KV3 >> (6 * u + n)) & 1) hb(std::integral_constant<int, KAT + 1 + __builtin_popcount(KV3 & ((1 << (6 * u + n)) - 1))>{}); } while (0)
                if constexpr (RX & 1) {
                kf4a(t0, e, k0[0], k0[1], kt[u][1][0], kt[u][1][1], kw[p]); K3(0); kf4b(t0, e, kw[p]); K3(1); kf4c(t0, vt[u][0][0], vt[u][0][1]); K3(2);
                kf4a(t1, e, kt[u][1][0], kt[u][1][1], kt[u][2][0], kt[u][2][1], kw[p]); K3(3); kf4b(t1, e, kw[p]); K3(4); kf4c(t1, vt[u][1][0], vt[u][1][1]);
                } else {
                kv4a(t0, e, k0[0], k0[1], kt[u][1][0], kt[u][1][1], kw[p]); K3(0); kv4b(t0, e, kw[p]); K3(1); kv4c(t0, vt[u][0][0], vt[u][0][1]); K3(2);
                kv4a(t1, e, kt[u][1][0], kt[u][1][1], kt[u][2][0], kt[u][2][1], kw[p]); K3(3); kv4b(t1, e, kw[p]); K3(4); kv4c(t1, vt[u][1][0], vt[u][1][1]);
                }
                kvo[u] = u32x4{t1[2], t1[0], t0[2], t0[0]};
                GST_B128_SC1(l16, kvo[u], kvd + (u >> 1) * 65536, (u & 1) * 1024);
                K3(5);
#undef K3
            });
            hb(std::integral_constant<int, KAT + 1 + __builtin_popcount(KV3)>{});
            __builtin_amdgcn_s_setprio(3);
        }
        TS(3);
        const char* usrc = ws + WS_KVT + (size_t)(xcd * 8 + (idx >> 2)) * 65536 + wm * 16384 + (idx & 3) * 64;
        const unsigned uo = (lane >> 2) * 256 + (lane & 3) * 16;
        const char* cs = ((wm & 2) ? a.cim : a.cre) + (size_t)(tn * 64 + (wm & 1) * 2) * 2048;
        unsigned co[4];
#pragma unroll
        for (int i = 0; i < 4; ++i) {
            const int pair = i * 4 + g4, v = r ^ pair, row = pair * 2 + (v >> 3);
            co[i] = ((row & 15) * 4 + (row >> 4)) * 2048 + (v & 7) * 16 - i * 1024;
        }
        bool kv_ok = false;
#pragma unroll
        for (int it = 0; it < 4; ++it) asm volatile("" : "=v"(uu[it]));
        auto cst = [&](int place, int slice) { const char* s = cs + (slice << 7); DMA(lb + place + wm * 4096, co[0], s); dmao_<1024>(co[1], s); dmao_<2048>(co[2], s); dmao_<3072>(co[3], s); };
        auto items = [&](auto pc) {
            constexpr int p = decltype(pc)::value;
            if constexpr (PU == p) {
                WAIT_V(it_cnt(PU - 1, 5, PU - 1)); TIE(fl);
                if (TCH >= 2) TIE(tch);
                if (TCH == 3) TIE(tch2);
                kv_ok = __all(fl[0] == tagA && fl[1] == tagB);
                const char* us = kv_ok ? usrc : a.x;
                for (int it = 0; it < 4; ++it) GLD_B128_SC1(uu[it], uo, us + it * 4096, 0);
            }
            if constexpr (PK == p) ld4_if<0>(0, l16, sw, krow);
            if constexpr (PC0 == p) cst(g2_place(0), SLICE(0));
            if constexpr (PC1 == p) cst(g2_place(1), SLICE(1));
            if constexpr (PQ == p) ld8_if<0>(0, qo, qv, qs, qs + 16384);
            if constexpr (PU == p + 1) GLD_B64_SC1(fl, fpoll, flg + FL_KV, 0);
            if constexpr (PC2 == p) cst(g2_place(5), SLICE(5));
            if constexpr (PC3 == p) cst(g2_place(6), SLICE(6));
        };
#define ITEMS(p) items(std::integral_constant<int, p>{})
        static_assert(KFB > KAT + 1 + __builtin_popcount(KV3) && KV3 < (1 << 23) && KFB <= 13 && KFB <= PC0 && KFB < PU - 1 && nib(PLT, 0) >= KAT && nib(PLT, 3) < PU - 1, "schedule");
        static_for<KAT + 2 + __builtin_popcount(KV3), 15>([&](auto nc) { hb(nc); if constexpr (decltype(nc)::value < 14) items(nc); });
        const unsigned ud = lb + (lane & 3) * CHSZ + UOFF + ((lane >> 2) & 1) * UPL + (16 + wm * 32 + (lane >> 3)) * 16;
        float dz = lane == 0 ? bf2f(dskl) : 0.f;
        if (RX & 2) TIE(dz);
        auto zero = [&]() {
            const u32x4 z4 = {0, 0, 0, 0};
            if (lane < 24) *LDSP(u32x4, lb + L_SY + l16) = z4;
            if (lane < 56) *LDSP(u32x4, lb + L_AGG + wm * 896 + l16) = z4;
            *LDSP(u32x4, lane < 32 ? cb + ((lane >> 3) & 1) * CST + (lane & 7) * 16 : cb + UOFF + (g4 & 1) * UPL + r * 16) = z4;
        };
        auto fill = [&]() {
#pragma unroll
            for (int it = 0; it < 4; ++it) TIE(sw[it]);
            if (RX & 2) sw[0][0] = pack2(bflo(sw[0][0]) + dz, bfhi(sw[0][0]));
#pragma unroll
            for (int it = 0; it < 4; ++it) {
                const unsigned q = cb + 128 + it * 1024 + l16;
                const unsigned d0 = sw[it][0], d1 = sw[it][1], d2 = sw[it][2], d3 = sw[it][3];
                const unsigned o1 = it ? __builtin_amdgcn_readlane(sw[it ? it - 1 : 0][3], 63) : 0u;
                const unsigned x1 = __builtin_amdgcn_update_dpp(o1, d3, 0x138, 0xf, 0xf, false);
                *LDSP(u32x4, q) = sw[it];
                *LDSP(u32x4, q + CST) = u32x4{__builtin_amdgcn_alignbyte(d0, x1, 2), __builtin_amdgcn_alignbyte(d1, d0, 2), __builtin_amdgcn_alignbyte(d2, d1, 2), __builtin_amdgcn_alignbyte(d3, d2, 2)};
            }
        };
        auto uput = [&]() {
            WAIT_V(it_cnt(PU, 0, UE ? 15 : 16));
#pragma unroll
            for (int it = 0; it < 4; ++it) TIE(uu[it]);
            if (!kv_ok) {
                TSE(23);
                for (unsigned spin = 0; spin < SPIN_CAP; ++spin) {
                    GLD_B64_SC1(fl, fpoll, flg + FL_KV, 0);
                    WAIT_V(0); TIE(fl);
                    if (__all(fl[0] == tagA && fl[1] == tagB)) break;
                    __builtin_amdgcn_s_sleep(8);
                }
#pragma unroll
                for (int it = 0; it < 4; ++it) GLD_B128_SC1(uu[it], uo, usrc + it * 4096, 0);
                WAIT_V(0);
#pragma unroll
                for (int it = 0; it < 4; ++it) TIE(uu[it]);
            }
#pragma unroll
            for (int it = 0; it < 4; ++it) *LDSP(u32x4, ud + it * 128) = uu[it];
        };
        static_assert(PU <= (UE ? 15 : 16) && (FP_ != 14 || nib(PLT, 3) <= 12), "u");
        ITEMS(14);
        if (UE) __builtin_amdgcn_s_setprio(UPRIO);
        if (P14 != 3) __builtin_amdgcn_s_setprio(P14);
        if (UE >= 2 || ZP_ == 14) zero();
        if (UE == 2 || FP_ == 14) { WAIT_V(it_cnt(12, 8, 14)); fill(); }
        BAR();
        if (P14 != 3) __builtin_amdgcn_s_setprio(3);
        ITEMS(15);
        TS(4);
        if (P15 != 3) __builtin_amdgcn_s_setprio(P15);
        if (UE < 2 && ZP_ == 15) zero();
        if ((UE & 1) || FP_ == 15) fill();
        if (UE) uput();
        WAIT_L(0);
        TS(5);
        BAR();
        __builtin_amdgcn_s_setprio(FPRIO);
        ITEMS(16);
        if (P16 != FPRIO) __builtin_amdgcn_s_setprio(P16);
        if (S16) __builtin_amdgcn_s_sleep(S16);
        TS(11);
        const unsigned pmine = lb + L_SY + 32 + wm * 4, pall = lb + L_SY + 32 + (lane & 3) * 4;
        static_assert(!PK || (!UE && PK > KFB && PK <= 16), "ssm_k");
        static_assert(!PQ || PQ > KFB, "q");
        if (!UE) { if (PK >= PU) { uput(); sy_post<0>(pmine); WAIT_V(it_cnt(PK, 1, 16)); fill(); } else { if (FP_ == 16) fill(); TS(13); uput(); sy_post<0>(pmine); } ITEMS(17); sy_wait<0>(pall); }
        if (!CPOST) { WAIT_V(0); sy_post<1>(pmine); }
        __builtin_amdgcn_s_setprio(CVPRIO);
        TS(6);
        f32x4 ca[8];
        {
            unsigned kb[4];
#pragma unroll
            for (int i = 0; i < 4; ++i) { kb[i] = cb + (~r & 1) * CST + 2 * (64 + 16 + r - 8 * g4 - 7 + (~r & 1)) + i * 1024; TIE(kb[i]); }
            unsigned ub = cb + UOFF + (g4 & 1) * UPL + (15 + r + (g4 >> 1)) * 16 - 64;
#pragma unroll
            for (int i = 0; i < 8; ++i) { ca[i] = f32x4{0, 0, 0, 0}; TIE(ca[i]); }
            s16x4 FA[4][2]; bf16x8 FB[2][8];
#define CV_A(s, o, e) dsr2_<16 * ((s) + ((o) >> 3)) + 128 * (cv_m((o) & 7) & 1) + 2 * (e)>(FA[(o) & 3][e], kb[cv_m((o) & 7) >> 1])
#define CV_B(s, jb) DSR128(FB[(s) & 1][jb], ub, 64 + 256 * (jb) - 32 * (s))
            static_for<0, 8>([&](auto ic) { constexpr int n = decltype(ic)::value; CV_B(0, n); CV_A(0, n >> 1, n & 1); });
            WAIT_L(0);
            auto body = [&]() {
                static_for<0, 72>([&](auto ic) {
                    constexpr int s = decltype(ic)::value / 36, p = decltype(ic)::value % 36, o = cv_grp(p), m = cv_m(o), jb = p - cv_first(o);
                    if constexpr (jb == 0) { WAIT_LB(cv_wait(o)); if constexpr (!(AMF & 1)) { TIE(FA[o & 3][0]); TIE(FA[o & 3][1]); } }
                    if constexpr (p == 0 && !(AMF & 1)) { for (int i = 0; i < 8; ++i) TIE(FB[s][i]); }
                    const bf16x8 fa = __builtin_shufflevector(FA[o & 3][0], FA[o & 3][1], 0, 1, 2, 3, 4, 5, 6, 7);
                    if constexpr (AMF & 1) mfma_(ca[jb + m], fa, FB[s][jb]); else
                    ca[jb + m] = __builtin_amdgcn_mfma_f32_16x16x32_bf16(fa, FB[s][jb], ca[jb + m], 0, 0, 0);
                    SB();
                    constexpr int rd = cv_read(p);
                    if constexpr (rd >= 32) CV_B(s + 1, rd - 32);
                    else if constexpr (rd >= 0) CV_A(s, rd >> 1, rd & 1);
                    SB();
                });
                for (int i = 0; i < 4; ++i) { kb[i] += 128; TIE(kb[i]); }
                ub -= 64;
            };
#pragma unroll 1
            for (int it = 0; it < QIT; ++it) body();
            {
                TS(19);
                if (CPOST) { WAIT_V(PQ == 17 && PC3 < 17 ? 8 : 0); sy_post<1>(pmine); WAIT_L(0); }
                static_assert(PC3 != 19 || (ZFB == 1 && (BARM & 0x3ff) == 0x220), "stages 5, 6 are covered by the second barrier");
                if (PC3 == 19 || PQ == 19) { sy_wait<0>(lb + L_ES + (lane & P19M) * 256 + 8); ITEMS(19); }
                if (CVLATE) sy_wait<1>(lb + L_SY + (CVLATE == 2 ? wm + (lane & 1) * 4 : lane & 7) * 4);
                TS(20);
                if (PQ == 20 && QSL) __builtin_amdgcn_s_sleep(QSL);
                ITEMS(20);
            }
#pragma unroll 1
            for (int it = QIT; it < 4; ++it) body();
            WAIT_L(0);
            if (AMF & 1) asm volatile("s_nop 7\n\ts_nop 7" : "+v"(ca[0]), "+v"(ca[1]), "+v"(ca[2]), "+v"(ca[3]), "+v"(ca[4]), "+v"(ca[5]), "+v"(ca[6]), "+v"(ca[7]));
#pragma unroll
            for (int i = 0; i < 4; ++i) { TIE(FA[i][0]); TIE(FA[i][1]); }
#pragma unroll
            for (int i = 0; i < 8; ++i) TIE(FB[0][i]);
#undef CV_A
#undef CV_B
        }
        TS(7);
        __builtin_amdgcn_s_setprio(3);
        constexpr int NBAR = __builtin_popcount(BARM & ~VBM);
        unsigned zpub = (xcd * 32 + l31) * 256 + idx * 8, zpoll = ZOWN ? (xcd * 32 + idx) * 256 + l31 * 8 : fpoll;
        {
            const float Dd = bf2f(dskl);
            const char* zd = ws + WS_ZT + (size_t)(xcd * 128 + wm * 32) * 4096 + idx * 128;
            u32x2 kvr[8];
#pragma unroll
            for (int jt = 0; jt < 8; ++jt) if (!(RX & 2)) kvr[jt] = *LDSP(u32x2, cb + UOFF + (g4 >> 1) * UPL + (16 + 16 * jt + r) * 16 + (1 - (g4 & 1)) * 8);
#pragma unroll
            for (int jt = 0; jt < 8; ++jt) {
                const f32x4 y = ca[jt];
                const u32x2 kq = kvr[jt];
                *LDSP(u32x2, cb + (jt * 4 + (r >> 2)) * 144 + (r & 3) * 32 + g4 * 8) = (RX & 2) ? u32x2{pack2(y[0], y[1]), pack2(y[2], y[3])} : ZM ?
                    u32x2{pack2(add1(rb1(y[0]), rb1(mul1(bfhi(kq[1]), Dd))), add1(rb1(y[1]), rb1(mul1(bflo(kq[1]), Dd)))), pack2(add1(rb1(y[2]), rb1(mul1(bfhi(kq[0]), Dd))), add1(rb1(y[3]), rb1(mul1(bflo(kq[0]), Dd))))} :
                    u32x2{pack2(rb1(y[0]) + rb1(bfhi(kq[1]) * Dd), rb1(y[1]) + rb1(bflo(kq[1]) * Dd)), pack2(rb1(y[2]) + rb1(bfhi(kq[0]) * Dd), rb1(y[3]) + rb1(bflo(kq[0]) * Dd))};
            }
            unsigned za = lb + (r >> 2) * CHSZ + (wm * 8 + (g4 >> 1)) * 144 + ((g4 & 1) * 2 + ((r >> 1) & 1)) * 32 + (r & 1) * 8, zo = (lane >> 3) * 4096 + (lane & 7) * 16;
            TIE(za); TIE(zo); TIE(zpub); TIE(zpoll);
            sy_post<3>(pmine); sy_wait<3, 0>(pall);
            v4i16 t[4][2];
#pragma unroll
            for (int it = 0; it < 4; ++it)
#pragma unroll
                for (int e = 0; e < 2; ++e) t[it][e] = __builtin_amdgcn_ds_read_tr16_b64_v4i16(LDSP(v4i16, za + it * 288 + e * 16));
#pragma unroll
            for (int it = 0; it < 4; ++it) { TIE(t[it][0]); TIE(t[it][1]); }
            if (ZSB) BAR();
#pragma unroll
            for (int it = 0; it < 4; ++it) { const bf16x8 f = __builtin_shufflevector(t[it][0], t[it][1], 0, 1, 2, 3, 4, 5, 6, 7); GST_B128_SC1(zo, f, zd + it * 32768, 0); }
        }
#pragma unroll
        for (int i = ZSB; i < ZFB; ++i) BAR();
        WAIT_V(0);
        sy_post<2>(pmine);
        if (wm == 0) {
            sy_wait<2>(pall);
            if (ZOWN) { if (lane < 32) GST_B64_SC1(zpub, tag, flg + FL_Z); }
            else if (lane == 0) GST_B64_SC1(fpub, tag, flg + FL_Z);
        }
        TS(8);
        unsigned qm[8][4];
        if (QM == 1 || QM == 2) {
#pragma unroll
            for (int n = 0; n < 8; ++n) {
                TIE(qv[n]);
                if (QM == 2) { qm[n][0] = qv[n][0] << 16; qm[n][2] = qv[n][1] << 16; } else { qm[n][0] = qv[n][0] & 0xffffu; qm[n][2] = qv[n][1] & 0xffffu; }
                qm[n][1] = qv[n][0] & 0xffff0000u; qm[n][3] = qv[n][1] & 0xffff0000u;
                TIE(qm[n][0]); TIE(qm[n][1]); TIE(qm[n][2]); TIE(qm[n][3]);
            }
        }
#pragma unroll
        for (int i = ZFB; i < NBAR - 1 - (ZE >= 0); ++i) { BAR();
        }
        if (ZSL) __builtin_amdgcn_s_sleep(ZSL);
        asm volatile("s_cmp_lg_u32 %3, 0\n\ts_cbranch_scc1 1f\n\tglobal_load_dwordx2 %0, %1, %2 sc1\n1:" : "=&v"(fl) : "v"(zpoll), "s"(flg + FL_Z), "s"(wm) : "scc", "memory");
        if (NBAR > ZFB) BAR();
        if (wm == 0) {
            unsigned left = SPIN_CAP;
            asm volatile("s_branch 2f\n1:\n\ts_sleep %5\n\tglobal_load_dwordx2 %0, %2, %3 sc1\n2:\n\ts_waitcnt vmcnt(0)\n\tv_cmp_ne_u64 vcc, %4, %0\n\ts_cbranch_vccz 3f\n\ts_sub_u32 %1, %1, 1\n\ts_cbranch_scc0 1b\n3:"
                         : "+v"(fl), "+s"(left) : "v"(zpoll), "s"(flg + FL_Z), "s"(tag), "n"(ZPS) : "vcc", "scc", "memory");
            sy_post<4>(pmine);
        } else sy_wait<4, 2>(lb + L_SY + 32);
        u32x4 zv[4];
        {
            ITEMS(18);
            const char* zsrc = ws + WS_ZT + (size_t)(xcd * 128 + idx * 4 + wm) * 4096;
            static_for<0, 4>([&](auto ic) { constexpr int it = decltype(ic)::value; GLD_B128_SC1(zv[it], (l31 >> 2) * 512 + lh * 64 + (lane & 3) * 16, zsrc, it * 128); });
        }
        WAIT_V(0);
        TS(9);
#pragma unroll
        for (int it = 0; it < 4; ++it) TIE(zv[it]);
        if (!(XAB & 1)) {
        quad_swap<0>(zv[0], zv[1]); quad_swap<0>(zv[2], zv[3]);
        quad_swap<1>(zv[0], zv[2]); quad_swap<1>(zv[1], zv[3]); }
#pragma unroll
        for (int it = 0; it < 4; ++it) TIE(zv[it]);
        char* const oa = a.out + (size_t)(idx * 64 + wm * 16) * 8192 + xcd * 512;
        if (OPRIO != 3) __builtin_amdgcn_s_setprio(OPRIO);
        if (OS0) __builtin_amdgcn_s_sleep(OS0);
        if (OSW && ZE < 0) for (int i = 0; i < wm; ++i) __builtin_amdgcn_s_sleep(OSW);
#pragma unroll
        for (int n = 0; n < 8; ++n) {
            if (!QM || QM == 3) TIE(qv[n]);
            const unsigned z0 = zv[n & 3][2 * (n >> 2)], z1 = zv[n & 3][2 * (n >> 2) + 1];
            const f32x4 o = (XAB & 2) ? __builtin_bit_cast(f32x4, zv[n & 3]) : QM == 1 ? mul4bf(u32x2{z0, z1}, qm[n]) :
                QM == 3 ? f32x4{mul1(bflo(qv[n][0]), bflo(z0)), mul1(bfhi(qv[n][0]), bfhi(z0)), mul1(bflo(qv[n][1]), bflo(z1)), mul1(bfhi(qv[n][1]), bfhi(z1))} :
                QM == 2 && (RX & 4) ? f32x4{mul1(__uint_as_float(qm[n][0]), bflo(z0)), mul1(__uint_as_float(qm[n][1]), bfhi(z0)), mul1(__uint_as_float(qm[n][2]), bflo(z1)), mul1(__uint_as_float(qm[n][3]), bfhi(z1))} :
                QM == 2 ? f32x4{rb1(mul1(__uint_as_float(qm[n][0]), bflo(z0))), rb1(mul1(__uint_as_float(qm[n][1]), bfhi(z0))), rb1(mul1(__uint_as_float(qm[n][2]), bflo(z1))), rb1(mul1(__uint_as_float(qm[n][3]), bfhi(z1)))} :
                f32x4{rb1(bflo(qv[n][0]) * bflo(z0)), rb1(bfhi(qv[n][0]) * bfhi(z0)), rb1(bflo(qv[n][1]) * bflo(z1)), rb1(bfhi(qv[n][1]) * bfhi(z1))};
            if (n == ZE) { BAR(); if (OSW) for (int i = 0; i < wm; ++i) __builtin_amdgcn_s_sleep(OSW); }
            if (n == 0 && OSG) for (int i = 0; i < (idx & 3); ++i) __builtin_amdgcn_s_sleep(OSG);
            if (!(XAB & 4))
            GST_B128_NT(lh * 32768 + l31 * 16, o, oa + ((n >> 2) * 8 + (n & 3)) * 8192, 0);
            if (OSL && n < 7 && n + 1 != ZE) __builtin_amdgcn_s_sleep(OSL);
        }
        asm volatile(".Lfk_code_a:");
        TSE(10);
        TS_FLUSH();
        return;
    }

    u32x2 fl;
    const unsigned* dv = kvv;
#define G1_ISSUE(st, i) do { if constexpr ((st) < DFN) { if constexpr ((i) > 0) dmaof_<(i) * 1024>(dv[i], g1src + ((((st) << 7) + rot7) & 1920)); else dmaf_(g1dst + g1_slot(st), dv[i], g1src + ((((st) << 7) + rot7) & 1920)); } else if constexpr (M1 && (i) > 0) dmao_<(i) * 1024>(dv[i], g1src + ((((st) << 7) + rot7) & 1920)); \
        else DMA(g1dst + g1_slot(st) + (i) * 1024, dv[i], g1src + ((((st) << 7) + rot7) & 1920)); } while (0)
    unsigned k0 = trow + wm * 32768 + g4 * 2048 + ((l15 ^ (2 * g4)) << 4), k1 = k0 + 7168, k2 = (k0 ^ 128) + 14336, k3 = k2 + 7168;
    auto pre = [&](auto ic) {
        constexpr int n = decltype(ic)::value;
        if constexpr (n == KVP) { if (!HKV) kv_dma(g1dst + L_KS, k0, k1, k2, k3, wn ? a.v : a.k); }
        else static_for<0, 4>([&](auto jc) { G1_ISSUE(n - (n > KVP), decltype(jc)::value); });
    };
    static_for<1, AP0 / 4>(pre);
    TS(14);
    SLICE_TABLE();
    const int j0 = 4 * run - (32 - 2 * tm);
    const int pneg = EALL ? 0 : 16 * (wm >> 1) + 15 - (32 - 2 * tm);
    unsigned long long em0 = __ballot(j0 >= 0), em1 = __ballot(j0 + 2 >= 0);
    if (EM == 2) asm volatile("" : "+s"(em0), "+s"(em1));
    unsigned eo[2], pes, plr, est, hw[2], ta[2], po, pmine, pall, pown, pzero, g2v[2], cad[2][2], to[2][2], xo, dso, fst, fld, cwa;
    unsigned cta = lb + L_TS + st * 16, ctb = lb + L_TR + run * 1024 + st * 16;
    TIE(cta); TIE(ctb);
    f32x4 ct[5]; f32x2 V1;
    const int gm = PAIR ? wid >> 1 : wm, gk = PAIR ? wid & 1 : wn, gs = gk * 4 + (gm & 2);
    auto addrs = [&]() {
        int ln = lane;
        TIE(ln);
        const int l15 = ln & 15, g4 = ln >> 4, st = sq * 16 + l15, run = (wm >> 1) * 4 + g4, j0 = 4 * run - (32 - 2 * tm);
        pes = lb + L_ES + wid * 256 + l15 * 16;
        plr = lb + L_AGG + st * 8 + run * 512; est = ((tm * 2 + (wm >> 1)) * 1024 + tn * 64 + st) * 16 + (EREP ? g4 << 19 : 0);
#pragma unroll
        for (int i = 0; i < 2; ++i) { eo[i] = (j0 + 2 * i < 0 ? 0 : j0 + 2 * i) * 16384 + (tn * 64 + st) * 16 + (EREP == 1 ? (tm >> 2) << 19 : EREP == 2 ? (tm & 3) << 19 : 0); TIE(eo[i]); }
        TIE(plr); TIE(pes); TIE(est);
        const unsigned ht = lb + L_HT + wid * 4096;
#pragma unroll
        for (int j = 0; j < 2; ++j) {
            const int pr = (8 * (g4 & 1) + 4 * j + (l15 >> 2)) ^ ((g4 & 1) * 4), pw = l15 ^ ((l15 >> 3) * 4);
            ta[j] = ht + (g4 >> 1) * 512 + pr * 32 + ((((l15 >> 1) ^ (pr >> 2)) & 1) << 4) + 8 * (ln & 1);
            hw[j] = ht + g4 * 1024 + pw * 32 + (((j ^ (pw >> 2)) & 1) << 4);
            TIE(ta[j]); TIE(hw[j]);
        }
        po = pes + ((wm & 2) ? -512 : 512);
        pmine = lb + L_SY + wid * 4; pall = lb + L_SY + (ln & 7) * 4; pown = lb + L_SY + (ln < 4 ? 32 + ln * 4 : gs * 4 + (ln & 4)); pzero = lb + L_SY;
        xo = (tm * 128 + gm * 32 + gk * 16 + g4 * 4) * 2048 + tn * 128 + l15 * 8 + 4096; dso = xo & 2047;
        fst = (tm * 16 + l15) * 128 + tn * 8; fld = (tm * 16 + tn) * 128 + l15 * 8;
#pragma unroll
        for (int i = 0; i < 2; ++i) {
            const int pair = i * 4 + g4, v = l15 ^ pair ^ ((wm & 1) * 8);
            g2v[i] = (pair * 2 + (v >> 3)) * 8192 + (v & 7) * 16 + (tn * 64 + wm) * 2048 - i * 1024;
            TIE(g2v[i]);
#pragma unroll
            for (int j = 0; j < 2; ++j) {
                cad[i][j] = lb + L_C + (img_off((g4 >> 1) * 64 + l15, 4 * gk + (g4 & 1)) ^ (i * 32) ^ (j * 128));
                to[i][j] = ta[j] + (i ^ gk) * 1024 + (gm & 1) * 2048 + (gs - wid) * 4096;
                TIE(cad[i][j]); TIE(to[i][j]);
            }
        }
        cwa = lb + wid * 2048 + ln * 16;
        TIE(cwa); TIE(po);
        TIE(pmine); TIE(pall); TIE(pown); TIE(pzero); TIE(xo); TIE(dso); TIE(fst); TIE(fld);
    };
    if (!APRE) addrs();
    unsigned aad[2][2], bad[2];
    {
        const int aoff = img_off((wm >> 1) * 64 + l15, g4), boff = img_off(128 + wn * 64 + (wm & 1) * 16 + l15, g4);
#pragma unroll
        for (int ks = 0; ks < 2; ++ks) { aad[ks][0] = lb + (aoff ^ (ks * 64)); aad[ks][1] = lb + (aoff ^ (ks * 64) ^ 128); bad[ks] = lb + (boff ^ (ks * 64)); TIE(aad[ks][0]); TIE(aad[ks][1]); TIE(bad[ks]); }
    }
    if (!HKV && KVP >= AP0 / 4) { TIE(k0); TIE(k1); TIE(k2); TIE(k3); }
    static_for<AP0 / 4, NBK + 1>(pre);
    TS(1);
    float nL1i, nC1r, nC1i;
    unsigned tagA, tagB;
    f32x4 acc[4][2];
    {
#pragma unroll
        for (int i = 0; i < 4; ++i) { acc[i][0] = f32x4{0, 0, 0, 0}; acc[i][1] = f32x4{0, 0, 0, 0}; }
        bf16x8 fa[2][4], fb[2][2];
        if (GPRIO) __builtin_amdgcn_s_setprio(GPRIO);
        WAIT_V(g1_cnt(0)); BAR();
        TS(2);
#define G1_FRAG(g, n) do { constexpr int bo_ = g1_slot((g) >> 1), hi_ = bo_ & ~65535, x_ = (n) < 3 ? (n) >> 1 : (n) - 2; \
        if constexpr ((n) == 0 || (n) == 2) DSR128(fb[(g) & 1][x_], bad[(g) & 1] + hi_, bo_ - hi_ + x_ * 4096); \
        else DSR128(fa[(g) & 1][x_], aad[(g) & 1][x_ & 1] + hi_, bo_ - hi_ + x_ * 2048); } while (0)
        static_for<0, 6>([&](auto ic) { constexpr int n = decltype(ic)::value; G1_FRAG(0, n); });
        static_for<0, 32>([&](auto ic) {
            constexpr int g = decltype(ic)::value, gn = g + 1, n0 = g1_upto(g - 1), nd = g1_upto(g) - n0;
            constexpr bool bar = (gn & 1) == 0 && gn < 32;
            if constexpr (g == 1) {
                DSR128(ct[0], cta, 0); DSR128(ct[1], cta, 1024); DSR128(ct[2], cta, 2048); DSR128(ct[3], ctb, 0); DSR128(ct[4], ctb, 8192); DSR64(V1, ctb, 16384);
            }
            if constexpr (g == 3) {
#pragma unroll
                for (int i = 0; i < 5; ++i) TIE(ct[i]);
                TIE(V1);
                nL1i = -ct[0][1]; nC1r = -ct[0][2]; nC1i = -ct[0][3];
                TIE(nL1i); TIE(nC1r); TIE(nC1i);
                tagA = __builtin_amdgcn_readfirstlane(__float_as_uint(ct[2][2])); tagB = __builtin_amdgcn_readfirstlane(__float_as_uint(ct[2][3]));
                ties_(tagA); ties_(tagB);
            }
            if constexpr (bar) {
                static_assert(g1_cnt(gn >> 1) >= 0, "schedule");
                WAIT_VB(g1_cnt(gn >> 1)); WAIT_LB(0); BAR();
                if constexpr (((gn >> 1) & 3) == 0 || gn >= 26) TS(gn >= 26 ? 23 + (gn >> 1) - 13 : 16 + (gn >> 3));
#pragma unroll
                for (int i = 0; i < 4; ++i) if (!(AMF & 2)) TIE(fa[g & 1][i]);
                if (!(AMF & 2)) { TIE(fb[g & 1][0]); TIE(fb[g & 1][1]); }
            }
            static_for<0, 8>([&](auto mc) {
                constexpr int m = decltype(mc)::value, i = m >> 1, j = m & 1, js = (SNK & 1) && (i & 1) ? 1 - j : j;
                if constexpr (W1 && !bar && g > 0 && g < 31) {
                    if constexpr (m == 0) WAIT_LB(W1 == 1 ? 3 : 2);
                    if constexpr (m == 2 && W1 == 1) WAIT_LB(4);
                    if constexpr (m == 4) WAIT_LB(4);
                } else
                if constexpr (!bar && (i == 0 || j == 0)) {
                    WAIT_LB(5 - (i == 0 ? j + 1 : i + 2) + (gn < 32 ? (m < 6 ? m : 6) : 0));
                    if constexpr (!(AMF & 2) || g == 0) { TIE(fa[g & 1][i]); TIE(fb[g & 1][j]); }
                }
                if constexpr ((AMF & 2) && g > 0) mfma_(acc[i][js], fa[g & 1][i], fb[g & 1][js]); else
                acc[i][j] = __builtin_amdgcn_mfma_f32_16x16x32_bf16(fa[g & 1][i], fb[g & 1][j], acc[i][j], 0, 0, 0);
                SB();
                if constexpr (gn < 32 && m < 6) G1_FRAG(gn, m);
                constexpr int d = nd > 2 ? ((m & 1) ? m >> 1 : -1) : (m == DP0 ? 0 : m == DP1 ? 1 : -1);
                if constexpr (d >= 0 && d < nd) G1_ISSUE((n0 + d) >> 2, (n0 + d) & 3);
                SB();
            });
        });
#undef G1_FRAG
    }
    TS(3);
    const unsigned tagG = tagA ^ tagB ^ 0x9E3779B9u;
    __builtin_amdgcn_s_setprio(MPRIO);
    f32x2 ur[8], ui[8], hr, hi;
    const f32x2 L1 = {ct[0][0], ct[0][1]}, C1 = {ct[0][2], ct[0][3]}, L16 = {ct[1][2], ct[1][3]}, Q2r = {ct[3][0], ct[3][1]}, Q2i = {ct[3][2], ct[3][3]};
    const float L8r = ct[1][0], L8i = ct[1][1], L64r = ct[2][0], L64i = ct[2][1];
    u32x4 eg[4], cr[2 * NR + 1];
    const char* g2src = wn ? a.cim : a.cre;
#define CR_LOAD(n0, n1) static_for<n0, n1>([&](auto ic) { constexpr int n = decltype(ic)::value; GLD_B128(cr[n], g2v[n & 1], g2src + (SLICE(7 + (n >> 1)) << 7), (n & 1) * 1024); })
#define E_LOADS "global_load_dwordx4 v[104:107], %[a0], %[b0] " EMOD "\n\tglobal_load_dwordx4 v[108:111], %[a0], %[b1] " EMOD "\n\t" \
                "global_load_dwordx4 v[112:115], %[a1], %[b0] " EMOD "\n\tglobal_load_dwordx4 v[116:119], %[a1], %[b1] " EMOD "\n"
#define E_POLL() asm volatile("s_cmp_lt_i32 %[on], 0\n\ts_cbranch_scc1 1f\n\ts_mov_b64 exec, %[m1]\n\tglobal_load_dwordx4 v[112:115], %[a1], %[b0] " EMOD "\n\tglobal_load_dwordx4 v[116:119], %[a1], %[b1] " EMOD "\n\t" \
        "s_mov_b64 exec, %[m0]\n\ts_cbranch_execz 2f\n\tglobal_load_dwordx4 v[104:107], %[a0], %[b0] " EMOD "\n\tglobal_load_dwordx4 v[108:111], %[a0], %[b1] " EMOD "\n2:\n\ts_mov_b64 exec, -1\n1:" \
        : "+{v[104:107]}"(eg[0]), "+{v[108:111]}"(eg[1]), "+{v[112:115]}"(eg[2]), "+{v[116:119]}"(eg[3]) \
        : [a0] "v"(eo[0]), [a1] "v"(eo[1]), [b0] "s"(ws + WS_E), [b1] "s"(ws + WS_E + 16384), [on] "s"(pneg), [m0] "s"(em0), [m1] "s"(em1) : "scc", "memory")
    {
        asm volatile("s_nop 7\n\ts_nop 3" : "+v"(acc[0][0]), "+v"(acc[0][1]), "+v"(acc[1][0]), "+v"(acc[1][1]), "+v"(acc[2][0]), "+v"(acc[2][1]), "+v"(acc[3][0]), "+v"(acc[3][1]));
#pragma unroll
        for (int p = 0; p < 8; ++p) { ur[p] = f32x2{acc[p >> 1][0][2 * (p & 1)], acc[p >> 1][0][2 * (p & 1) + 1]}; ui[p] = f32x2{acc[p >> 1][1][2 * (p & 1)], acc[p >> 1][1][2 * (p & 1) + 1]}; }
        f32x2 gr = ur[0], gi = ui[0];
#pragma unroll
        for (int p = 1; p < 8; ++p) { f32x2 nr, ni; if (PLN & 1) c2_mad(nr, ni, L1[0], nL1i, L1[0], L1[1], gr, gi, ur[p], ui[p]); else pk_cmad(nr, ni, L1, gr, gi, ur[p], ui[p]); gr = nr; gi = ni; }
        float fr, fi;
        cmad(fr, fi, L8r, L8i, gr[0], gi[0], gr[1], gi[1]);
        {
            const f32x2 w = cmul4(V1, f32x2{fr, fi});
            const float wr = sum4(w[0]), wi = sum4(w[1]);
            const u32x4 gq = {__float_as_uint(wr), tagA, __float_as_uint(wi), tagG};
            if (EREP ? (EALL || EREP == 2 || g4 >= (tm >> 2)) : g4 == 0) GST_B128_SC1(est, gq, ws + WS_E, 0);
        }
        *LDSP(f32x2, plr + 7 * 512) = f32x2{fr, fi};
        *LDSP(unsigned, pes + 8) = 0;
        if (EM == 2) {
#pragma unroll
            for (int i = 0; i < 4; ++i) eg[i] = u32x4{0u, tagA, 0u, tagG};
            asm volatile("" : "+{v[104:107]}"(eg[0]), "+{v[108:111]}"(eg[1]), "+{v[112:115]}"(eg[2]), "+{v[116:119]}"(eg[3]));
        }
        TS(21);
        if (EP == 2) E_POLL();
        auto coef = [&]() {
#pragma unroll
            for (int p = 0; p < 8; ++p) { f32x2 nr, ni; if (PLN & 2) c2_mul(nr, ni, C1[0], nC1i, nC1r, nC1i, ur[p], ui[p]); else pk_conj_mul(nr, ni, C1, ur[p], ui[p]); ur[p] = nr; ui[p] = ni; TIE(ur[p]); TIE(ui[p]); }
        };
        if (CFB) coef();
        WAIT_L(0); BAR();
        f32x2 e[7];
#pragma unroll
        for (int j = 0; j < 7; ++j) e[j] = *LDSP(f32x2, plr + j * 512);
        if (EP == 1) E_POLL();
        CR_LOAD(0, CR1);
        if (!CFB) coef();
#pragma unroll
        for (int j = 1; j < 7; ++j) { if (PLN & 4) { float xr = e[j][0], xi = e[j][1]; cmac(xr, xi, L16[0], L16[1], e[j - 1][0], e[j - 1][1]); e[j] = f32x2{xr, xi}; } else cmac2(e[j], L16, e[j - 1]); }
        float mr, mi;
        cmad(mr, mi, L8r, L8i, e[6][0], e[6][1], gr[0], gi[0]);
        if (PLN & 4) c2_mul(hr, hi, C1[0], nC1i, nC1r, nC1i, f32x2{e[6][0], mr}, f32x2{e[6][1], mi}); else pk_conj_mul(hr, hi, C1, f32x2{e[6][0], mr}, f32x2{e[6][1], mi});
        TIE(hr); TIE(hi);
        CR_LOAD(CR1, CR1 + CR2);
        TS(4);
    }
    f32x2 G;
    {
        float pr = 0.f, pi = 0.f;
        if (!EP) E_POLL();
        if (pneg >= 0) {
            unsigned t0, t1, t2, left = SPIN_CAP;
            asm volatile("s_waitcnt vmcnt(%[c])\n\ts_branch 2f\n1:\n\ts_sleep %[sl]\n\t" EM_ON E_LOADS EM_OFF "\ts_waitcnt vmcnt(0)\n2:\n\t"
                         "v_bitop3_b32 %[t0], v105, v109, %[ta] bitop3:0x7e\n\tv_bitop3_b32 %[t1], v113, v117, %[ta] bitop3:0x7e\n\tv_bitop3_b32 %[t2], v107, v111, %[tg] bitop3:0x7e\n\t"
                         "v_or3_b32 %[t0], %[t0], %[t1], %[t2]\n\tv_bitop3_b32 %[t1], v115, v119, %[tg] bitop3:0x7e\n\tv_or_b32 %[t0], %[t0], %[t1]\n\t"
                         "v_cmp_ne_u32 vcc, 0, %[t0]\n\ts_cbranch_vccz 3f\n\ts_sub_u32 %[n], %[n], 1\n\ts_cbranch_scc0 1b\n3:"
                         : "+{v[104:107]}"(eg[0]), "+{v[108:111]}"(eg[1]), "+{v[112:115]}"(eg[2]), "+{v[116:119]}"(eg[3]), [t0] "=&v"(t0), [t1] "=&v"(t1), [t2] "=&v"(t2), [n] "+s"(left)
                         : [a0] "v"(eo[0]), [a1] "v"(eo[1]), [b0] "s"(ws + WS_E), [b1] "s"(ws + WS_E + 16384), [ta] "s"(tagA), [tg] "s"(tagG), [sl] "n"(ESL), [c] "n"(CR1 + CR2) : "vcc", "scc", "memory");
            float ar = __uint_as_float(eg[1][0]), ai = __uint_as_float(eg[1][2]), br = __uint_as_float(eg[3][0]), bi = __uint_as_float(eg[3][2]);
            cmac(ar, ai, L64r, L64i, __uint_as_float(eg[0][0]), __uint_as_float(eg[0][2]));
            cmac(br, bi, L64r, L64i, __uint_as_float(eg[2][0]), __uint_as_float(eg[2][2]));
            if (EALL) { if (j0 < 0) ar = ai = 0.f; if (j0 + 2 < 0) br = bi = 0.f; }
            const f32x2 pu = cmul4(f32x2{ct[4][2], ct[4][3]}, f32x2{ar, ai});
            pr = pu[0]; pi = pu[1];
            cmac(pr, pi, ct[4][0], ct[4][1], br, bi);
            pr = sum4(pr); pi = sum4(pi);
        }
        TS(5);
        *LDSP(u32x4, pes) = u32x4{__float_as_uint(pr), __float_as_uint(pi), 1u, 0u};
        CR_LOAD(CR1 + CR2, CR1 + CR2 + CR3);
        u32x4 o;
        asm volatile("1:\n\tds_read_b128 %0, %1\n\ts_waitcnt lgkmcnt(0)\n\tv_cmp_eq_u32 vcc, 0, v122\n\ts_cbranch_vccnz 1b" : "=&{v[120:123]}"(o) : "v"(po) : "vcc", "memory");
        G = f32x2{pr + __uint_as_float(o[0]), pi + __uint_as_float(o[1])};
    }
    TS(6);
    {
        if (PLN & 8) c2_mac(hr, hi, G[0], -G[1], -G[0], -G[1], Q2r, Q2i); else pk_conj_mac(hr, hi, G, Q2r, Q2i);
#pragma unroll
        for (int p = 0; p < 8; ++p) { if (PLN & 8) c2_mac(ur[p], ui[p], L1[0], L1[1], L1[0], nL1i, hr, hi); else pk_cmac_conj(ur[p], ui[p], L1, hr, hi); hr = ur[p]; hi = ui[p]; }
        const char* hd = ws + WS_H + (size_t)((tm * 4 + (wm & 2)) * 2 + wn) * HP + (tn * 4 + (wm & 1) * 2) * 1024;
#pragma unroll
        for (int e2 = 0; e2 < 2; ++e2)
#pragma unroll
            for (int c = 0; c < 2; ++c) {
                const f32x2* o = c ? ui : ur;
                *LDSP(u32x4, hw[e2] + c * 512) = u32x4{pack2(o[0][e2], o[1][e2]), pack2(o[2][e2], o[3][e2]), pack2(o[4][e2], o[5][e2]), pack2(o[6][e2], o[7][e2])};
            }
        v4i16 t[4][2];
#pragma unroll
        for (int n = 0; n < 4; ++n)
#pragma unroll
            for (int j = 0; j < 2; ++j) t[n][j] = __builtin_amdgcn_ds_read_tr16_b64_v4i16(LDSP(v4i16, ta[j] + n * 1024));
        sy_post<1>(pmine);
        static_for<0, 4>([&](auto ic) {
            constexpr int n = decltype(ic)::value;
            const bf16x8 f = __builtin_shufflevector(t[n][0], t[n][1], 0, 1, 2, 3, 4, 5, 6, 7);
            GST_B128_SC1(l16, f, hd + (n >> 1) * 2 * HP + ((n & 1) ^ wn) * 1024, 0);
        });
    }
    TS(7);
    bf16x8 fa2[64];
    u32x2 xv[4], dsv;
    const char* Aw = ws + WS_H + (size_t)((tm * 4 + gm) * 2 + gk) * HP;
    const unsigned g2dst = lb + wid * 2048;
#define G2_ISSUE(st, i) do { if constexpr (i) dmao_<1024>(g2v[1], g2src + (SLICE(st) << 7)); else DMA(g2dst + g2_place(st), g2v[0], g2src + (SLICE(st) << 7)); } while (0)
#define G2_A(g) GLD_B128_SC1(fa2[g], l16, Aw + (SLICE((g) >> 2) << 12), ((g) & 3) * 1024)
    {
        {
            sy_wait<1>(pown);
            s16x4 fo[4][2]; bf16x8 c[8];
            static_for<0, 2>([&](auto kc) {
                constexpr int ks = decltype(kc)::value;
                static_for<0, 4>([&](auto ic) { constexpr int i = decltype(ic)::value; dsrtr_<ks * 4096>(fo[2 * ks + (i >> 1)][i & 1], to[i >> 1][i & 1]); });
                static_for<0, 4>([&](auto ic) { constexpr int i = decltype(ic)::value; DSR128(c[4 * ks + i], cad[ks][i & 1], 2 * G2STAGE + i * 2048); });
            });
            static_for<0, 8>([&](auto ic) {
                constexpr int n = decltype(ic)::value, ks = n >> 2, i = n & 3;
                WAIT_L(ks ? 3 - i : 11 - i);
                TIE(c[n]);
                if constexpr (i == 0) { TIE(fo[2 * ks][0]); TIE(fo[2 * ks][1]); TIE(fo[2 * ks + 1][0]); TIE(fo[2 * ks + 1][1]); }
#pragma unroll
                for (int j = 0; j < 2; ++j)
                    acc[i][j] = __builtin_amdgcn_mfma_f32_16x16x32_bf16(__builtin_shufflevector(fo[2 * ks + j][0], fo[2 * ks + j][1], 0, 1, 2, 3, 4, 5, 6, 7), c[n], ks ? acc[i][j] : f32x4{0, 0, 0, 0}, 0, 0, 0);
                SB();
            });
#pragma unroll
            for (int i = 0; i < 4; ++i) { TIE(acc[i][0]); TIE(acc[i][1]); }
        }
        if (CEARLY) sy_post<0>(pmine);
        static_for<0, 4>([&](auto ic) { constexpr int it = decltype(ic)::value; GLD_B64(xv[it], xo, a.x, it * 2048 - 4096); });
        GLD_B64(dsv, dso, a.dskip, 0);
        if (CEARLY) {
            sy_wait<0>(pall);
            static_for<4, 4 + CEARLY>([&](auto ic) { constexpr int n = decltype(ic)::value; G2_ISSUE(n >> 1, n & 1); });
        }
        TS(16);
        WAIT_V(5 + CEARLY);
        sy_post<2>(pmine);
        if (!CEARLY || wid == (NOOWN ? 1 : 0) || (NOOWN == 1 && wid == 0)) sy_wait<2, 0>(pall);
        const u32x2 tagv = {__float_as_uint(ct[2][2]), __float_as_uint(ct[2][3])};
        if (wid == (NOOWN ? 1 : 0)) asm volatile("s_mov_b64 exec, 0xffff\n\tglobal_store_dwordx2 %0, %1, %2 sc1\n\ts_mov_b64 exec, -1" :: "v"(fst), "v"(tagv), "s"(flg + FL_H) : "memory");
        if (PFIRST) asm volatile("s_cmp_lg_u32 %3, 0\n\ts_cbranch_scc1 1f\n\tglobal_load_dwordx2 %0, %1, %2 sc1\n1:" : "=&v"(fl) : "v"(fld), "s"(flg + FL_H), "s"(wid) : "scc", "memory");
        TS(8);
        static_for<4 + CEARLY, 10>([&](auto ic) { constexpr int n = decltype(ic)::value; G2_ISSUE(n >> 1, n & 1); });
        CR_LOAD(2 * NR - CRL, 2 * NR);
        TS(13);
        if (wid == 0) {
            unsigned left = SPIN_CAP;
            if (PFIRST)
            asm volatile("s_waitcnt vmcnt(6)\n\ts_branch 3f\n1:\n\tglobal_load_dwordx2 %0, %2, %3 sc1\n\ts_waitcnt vmcnt(0)\n3:\n\tv_cmp_ne_u64 vcc, %0, %4\n\ts_cbranch_vccz 2f\n\ts_sub_u32 %1, %1, 1\n\ts_cbranch_scc0 1b\n2:"
                         : "+v"(fl), "+s"(left) : "v"(fld), "s"(flg + FL_H), "v"(tagv) : "vcc", "scc", "memory");
            else
            asm volatile("1:\n\tglobal_load_dwordx2 %0, %2, %3 sc1\n\ts_waitcnt vmcnt(0)\n\tv_cmp_ne_u64 vcc, %0, %4\n\ts_cbranch_vccz 2f\n\ts_sub_u32 %1, %1, 1\n\ts_cbranch_scc0 1b\n2:"
                         : "=&v"(fl), "+s"(left) : "v"(fld), "s"(flg + FL_H), "v"(tagv) : "vcc", "scc", "memory");
            sy_post<3>(pmine);
            if (b == 255 && lane == 0) GST_B64_SC1(0u * lane, tagv, flg + FL_EPOCH);
        } else if (g2_bar(3)) sy_wait<3>(pzero);
        else { WAIT_V(CRL); sy_post<3>(pmine); }
        if (!g2_bar(3)) sy_wait<3>(pall);
        TS(20);
        static_for<4, 4 + AW>([&](auto ic) { constexpr int g = decltype(ic)::value; G2_A(g); });
    }
    TS(9);
    __builtin_amdgcn_s_setprio(0);
    unsigned yw, yr, yp, yo, lzw;
    static_assert(PAIR, "yr");
    unsigned dm[4];
    auto dmask = [&]() { TIE(dsv); dm[0] = dsv[0] & 0xffffu; dm[1] = dsv[0] & 0xffff0000u; dm[2] = dsv[1] & 0xffffu; dm[3] = dsv[1] & 0xffff0000u; TIE(dm[0]); TIE(dm[1]); TIE(dm[2]); TIE(dm[3]); };
    auto skip = [&](int it) { TIE(xv[it]); const f32x4 y = mul4bf(xv[it], dm); for (int i = 0; i < 4; ++i) acc[i][0][it] = add1(acc[i][0][it], y[i]); if (it & 1) { for (int i = 0; i < 4; ++i) TIE(acc[i][0]); } };
    {
        bf16x8 fb[2][4];
#define G2_FRAG(g, i) do { constexpr int pl_ = g2_place((g) >> 1), b_ = pl_ >= L_C ? L_C : pl_ >= 32768 ? 32768 : 0; DSR128(fb[(g) & 1][i], cad[(g) & 1][(i) & 1] + (b_ - L_C), pl_ - b_ + (i) * 2048); } while (0)
        static_for<0, 4>([&](auto ic) { constexpr int i = decltype(ic)::value; G2_FRAG(2, i); });
        if (YS == 0) { WAIT_V(AW + 6 + CRL); dmask(); skip(0); skip(1); skip(2); skip(3); }
        static_for<2, 32>([&](auto ic) {
            constexpr int g = decltype(ic)::value, gn = g + 1;
            constexpr bool bar = g2_bar(g) && !((VBM >> g) & 1);
            constexpr int n0 = g2_upto(g - 1), n1 = g2_upto(g);
            if constexpr (bar) {
                constexpr int ca = g2_cnt(2 * g, 2 * g), cs = g2_cover(g) >= 7 + NR && !(LZ && g == LASTB) ? g2_cnt(100 + 2 * g2_cover(g) + 1, 2 * g) : 63;
                static_assert(cs >= 0 && g2_wr(g - 1) >= 2 * (g2_cover(g) < 6 + NR ? g2_cover(g) - 6 : NR), "schedule");
                WAIT_VB(AWB && ca < cs ? ca : cs); WAIT_LB(0); BAR();
                if constexpr (!AWB) WAIT_V(ca);
#pragma unroll
                for (int i = 0; i < 4; ++i) if (!(AMF & 4)) TIE(fb[g & 1][i]);
                if (!(AMF & 4)) TIE(fa2[2 * g]);
            }
            if constexpr (LZ && g == lz_chk()) { WAIT_LB(0); sy_chk<0>(lzw, pall); }
            constexpr int xl = g2_wr(g - 1) - g2_wr(g - 2) + (g - 1 == EGX) + (LZ && g - 1 == lz_post());
            static_for<0, 8>([&](auto mc) {
                constexpr int m = decltype(mc)::value, i = m & 3, j = m >> 2, f = 2 * g + j;
                static_assert(g2_cnt(f, f) >= 0, "schedule");
                constexpr int jj = LSW && g == 31 ? 1 - j : j, ff = 2 * g + jj;
                if constexpr (i == 0 && (j || !bar)) { WAIT_VB(LSW && g == 31 ? 0 : g2_cnt(f, f)); if constexpr (!(AMF & 4) || g == 2 || g == 31) TIE(fa2[ff]); }
                if constexpr (W2 && g > 2 && g < 31) { if constexpr (!bar && j == 0 && (i & W2) == 0) WAIT_LB(3 - W2 + xl); } else
                if constexpr (!bar && j == 0) { WAIT_LB((gn < 32 ? 3 : 3 - i) + xl); if constexpr (!(AMF & 4) || g == 2 || g == 31) TIE(fb[g & 1][i]); }
                constexpr int is = (SNK & 2) && j ? 3 - i : i;
                if constexpr ((AMF & 4) && g > 2 && g < 31) mfma_(acc[is][jj], fa2[ff], fb[g & 1][is]); else
                acc[i][jj] = __builtin_amdgcn_mfma_f32_16x16x32_bf16(fa2[ff], fb[g & 1][i], acc[i][jj], 0, 0, 0);
                SB();
                if constexpr (LSW && g == 31 && j) *LDSP(f32x4, yw + i * 1024) = acc[i][1];
                if constexpr (g == EGX && m == 4) sy_post<5>(pmine);
                if constexpr (LZ && g == lz_post() && m == 4) sy_post<0>(pmine);
                if constexpr (LZ && g + 1 == lz_chk() && m == 4) sy_pre<0>(lzw, pall);
                if constexpr (g == 29 && m == 5) { yw = cwa + L_Y + wid * 2048; yr = yw ^ (PAIR ? 4096 : 16384); yp = pmine ^ (PAIR ? 4 : 16); yo = (cwa & 255) + ((cwa & 768) << 7); TIE(yw); TIE(yr); TIE(yp); TIE(yo); }
                if constexpr (gn < 32 && m < 4) G2_FRAG(gn, m);
                if constexpr (m >= 4 && g2_wr(g - 1) + m - 4 < g2_wr(g)) {
                    constexpr int n = g2_wr(g - 1) + m - 4, pl = g2_place(7 + (n >> 1)), pb = pl >= L_C ? L_C : 0;
                    asm volatile("ds_write_b128 %0, %1 offset:%2" :: "v"(cwa + pb), "v"(cr[n]), "n"(pl - pb + (n & 1) * 1024) : "memory");
                }
                if constexpr (i == 1 && n0 + j < n1) G2_ISSUE((n0 + j) >> 1, (n0 + j) & 1);
                if constexpr (i >= 2 && g2_ald(f, i & 1) >= 0) G2_A(g2_ald(f, i & 1));
                SB();
            });
        });
#undef G2_FRAG
    }
    TS(11);
    if (EPRIO) __builtin_amdgcn_s_setprio(EPRIO);
    {
#pragma unroll
        for (int i = 0; i < 4; ++i) if (!LSW) *LDSP(f32x4, yw + i * 1024) = acc[i][1];
        sy_post<4>(pmine);
        sy_wait<4, 0>(yp);
        char* const ob = a.out + (size_t)(tm * 128 + gm * 32 + gk * 16) * 8192 + 4096 + tn * 256;
        f32x4 w[4];
#pragma unroll
        for (int i = 0; i < 4; ++i) w[i] = *LDSP(f32x4, yr + i * 1024);
        f32x4 o[4];
#pragma unroll
        for (int r = 0; r < 4; ++r) { o[r] = f32x4{add1(w[0][r], acc[0][0][r]), add1(w[1][r], acc[1][0][r]), add1(w[2][r], acc[2][0][r]), add1(w[3][r], acc[3][0][r])}; if (EGX) TIE(o[r]); }
        if (EGX && !wn) sy_wait<5, 1>(pall);
#pragma unroll
        for (int r = 0; r < 4; ++r) GST_B128_NT(yo, o[r], ob + r * 8192, 0);
    }
    asm volatile(".Lfk_code_b:");
    TSE(12);
    TS_FLUSH();
}

}

extern "C" void kernel_launch(void* const* d_in, const int* in_sizes, int n_in, void* d_out, int out_size, void* d_ws, size_t ws_size,
                              hipStream_t stream) {
    (void)in_sizes; (void)n_in; (void)out_size; (void)ws_size;
    fk::Args a;
    a.q = (const char*)d_in[0]; a.k = (const char*)d_in[1]; a.v = (const char*)d_in[2]; a.sk = (const char*)d_in[3];
    a.ssmk = (const char*)d_in[4]; a.ssmD = (const char*)d_in[5]; a.x = (const char*)d_in[6]; a.lre = (const char*)d_in[7];
    a.lim = (const char*)d_in[8]; a.bre = (const char*)d_in[9]; a.bim = (const char*)d_in[10]; a.cre = (const char*)d_in[11];
    a.cim = (const char*)d_in[12]; a.logdt = (const char*)d_in[13]; a.dskip = (const char*)d_in[14];
    a.out = (char*)d_out; a.ws = (char*)d_ws;
    hipLaunchKernelGGL(fk::fused, dim3(256), dim3(768), 0, stream, a);
}
```

```cpp
#include <hip/hip_runtime.h>
#include <stdint.h>
#include <type_traits>

#define ZM 0
#define RX 11
#define P19M 7
#define QSL 2
#define ZP_ 15
#define FP_ 16
#define P14 3
#define P15 3
#define P16 3
#define S16 0
#define OPRIO 3
#define OS0 0
#define OSW 0
#define ZE -1
#define EM 2
#define EM_ON "s_mov_b64 exec, vcc\n\t"
#define EM_OFF "\ts_mov_b64 exec, -1\n"
#define EMD 0
#define EMOD "sc1"
#define TCH 0
#define TCB 10
#define SQ 1
#define SN 2
#define FMD 0
#define FMOD "sc1"
#define SMD 0
#define SMOD "sc1"
#define FX 0
#define FX_ON
#define FX_OFF
#define CVE 0
#define CVS 1
#define CVN 4
#define G2PRIO 0
#define CVZ 0
#define QP 0
#define FSL 0
#define SNK 0
namespace fk {

constexpr int S = 2048, D = 1024, P = 1024;

typedef __attribute__((ext_vector_type(8))) short bf16x8;
typedef __attribute__((ext_vector_type(4))) short s16x4;
typedef __attribute__((ext_vector_type(16))) float f32x16;
typedef __attribute__((ext_vector_type(4))) float f32x4;
typedef __attribute__((ext_vector_type(2))) float f32x2;
typedef __attribute__((ext_vector_type(4))) unsigned u32x4;
typedef __attribute__((ext_vector_type(2))) unsigned u32x2;
#define LDSP(T, a) ((__attribute__((address_space(3))) T*)(uintptr_t)(unsigned)(a))

__device__ __forceinline__ float bf2f(unsigned v) { return __uint_as_float(v << 16); }
__device__ __forceinline__ float bflo(unsigned w) { return __uint_as_float(w << 16); }
__device__ __forceinline__ float bfhi(unsigned w) { return __uint_as_float(w & 0xffff0000u); }
__device__ __forceinline__ uint16_t f2bf(float f) { __bf16 b = (__bf16)f; return __builtin_bit_cast(uint16_t, b); }
__device__ __forceinline__ float rbf(float f) { return bf2f(f2bf(f)); }
typedef __attribute__((ext_vector_type(2))) __bf16 bf16x2;
__device__ __forceinline__ unsigned pack2(float lo, float hi) { return __builtin_bit_cast(unsigned, __builtin_convertvector(f32x2{lo, hi}, bf16x2)); }
__device__ __forceinline__ float rb1(float x) { return __uint_as_float(pack2(0.f, x)); }
__device__ __forceinline__ void cmac(float& xr, float& xi, float ar, float ai, float hr, float hi) {
    asm("v_fmac_f32 %0, %2, %4\n\tv_fmac_f32 %1, %2, %5\n\tv_fma_f32 %0, -%3, %5, %0\n\tv_fmac_f32 %1, %3, %4" : "+v"(xr), "+v"(xi) : "v"(ar), "v"(ai), "v"(hr), "v"(hi));
}
__device__ __forceinline__ void cmad(float& o_r, float& o_i, float ar, float ai, float hr, float hi, float er, float ei) {
    asm("v_fma_f32 %0, %2, %4, %6\n\tv_fma_f32 %1, %2, %5, %7\n\tv_fma_f32 %0, -%3, %5, %0\n\tv_fmac_f32 %1, %3, %4" : "=&v"(o_r), "=&v"(o_i) : "v"(ar), "v"(ai), "v"(hr), "v"(hi), "v"(er), "v"(ei));
}
__device__ __forceinline__ f32x2 cmul4(f32x2 a, f32x2 h) {
    float o_r, o_i;
    asm("v_mul_f32 %0, %2, %4\n\tv_mul_f32 %1, %2, %5\n\tv_fma_f32 %0, -%3, %5, %0\n\tv_fmac_f32 %1, %3, %4" : "=&v"(o_r), "=&v"(o_i) : "v"(a[0]), "v"(a[1]), "v"(h[0]), "v"(h[1]));
    return f32x2{o_r, o_i};
}

#define PK_LO " op_sel:[0,0,0] op_sel_hi:[0,1,1]"
#define PK_HI " op_sel:[1,0,0] op_sel_hi:[1,1,1]"
#define PK_NEG " neg_lo:[1,0,0] neg_hi:[1,0,0]"
__device__ __forceinline__ void pk_cmad(f32x2& o_r, f32x2& o_i, f32x2 a, f32x2 hr, f32x2 hi, f32x2 er, f32x2 ei) {
    asm("v_pk_fma_f32 %0, %2, %3, %5" PK_LO "\n\tv_pk_fma_f32 %1, %2, %4, %6" PK_LO "\n\tv_pk_fma_f32 %0, %2, %4, %0" PK_HI PK_NEG "\n\tv_pk_fma_f32 %1, %2, %3, %1" PK_HI
        : "=&v"(o_r), "=&v"(o_i) : "v"(a), "v"(hr), "v"(hi), "v"(er), "v"(ei));
}
__device__ __forceinline__ void pk_cmac_conj(f32x2& xr, f32x2& xi, f32x2 a, f32x2 hr, f32x2 hi) {
    asm("v_pk_fma_f32 %0, %2, %3, %0" PK_LO "\n\tv_pk_fma_f32 %1, %2, %4, %1" PK_LO "\n\tv_pk_fma_f32 %0, %2, %4, %0" PK_HI "\n\tv_pk_fma_f32 %1, %2, %3, %1" PK_HI PK_NEG
        : "+v"(xr), "+v"(xi) : "v"(a), "v"(hr), "v"(hi));
}
__device__ __forceinline__ void pk_conj_mul(f32x2& o_r, f32x2& o_i, f32x2 a, f32x2 hr, f32x2 hi) {
    asm("v_pk_mul_f32 %0, %2, %3 op_sel:[0,0] op_sel_hi:[0,1]\n\tv_pk_mul_f32 %1, %2, %4 op_sel:[0,0] op_sel_hi:[0,1] neg_lo:[1,0] neg_hi:[1,0]\n\t"
        "v_pk_fma_f32 %0, %2, %4, %0" PK_HI PK_NEG "\n\tv_pk_fma_f32 %1, %2, %3, %1" PK_HI PK_NEG : "=&v"(o_r), "=&v"(o_i) : "v"(a), "v"(hr), "v"(hi));
}
__device__ __forceinline__ void pk_conj_mac(f32x2& xr, f32x2& xi, f32x2 a, f32x2 hr, f32x2 hi) {
    asm("v_pk_fma_f32 %0, %2, %3, %0" PK_LO "\n\tv_pk_fma_f32 %1, %2, %4, %1" PK_LO PK_NEG "\n\tv_pk_fma_f32 %0, %2, %4, %0" PK_HI PK_NEG "\n\tv_pk_fma_f32 %1, %2, %3, %1" PK_HI PK_NEG
        : "+v"(xr), "+v"(xi) : "v"(a), "v"(hr), "v"(hi));
}
__device__ __forceinline__ void cmac2(f32x2& x, f32x2 a, f32x2 h) {
    asm("v_pk_fma_f32 %0, %1, %2, %0" PK_LO "\n\tv_pk_fma_f32 %0, %1, %2, %0 op_sel:[1,1,0] op_sel_hi:[1,0,1] neg_lo:[1,0,0]" : "+v"(x) : "v"(a), "v"(h));
}
#define PLN 0
__device__ __forceinline__ void c2_mac(f32x2& xr, f32x2& xi, float p, float q, float r, float s, f32x2 hr, f32x2 hi) {
    float a = xr[0], b = xi[0], c = xr[1], d = xi[1];
    asm("v_fmac_f32 %0, %4, %8\n\tv_fmac_f32 %1, %6, %9\n\tv_fmac_f32 %2, %4, %10\n\tv_fmac_f32 %3, %6, %11\n\t"
        "v_fmac_f32 %0, %5, %9\n\tv_fmac_f32 %1, %7, %8\n\tv_fmac_f32 %2, %5, %11\n\tv_fmac_f32 %3, %7, %10"
        : "+v"(a), "+v"(b), "+v"(c), "+v"(d) : "v"(p), "v"(q), "v"(r), "v"(s), "v"(hr[0]), "v"(hi[0]), "v"(hr[1]), "v"(hi[1]));
    xr = f32x2{a, c}; xi = f32x2{b, d};
}
__device__ __forceinline__ void c2_mad(f32x2& xr, f32x2& xi, float p, float q, float r, float s, f32x2 hr, f32x2 hi, f32x2 er, f32x2 ei) {
    float a, b, c, d;
    asm("v_fma_f32 %0, %4, %8, %12\n\tv_fma_f32 %1, %6, %9, %13\n\tv_fma_f32 %2, %4, %10, %14\n\tv_fma_f32 %3, %6, %11, %15\n\t"
        "v_fmac_f32 %0, %5, %9\n\tv_fmac_f32 %1, %7, %8\n\tv_fmac_f32 %2, %5, %11\n\tv_fmac_f32 %3, %7, %10"
        : "=&v"(a), "=&v"(b), "=&v"(c), "=&v"(d) : "v"(p), "v"(q), "v"(r), "v"(s), "v"(hr[0]), "v"(hi[0]), "v"(hr[1]), "v"(hi[1]), "v"(er[0]), "v"(ei[0]), "v"(er[1]), "v"(ei[1]));
    xr = f32x2{a, c}; xi = f32x2{b, d};
}
__device__ __forceinline__ void c2_mul(f32x2& xr, f32x2& xi, float p, float q, float r, float s, f32x2 hr, f32x2 hi) {
    float a, b, c, d;
    asm("v_mul_f32 %0, %4, %8\n\tv_mul_f32 %1, %6, %9\n\tv_mul_f32 %2, %4, %10\n\tv_mul_f32 %3, %6, %11\n\t"
        "v_fmac_f32 %0, %5, %9\n\tv_fmac_f32 %1, %7, %8\n\tv_fmac_f32 %2, %5, %11\n\tv_fmac_f32 %3, %7, %10"
        : "=&v"(a), "=&v"(b), "=&v"(c), "=&v"(d) : "v"(p), "v"(q), "v"(r), "v"(s), "v"(hr[0]), "v"(hi[0]), "v"(hr[1]), "v"(hi[1]));
    xr = f32x2{a, c}; xi = f32x2{b, d};
}
__device__ __forceinline__ float mul1(float a, float b) { float c; asm("v_mul_f32 %0, %1, %2" : "=v"(c) : "v"(a), "v"(b)); return c; }
__device__ __forceinline__ float add1(float a, float b) { float c; asm("v_add_f32 %0, %1, %2" : "=v"(c) : "v"(a), "v"(b)); return c; }
__device__ __forceinline__ void mfma_(f32x4& c, bf16x8 a, bf16x8 b) { asm volatile("v_mfma_f32_16x16x32_bf16 %0, %1, %2, %0" : "+v"(c) : "v"(a), "v"(b)); }
__device__ __forceinline__ float sum4(float x) {
    const auto p = __builtin_amdgcn_permlane32_swap(__float_as_uint(x), __float_as_uint(x), false, false);
    x = __uint_as_float(p[0]) + __uint_as_float(p[1]);
    const auto q = __builtin_amdgcn_permlane16_swap(__float_as_uint(x), __float_as_uint(x), false, false);
    return __uint_as_float(q[0]) + __uint_as_float(q[1]);
}

template <int I, int N, class F>
__device__ __forceinline__ void static_for(F&& f) { if constexpr (I < N) { f(std::integral_constant<int, I>{}); static_for<I + 1, N>(f); } }

#define TS_DECL
#define TS0a()
#define TS0b()
#define TSE(i)
#define TS(i)
#define TS_FLUSH()

#define WAIT_V(n) asm volatile("s_waitcnt vmcnt(%0)" ::"n"((n) > 63 ? 63 : (n)) : "memory")
#define WAIT_L(n) asm volatile("s_waitcnt lgkmcnt(%0)" ::"n"((n) > 15 ? 15 : (n)) : "memory")
#define BW 1
constexpr int wenc(int v, int l) { v = v > 63 ? 63 : v; l = l > 15 ? 15 : l; return (v & 15) | 0x70 | (l << 8) | ((v >> 4) << 14); }
#define WAIT_VB(n) do { if constexpr (BW) __builtin_amdgcn_s_waitcnt(wenc(n, 15)); else WAIT_V(n); } while (0)
#define WAIT_LB(n) do { if constexpr (BW) __builtin_amdgcn_s_waitcnt(wenc(63, n)); else WAIT_L(n); } while (0)
#define BAR() asm volatile("s_barrier" ::: "memory")
#define SB() __builtin_amdgcn_sched_barrier(0)
template <class T> __device__ __forceinline__ void tie_(T& x) { asm volatile("" : "+v"(x)); }
#define TIE(x) tie_(x)
template <class T> __device__ __forceinline__ void ties_(T& x) { asm volatile("" : "+s"(x)); }
__device__ __forceinline__ unsigned take(unsigned x) { unsigned y; asm volatile("v_mov_b32 %0, %1" : "=v"(y) : "v"(x)); return y; }
#define DEF_LD(name, ins) template <int OFF, class T> __device__ __forceinline__ void name(T& dst, unsigned voff, const char* sb) { \
    asm volatile(ins : "=v"(dst) : "v"(voff), "s"(sb), "n"(OFF) : "memory"); }
DEF_LD(gld_u16_, "global_load_ushort %0, %1, %2 offset:%3")
DEF_LD(gld_b32_, "global_load_dword %0, %1, %2 offset:%3")
DEF_LD(gld_b64_, "global_load_dwordx2 %0, %1, %2 offset:%3")
DEF_LD(gld_b128_, "global_load_dwordx4 %0, %1, %2 offset:%3")
#define NTQ " nt"
#define NTK " nt"
DEF_LD(gld_b64_nt_, "global_load_dwordx2 %0, %1, %2 offset:%3" NTQ)
DEF_LD(gld_b128_nt_, "global_load_dwordx4 %0, %1, %2 offset:%3" NTQ)
DEF_LD(gld_b64_sc1_, "global_load_dwordx2 %0, %1, %2 offset:%3 sc1")
DEF_LD(gld_b128_sc1_, "global_load_dwordx4 %0, %1, %2 offset:%3 sc1")
#define DEF_ST(name, ins) template <int OFF, class T> __device__ __forceinline__ void name(unsigned voff, const T& v, const char* sb) { \
    asm volatile(ins :: "v"(voff), "v"(v), "s"(sb), "n"(OFF) : "memory"); }
DEF_ST(gst_b128_, "global_store_dwordx4 %0, %1, %2 offset:%3\n\ts_nop 1")
DEF_ST(gst_b128_nt_, "global_store_dwordx4 %0, %1, %2 offset:%3 nt\n\ts_nop 1")
DEF_ST(gst_b128_sc1_, "global_store_dwordx4 %0, %1, %2 offset:%3 sc1\n\ts_nop 1")
DEF_ST(gst_b64_sc1_, "global_store_dwordx2 %0, %1, %2 offset:%3 sc1")
#define GLD_U16(dst, voff, sb) gld_u16_<0>(dst, voff, sb)
#define GLD_B32(dst, voff, sb, off) gld_b32_<(off)>(dst, voff, sb)
#define GLD_B64(dst, voff, sb, off) gld_b64_<(off)>(dst, voff, sb)
#define GLD_B128(dst, voff, sb, off) gld_b128_<(off)>(dst, voff, sb)
#define GLD_B64_NT(dst, voff, sb, off) gld_b64_nt_<(off)>(dst, voff, sb)
#define GLD_B128_NT(dst, voff, sb, off) gld_b128_nt_<(off)>(dst, voff, sb)
#define GLD_B64_SC1(dst, voff, sb, off) gld_b64_sc1_<(off)>(dst, voff, sb)
#define GLD_B128_SC1(dst, voff, sb, off) gld_b128_sc1_<(off)>(dst, voff, sb)
#define GST_B128(voff, v, sb, off) gst_b128_<(off)>(voff, v, sb)
#define GST_B128_NT(voff, v, sb, off) gst_b128_nt_<(off)>(voff, v, sb)
#define GST_B128_SC1(voff, v, sb, off) gst_b128_sc1_<(off)>(voff, v, sb)
#define GST_B64_SC1(voff, v, sb) gst_b64_sc1_<0>(voff, v, sb)
__device__ __forceinline__ void dma_(unsigned ldsaddr, unsigned voff, const char* sb) {
    asm volatile("s_mov_b32 m0, %0\n\ts_nop 0\n\tglobal_load_lds_dwordx4 %1, %2" :: "s"(ldsaddr), "v"(voff), "s"(sb) : "memory"); }
__device__ __forceinline__ void dma_nt_(unsigned ldsaddr, unsigned voff, const char* sb) {
    asm volatile("s_mov_b32 m0, %0\n\ts_nop 0\n\tglobal_load_lds_dwordx4 %1, %2 nt" :: "s"(ldsaddr), "v"(voff), "s"(sb) : "memory"); }
#define DF 0
#define DFN 4
#define FL_0 ""
#define FL_1 " nt"
#define FL_2 " sc1"
#define FL_3 " sc1 nt"
#define FL_4 " sc0 sc1"
#define FL_5 " sc0 sc1 nt"
#define FL_6 " sc0 nt"
#define FLX_(x) FL_##x
#define FLX(x) FLX_(x)
__device__ __forceinline__ void dmaf_(unsigned ldsaddr, unsigned voff, const char* sb) {
    asm volatile("s_mov_b32 m0, %0\n\ts_nop 0\n\tglobal_load_lds_dwordx4 %1, %2" FLX(DF) :: "s"(ldsaddr), "v"(voff), "s"(sb) : "memory"); }
template <int OFF> __device__ __forceinline__ void dmaof_(unsigned voff, const char* sb) {
    asm volatile("global_load_lds_dwordx4 %0, %1 offset:%2" FLX(DF) :: "v"(voff), "s"(sb), "n"(OFF) : "memory"); }
#define DMA(ldsaddr, voff, sb) dma_(ldsaddr, voff, sb)
template <int OFF> __device__ __forceinline__ void dmao_(unsigned voff, const char* sb) {
    asm volatile("global_load_lds_dwordx4 %0, %1 offset:%2" :: "v"(voff), "s"(sb), "n"(OFF) : "memory"); }
template <int OFF, class T> __device__ __forceinline__ void dsr128_(T& dst, unsigned addr) { asm volatile("ds_read_b128 %0, %1 offset:%2" : "=v"(dst) : "v"(addr), "n"(OFF)); }
template <int OFF, class T> __device__ __forceinline__ void dsr64_(T& dst, unsigned addr) { asm volatile("ds_read_b64 %0, %1 offset:%2" : "=v"(dst) : "v"(addr), "n"(OFF)); }
template <int OFF, class T> __device__ __forceinline__ void dsr2_(T& dst, unsigned addr) { asm volatile("ds_read2_b32 %0, %1 offset0:%2 offset1:%3" : "=v"(dst) : "v"(addr), "n"(OFF), "n"(OFF + 1)); }
#define DSR128(dst, addr, off) dsr128_<(off)>(dst, addr)
#define DSR64(dst, addr, off) dsr64_<(off)>(dst, addr)

__device__ __forceinline__ void kv4(unsigned& o01, unsigned& o23, unsigned ka, unsigned kb, unsigned kc, unsigned kd, const unsigned* w, unsigned v0, unsigned v1) {
    unsigned t1, t3, p0, p1, p2, p3;
    asm(
        "v_dot2_f32_bf16 %0, %8, %19, 0\n\tv_dot2_f32_bf16 %1, %9, %18, 0\n\tv_dot2_f32_bf16 %2, %9, %19, 0\n\tv_dot2_f32_bf16 %3, %10, %18, 0\n\t"
        "v_dot2_f32_bf16 %4, %9, %16, 0\n\tv_dot2_f32_bf16 %5, %9, %17, 0\n\tv_dot2_f32_bf16 %6, %10, %16, 0\n\tv_dot2_f32_bf16 %7, %10, %17, 0\n\t"
        "v_cvt_pk_bf16_f32 %0, %0, %4\n\tv_cvt_pk_bf16_f32 %1, %1, %5\n\tv_cvt_pk_bf16_f32 %2, %2, %6\n\tv_cvt_pk_bf16_f32 %3, %3, %7\n\t"
        "v_dot2_f32_bf16 %4, %9, %15, 0\n\tv_dot2_f32_bf16 %5, %10, %14, 0\n\tv_dot2_f32_bf16 %6, %10, %15, 0\n\tv_dot2_f32_bf16 %7, %11, %14, 0\n\t"
        "v_dot2_f32_bf16 %0, %0, %22, 0\n\tv_dot2_f32_bf16 %1, %1, %22, 0\n\tv_dot2_f32_bf16 %2, %2, %22, 0\n\tv_dot2_f32_bf16 %3, %3, %22, 0\n\t"
        "v_cvt_pk_bf16_f32 %0, %0, %4\n\tv_cvt_pk_bf16_f32 %1, %1, %5\n\tv_cvt_pk_bf16_f32 %2, %2, %6\n\tv_cvt_pk_bf16_f32 %3, %3, %7\n\t"
        "v_dot2_f32_bf16 %4, %10, %12, 0\n\tv_dot2_f32_bf16 %5, %10, %13, 0\n\tv_dot2_f32_bf16 %6, %11, %12, 0\n\tv_dot2_f32_bf16 %7, %11, %13, 0\n\t"
        "v_dot2_f32_bf16 %0, %0, %22, 0\n\tv_dot2_f32_bf16 %1, %1, %22, 0\n\tv_dot2_f32_bf16 %2, %2, %22, 0\n\tv_dot2_f32_bf16 %3, %3, %22, 0\n\t"
        "v_cvt_pk_bf16_f32 %0, %0, %4\n\tv_cvt_pk_bf16_f32 %1, %1, %5\n\tv_cvt_pk_bf16_f32 %2, %2, %6\n\tv_cvt_pk_bf16_f32 %3, %3, %7\n\t"
        "v_dot2_f32_bf16 %0, %0, %22, 0\n\tv_dot2_f32_bf16 %1, %1, %22, 0\n\tv_dot2_f32_bf16 %2, %2, %22, 0\n\tv_dot2_f32_bf16 %3, %3, %22, 0\n\t"
        "v_cvt_pk_bf16_f32 %0, %0, 0\n\tv_cvt_pk_bf16_f32 %1, 0, %1\n\tv_cvt_pk_bf16_f32 %2, %2, 0\n\tv_cvt_pk_bf16_f32 %3, 0, %3\n\t"
        "v_dot2_f32_bf16 %0, %0, %20, 0\n\tv_dot2_f32_bf16 %1, %1, %20, 0\n\tv_dot2_f32_bf16 %2, %2, %21, 0\n\tv_dot2_f32_bf16 %3, %3, %21, 0\n\t"
        "s_nop 1\n\tv_cvt_pk_bf16_f32 %0, %1, %0\n\tv_cvt_pk_bf16_f32 %2, %3, %2\n\t"
        : "=&v"(o01), "=&v"(t1), "=&v"(o23), "=&v"(t3), "=&v"(p0), "=&v"(p1), "=&v"(p2), "=&v"(p3)
        : "v"(ka), "v"(kb), "v"(kc), "v"(kd), "v"(w[0]), "v"(w[1]), "v"(w[2]), "v"(w[3]), "v"(w[4]), "v"(w[5]), "v"(w[6]), "v"(w[7]), "v"(v0), "v"(v1), "s"(0x3f803f80u));
}
__device__ __forceinline__ void kv4a(unsigned* t, unsigned* e, unsigned ka, unsigned kb, unsigned kc, unsigned kd, const unsigned* w) {
    asm volatile(
        "v_and_b32 %8, %21, %15\n\tv_lshlrev_b32 %9, 16, %16\n\tv_and_b32 %10, %21, %16\n\tv_lshlrev_b32 %11, 16, %17\n\t"
        "v_and_b32 %12, %21, %17\n\tv_mul_f32 %0, %20, %8\n\tv_mul_f32 %1, %20, %9\n\tv_mul_f32 %2, %20, %10\n\t"
        "v_mul_f32 %3, %20, %11\n\tv_mul_f32 %4, %19, %9\n\tv_mul_f32 %5, %19, %10\n\tv_mul_f32 %6, %19, %11\n\t"
        "v_mul_f32 %7, %19, %12\n\tv_lshlrev_b32 %13, 16, %18\n\tv_and_b32 %14, %21, %18\n\tv_cvt_pk_bf16_f32 %0, 0, %0\n\t"
        "v_cvt_pk_bf16_f32 %1, 0, %1\n\tv_cvt_pk_bf16_f32 %2, 0, %2\n\tv_cvt_pk_bf16_f32 %3, 0, %3\n\tv_cvt_pk_bf16_f32 %4, 0, %4\n\t"
        "v_cvt_pk_bf16_f32 %5, 0, %5\n\tv_cvt_pk_bf16_f32 %6, 0, %6\n\tv_cvt_pk_bf16_f32 %7, 0, %7\n\t"
        : "=&v"(t[0]), "=&v"(t[1]), "=&v"(t[2]), "=&v"(t[3]), "=&v"(t[4]), "=&v"(t[5]), "=&v"(t[6]), "=&v"(t[7]), "=&v"(e[0]), "=&v"(e[1]), "=&v"(e[2]), "=&v"(e[3]), "=&v"(e[4]), "=&v"(e[5]), "=&v"(e[6])
        : "v"(ka), "v"(kb), "v"(kc), "v"(kd), "v"(w[2]), "v"(w[3]), "s"(0xffff0000u));
}
__device__ __forceinline__ void kv4b(unsigned* t, const unsigned* e, const unsigned* w) {
    asm volatile(
        "v_add_f32 %0, %0, %4\n\tv_add_f32 %1, %1, %5\n\tv_add_f32 %2, %2, %6\n\tv_add_f32 %3, %3, %7\n\t"
        "v_mul_f32 %4, %14, %8\n\tv_mul_f32 %5, %14, %9\n\tv_mul_f32 %6, %14, %10\n\tv_mul_f32 %7, %14, %11\n\t"
        "v_cvt_pk_bf16_f32 %0, 0, %0\n\tv_cvt_pk_bf16_f32 %1, 0, %1\n\tv_cvt_pk_bf16_f32 %2, 0, %2\n\tv_cvt_pk_bf16_f32 %3, 0, %3\n\t"
        "v_cvt_pk_bf16_f32 %4, 0, %4\n\tv_cvt_pk_bf16_f32 %5, 0, %5\n\tv_cvt_pk_bf16_f32 %6, 0, %6\n\tv_cvt_pk_bf16_f32 %7, 0, %7\n\t"
        "v_add_f32 %0, %0, %4\n\tv_add_f32 %1, %1, %5\n\tv_add_f32 %2, %2, %6\n\tv_add_f32 %3, %3, %7\n\t"
        "v_mul_f32 %4, %13, %9\n\tv_mul_f32 %5, %13, %10\n\tv_mul_f32 %6, %13, %11\n\tv_mul_f32 %7, %13, %12\n\t"
        : "+v"(t[0]), "+v"(t[1]), "+v"(t[2]), "+v"(t[3]), "+v"(t[4]), "+v"(t[5]), "+v"(t[6]), "+v"(t[7]) : "v"(e[2]), "v"(e[3]), "v"(e[4]), "v"(e[5]), "v"(e[6]), "v"(w[0]), "v"(w[1]));
}
__device__ __forceinline__ void kv4c(unsigned* t, unsigned v0, unsigned v1) {
    asm volatile(
        "v_cvt_pk_bf16_f32 %0, 0, %0\n\tv_cvt_pk_bf16_f32 %1, 0, %1\n\tv_cvt_pk_bf16_f32 %2, 0, %2\n\tv_cvt_pk_bf16_f32 %3, 0, %3\n\t"
        "v_cvt_pk_bf16_f32 %4, 0, %4\n\tv_cvt_pk_bf16_f32 %5, 0, %5\n\tv_cvt_pk_bf16_f32 %6, 0, %6\n\tv_cvt_pk_bf16_f32 %7, 0, %7\n\t"
        "v_add_f32 %0, %0, %4\n\tv_add_f32 %1, %1, %5\n\tv_add_f32 %2, %2, %6\n\tv_add_f32 %3, %3, %7\n\t"
        "v_lshlrev_b32 %4, 16, %8\n\tv_and_b32 %5, %10, %8\n\tv_lshlrev_b32 %6, 16, %9\n\tv_and_b32 %7, %10, %9\n\t"
        "v_cvt_pk_bf16_f32 %0, 0, %0\n\tv_cvt_pk_bf16_f32 %1, 0, %1\n\tv_cvt_pk_bf16_f32 %2, 0, %2\n\tv_cvt_pk_bf16_f32 %3, 0, %3\n\t"
        "v_mul_f32 %0, %0, %4\n\tv_mul_f32 %1, %1, %5\n\tv_mul_f32 %2, %2, %6\n\tv_mul_f32 %3, %3, %7\n\t"
        "v_cvt_pk_bf16_f32 %0, %1, %0\n\tv_cvt_pk_bf16_f32 %2, %3, %2\n\t"
        : "+v"(t[0]), "+v"(t[1]), "+v"(t[2]), "+v"(t[3]), "+v"(t[4]), "+v"(t[5]), "+v"(t[6]), "+v"(t[7]) : "v"(v0), "v"(v1), "s"(0xffff0000u));
}
__device__ __forceinline__ void kf4a(unsigned* t, unsigned* e, unsigned ka, unsigned kb, unsigned kc, unsigned kd, const unsigned* w) {
    asm volatile(
        "v_and_b32 %4, %16, %11\n\tv_lshlrev_b32 %5, 16, %12\n\tv_and_b32 %6, %16, %12\n\tv_lshlrev_b32 %7, 16, %13\n\t"
        "v_and_b32 %8, %16, %13\n\tv_mul_f32 %0, %15, %4\n\tv_mul_f32 %1, %15, %5\n\tv_mul_f32 %2, %15, %6\n\t"
        "v_mul_f32 %3, %15, %7\n\tv_lshlrev_b32 %9, 16, %14\n\tv_and_b32 %10, %16, %14\n\t"
        : "=&v"(t[0]), "=&v"(t[1]), "=&v"(t[2]), "=&v"(t[3]), "=&v"(e[0]), "=&v"(e[1]), "=&v"(e[2]), "=&v"(e[3]), "=&v"(e[4]), "=&v"(e[5]), "=&v"(e[6])
        : "v"(ka), "v"(kb), "v"(kc), "v"(kd), "v"(w[3]), "s"(0xffff0000u));
}
__device__ __forceinline__ void kf4b(unsigned* t, const unsigned* e, const unsigned* w) {
    asm volatile(
        "v_fmac_f32 %0, %12, %4\n\tv_fmac_f32 %1, %12, %5\n\tv_fmac_f32 %2, %12, %6\n\tv_fmac_f32 %3, %12, %7\n\t"
        "v_fmac_f32 %0, %11, %5\n\tv_fmac_f32 %1, %11, %6\n\tv_fmac_f32 %2, %11, %7\n\tv_fmac_f32 %3, %11, %8\n\t"
        "v_fmac_f32 %0, %10, %6\n\tv_fmac_f32 %1, %10, %7\n\tv_fmac_f32 %2, %10, %8\n\tv_fmac_f32 %3, %10, %9\n\t"
        : "+v"(t[0]), "+v"(t[1]), "+v"(t[2]), "+v"(t[3]) : "v"(e[1]), "v"(e[2]), "v"(e[3]), "v"(e[4]), "v"(e[5]), "v"(e[6]), "v"(w[0]), "v"(w[1]), "v"(w[2]));
}
__device__ __forceinline__ void kf4c(unsigned* t, unsigned v0, unsigned v1) {
    asm volatile(
        "v_lshlrev_b32 %4, 16, %8\n\tv_and_b32 %5, %10, %8\n\tv_lshlrev_b32 %6, 16, %9\n\tv_and_b32 %7, %10, %9\n\t"
        "v_mul_f32 %0, %0, %4\n\tv_mul_f32 %1, %1, %5\n\tv_mul_f32 %2, %2, %6\n\tv_mul_f32 %3, %3, %7\n\t"
        "v_cvt_pk_bf16_f32 %0, %1, %0\n\tv_cvt_pk_bf16_f32 %2, %3, %2\n\t"
        : "+v"(t[0]), "+v"(t[1]), "+v"(t[2]), "+v"(t[3]), "=&v"(t[4]), "=&v"(t[5]), "=&v"(t[6]), "=&v"(t[7]) : "v"(v0), "v"(v1), "s"(0xffff0000u));
}
__device__ __forceinline__ f32x4 mul4bf(u32x2 x, const unsigned* d) {
    float a, b, c, e;
    asm("v_dot2_f32_bf16 %0, %4, %6, 0\n\tv_dot2_f32_bf16 %1, %4, %7, 0\n\tv_dot2_f32_bf16 %2, %5, %8, 0\n\tv_dot2_f32_bf16 %3, %5, %9, 0\n\t"
        "v_cvt_pk_bf16_f32 %0, 0, %0\n\tv_cvt_pk_bf16_f32 %1, 0, %1\n\tv_cvt_pk_bf16_f32 %2, 0, %2\n\tv_cvt_pk_bf16_f32 %3, 0, %3"
        : "=&v"(a), "=&v"(b), "=&v"(c), "=&v"(e) : "v"(x[0]), "v"(x[1]), "v"(d[0]), "v"(d[1]), "v"(d[2]), "v"(d[3]));
    return f32x4{a, b, c, e};
}
template <int BIT> __device__ __forceinline__ void quad_swap(u32x4& a, u32x4& b) {
    unsigned n0, n1, n2, n3, b0 = b[0], b1 = b[1], b2 = b[2], b3 = b[3];
#define QS_(d, s, k) "v_cndmask_b32_dpp " d ", " s ", " k ", vcc quad_perm:[%14,%15,%16,%17] row_mask:0xf bank_mask:0xf\n\t"
    asm("s_mov_b64 vcc, %12\n\ts_nop 0\n\t" QS_("%0", "%4", "%8") QS_("%1", "%5", "%9") QS_("%2", "%6", "%10") QS_("%3", "%7", "%11")
        "s_mov_b64 vcc, %13\n\t" QS_("%4", "%8", "%4") QS_("%5", "%9", "%5") QS_("%6", "%10", "%6") QS_("%7", "%11", "%7")
        : "=&v"(n0), "=&v"(n1), "=&v"(n2), "=&v"(n3), "+v"(b0), "+v"(b1), "+v"(b2), "+v"(b3)
        : "v"(a[0]), "v"(a[1]), "v"(a[2]), "v"(a[3]), "s"(BIT ? 0x3333333333333333ull : 0x5555555555555555ull), "s"(BIT ? 0xccccccccccccccccull : 0xaaaaaaaaaaaaaaaaull),
          "n"(BIT ? 2 : 1), "n"(BIT ? 3 : 0), "n"(BIT ? 0 : 3), "n"(BIT ? 1 : 2) : "vcc");
#undef QS_
    a = u32x4{n0, n1, n2, n3}; b = u32x4{b0, b1, b2, b3};
}
__device__ __forceinline__ void kv_dma(unsigned ldsaddr, unsigned k0, unsigned k1, unsigned k2, unsigned k3, const char* sb) {
    asm volatile("s_mov_b32 m0, %5\n\ts_nop 0\n\tglobal_load_lds_dwordx4 %0, %4" NTK "\n\tglobal_load_lds_dwordx4 %1, %4 offset:1024" NTK "\n\t"
                 "global_load_lds_dwordx4 %2, %4 offset:2048" NTK "\n\tglobal_load_lds_dwordx4 %3, %4 offset:3072" NTK
                 :: "v"(k0), "v"(k1), "v"(k2), "v"(k3), "s"(sb), "s"(ldsaddr) : "memory");
}
#define IF_(c, C) "s_cmp_lg_u32 " c ", " C "\n\ts_cbranch_scc1 1f\n\t"
template <int C, int N> __device__ __forceinline__ void wait_if(int c) { asm volatile(IF_("%0", "%1") "s_waitcnt vmcnt(%2)\n1:" :: "s"(c), "n"(C), "n"(N) : "scc", "memory"); }
template <int C, int M> __device__ __forceinline__ void flag_if(int c, unsigned voff, u32x2 v, const char* sb) {
    asm volatile(IF_("%0", "%1") "s_mov_b32 exec_lo, %5\n\ts_mov_b32 exec_hi, 0\n\tglobal_store_dwordx2 %2, %3, %4 sc1\n\ts_mov_b64 exec, -1\n1:" :: "s"(c), "n"(C), "v"(voff), "v"(v), "s"(sb), "n"(M) : "scc", "memory"); }
template <int C> __device__ __forceinline__ void st4_if(int c, unsigned voff, const u32x4* v, const char* s0, const char* s1) {
    asm volatile(IF_("%0", "%1") "global_store_dwordx4 %2, %3, %7 sc1\n\tglobal_store_dwordx4 %2, %4, %7 offset:1024 sc1\n\tglobal_store_dwordx4 %2, %5, %8 sc1\n\tglobal_store_dwordx4 %2, %6, %8 offset:1024 sc1\n\ts_nop 1\n1:"
                 :: "s"(c), "n"(C), "v"(voff), "v"(v[0]), "v"(v[1]), "v"(v[2]), "v"(v[3]), "s"(s0), "s"(s1) : "scc", "memory"); }
template <int C> __device__ __forceinline__ void ld4_if(int c, unsigned voff, u32x4* v, const char* s0) {
    asm volatile(IF_("%4", "%5") "global_load_dwordx4 %0, %6, %7 nt\n\tglobal_load_dwordx4 %1, %6, %7 offset:1024 nt\n\tglobal_load_dwordx4 %2, %6, %7 offset:2048 nt\n\tglobal_load_dwordx4 %3, %6, %7 offset:3072 nt\n1:"
                 : "+v"(v[0]), "+v"(v[1]), "+v"(v[2]), "+v"(v[3]) : "s"(c), "n"(C), "v"(voff), "s"(s0) : "scc", "memory"); }
template <int C> __device__ __forceinline__ void ld8_if(int c, unsigned voff, u32x2* v, const char* s0, const char* s1) {
    asm volatile(IF_("%8", "%9") "global_load_dwordx2 %0, %10, %11 offset:-4096 nt\n\tglobal_load_dwordx2 %1, %10, %11 offset:-2048 nt\n\tglobal_load_dwordx2 %2, %10, %11 nt\n\tglobal_load_dwordx2 %3, %10, %11 offset:2048 nt\n\t"
                 "global_load_dwordx2 %4, %10, %12 offset:-4096 nt\n\tglobal_load_dwordx2 %5, %10, %12 offset:-2048 nt\n\tglobal_load_dwordx2 %6, %10, %12 nt\n\tglobal_load_dwordx2 %7, %10, %12 offset:2048 nt\n1:"
                 : "+v"(v[0]), "+v"(v[1]), "+v"(v[2]), "+v"(v[3]), "+v"(v[4]), "+v"(v[5]), "+v"(v[6]), "+v"(v[7]) : "s"(c), "n"(C), "v"(voff), "s"(s0), "s"(s1) : "scc", "memory"); }
template <int E> __device__ __forceinline__ void sy_post(unsigned myaddr) { asm volatile("ds_write_b32 %0, %0 offset:%1" :: "v"(myaddr), "n"(E * 64) : "memory"); }
template <int E, int SL = 1> __device__ __forceinline__ unsigned sy_wait(unsigned addr) {
    unsigned v;
    asm volatile("s_branch 2f\n1:\n\ts_sleep %3\n2:\n\tds_read_b32 %0, %1 offset:%2\n\ts_waitcnt lgkmcnt(0)\n\tv_cmp_eq_u32 vcc, 0, %0\n\ts_cbranch_vccnz 1b"
                 : "=&v"(v) : "v"(addr), "n"(E * 64), "n"(SL) : "vcc", "memory");
    return v;
}
template <int E> __device__ __forceinline__ void sy_pre(unsigned& v, unsigned addr) { asm volatile("ds_read_b32 %0, %1 offset:%2" : "=v"(v) : "v"(addr), "n"(E * 64) : "memory"); }
template <int E> __device__ __forceinline__ void sy_chk(unsigned& v, unsigned addr) {
    asm volatile("s_branch 2f\n1:\n\tds_read_b32 %0, %1 offset:%2\n\ts_waitcnt lgkmcnt(0)\n2:\n\tv_cmp_eq_u32 vcc, 0, %0\n\ts_cbranch_vccnz 1b"
                 : "+v"(v) : "v"(addr), "n"(E * 64) : "vcc", "memory");
}
template <int OFF, class T> __device__ __forceinline__ void dsrtr_(T& dst, unsigned addr) { asm volatile("ds_read_b64_tr_b16 %0, %1 offset:%2" : "=v"(dst) : "v"(addr), "n"(OFF)); }

constexpr size_t WS_H = 0, WS_KVT = (size_t)10 << 20, WS_ZT = (size_t)15 << 20, WS_E = (size_t)20 << 20, WS_FLAG = WS_E + (2048 << 10);
#define HP 65536
#define KP 8192
#define ZP 4096
constexpr int FL_KV = 0, FL_Z = 65536, FL_H = 131072, FL_EPOCH = 196608;
constexpr unsigned SPIN_CAP = 1u << 17;
#define ABL 0
#define CSPLIT 1
#define MERGE 3
#define CB 13
#define PAUSE 0
#define QLATE 0
#define CVPRIO 0
#define STG_SEL (idx & 3)
#define SP0 3
#define SDP 1
#define LP0 3
#define LDP 2
#define KVS 5
#define KVU 3
#define QIT 0
#define ZB 3
#define OS 2
#define MPRIO 2

#define ROT 0x4042
#define R2 0x14
#define CPF 0
#define PRE 4
#define PRO 9
#define G1NT 0
#define G1NTS 16
#define TMIX 0
#define TLBT 0
#define CDEF 0
#define CVLATE 1
#define FPRIO 3
#define YS 0
#define DP0 1
#define DP1 5
#define AWB 1
#define PK 0
#define PU 15
#define PC0 13
#define PC1 14
#define PC2 16
#define PC3 16
#define PQ 0
#define SPF 0
#define SPH 0
#define NB1 4
#define EP 0
#define ESL 0
#define CFB 0
#define P1 1
#define P0 1
#define BKE 0
#define EALL 0
#define EREP 0
#define TOUCH 0
#define FILLB 0
#define CPOST 1
#define CEARLY 0
#define NOOWN 0
#define PFIRST 0
#define ZB0 0
#define CATCH 4
#define PWE 0
#define SWAP 1
#define KVM 0x0FE
#define KFD 3
#define ZW 2
#define ZX 1
#define ZP0 3
#define BARM 0x22220
#define NR 6
#define WPS 2
#define CD0 3
#define AWX 8
#define VBM 0
#define ZOWN 1
#define ZPS 4
#define XAB 0
#define QM 0
#define OSG 0
#define KPL 0xFE
#define KPRIO 1
#define GPRIO 1
#define PST 0x7654
#define PLT 0xB964
#define LC 0
#define PRE2 PRE
#define KVD 0
#define PAIR 1
#define G2P1 0
#define EPRIO 0
#define LSW 1
#define EGX 28
#define CR1 0
#define CR2 0
#define CR3 0
#define CR4 0
#define CR5 0
#define CRL (2 * NR - CR1 - CR2 - CR3 - CR4 - CR5)
#define UE 0
#define UPRIO 3
#define AMF 7
#define M1 1
#define W2 1
#define W1 0
#define NBK 3
#define XB 4
#define KFB 9
#define APRE 0
#define CB0 1
#define AP0 8
#define HTSE(i)
#define KAT 2
#define SD 2
#define KV3 0x088888
#define KOWN 1
#define HKV 0
#define KVP NBK
#define LZ 1
#define AWN 8
#define CRATE 2
#define ZSB 0
#define ZFB 1
#define ZSL 4
#define OSL 0
constexpr int it_cnt(int p, int k, int last) {
    const int pos[8] = {PU, PK, PC0, PC1, PQ, PU - 1, PC2, PC3}, n[8] = {4, 4, 4, 4, 8, 1, 4, 4};
    int c = 0;
    for (int i = 0; i < 8; ++i) if (pos[i] <= last && (pos[i] > p || (pos[i] == p && i > k))) c += n[i];
    return c;
}
constexpr int nib(int t, int c) { return (t >> (4 * c)) & 15; }
constexpr bool nib_any(int t, int v) { return nib(t, 0) == v || nib(t, 1) == v || nib(t, 2) == v || nib(t, 3) == v; }
constexpr int kv_wait(int late) {
    int t = 0;
    for (int c = 0; c < 4; ++c) t |= ((nib(PLT, c) >= nib(PST, c) && nib(PLT, c) < nib(PST, c) + KFD) == late ? nib(PST, c) + KFD : 15) << (4 * c);
    return t;
}
constexpr unsigned long long sl_seq(int i, int inv) {
    int q[16] = {};
    const int near[4] = {1, 5, 6, SN}, far[13] = {2, 3, 4, 7, 8, 9, 10, 11, 12, 13, 14, 15, 0};
    for (int k = 0; k < 4; ++k) q[near[k]] = (i + k) & 3;
    for (int k = 0, n = 0; k < 13; ++k) if (far[k] != SN) q[far[k]] = 4 + (n++ + SQ * i) % 12;
    unsigned long long t = 0;
    for (int k = 0; k < 16; ++k) t |= inv ? (unsigned long long)k << (4 * q[k]) : (unsigned long long)q[k] << (4 * k);
    return t;
}
constexpr int G1STAGE = 32768;
constexpr int g1_slot(int s) { return (NB1 == 5 ? (s + 3) % 5 : s & 3) * G1STAGE; }
constexpr int L_KS = NB1 == 5 ? 65536 : 131072, L_VS = L_KS + 16384;
constexpr int L_TS = 3 * G1STAGE, L_TR = L_TS + 3072;
constexpr int CST = 4288;
constexpr int UOFF = 2 * CST + 128, UPL = 2304;
constexpr int CHSZ = UOFF + 2 * UPL + 32;
constexpr int L_AGG = 4 * CHSZ, L_ES = L_AGG + 15 * 512, L_SY = L_ES + 2048;
constexpr int L_HT = 98304;
constexpr int L_C = 98304, G2STAGE = 16384;
constexpr int L_Y = 16384;
static_assert(L_SY + 448 <= 65536 && L_AGG >= 49152 && UOFF % 16 == 0 && CHSZ % 256 == 32, "lds");

__device__ __forceinline__ int img_off(int r, int c) { return (r >> 1) * 256 + ((((r & 1) * 8 + c) ^ ((r >> 1) & 15)) << 4); }

constexpr int g1_upto(int g) {
    int n = 4 * NBK;
    for (int t = SD; t <= g; ++t) {
        int sb = (t + 1) / 2, sub = (t + 1) % 2, allowed = sb ? 4 * (sb + 3) + 2 * (sub + 1) : 12;
        if (allowed > 64) allowed = 64;
        n = n + CATCH < allowed ? n + CATCH : allowed;
    }
    return n;
}
constexpr int g1_cnt(int s) { return HKV || s >= KVP ? g1_upto(2 * s - 2) - 4 * (s + 1) : s >= KAT ? g1_upto(2 * s - 2) - 4 * KVP : g1_upto(2 * s - 2) - 4 * (s + 1) + 4; }
static_assert(KVP >= 1 && KVP <= NBK && AP0 % 4 == 0, "schedule");
static_assert(g1_upto(28) == 64, "schedule");
constexpr int AW = AWN;
constexpr int g2_place(int s) { return s < 5 ? L_C + ((s + 2) & 3) * G2STAGE : s < 7 ? (s - 1) * G2STAGE : s < 10 ? (9 - s) * G2STAGE : g2_place(s - 9); }
constexpr bool g2_bar(int g) { return (BARM >> g) & 1; }
struct G2Plan { int upto[33], wr[33], ald[64][2], seq[164], use[64]; };
constexpr G2Plan g2_plan() {
    G2Plan p = {};
    int nc = 14 + 2 * NR, nw = 0, nx = 4, lastb = 0, n = 0;
    for (int i = 0; i < 164; ++i) p.seq[i] = 1000;
    for (; nx < 4 + AW; ++nx) p.seq[nx] = ++n;
    for (int t = 0; t < 32; ++t) {
        p.upto[t] = nc; p.wr[t] = nw;
        if (t < 2) continue;
        if (g2_bar(t)) lastb = t;
        const int w = AW + nw < AWX ? AW + nw : AWX;
        for (int f = 2 * t; f < 2 * t + 2; ++f) {
            p.use[f] = n;
            if (nc < 32 && nc == p.upto[t] + (f & 1) && (f & 1) < CRATE && t >= CD0 && lastb >= (nc < 20 ? 1 : (nc | 1) - 18)) p.seq[100 + nc++] = ++n;
            for (int k = 0; k < 2; ++k) {
                p.ald[f][k] = -1;
                if (nx < 64 && nx - f - k < w) { p.ald[f][k] = nx; p.seq[nx++] = ++n; }
            }
        }
        for (int k = 0; k < WPS; ++k) if (nw < 2 * NR && lastb >= (nw < 6 ? 1 : (nw | 1) - 4)) ++nw;
    }
    p.upto[32] = nc; p.wr[32] = nw;
    return p;
}
constexpr G2Plan G2P = g2_plan();
constexpr int g2_upto(int g) { return G2P.upto[g + 1]; }
constexpr int g2_wr(int g) { return G2P.wr[g + 1]; }
constexpr int g2_ald(int f, int k) { return G2P.ald[f][k]; }
constexpr int g2_cnt(int id, int at) { return G2P.use[at] - G2P.seq[id]; }
constexpr int LASTB = 31 - __builtin_clz(BARM);
constexpr int lz_post() { int g = LASTB; while (G2P.seq[2 * g] < G2P.seq[131]) ++g; return g; }
constexpr int lz_chk() { int s = 7 + NR; while (G2P.seq[100 + 2 * s + 1] < G2P.seq[2 * LASTB]) ++s; return 2 * s - 1; }
static_assert(!LZ || (AWB && lz_post() + 1 < lz_chk() && lz_chk() < EGX), "schedule");
constexpr int g2_cover(int g) { int s = (g + 1) / 2; while (s < 15 && !g2_bar(2 * s + 1)) ++s; return s; }
static_assert(BARM >> 17 && g2_cover(3) < 7 && CRATE <= 2, "the epilogue needs stage 8 behind a barrier");

constexpr int cv_m(int o) { return (o & 1) ? 7 - (o >> 1) : o >> 1; }
constexpr int cv_first(int o) { int p = 0; for (int i = 0; i < o; ++i) p += 8 - cv_m(i); return p; }
constexpr int cv_grp(int p) { int o = 0; while (cv_first(o + 1) <= p) ++o; return o; }
constexpr int cv_read(int p) {
    int slot[40] = {};
    for (int i = 0; i < 40; ++i) slot[i] = -1;
    slot[0] = 2 * 3 + 1;
    for (int o = 0; o < 8; ++o) {
        int q = cv_first(o + 1) - 1;
        for (int h = 0; h < 2; ++h) { while (slot[q] >= 0) ++q; slot[q] = 2 * (o + 4) + h; }
    }
    const int bs[8] = {1, 3, 5, 11, 12, 13, 19, 21};
    for (int jb = 0; jb < 8; ++jb) slot[bs[jb]] = 32 + jb;
    return slot[p];
}
static_assert(cv_read(35) == 22 && cv_read(36) == 23 && cv_read(37) == -1 && cv_read(10) == 11 && cv_read(15) == 12 && cv_read(18) == 15 && cv_read(24) == 17, "conv schedule");
constexpr int cv_wait(int o) {
    int n = 0;
    for (int q = cv_first(o) - 1;; --q) {
        const int rd = cv_read(q < 0 ? q + 36 : q);
        if (rd == 2 * (o + (q < 0 ? 8 : 0)) + 1) return n;
        if (rd >= 0) ++n;
    }
}

struct Args {
    const char *q, *k, *v, *sk, *ssmk, *ssmD, *x, *lre, *lim, *bre, *bim, *cre, *cim, *logdt, *dskip;
    char *out, *ws;
};

typedef __attribute__((ext_vector_type(8))) unsigned long long u64x8;
__global__ __launch_bounds__(768, 3) void fused(Args) {
    __shared__ __attribute__((aligned(1024))) char shm[163840];
    TS0a();
    u64x8 ka0, ka1; unsigned long long ka2;
    const char* pc0;
    int code_n;
    asm volatile("s_load_dwordx16 %0, %5, 0x0\n\ts_load_dwordx16 %1, %5, 0x40\n\ts_load_dwordx2 %2, %5, 0x80\n\t"
                 "s_getpc_b64 %3\n.Lfk_code_0:\n\ts_mov_b32 %4, .Lfk_code_a-.Lfk_code_0-4\n\ts_max_i32 %4, %4, .Lfk_code_b-.Lfk_code_0-4"
                 : "=&s"(ka0), "=&s"(ka1), "=&s"(ka2), "=&s"(pc0), "=&s"(code_n) : "s"(__builtin_amdgcn_kernarg_segment_ptr()) : "scc");
    const int tid = threadIdx.x, lane = tid & 63, wraw = __builtin_amdgcn_readfirstlane(tid >> 6);
    const int wid = !SWAP ? wraw : wraw < 4 ? wraw + 8 : wraw - 4;
    const int b = blockIdx.x, xcd = b & 7, idx = b >> 3;
    asm volatile("s_cmp_lt_u32 %0, 8\n\ts_cbranch_scc1 1f\n\ts_setprio 3\n\ts_cmp_lg_u32 %0, 8\n\ts_cbranch_scc1 1f\n\t"
                 "v_lshl_add_u32 v161, %1, 7, %2\n\tv_min_i32 v161, %3, v161\n\ts_mov_b64 exec, 0xffff\n\tglobal_load_dword v161, v161, %4\n\ts_mov_b64 exec, -1\n1:"
                 :: "s"(wid), "v"(lane), "s"(idx * 2048), "s"(code_n), "s"(pc0) : "scc", "memory", "v161");
    TS_DECL
    const int tm = (xcd >> 1) * 4 + (idx >> 3), tn = (xcd & 1) * 8 + (idx & 7);
    const int wm = wid & 3, wn = (wid >> 2) & 1, l31 = lane & 31, lh = lane >> 5;
    const unsigned lb = (unsigned)(uintptr_t)shm;
    const unsigned l16 = lane * 16;
    const int l15 = lane & 15, g4 = lane >> 4, sq = 2 * wn + (wm & 1), st = sq * 16 + l15, run = (wm >> 1) * 4 + g4;
    unsigned kvv[4];
    const int trow = idx * 64 * 2048 + xcd * 256;
    if (wid < 8) {
        const int v = l15 ^ g4, hi = v >> 3;
        const int r0 = wn ? tn * 64 + (wm >> 1) * 32 : tm * 128 + (wm >> 1) * 64 + (wm & 1) * 4;
        const unsigned d0 = (r0 + ((g4 * (wn ? 2 : 9)) & (wn ? 6 : 17))) * 2048 + (v & 7) * 16, odd = wn ? 11 : 14;
        kvv[0] = d0 + (hi << odd); kvv[2] = d0 + ((hi ^ 1) << odd) + (wn ? 32768 - 2048 : 4096 - 2048);
        kvv[1] = (kvv[0] ^ 64) + (wn ? 16384 - 1024 : 65536 - 1024); kvv[3] = (kvv[2] ^ 64) + (wn ? 16384 - 1024 : 65536 - 1024);
    } else {
        kvv[0] = trow + (idx ? -4 * 2048 : 0) + g4 * 2048 + wm * 64 + l15 * 4;
        asm volatile("v_writelane_b32 %0, 0, 0\n\tv_writelane_b32 %0, 4, 1\n\tv_writelane_b32 %0, %1, 2" : "+v"(kvv[0]) : "s"((xcd * 64 + idx * 2 + (wm >> 1)) * 4));
        kvv[1] = (xcd * 128 + wm * 32 + l31) * 8;
        kvv[2] = kvv[3] = (tn * 64 + lane) * 2;
    }
    unsigned g1dst = lb + wid * 4096;
    int rot7 = ((((ROT >> 12) & 15) * (idx & 7) + ((ROT >> 8) & 15) * (idx >> 3) + ((ROT >> 4) & 15) * (xcd >> 1) + (ROT & 15) * (xcd & 1)) & 15) << 7;
    asm volatile("s_waitcnt lgkmcnt(0)" : "+s"(ka0), "+s"(ka1), "+s"(ka2), "+s"(g1dst), "+s"(rot7), "+v"(kvv[0]), "+v"(kvv[1]), "+v"(kvv[2]), "+v"(kvv[3]));
    Args a;
    a.q = (const char*)ka0[0]; a.k = (const char*)ka0[1]; a.v = (const char*)ka0[2]; a.sk = (const char*)ka0[3]; a.ssmk = (const char*)ka0[4]; a.ssmD = (const char*)ka0[5];
    a.x = (const char*)ka0[6]; a.lre = (const char*)ka0[7]; a.lim = (const char*)ka1[0]; a.bre = (const char*)ka1[1]; a.bim = (const char*)ka1[2]; a.cre = (const char*)ka1[3];
    a.cim = (const char*)ka1[4]; a.logdt = (const char*)ka1[5]; a.dskip = (const char*)ka1[6]; a.out = (char*)ka1[7]; a.ws = (char*)ka2;
    char* const ws = a.ws;
    const char* const flg = ws + WS_FLAG;
    TS(26);
    const char* g1src = wn ? ((wm & 1) ? a.bim : a.bre) : a.x;
    asm volatile("s_mov_b32 m0, %[m]\n\ts_cmp_gt_u32 %[w], 7\n\ts_cbranch_scc1 1f\n\t"
                 "global_load_lds_dwordx4 %[k0], %[g]" FLX(DF) "\n\tglobal_load_lds_dwordx4 %[k1], %[g] offset:1024" FLX(DF) "\n\t"
                 "global_load_lds_dwordx4 %[k2], %[g] offset:2048" FLX(DF) "\n\tglobal_load_lds_dwordx4 %[k3], %[g] offset:3072" FLX(DF) "\n\t"
                 "s_branch 2f\n1:\n\tglobal_load_ushort v164, %[k2], %[c1]\n\tglobal_load_ushort v165, %[k2], %[c2]\n\tglobal_load_ushort v166, %[k2], %[c3]\n\t"
                 "v_mov_b64 v[162:163], %[kv]\n\ts_mov_b64 exec, 7\n\tv_mov_b64 v[162:163], %[d]\n\ts_mov_b64 exec, 3\n\tv_mov_b64 v[162:163], %[e]\n\ts_mov_b64 exec, -1\n\t"
                 "v_mad_u64_u32 v[162:163], vcc, %[k0], 1, v[162:163]\n\tglobal_load_dword v160, v[162:163], off sc1\n\tglobal_load_dwordx2 v[162:163], %[k1], %[sk]\n2:"
                 :
                 : [w] "s"(wid), [c1] "s"(a.logdt), [c2] "s"(a.lre), [c3] "s"(a.lim), [d] "s"(a.ssmD), [e] "s"(flg + FL_EPOCH), [sk] "s"(a.sk),
                   [k0] "v"(kvv[0]), [k1] "v"(kvv[1]), [k2] "v"(kvv[2]), [k3] "v"(kvv[3]), [kv] "s"(a.k), [g] "s"(g1src + rot7), [m] "s"(g1dst)
                 : "scc", "vcc", "memory", "v160", "v161", "v162", "v163", "v164", "v165", "v166");
    TS0b();
    TS(27);
    if (P1) BAR();
#define SLICE_TABLE() unsigned slo, shi; { int ix = idx; asm volatile("" : "+s"(ix)); \
        const unsigned long long t0 = (ix & 16) ? ((ix & 8) ? sl_seq(3, 0) : sl_seq(2, 0)) : ((ix & 8) ? sl_seq(1, 0) : sl_seq(0, 0)), iv = (ix & 16) ? ((ix & 8) ? sl_seq(3, 1) : sl_seq(2, 1)) : ((ix & 8) ? sl_seq(1, 1) : sl_seq(0, 1)); \
        const unsigned x4 = (xcd >> 1) * 0x44444444u, own = tn ^ (x4 & 15), es = (unsigned)(iv >> (4 * own)) & 15; \
        const unsigned long long df = ((unsigned)t0 & 15) ^ own, tab = t0 ^ (df << (4 * es)) ^ df; \
        slo = (unsigned)tab ^ x4; shi = (unsigned)(tab >> 32) ^ x4; asm volatile("" : "+s"(slo), "+s"(shi)); }
#define SLICE(st) ((((st) < 8 ? slo : shi) >> (4 * ((st) & 7))) & 15)
    typedef __attribute__((ext_vector_type(4))) short v4i16;
    int role = wid;
    asm volatile("" : "+s"(role));

    if (role >= 8) {
        int lane;
        asm volatile("v_mbcnt_lo_u32_b32 %0, -1, 0\n\tv_mbcnt_hi_u32_b32 %0, -1, %0" : "=v"(lane));
        __builtin_assume(lane >= 0 && lane < 64);
        const int l31 = lane & 31, lh = lane >> 5, l15 = lane & 15, g4 = lane >> 4;
        const unsigned l16 = lane * 16;
        const int chn = xcd * 128 + idx * 4 + wm;
        const unsigned cb = lb + wm * CHSZ;
        const int r = l15;
        const unsigned fpub = KOWN ? (xcd * 32 + l31) * 256 + idx * 8 : xcd * 256 + idx * 8, fpoll = KOWN ? (xcd * 32 + idx) * 256 + l31 * 8 : xcd * 256 + l31 * 8;
        u32x2 fl; u32x4 sw[4], uu[4];
        unsigned tch = 0, tch2 = 0;
        if (TOUCH) {
            const int ln = wm * 4 + (lane & 3);
            const unsigned fo = (unsigned)(WS_FLAG - WS_E) + (wm == 0 ? FL_H + (tm * 16 + tn) * 128 : (wm == 1 ? FL_KV : FL_Z) + xcd * 256 + (lane & 1) * 128);
            const unsigned o = (lane & 4) && (TOUCH & 2) ? fo : ((tm * 2 + (ln >> 3)) * 1024 + tn * 64) * 16 + (ln & 7) * 128;
            asm volatile("s_mov_b64 exec, 0xff\n\tglobal_load_dword %0, %1, %2\n\ts_mov_b64 exec, -1" : "=&v"(tch) : "v"(o), "s"(ws + WS_E) : "memory");
        }
        SLICE_TABLE();
        TS(1);
        unsigned sm, cl[3]; u32x2 kw0;
        asm volatile("s_waitcnt vmcnt(0)" : "={v160}"(sm), "={v[162:163]}"(kw0), "={v164}"(cl[0]), "={v165}"(cl[1]), "={v166}"(cl[2]) :: "memory", "v161");
        TS(22);
        const unsigned hal = take(sm);
        const unsigned tagA = __builtin_amdgcn_readlane(hal, 0) + 1, tagB = __builtin_amdgcn_readlane(hal, 1) + (tagA == 0 ? 1u : 0u);
        const u32x2 tag = {tagA, tagB};
        f32x2 L1, C1;
        {
            const float dt = rbf(expf(bf2f(take(cl[0]))));
            const float lr = bf2f(take(cl[1])), li = bf2f(take(cl[2]));
            const float e = expf(dt * lr);
            float sn, cs;
            sincosf(dt * li, &sn, &cs);
            L1 = f32x2{e * cs, e * sn};
            const float nr = L1[0] - 1.f, ni = L1[1], den = lr * lr + li * li;
            C1 = f32x2{2.f * ((nr * lr + ni * li) / den), 2.f * ((ni * lr - nr * li) / den)};
            TIE(L1); TIE(C1);
        }

        f32x2 p[11];
        p[0] = L1;
        if (PWE) {
#pragma unroll
            for (int i = 1; i <= 10; ++i) { p[i] = cmul4(p[i - 1], p[i - 1]); TIE(p[i]); }
        }
        {
            if (!PWE) {
#pragma unroll
                for (int i = 1; i <= 10; ++i) p[i] = cmul4(p[i - 1], p[i - 1]);
            }
            const f32x2 one = {1.f, 0.f}, zero = {0.f, 0.f};
            const unsigned ts = lb + L_TS + l16, tr = lb + L_TR + wm * 2048 + l16;
            if (wm == 0) {
                *LDSP(f32x4, ts) = f32x4{L1[0], L1[1], C1[0], C1[1]};
                *LDSP(f32x4, ts + 1024) = f32x4{p[3][0], p[3][1], p[4][0], p[4][1]};
                *LDSP(f32x4, ts + 2048) = f32x4{p[6][0], p[6][1], __uint_as_float(tagA), __uint_as_float(tagB)};
            }
            f32x2 Q[2], V[2], W[2];
            Q[0] = cmul4(cmul4(C1, (wm & 1) ? p[5] : one), (wm & 2) ? p[6] : one); Q[1] = cmul4(Q[0], p[4]);
            V[1] = (wm & 1) ? one : p[5]; V[0] = cmul4(V[1], p[4]);
            W[1] = cmul4((wm & 1) ? one : p[9], (wm & 2) ? one : p[10]); W[0] = cmul4(W[1], p[8]);
#pragma unroll
            for (int i = 0; i < 2; ++i) {
                const f32x2 Q8 = cmul4(Q[i], p[3]), U = 4 * wm + 2 * i >= 16 - tm ? cmul4(W[i], p[7]) : zero, Wz = 4 * wm + 2 * i + 1 >= 16 - tm ? W[i] : zero;
                *LDSP(f32x4, tr + i * 1024) = f32x4{Q[i][0], Q8[0], Q[i][1], Q8[1]};
                *LDSP(f32x4, tr + i * 1024 + 8192) = f32x4{Wz[0], Wz[1], U[0], U[1]};
                *LDSP(f32x2, tr + i * 1024 + 16384) = V[i];
            }
        }
        auto b0 = [&]() {
            unsigned k0 = trow + wm * 32768 + g4 * 2048 + ((l15 ^ (2 * g4)) << 4), k1 = k0 + 7168, k2 = (k0 ^ 128) + 14336, k3 = k2 + 7168;
            if (HKV) { TIE(k0); TIE(k1); TIE(k2); TIE(k3); }
            if (HKV == 1) { kv_dma(lb + L_KS + wm * 4096, k0, k1, k2, k3, a.k); kv_dma(lb + L_VS + wm * 4096, k0, k1, k2, k3, a.v); }
            WAIT_L(0); BAR(); HTSE(1);
            if (HKV == 2) { kv_dma(lb + L_KS + wm * 4096, k0, k1, k2, k3, a.k); kv_dma(lb + L_VS + wm * 4096, k0, k1, k2, k3, a.v); }
        };
        if (CB0 == 0) b0();
        const unsigned dskl = (__builtin_amdgcn_readlane(hal, 2) >> ((wm & 1) * 16)) & 0xffffu;
        u32x4 kvo[4];
        u32x2 qv[8];
        const char* krow = a.ssmk + (size_t)chn * 4096;
        const char* qs = a.q + (size_t)(idx * 64 + wm * 16) * 2048 + xcd * 256;
        const char* kvd = ws + WS_KVT + (size_t)(xcd * 8 + wm * 2) * 65536 + idx * 2048;
        const int pl = (PLT >> ((STG_SEL) * 4)) & 15;
        const unsigned qo = lh * 8192 + l31 * 8 + 4096;
        auto hb = [&](auto nc) {
            constexpr int nb = decltype(nc)::value;
            if constexpr (nb == KAT && HKV) WAIT_V(0);
            if constexpr (nb == KFB) { WAIT_V((PK ? 0 : 4) + (PQ ? 0 : 8)); int lm = (1 << KFB) - (2 << (KAT + __builtin_popcount(KV3))); ties_(lm); wait_if<0, 0>((lm >> pl) & 1); }
            BAR();
            if constexpr (nb == KFB) flag_if<0, KOWN ? -1 : 1>(wm, fpub, tag, flg + FL_KV);
            if constexpr (TCH && nb == TCB) {
                if (TCH == 1) { const u32x4 nz = {~tagA, ~tagA, ~tagA, ~tagA}; if (lane < 32) GST_B128_SC1(((tm * 2 + (wm >> 1)) * 1024 + tn * 64 + (wm & 1) * 32) * 16 + l16, nz, ws + WS_E, 0); }
                else if (TCH == 3) {
                    const unsigned o = (tn * 64 + lane) * 2048 + ((tm & 3) * 4 + wm) * 128;
                    asm volatile("global_load_dword %0, %2, %3\n\tglobal_load_dword %1, %2, %4" : "=&v"(tch), "=&v"(tch2) : "v"(o), "s"(a.cre), "s"(a.cim) : "memory");
                } else if (TCH == 4) {
                    asm volatile("global_load_dword %0, %1, %2" : "=&v"(tch) : "v"((tn * 64 + lane) * 2048), "s"(((wm & 1) ? a.cim : a.cre) + (((wm & 2) ? SLICE(6) : SLICE(5)) << 7)) : "memory");
                }
                else asm volatile("global_load_dword %0, %1, %2 sc1" : "=&v"(tch) : "v"((lane >> 1) * 16384 + tn * 1024 + wm * 256 + (lane & 1) * 128), "s"(ws + WS_E) : "memory");
            }
            if constexpr (nib_any(PLT, nb)) { if (!PK) ld4_if<nb>(pl, l16, sw, krow); if (!PQ) ld8_if<nb>(pl, qo, qv, qs, qs + 16384); }
        };
#define HB(n) hb(std::integral_constant<int, (n) + NBK>{})
        {
            unsigned kw[2][4], ta[2], th[2];
            u32x2 hm[2];
#pragma unroll
            for (int j = 0; j < 2; ++j) {
                const unsigned x = take(kw0[j]);
                const auto y = __builtin_amdgcn_permlane16_swap(x, x, false, false);
#pragma unroll
                for (int p = 0; p < 2; ++p) { kw[p][2 * j] = y[p] << 16; kw[p][2 * j + 1] = y[p] & 0xffff0000u; }
            }
#pragma unroll
            for (int p = 0; p < 2; ++p) {
                unsigned h[3];
#pragma unroll
                for (int j = 0; j < 3; ++j) h[j] = (__shfl(hal, p * 8 + (r >> 1) + 16 * (j + 1)) >> ((r & 1) * 16)) & 0xffffu;
                hm[p] = idx ? u32x2{h[0] << 16, h[1] | (h[2] << 16)} : u32x2{0u, 0u};
                const int q = r >> 2, pp = r & 3;
                ta[p] = lb + L_KS + (8 * g4 + q) * 256 + (((wm * 4 + p * 2 + (pp >> 1)) ^ (2 * (q | ((g4 & 1) << 2)))) << 4) + (pp & 1) * 8;
                th[p] = (ta[p] ^ 128) - 1024;
                TIE(hm[p]); TIE(ta[p]); TIE(th[p]);
#pragma unroll
                for (int j = 0; j < 4; ++j) TIE(kw[p][j]);
            }
            TS(21);
            if (CB0 == 1) b0();
#pragma unroll
            for (int i = 0; i < 4; ++i) asm volatile("" : "=v"(sw[i]), "=v"(qv[2 * i]), "=v"(qv[2 * i + 1]));
            static_for<1, KAT + 1>(hb);
            TS(2);
            u32x2 kt[4][3], vt[4][2];
#pragma unroll
            for (int u = 0; u < 4; ++u) {
                const int p = u >> 1, o = (u & 1) * 8192;
                auto tr = [&](unsigned ad) -> u32x2 { return __builtin_bit_cast(u32x2, __builtin_amdgcn_ds_read_tr16_b64_v4i16(LDSP(v4i16, ad))); };
                kt[u][0] = tr(th[p] + o); kt[u][1] = tr(ta[p] + o); vt[u][0] = tr(ta[p] + o + 16384); kt[u][2] = tr(ta[p] + o + 1024); vt[u][1] = tr(ta[p] + o + 17408);
                SB();
            }
            TS(14);
            __builtin_amdgcn_s_setprio(KPRIO);
            static_for<0, 4>([&](auto uc) {
                constexpr int u = decltype(uc)::value, p = u >> 1, oh = u & 1;
                const u32x2 k0 = oh == 0 && g4 == 0 ? hm[p] : kt[u][0];
                unsigned t0[8], t1[8], e[7];
#define K3(n) do { if constexpr ((KV3 >> (6 * u + n)) & 1) hb(std::integral_constant<int, KAT + 1 + __builtin_popcount(KV3 & ((1 << (6 * u + n)) - 1))>{}); } while (0)
                if constexpr (RX & 1) {
                kf4a(t0, e, k0[0], k0[1], kt[u][1][0], kt[u][1][1], kw[p]); K3(0); kf4b(t0, e, kw[p]); K3(1); kf4c(t0, vt[u][0][0], vt[u][0][1]); K3(2);
                kf4a(t1, e, kt[u][1][0], kt[u][1][1], kt[u][2][0], kt[u][2][1], kw[p]); K3(3); kf4b(t1, e, kw[p]); K3(4); kf4c(t1, vt[u][1][0], vt[u][1][1]);
                } else {
                kv4a(t0, e, k0[0], k0[1], kt[u][1][0], kt[u][1][1], kw[p]); K3(0); kv4b(t0, e, kw[p]); K3(1); kv4c(t0, vt[u][0][0], vt[u][0][1]); K3(2);
                kv4a(t1, e, kt[u][1][0], kt[u][1][1], kt[u][2][0], kt[u][2][1], kw[p]); K3(3); kv4b(t1, e, kw[p]); K3(4); kv4c(t1, vt[u][1][0], vt[u][1][1]);
                }
                kvo[u] = u32x4{t1[2], t1[0], t0[2], t0[0]};
                GST_B128_SC1(l16, kvo[u], kvd + (u >> 1) * 65536, (u & 1) * 1024);
                K3(5);
#undef K3
            });
            hb(std::integral_constant<int, KAT + 1 + __builtin_popcount(KV3)>{});
            __builtin_amdgcn_s_setprio(3);
        }
        TS(3);
        const char* usrc = ws + WS_KVT + (size_t)(xcd * 8 + (idx >> 2)) * 65536 + wm * 16384 + (idx & 3) * 64;
        const unsigned uo = (lane >> 2) * 256 + (lane & 3) * 16;
        const char* cs = ((wm & 2) ? a.cim : a.cre) + (size_t)(tn * 64 + (wm & 1) * 2) * 2048;
        unsigned co[4];
#pragma unroll
        for (int i = 0; i < 4; ++i) {
            const int pair = i * 4 + g4, v = r ^ pair, row = pair * 2 + (v >> 3);
            co[i] = ((row & 15) * 4 + (row >> 4)) * 2048 + (v & 7) * 16 - i * 1024;
        }
        bool kv_ok = false;
#pragma unroll
        for (int it = 0; it < 4; ++it) asm volatile("" : "=v"(uu[it]));
        auto cst = [&](int place, int slice) { const char* s = cs + (slice << 7); DMA(lb + place + wm * 4096, co[0], s); dmao_<1024>(co[1], s); dmao_<2048>(co[2], s); dmao_<3072>(co[3], s); };
        auto items = [&](auto pc) {
            constexpr int p = decltype(pc)::value;
            if constexpr (PU == p) {
                WAIT_V(it_cnt(PU - 1, 5, PU - 1)); TIE(fl);
                if (TCH >= 2) TIE(tch);
                if (TCH == 3) TIE(tch2);
                kv_ok = __all(fl[0] == tagA && fl[1] == tagB);
                const char* us = kv_ok ? usrc : a.x;
                for (int it = 0; it < 4; ++it) GLD_B128_SC1(uu[it], uo, us + it * 4096, 0);
            }
            if constexpr (PK == p) ld4_if<0>(0, l16, sw, krow);
            if constexpr (PC0 == p) cst(g2_place(0), SLICE(0));
            if constexpr (PC1 == p) cst(g2_place(1), SLICE(1));
            if constexpr (PQ == p) ld8_if<0>(0, qo, qv, qs, qs + 16384);
            if constexpr (PU == p + 1) GLD_B64_SC1(fl, fpoll, flg + FL_KV, 0);
            if constexpr (PC2 == p) cst(g2_place(5), SLICE(5));
            if constexpr (PC3 == p) cst(g2_place(6), SLICE(6));
        };
#define ITEMS(p) items(std::integral_constant<int, p>{})
        static_assert(KFB > KAT + 1 + __builtin_popcount(KV3) && KV3 < (1 << 23) && KFB <= 13 && KFB <= PC0 && KFB < PU - 1 && nib(PLT, 0) >= KAT && nib(PLT, 3) < PU - 1, "schedule");
        static_for<KAT + 2 + __builtin_popcount(KV3), 15>([&](auto nc) { hb(nc); if constexpr (decltype(nc)::value < 14) items(nc); });
        const unsigned ud = lb + (lane & 3) * CHSZ + UOFF + ((lane >> 2) & 1) * UPL + (16 + wm * 32 + (lane >> 3)) * 16;
        float dz = lane == 0 ? bf2f(dskl) : 0.f;
        if (RX & 2) TIE(dz);
        auto zero = [&]() {
            const u32x4 z4 = {0, 0, 0, 0};
            if (lane < 28) *LDSP(u32x4, lb + L_SY + l16) = z4;
            if (lane < 56) *LDSP(u32x4, lb + L_AGG + wm * 896 + l16) = z4;
            *LDSP(u32x4, lane < 32 ? cb + ((lane >> 3) & 1) * CST + (lane & 7) * 16 : cb + UOFF + (g4 & 1) * UPL + r * 16) = z4;
        };
        auto fill = [&]() {
#pragma unroll
            for (int it = 0; it < 4; ++it) TIE(sw[it]);
            if (RX & 2) sw[0][0] = pack2(bflo(sw[0][0]) + dz, bfhi(sw[0][0]));
#pragma unroll
            for (int it = 0; it < 4; ++it) {
                const unsigned q = cb + 128 + it * 1024 + l16;
                const unsigned d0 = sw[it][0], d1 = sw[it][1], d2 = sw[it][2], d3 = sw[it][3];
                const unsigned o1 = it ? __builtin_amdgcn_readlane(sw[it ? it - 1 : 0][3], 63) : 0u;
                const unsigned x1 = __builtin_amdgcn_update_dpp(o1, d3, 0x138, 0xf, 0xf, false);
                *LDSP(u32x4, q) = sw[it];
                *LDSP(u32x4, q + CST) = u32x4{__builtin_amdgcn_alignbyte(d0, x1, 2), __builtin_amdgcn_alignbyte(d1, d0, 2), __builtin_amdgcn_alignbyte(d2, d1, 2), __builtin_amdgcn_alignbyte(d3, d2, 2)};
            }
        };
        auto uput = [&]() {
            WAIT_V(it_cnt(PU, 0, UE ? 15 : 16));
#pragma unroll
            for (int it = 0; it < 4; ++it) TIE(uu[it]);
            if (!kv_ok) {
                TSE(23);
                for (unsigned spin = 0; spin < SPIN_CAP; ++spin) {
                    GLD_B64_SC1(fl, fpoll, flg + FL_KV, 0);
                    WAIT_V(0); TIE(fl);
                    if (__all(fl[0] == tagA && fl[1] == tagB)) break;
                    __builtin_amdgcn_s_sleep(8);
                }
#pragma unroll
                for (int it = 0; it < 4; ++it) GLD_B128_SC1(uu[it], uo, usrc + it * 4096, 0);
                WAIT_V(0);
#pragma unroll
                for (int it = 0; it < 4; ++it) TIE(uu[it]);
            }
#pragma unroll
            for (int it = 0; it < 4; ++it) *LDSP(u32x4, ud + it * 128) = uu[it];
        };
        static_assert(PU <= (UE ? 15 : 16) && (FP_ != 14 || nib(PLT, 3) <= 12), "u");
        ITEMS(14);
        if (UE) __builtin_amdgcn_s_setprio(UPRIO);
        if (P14 != 3) __builtin_amdgcn_s_setprio(P14);
        if (UE >= 2 || ZP_ == 14) zero();
        if (UE == 2 || FP_ == 14) { WAIT_V(it_cnt(12, 8, 14)); fill(); }
        BAR();
        if (P14 != 3) __builtin_amdgcn_s_setprio(3);
        ITEMS(15);
        TS(4);
        if (P15 != 3) __builtin_amdgcn_s_setprio(P15);
        if (UE < 2 && ZP_ == 15) zero();
        if (FSL) __builtin_amdgcn_s_sleep(FSL);
        if ((UE & 1) || FP_ == 15) fill();
        if (UE) uput();
        WAIT_L(0);
        TS(5);
        BAR();
        __builtin_amdgcn_s_setprio(FPRIO);
        ITEMS(16);
        if (P16 != FPRIO) __builtin_amdgcn_s_setprio(P16);
        if (S16) __builtin_amdgcn_s_sleep(S16);
        TS(11);
        const unsigned pmine = lb + L_SY + 32 + wm * 4, pall = lb + L_SY + 32 + (lane & 3) * 4;
        static_assert(!PK || (!UE && PK > KFB && PK <= 16), "ssm_k");
        static_assert(!PQ || PQ > KFB, "q");
        if (!UE) { if (PK >= PU) { uput(); sy_post<0>(pmine); WAIT_V(it_cnt(PK, 1, 16)); fill(); } else { if (FP_ == 16) fill(); TS(13); uput(); sy_post<0>(pmine); } ITEMS(17); sy_wait<0>(pall); }
        if (!CPOST) { WAIT_V(0); sy_post<1>(pmine); }
        __builtin_amdgcn_s_setprio(CVPRIO);
        TS(6);
        f32x4 ca[8];
        {
            unsigned kb[4];
#pragma unroll
            for (int i = 0; i < 4; ++i) { kb[i] = cb + (~r & 1) * CST + 2 * (64 + 16 + r - 8 * g4 - 7 + (~r & 1)) + i * 1024; TIE(kb[i]); }
            unsigned ub = cb + UOFF + (g4 & 1) * UPL + (15 + r + (g4 >> 1)) * 16 - 64;
#pragma unroll
            for (int i = 0; i < 8; ++i) { ca[i] = f32x4{0, 0, 0, 0}; TIE(ca[i]); }
            s16x4 FA[4][2]; bf16x8 FB[2][8];
#define CV_A(s, o, e) dsr2_<16 * ((s) + ((o) >> 3)) + 128 * (cv_m((o) & 7) & 1) + 2 * (e)>(FA[(o) & 3][e], kb[cv_m((o) & 7) >> 1])
#define CV_B(s, jb) DSR128(FB[(s) & 1][jb], ub, 64 + 256 * (jb) - 32 * (s))
            static_for<0, 8>([&](auto ic) { constexpr int n = decltype(ic)::value; CV_B(0, n); CV_A(0, n >> 1, n & 1); });
            WAIT_L(0);
            unsigned pw = lb + L_SY + (CVLATE == 2 ? wm + (lane & 1) * 4 : lane & 7) * 4;
            if (QP) TIE(pw);
            auto body = [&](int it) {
                static_for<0, 72>([&](auto ic) {
                    constexpr int s = decltype(ic)::value / 36, p = decltype(ic)::value % 36, o = cv_grp(p), m = cv_m(o), jb = p - cv_first(o);
                    if constexpr (QP && decltype(ic)::value == QP) {
                        unsigned t;
                        asm volatile("s_cmp_lg_u32 %2, 0\n\ts_cbranch_scc1 3f\n\ts_waitcnt vmcnt(0)\n\tds_write_b32 %3, %3 offset:64\n\ts_branch 2f\n1:\n\ts_sleep 1\n2:\n\tds_read_b32 %0, %1 offset:64\n\ts_waitcnt lgkmcnt(0)\n\t"
                                     "v_cmp_eq_u32 vcc, 0, %0\n\ts_cbranch_vccnz 1b\n3:" : "=&v"(t) : "v"(pw), "s"(it), "v"(pmine) : "vcc", "scc", "memory");
                    }
                    if constexpr (jb == 0) { WAIT_LB(cv_wait(o)); if constexpr (!(AMF & 1)) { TIE(FA[o & 3][0]); TIE(FA[o & 3][1]); } }
                    if constexpr (p == 0 && !(AMF & 1)) { for (int i = 0; i < 8; ++i) TIE(FB[s][i]); }
                    const bf16x8 fa = __builtin_shufflevector(FA[o & 3][0], FA[o & 3][1], 0, 1, 2, 3, 4, 5, 6, 7);
                    if constexpr (AMF & 1) mfma_(ca[jb + m], fa, FB[s][jb]); else
                    ca[jb + m] = __builtin_amdgcn_mfma_f32_16x16x32_bf16(fa, FB[s][jb], ca[jb + m], 0, 0, 0);
                    SB();
                    constexpr int rd = cv_read(p);
                    if constexpr (rd >= 32) CV_B(s + 1, rd - 32);
                    else if constexpr (rd >= 0) CV_A(s, rd >> 1, rd & 1);
                    SB();
                });
                for (int i = 0; i < 4; ++i) { kb[i] += 128; TIE(kb[i]); }
                ub -= 64;
            };
#pragma unroll 1
            for (int it = 0; it < QIT; ++it) body(it);
            {
                TS(19);
                static_assert(!QP || (CPOST && !QIT && !EP && !CVE && CVLATE && PC3 < 19), "pause");
                if (QP) sy_wait<6, 0>(pw); else
                if (CPOST) { WAIT_V(PQ == 17 && PC3 < 17 ? 8 : 0); sy_post<1>(pmine); WAIT_L(0); }
                static_assert(PC3 != 19 || (ZFB == 1 && (BARM & 0x3ff) == 0x220), "stages 5, 6 are covered by the second barrier");
                if (PC3 == 19 || PQ == 19) { sy_wait<0>(lb + L_ES + (lane & P19M) * 256 + 8); ITEMS(19); }
                if (CVLATE && !QP) sy_wait<CVE ? 6 : 1, CVS>(lb + L_SY + (CVLATE == 2 ? wm + (lane & 1) * 4 : lane & 7) * 4);
                TS(20);
                if (PQ == 20 && QSL) __builtin_amdgcn_s_sleep(QSL);
                ITEMS(20);
            }
#pragma unroll 1
            for (int it = QIT; it < CVN; ++it) body(it);
            if (CVZ) __builtin_amdgcn_s_sleep(CVZ);
            WAIT_L(0);
            if (AMF & 1) asm volatile("s_nop 7\n\ts_nop 7" : "+v"(ca[0]), "+v"(ca[1]), "+v"(ca[2]), "+v"(ca[3]), "+v"(ca[4]), "+v"(ca[5]), "+v"(ca[6]), "+v"(ca[7]));
#pragma unroll
            for (int i = 0; i < 4; ++i) { TIE(FA[i][0]); TIE(FA[i][1]); }
#pragma unroll
            for (int i = 0; i < 8; ++i) TIE(FB[0][i]);
#undef CV_A
#undef CV_B
        }
        TS(7);
        __builtin_amdgcn_s_setprio(3);
        constexpr int NBAR = __builtin_popcount(BARM & ~VBM);
        unsigned zpub = (xcd * 32 + l31) * 256 + idx * 8, zpoll = ZOWN ? (xcd * 32 + idx) * 256 + l31 * 8 : fpoll;
        {
            const float Dd = bf2f(dskl);
            const char* zd = ws + WS_ZT + (size_t)(xcd * 128 + wm * 32) * 4096 + idx * 128;
            u32x2 kvr[8];
#pragma unroll
            for (int jt = 0; jt < 8; ++jt) if (!(RX & 2)) kvr[jt] = *LDSP(u32x2, cb + UOFF + (g4 >> 1) * UPL + (16 + 16 * jt + r) * 16 + (1 - (g4 & 1)) * 8);
#pragma unroll
            for (int jt = 0; jt < 8; ++jt) {
                const f32x4 y = ca[jt];
                const u32x2 kq = kvr[jt];
                *LDSP(u32x2, cb + (jt * 4 + (r >> 2)) * 144 + (r & 3) * 32 + g4 * 8) = (RX & 2) ? u32x2{pack2(y[0], y[1]), pack2(y[2], y[3])} : ZM ?
                    u32x2{pack2(add1(rb1(y[0]), rb1(mul1(bfhi(kq[1]), Dd))), add1(rb1(y[1]), rb1(mul1(bflo(kq[1]), Dd)))), pack2(add1(rb1(y[2]), rb1(mul1(bfhi(kq[0]), Dd))), add1(rb1(y[3]), rb1(mul1(bflo(kq[0]), Dd))))} :
                    u32x2{pack2(rb1(y[0]) + rb1(bfhi(kq[1]) * Dd), rb1(y[1]) + rb1(bflo(kq[1]) * Dd)), pack2(rb1(y[2]) + rb1(bfhi(kq[0]) * Dd), rb1(y[3]) + rb1(bflo(kq[0]) * Dd))};
            }
            unsigned za = lb + (r >> 2) * CHSZ + (wm * 8 + (g4 >> 1)) * 144 + ((g4 & 1) * 2 + ((r >> 1) & 1)) * 32 + (r & 1) * 8, zo = (lane >> 3) * 4096 + (lane & 7) * 16;
            TIE(za); TIE(zo); TIE(zpub); TIE(zpoll);
            sy_post<3>(pmine); sy_wait<3, 0>(pall);
            v4i16 t[4][2];
#pragma unroll
            for (int it = 0; it < 4; ++it)
#pragma unroll
                for (int e = 0; e < 2; ++e) t[it][e] = __builtin_amdgcn_ds_read_tr16_b64_v4i16(LDSP(v4i16, za + it * 288 + e * 16));
#pragma unroll
            for (int it = 0; it < 4; ++it) { TIE(t[it][0]); TIE(t[it][1]); }
            if (ZSB) BAR();
#pragma unroll
            for (int it = 0; it < 4; ++it) { const bf16x8 f = __builtin_shufflevector(t[it][0], t[it][1], 0, 1, 2, 3, 4, 5, 6, 7); GST_B128_SC1(zo, f, zd + it * 32768, 0); }
        }
#pragma unroll
        for (int i = ZSB; i < ZFB; ++i) BAR();
        WAIT_V(0);
        sy_post<2>(pmine);
        if (wm == 0) {
            sy_wait<2>(pall);
            if (ZOWN) { if (lane < 32) GST_B64_SC1(zpub, tag, flg + FL_Z); }
            else if (lane == 0) GST_B64_SC1(fpub, tag, flg + FL_Z);
        }
        TS(8);
        unsigned qm[8][4];
        if (QM == 1 || QM == 2) {
#pragma unroll
            for (int n = 0; n < 8; ++n) {
                TIE(qv[n]);
                if (QM == 2) { qm[n][0] = qv[n][0] << 16; qm[n][2] = qv[n][1] << 16; } else { qm[n][0] = qv[n][0] & 0xffffu; qm[n][2] = qv[n][1] & 0xffffu; }
                qm[n][1] = qv[n][0] & 0xffff0000u; qm[n][3] = qv[n][1] & 0xffff0000u;
                TIE(qm[n][0]); TIE(qm[n][1]); TIE(qm[n][2]); TIE(qm[n][3]);
            }
        }
#pragma unroll
        for (int i = ZFB; i < NBAR - 1 - (ZE >= 0); ++i) { BAR();
        }
        if (ZSL) __builtin_amdgcn_s_sleep(ZSL);
        asm volatile("s_cmp_lg_u32 %3, 0\n\ts_cbranch_scc1 1f\n\tglobal_load_dwordx2 %0, %1, %2 sc1\n1:" : "=&v"(fl) : "v"(zpoll), "s"(flg + FL_Z), "s"(wm) : "scc", "memory");
        if (NBAR > ZFB) BAR();
        if (wm == 0) {
            unsigned left = SPIN_CAP;
            asm volatile("s_branch 2f\n1:\n\ts_sleep %5\n\tglobal_load_dwordx2 %0, %2, %3 sc1\n2:\n\ts_waitcnt vmcnt(0)\n\tv_cmp_ne_u64 vcc, %4, %0\n\ts_cbranch_vccz 3f\n\ts_sub_u32 %1, %1, 1\n\ts_cbranch_scc0 1b\n3:"
                         : "+v"(fl), "+s"(left) : "v"(zpoll), "s"(flg + FL_Z), "s"(tag), "n"(ZPS) : "vcc", "scc", "memory");
            sy_post<4>(pmine);
        } else sy_wait<4, 2>(lb + L_SY + 32);
        u32x4 zv[4];
        {
            ITEMS(18);
            const char* zsrc = ws + WS_ZT + (size_t)(xcd * 128 + idx * 4 + wm) * 4096;
            static_for<0, 4>([&](auto ic) { constexpr int it = decltype(ic)::value; GLD_B128_SC1(zv[it], (l31 >> 2) * 512 + lh * 64 + (lane & 3) * 16, zsrc, it * 128); });
        }
        WAIT_V(0);
        TS(9);
#pragma unroll
        for (int it = 0; it < 4; ++it) TIE(zv[it]);
        if (!(XAB & 1)) {
        quad_swap<0>(zv[0], zv[1]); quad_swap<0>(zv[2], zv[3]);
        quad_swap<1>(zv[0], zv[2]); quad_swap<1>(zv[1], zv[3]); }
#pragma unroll
        for (int it = 0; it < 4; ++it) TIE(zv[it]);
        char* const oa = a.out + (size_t)(idx * 64 + wm * 16) * 8192 + xcd * 512;
        if (OPRIO != 3) __builtin_amdgcn_s_setprio(OPRIO);
        if (OS0) __builtin_amdgcn_s_sleep(OS0);
        if (OSW && ZE < 0) for (int i = 0; i < wm; ++i) __builtin_amdgcn_s_sleep(OSW);
#pragma unroll
        for (int n = 0; n < 8; ++n) {
            if (!QM || QM == 3) TIE(qv[n]);
            const unsigned z0 = zv[n & 3][2 * (n >> 2)], z1 = zv[n & 3][2 * (n >> 2) + 1];
            const f32x4 o = (XAB & 2) ? __builtin_bit_cast(f32x4, zv[n & 3]) : QM == 1 ? mul4bf(u32x2{z0, z1}, qm[n]) :
                QM == 3 ? f32x4{mul1(bflo(qv[n][0]), bflo(z0)), mul1(bfhi(qv[n][0]), bfhi(z0)), mul1(bflo(qv[n][1]), bflo(z1)), mul1(bfhi(qv[n][1]), bfhi(z1))} :
                QM == 2 && (RX & 4) ? f32x4{mul1(__uint_as_float(qm[n][0]), bflo(z0)), mul1(__uint_as_float(qm[n][1]), bfhi(z0)), mul1(__uint_as_float(qm[n][2]), bflo(z1)), mul1(__uint_as_float(qm[n][3]), bfhi(z1))} :
                QM == 2 ? f32x4{rb1(mul1(__uint_as_float(qm[n][0]), bflo(z0))), rb1(mul1(__uint_as_float(qm[n][1]), bfhi(z0))), rb1(mul1(__uint_as_float(qm[n][2]), bflo(z1))), rb1(mul1(__uint_as_float(qm[n][3]), bfhi(z1)))} :
                f32x4{rb1(bflo(qv[n][0]) * bflo(z0)), rb1(bfhi(qv[n][0]) * bfhi(z0)), rb1(bflo(qv[n][1]) * bflo(z1)), rb1(bfhi(qv[n][1]) * bfhi(z1))};
            if (n == ZE) { BAR(); if (OSW) for (int i = 0; i < wm; ++i) __builtin_amdgcn_s_sleep(OSW); }
            if (n == 0 && OSG) for (int i = 0; i < (idx & 3); ++i) __builtin_amdgcn_s_sleep(OSG);
            if (!(XAB & 4))
            GST_B128_NT(lh * 32768 + l31 * 16, o, oa + ((n >> 2) * 8 + (n & 3)) * 8192, 0);
            if (OSL && n < 7 && n + 1 != ZE) __builtin_amdgcn_s_sleep(OSL);
        }
        asm volatile(".Lfk_code_a:");
        TSE(10);
        TS_FLUSH();
        return;
    }

    u32x2 fl;
    const unsigned* dv = kvv;
#define G1_ISSUE(st, i) do { if constexpr ((st) < DFN) { if constexpr ((i) > 0) dmaof_<(i) * 1024>(dv[i], g1src + ((((st) << 7) + rot7) & 1920)); else dmaf_(g1dst + g1_slot(st), dv[i], g1src + ((((st) << 7) + rot7) & 1920)); } else if constexpr (M1 && (i) > 0) dmao_<(i) * 1024>(dv[i], g1src + ((((st) << 7) + rot7) & 1920)); \
        else DMA(g1dst + g1_slot(st) + (i) * 1024, dv[i], g1src + ((((st) << 7) + rot7) & 1920)); } while (0)
    unsigned k0 = trow + wm * 32768 + g4 * 2048 + ((l15 ^ (2 * g4)) << 4), k1 = k0 + 7168, k2 = (k0 ^ 128) + 14336, k3 = k2 + 7168;
    auto pre = [&](auto ic) {
        constexpr int n = decltype(ic)::value;
        if constexpr (n == KVP) { if (!HKV) kv_dma(g1dst + L_KS, k0, k1, k2, k3, wn ? a.v : a.k); }
        else static_for<0, 4>([&](auto jc) { G1_ISSUE(n - (n > KVP), decltype(jc)::value); });
    };
    static_for<1, AP0 / 4>(pre);
    TS(14);
    SLICE_TABLE();
    const int j0 = 4 * run - (32 - 2 * tm);
    const int pneg = EALL ? 0 : 16 * (wm >> 1) + 15 - (32 - 2 * tm);
    unsigned long long em0 = __ballot(j0 >= 0), em1 = __ballot(j0 + 2 >= 0);
    if (EM == 2) asm volatile("" : "+s"(em0), "+s"(em1));
    unsigned eo[2], pes, plr, est, hw[2], ta[2], po, pmine, pall, pown, pzero, g2v[2], cad[2][2], to[2][2], xo, dso, fst, fld, cwa;
    unsigned cta = lb + L_TS + st * 16, ctb = lb + L_TR + run * 1024 + st * 16;
    TIE(cta); TIE(ctb);
    f32x4 ct[5]; f32x2 V1;
    const int gm = PAIR ? wid >> 1 : wm, gk = PAIR ? wid & 1 : wn, gs = gk * 4 + (gm & 2);
    auto addrs = [&]() {
        int ln = lane;
        TIE(ln);
        const int l15 = ln & 15, g4 = ln >> 4, st = sq * 16 + l15, run = (wm >> 1) * 4 + g4, j0 = 4 * run - (32 - 2 * tm);
        pes = lb + L_ES + wid * 256 + l15 * 16;
        plr = lb + L_AGG + st * 8 + run * 512; est = ((tm * 2 + (wm >> 1)) * 1024 + tn * 64 + st) * 16 + (EREP ? g4 << 19 : 0);
#pragma unroll
        for (int i = 0; i < 2; ++i) { eo[i] = (j0 + 2 * i < 0 ? 0 : j0 + 2 * i) * 16384 + (tn * 64 + st) * 16 + (EREP == 1 ? (tm >> 2) << 19 : EREP == 2 ? (tm & 3) << 19 : 0); TIE(eo[i]); }
        TIE(plr); TIE(pes); TIE(est);
        const unsigned ht = lb + L_HT + wid * 4096;
#pragma unroll
        for (int j = 0; j < 2; ++j) {
            const int pr = (8 * (g4 & 1) + 4 * j + (l15 >> 2)) ^ ((g4 & 1) * 4), pw = l15 ^ ((l15 >> 3) * 4);
            ta[j] = ht + (g4 >> 1) * 512 + pr * 32 + ((((l15 >> 1) ^ (pr >> 2)) & 1) << 4) + 8 * (ln & 1);
            hw[j] = ht + g4 * 1024 + pw * 32 + (((j ^ (pw >> 2)) & 1) << 4);
            TIE(ta[j]); TIE(hw[j]);
        }
        po = pes + ((wm & 2) ? -512 : 512);
        pmine = lb + L_SY + wid * 4; pall = lb + L_SY + (ln & 7) * 4; pown = lb + L_SY + (ln < 4 ? 32 + ln * 4 : gs * 4 + (ln & 4)); pzero = lb + L_SY;
        xo = (tm * 128 + gm * 32 + gk * 16 + g4 * 4) * 2048 + tn * 128 + l15 * 8 + 4096; dso = xo & 2047;
        fst = (tm * 16 + l15) * 128 + tn * 8; fld = (tm * 16 + tn) * 128 + l15 * 8;
#pragma unroll
        for (int i = 0; i < 2; ++i) {
            const int pair = i * 4 + g4, v = l15 ^ pair ^ ((wm & 1) * 8);
            g2v[i] = (pair * 2 + (v >> 3)) * 8192 + (v & 7) * 16 + (tn * 64 + wm) * 2048 - i * 1024;
            TIE(g2v[i]);
#pragma unroll
            for (int j = 0; j < 2; ++j) {
                cad[i][j] = lb + L_C + (img_off((g4 >> 1) * 64 + l15, 4 * gk + (g4 & 1)) ^ (i * 32) ^ (j * 128));
                to[i][j] = ta[j] + (i ^ gk) * 1024 + (gm & 1) * 2048 + (gs - wid) * 4096;
                TIE(cad[i][j]); TIE(to[i][j]);
            }
        }
        cwa = lb + wid * 2048 + ln * 16;
        TIE(cwa); TIE(po);
        TIE(pmine); TIE(pall); TIE(pown); TIE(pzero); TIE(xo); TIE(dso); TIE(fst); TIE(fld);
    };
    if (!APRE) addrs();
    unsigned aad[2][2], bad[2];
    {
        const int aoff = img_off((wm >> 1) * 64 + l15, g4), boff = img_off(128 + wn * 64 + (wm & 1) * 16 + l15, g4);
#pragma unroll
        for (int ks = 0; ks < 2; ++ks) { aad[ks][0] = lb + (aoff ^ (ks * 64)); aad[ks][1] = lb + (aoff ^ (ks * 64) ^ 128); bad[ks] = lb + (boff ^ (ks * 64)); TIE(aad[ks][0]); TIE(aad[ks][1]); TIE(bad[ks]); }
    }
    if (!HKV && KVP >= AP0 / 4) { TIE(k0); TIE(k1); TIE(k2); TIE(k3); }
    static_for<AP0 / 4, NBK + 1>(pre);
    TS(1);
    float nL1i, nC1r, nC1i;
    unsigned tagA, tagB;
    f32x4 acc[4][2];
    {
#pragma unroll
        for (int i = 0; i < 4; ++i) { acc[i][0] = f32x4{0, 0, 0, 0}; acc[i][1] = f32x4{0, 0, 0, 0}; }
        bf16x8 fa[2][4], fb[2][2];
        if (GPRIO) __builtin_amdgcn_s_setprio(GPRIO);
        WAIT_V(g1_cnt(0)); BAR();
        TS(2);
#define G1_FRAG(g, n) do { constexpr int bo_ = g1_slot((g) >> 1), hi_ = bo_ & ~65535, x_ = (n) < 3 ? (n) >> 1 : (n) - 2; \
        if constexpr ((n) == 0 || (n) == 2) DSR128(fb[(g) & 1][x_], bad[(g) & 1] + hi_, bo_ - hi_ + x_ * 4096); \
        else DSR128(fa[(g) & 1][x_], aad[(g) & 1][x_ & 1] + hi_, bo_ - hi_ + x_ * 2048); } while (0)
        static_for<0, 6>([&](auto ic) { constexpr int n = decltype(ic)::value; G1_FRAG(0, n); });
        static_for<0, 32>([&](auto ic) {
            constexpr int g = decltype(ic)::value, gn = g + 1, n0 = g1_upto(g - 1), nd = g1_upto(g) - n0;
            constexpr bool bar = (gn & 1) == 0 && gn < 32;
            if constexpr (g == 1) {
                DSR128(ct[0], cta, 0); DSR128(ct[1], cta, 1024); DSR128(ct[2], cta, 2048); DSR128(ct[3], ctb, 0); DSR128(ct[4], ctb, 8192); DSR64(V1, ctb, 16384);
            }
            if constexpr (g == 3) {
#pragma unroll
                for (int i = 0; i < 5; ++i) TIE(ct[i]);
                TIE(V1);
                nL1i = -ct[0][1]; nC1r = -ct[0][2]; nC1i = -ct[0][3];
                TIE(nL1i); TIE(nC1r); TIE(nC1i);
                tagA = __builtin_amdgcn_readfirstlane(__float_as_uint(ct[2][2])); tagB = __builtin_amdgcn_readfirstlane(__float_as_uint(ct[2][3]));
                ties_(tagA); ties_(tagB);
            }
            if constexpr (bar) {
                static_assert(g1_cnt(gn >> 1) >= 0, "schedule");
                WAIT_VB(g1_cnt(gn >> 1)); WAIT_LB(0); BAR();
                if constexpr (((gn >> 1) & 3) == 0 || gn >= 26) TS(gn >= 26 ? 23 + (gn >> 1) - 13 : 16 + (gn >> 3));
#pragma unroll
                for (int i = 0; i < 4; ++i) if (!(AMF & 2)) TIE(fa[g & 1][i]);
                if (!(AMF & 2)) { TIE(fb[g & 1][0]); TIE(fb[g & 1][1]); }
            }
            static_for<0, 8>([&](auto mc) {
                constexpr int m = decltype(mc)::value, i = m >> 1, j = m & 1, js = (SNK & 1) && (i & 1) ? 1 - j : j;
                if constexpr (W1 && !bar && g > 0 && g < 31) {
                    if constexpr (m == 0) WAIT_LB(W1 == 1 ? 3 : 2);
                    if constexpr (m == 2 && W1 == 1) WAIT_LB(4);
                    if constexpr (m == 4) WAIT_LB(4);
                } else
                if constexpr (!bar && (i == 0 || j == 0)) {
                    WAIT_LB(5 - (i == 0 ? j + 1 : i + 2) + (gn < 32 ? (m < 6 ? m : 6) : 0));
                    if constexpr (!(AMF & 2) || g == 0) { TIE(fa[g & 1][i]); TIE(fb[g & 1][j]); }
                }
                if constexpr ((AMF & 2) && g > 0) mfma_(acc[i][js], fa[g & 1][i], fb[g & 1][js]); else
                acc[i][j] = __builtin_amdgcn_mfma_f32_16x16x32_bf16(fa[g & 1][i], fb[g & 1][j], acc[i][j], 0, 0, 0);
                SB();
                if constexpr (gn < 32 && m < 6) G1_FRAG(gn, m);
                constexpr int d = nd > 2 ? ((m & 1) ? m >> 1 : -1) : (m == DP0 ? 0 : m == DP1 ? 1 : -1);
                if constexpr (d >= 0 && d < nd) G1_ISSUE((n0 + d) >> 2, (n0 + d) & 3);
                SB();
            });
        });
#undef G1_FRAG
    }
    TS(3);
    const unsigned tagG = tagA ^ tagB ^ 0x9E3779B9u;
    __builtin_amdgcn_s_setprio(MPRIO);
    f32x2 ur[8], ui[8], hr, hi;
    const f32x2 L1 = {ct[0][0], ct[0][1]}, C1 = {ct[0][2], ct[0][3]}, L16 = {ct[1][2], ct[1][3]}, Q2r = {ct[3][0], ct[3][1]}, Q2i = {ct[3][2], ct[3][3]};
    const float L8r = ct[1][0], L8i = ct[1][1], L64r = ct[2][0], L64i = ct[2][1];
    u32x4 eg[4], cr[2 * NR + 1];
    const char* g2src = wn ? a.cim : a.cre;
#define CR_LOAD(n0, n1) static_for<n0, n1>([&](auto ic) { constexpr int n = decltype(ic)::value; GLD_B128(cr[n], g2v[n & 1], g2src + (SLICE(7 + (n >> 1)) << 7), (n & 1) * 1024); })
#define E_LOADS "global_load_dwordx4 v[104:107], %[a0], %[b0] " EMOD "\n\tglobal_load_dwordx4 v[108:111], %[a0], %[b1] " EMOD "\n\t" \
                "global_load_dwordx4 v[112:115], %[a1], %[b0] " EMOD "\n\tglobal_load_dwordx4 v[116:119], %[a1], %[b1] " EMOD "\n"
#define E_POLL() asm volatile("s_cmp_lt_i32 %[on], 0\n\ts_cbranch_scc1 1f\n\ts_mov_b64 exec, %[m1]\n\tglobal_load_dwordx4 v[112:115], %[a1], %[b0] " EMOD "\n\tglobal_load_dwordx4 v[116:119], %[a1], %[b1] " EMOD "\n\t" \
        "s_mov_b64 exec, %[m0]\n\ts_cbranch_execz 2f\n\tglobal_load_dwordx4 v[104:107], %[a0], %[b0] " EMOD "\n\tglobal_load_dwordx4 v[108:111], %[a0], %[b1] " EMOD "\n2:\n\ts_mov_b64 exec, -1\n1:" \
        : "+{v[104:107]}"(eg[0]), "+{v[108:111]}"(eg[1]), "+{v[112:115]}"(eg[2]), "+{v[116:119]}"(eg[3]) \
        : [a0] "v"(eo[0]), [a1] "v"(eo[1]), [b0] "s"(ws + WS_E), [b1] "s"(ws + WS_E + 16384), [on] "s"(pneg), [m0] "s"(em0), [m1] "s"(em1) : "scc", "memory")
    {
        asm volatile("s_nop 7\n\ts_nop 3" : "+v"(acc[0][0]), "+v"(acc[0][1]), "+v"(acc[1][0]), "+v"(acc[1][1]), "+v"(acc[2][0]), "+v"(acc[2][1]), "+v"(acc[3][0]), "+v"(acc[3][1]));
#pragma unroll
        for (int p = 0; p < 8; ++p) { ur[p] = f32x2{acc[p >> 1][0][2 * (p & 1)], acc[p >> 1][0][2 * (p & 1) + 1]}; ui[p] = f32x2{acc[p >> 1][1][2 * (p & 1)], acc[p >> 1][1][2 * (p & 1) + 1]}; }
        f32x2 gr = ur[0], gi = ui[0];
#pragma unroll
        for (int p = 1; p < 8; ++p) { f32x2 nr, ni; if (PLN & 1) c2_mad(nr, ni, L1[0], nL1i, L1[0], L1[1], gr, gi, ur[p], ui[p]); else pk_cmad(nr, ni, L1, gr, gi, ur[p], ui[p]); gr = nr; gi = ni; }
        float fr, fi;
        cmad(fr, fi, L8r, L8i, gr[0], gi[0], gr[1], gi[1]);
        {
            const f32x2 w = cmul4(V1, f32x2{fr, fi});
            const float wr = sum4(w[0]), wi = sum4(w[1]);
            const u32x4 gq = {__float_as_uint(wr), tagA, __float_as_uint(wi), tagG};
            if (EREP ? (EALL || EREP == 2 || g4 >= (tm >> 2)) : g4 == 0) GST_B128_SC1(est, gq, ws + WS_E, 0);
        }
        *LDSP(f32x2, plr + 7 * 512) = f32x2{fr, fi};
        *LDSP(unsigned, pes + 8) = 0;
        if (EM == 2) {
#pragma unroll
            for (int i = 0; i < 4; ++i) eg[i] = u32x4{0u, tagA, 0u, tagG};
            asm volatile("" : "+{v[104:107]}"(eg[0]), "+{v[108:111]}"(eg[1]), "+{v[112:115]}"(eg[2]), "+{v[116:119]}"(eg[3]));
        }
        TS(21);
        if (EP == 2) E_POLL();
        auto coef = [&]() {
#pragma unroll
            for (int p = 0; p < 8; ++p) { f32x2 nr, ni; if (PLN & 2) c2_mul(nr, ni, C1[0], nC1i, nC1r, nC1i, ur[p], ui[p]); else pk_conj_mul(nr, ni, C1, ur[p], ui[p]); ur[p] = nr; ui[p] = ni; TIE(ur[p]); TIE(ui[p]); }
        };
        if (CFB) coef();
        WAIT_L(0); BAR();
        f32x2 e[7];
#pragma unroll
        for (int j = 0; j < 7; ++j) e[j] = *LDSP(f32x2, plr + j * 512);
        if (EP == 1) E_POLL();
        CR_LOAD(0, CR1);
        if (!CFB) coef();
#pragma unroll
        for (int j = 1; j < 7; ++j) { if (PLN & 4) { float xr = e[j][0], xi = e[j][1]; cmac(xr, xi, L16[0], L16[1], e[j - 1][0], e[j - 1][1]); e[j] = f32x2{xr, xi}; } else cmac2(e[j], L16, e[j - 1]); }
        float mr, mi;
        cmad(mr, mi, L8r, L8i, e[6][0], e[6][1], gr[0], gi[0]);
        if (PLN & 4) c2_mul(hr, hi, C1[0], nC1i, nC1r, nC1i, f32x2{e[6][0], mr}, f32x2{e[6][1], mi}); else pk_conj_mul(hr, hi, C1, f32x2{e[6][0], mr}, f32x2{e[6][1], mi});
        TIE(hr); TIE(hi);
        CR_LOAD(CR1, CR1 + CR2);
        TS(4);
    }
    f32x2 G;
    {
        float pr = 0.f, pi = 0.f;
        if (QP) sy_post<6>(pmine);
        if (!EP) E_POLL();
        if (pneg >= 0) {
            unsigned t0, t1, t2, left = SPIN_CAP;
            asm volatile("s_waitcnt vmcnt(%[c])\n\ts_branch 2f\n1:\n\ts_sleep %[sl]\n\t" EM_ON E_LOADS EM_OFF "\ts_waitcnt vmcnt(0)\n2:\n\t"
                         "v_bitop3_b32 %[t0], v105, v109, %[ta] bitop3:0x7e\n\tv_bitop3_b32 %[t1], v113, v117, %[ta] bitop3:0x7e\n\tv_bitop3_b32 %[t2], v107, v111, %[tg] bitop3:0x7e\n\t"
                         "v_or3_b32 %[t0], %[t0], %[t1], %[t2]\n\tv_bitop3_b32 %[t1], v115, v119, %[tg] bitop3:0x7e\n\tv_or_b32 %[t0], %[t0], %[t1]\n\t"
                         "v_cmp_ne_u32 vcc, 0, %[t0]\n\ts_cbranch_vccz 3f\n\ts_sub_u32 %[n], %[n], 1\n\ts_cbranch_scc0 1b\n3:"
                         : "+{v[104:107]}"(eg[0]), "+{v[108:111]}"(eg[1]), "+{v[112:115]}"(eg[2]), "+{v[116:119]}"(eg[3]), [t0] "=&v"(t0), [t1] "=&v"(t1), [t2] "=&v"(t2), [n] "+s"(left)
                         : [a0] "v"(eo[0]), [a1] "v"(eo[1]), [b0] "s"(ws + WS_E), [b1] "s"(ws + WS_E + 16384), [ta] "s"(tagA), [tg] "s"(tagG), [sl] "n"(ESL), [c] "n"(CR1 + CR2) : "vcc", "scc", "memory");
            float ar = __uint_as_float(eg[1][0]), ai = __uint_as_float(eg[1][2]), br = __uint_as_float(eg[3][0]), bi = __uint_as_float(eg[3][2]);
            cmac(ar, ai, L64r, L64i, __uint_as_float(eg[0][0]), __uint_as_float(eg[0][2]));
            cmac(br, bi, L64r, L64i, __uint_as_float(eg[2][0]), __uint_as_float(eg[2][2]));
            if (EALL) { if (j0 < 0) ar = ai = 0.f; if (j0 + 2 < 0) br = bi = 0.f; }
            const f32x2 pu = cmul4(f32x2{ct[4][2], ct[4][3]}, f32x2{ar, ai});
            pr = pu[0]; pi = pu[1];
            cmac(pr, pi, ct[4][0], ct[4][1], br, bi);
            pr = sum4(pr); pi = sum4(pi);
        }
        TS(5);
        *LDSP(u32x4, pes) = u32x4{__float_as_uint(pr), __float_as_uint(pi), 1u, 0u};
        CR_LOAD(CR1 + CR2, CR1 + CR2 + CR3);
        u32x4 o;
        asm volatile("1:\n\tds_read_b128 %0, %1\n\ts_waitcnt lgkmcnt(0)\n\tv_cmp_eq_u32 vcc, 0, v122\n\ts_cbranch_vccnz 1b" : "=&{v[120:123]}"(o) : "v"(po) : "vcc", "memory");
        G = f32x2{pr + __uint_as_float(o[0]), pi + __uint_as_float(o[1])};
    }
    TS(6);
    {
        if (PLN & 8) c2_mac(hr, hi, G[0], -G[1], -G[0], -G[1], Q2r, Q2i); else pk_conj_mac(hr, hi, G, Q2r, Q2i);
#pragma unroll
        for (int p = 0; p < 8; ++p) { if (PLN & 8) c2_mac(ur[p], ui[p], L1[0], L1[1], L1[0], nL1i, hr, hi); else pk_cmac_conj(ur[p], ui[p], L1, hr, hi); hr = ur[p]; hi = ui[p];
            if (CVE && p == 8 - CVE) { TIE(hr); TIE(hi); sy_post<6>(pmine); } }
        const char* hd = ws + WS_H + (size_t)((tm * 4 + (wm & 2)) * 2 + wn) * HP + (tn * 4 + (wm & 1) * 2) * 1024;
#pragma unroll
        for (int e2 = 0; e2 < 2; ++e2)
#pragma unroll
            for (int c = 0; c < 2; ++c) {
                const f32x2* o = c ? ui : ur;
                *LDSP(u32x4, hw[e2] + c * 512) = u32x4{pack2(o[0][e2], o[1][e2]), pack2(o[2][e2], o[3][e2]), pack2(o[4][e2], o[5][e2]), pack2(o[6][e2], o[7][e2])};
            }
        v4i16 t[4][2];
#pragma unroll
        for (int n = 0; n < 4; ++n)
#pragma unroll
            for (int j = 0; j < 2; ++j) t[n][j] = __builtin_amdgcn_ds_read_tr16_b64_v4i16(LDSP(v4i16, ta[j] + n * 1024));
        sy_post<1>(pmine);
        static_for<0, 4>([&](auto ic) {
            constexpr int n = decltype(ic)::value;
            const bf16x8 f = __builtin_shufflevector(t[n][0], t[n][1], 0, 1, 2, 3, 4, 5, 6, 7);
            GST_B128_SC1(l16, f, hd + (n >> 1) * 2 * HP + ((n & 1) ^ wn) * 1024, 0);
        });
    }
    CR_LOAD(CR1 + CR2 + CR3, CR1 + CR2 + CR3 + CR5);
    TS(7);
    bf16x8 fa2[64];
    u32x2 xv[4], dsv;
    const char* Aw = ws + WS_H + (size_t)((tm * 4 + gm) * 2 + gk) * HP;
    const unsigned g2dst = lb + wid * 2048;
#define G2_ISSUE(st, i) do { if constexpr (i) dmao_<1024>(g2v[1], g2src + (SLICE(st) << 7)); else DMA(g2dst + g2_place(st), g2v[0], g2src + (SLICE(st) << 7)); } while (0)
#define G2_A(g) GLD_B128_SC1(fa2[g], l16, Aw + (SLICE((g) >> 2) << 12), ((g) & 3) * 1024)
    {
        {
            sy_wait<1>(pown);
            s16x4 fo[4][2]; bf16x8 c[8];
            static_for<0, 2>([&](auto kc) {
                constexpr int ks = decltype(kc)::value;
                static_for<0, 4>([&](auto ic) { constexpr int i = decltype(ic)::value; dsrtr_<ks * 4096>(fo[2 * ks + (i >> 1)][i & 1], to[i >> 1][i & 1]); });
                static_for<0, 4>([&](auto ic) { constexpr int i = decltype(ic)::value; DSR128(c[4 * ks + i], cad[ks][i & 1], 2 * G2STAGE + i * 2048); });
            });
            static_for<0, 8>([&](auto ic) {
                constexpr int n = decltype(ic)::value, ks = n >> 2, i = n & 3;
                WAIT_L(ks ? 3 - i : 11 - i);
                TIE(c[n]);
                if constexpr (i == 0) { TIE(fo[2 * ks][0]); TIE(fo[2 * ks][1]); TIE(fo[2 * ks + 1][0]); TIE(fo[2 * ks + 1][1]); }
#pragma unroll
                for (int j = 0; j < 2; ++j)
                    acc[i][j] = __builtin_amdgcn_mfma_f32_16x16x32_bf16(__builtin_shufflevector(fo[2 * ks + j][0], fo[2 * ks + j][1], 0, 1, 2, 3, 4, 5, 6, 7), c[n], ks ? acc[i][j] : f32x4{0, 0, 0, 0}, 0, 0, 0);
                SB();
            });
#pragma unroll
            for (int i = 0; i < 4; ++i) { TIE(acc[i][0]); TIE(acc[i][1]); }
        }
        if (CEARLY) sy_post<0>(pmine);
        static_for<0, 4>([&](auto ic) { constexpr int it = decltype(ic)::value; GLD_B64(xv[it], xo, a.x, it * 2048 - 4096); });
        GLD_B64(dsv, dso, a.dskip, 0);
        if (CEARLY) {
            sy_wait<0>(pall);
            static_for<4, 4 + CEARLY>([&](auto ic) { constexpr int n = decltype(ic)::value; G2_ISSUE(n >> 1, n & 1); });
        }
        CR_LOAD(CR1 + CR2 + CR3 + CR5, CR1 + CR2 + CR3 + CR5 + CR4);
        TS(16);
        WAIT_V(5 + CEARLY + CR4 + CR5);
        sy_post<2>(pmine);
        if (!CEARLY || wid == (NOOWN ? 1 : 0) || (NOOWN == 1 && wid == 0)) sy_wait<2, 0>(pall);
        const u32x2 tagv = {__float_as_uint(ct[2][2]), __float_as_uint(ct[2][3])};
        if (wid == (NOOWN ? 1 : 0)) asm volatile("s_mov_b64 exec, 0xffff\n\tglobal_store_dwordx2 %0, %1, %2 " SMOD "\n\ts_mov_b64 exec, -1" :: "v"(fst), "v"(tagv), "s"(flg + FL_H) : "memory");
        if (PFIRST) asm volatile("s_cmp_lg_u32 %3, 0\n\ts_cbranch_scc1 1f\n\tglobal_load_dwordx2 %0, %1, %2 sc1\n1:" : "=&v"(fl) : "v"(fld), "s"(flg + FL_H), "s"(wid) : "scc", "memory");
        TS(8);
        static_for<4 + CEARLY, 10>([&](auto ic) { constexpr int n = decltype(ic)::value; G2_ISSUE(n >> 1, n & 1); });
        CR_LOAD(2 * NR - CRL, 2 * NR);
        TS(13);
        if (wid == 0) {
            unsigned left = SPIN_CAP;
            if (PFIRST)
            asm volatile("s_waitcnt vmcnt(6)\n\ts_branch 3f\n1:\n\tglobal_load_dwordx2 %0, %2, %3 sc1\n\ts_waitcnt vmcnt(0)\n3:\n\tv_cmp_ne_u64 vcc, %0, %4\n\ts_cbranch_vccz 2f\n\ts_sub_u32 %1, %1, 1\n\ts_cbranch_scc0 1b\n2:"
                         : "+v"(fl), "+s"(left) : "v"(fld), "s"(flg + FL_H), "v"(tagv) : "vcc", "scc", "memory");
            else
            asm volatile(FX_ON "1:\n\tglobal_load_dwordx2 %0, %2, %3 " FMOD "\n\ts_waitcnt vmcnt(0)\n\tv_cmp_ne_u64 vcc, %0, %4\n\ts_cbranch_vccz 2f\n\ts_sub_u32 %1, %1, 1\n\ts_cbranch_scc0 1b\n2:" FX_OFF
                         : "=&v"(fl), "+s"(left) : "v"(fld), "s"(flg + FL_H), "v"(tagv) : "vcc", "scc", "memory");
            sy_post<3>(pmine);
            if (b == 255 && lane == 0) GST_B64_SC1(0u * lane, tagv, flg + FL_EPOCH);
        } else if (g2_bar(3)) sy_wait<3>(pzero);
        else { WAIT_V(CRL); sy_post<3>(pmine); }
        if (!g2_bar(3)) sy_wait<3>(pall);
        TS(20);
        static_for<4, 4 + AW>([&](auto ic) { constexpr int g = decltype(ic)::value; G2_A(g); });
    }
    TS(9);
    static_assert(EP || CR1 + CR2 == 0, "count");
    __builtin_amdgcn_s_setprio(G2PRIO);
    unsigned yw, yr, yp, yo, lzw;
    static_assert(PAIR, "yr");
    unsigned dm[4];
    auto dmask = [&]() { TIE(dsv); dm[0] = (RX & 8) ? dsv[0] << 16 : dsv[0] & 0xffffu; dm[1] = dsv[0] & 0xffff0000u; dm[2] = (RX & 8) ? dsv[1] << 16 : dsv[1] & 0xffffu; dm[3] = dsv[1] & 0xffff0000u; TIE(dm[0]); TIE(dm[1]); TIE(dm[2]); TIE(dm[3]); };
    auto skip = [&](int it) { TIE(xv[it]);
        if (RX & 8) {
            const float x4[4] = {bflo(xv[it][0]), bfhi(xv[it][0]), bflo(xv[it][1]), bfhi(xv[it][1])};
            for (int i = 0; i < 4; ++i) { float t = acc[i][0][it]; asm("v_fmac_f32 %0, %1, %2" : "+v"(t) : "v"(x4[i]), "v"(dm[i])); acc[i][0][it] = t; }
            if (it & 1) { for (int i = 0; i < 4; ++i) TIE(acc[i][0]); }
            return;
        }
        const f32x4 y = mul4bf(xv[it], dm); for (int i = 0; i < 4; ++i) acc[i][0][it] = add1(acc[i][0][it], y[i]); if (it & 1) { for (int i = 0; i < 4; ++i) TIE(acc[i][0]); } };
    {
        bf16x8 fb[2][4];
#define G2_FRAG(g, i) do { constexpr int pl_ = g2_place((g) >> 1), b_ = pl_ >= L_C ? L_C : pl_ >= 32768 ? 32768 : 0; DSR128(fb[(g) & 1][i], cad[(g) & 1][(i) & 1] + (b_ - L_C), pl_ - b_ + (i) * 2048); } while (0)
        static_for<0, 4>([&](auto ic) { constexpr int i = decltype(ic)::value; G2_FRAG(2, i); });
        if (YS == 0) { WAIT_V(AW + 6 + CRL + CR4); dmask(); skip(0); skip(1); skip(2); skip(3); }
        static_for<2, 32>([&](auto ic) {
            constexpr int g = decltype(ic)::value, gn = g + 1;
            constexpr bool bar = g2_bar(g) && !((VBM >> g) & 1);
            constexpr int n0 = g2_upto(g - 1), n1 = g2_upto(g);
            if constexpr (bar) {
                constexpr int ca = g2_cnt(2 * g, 2 * g), cs = g2_cover(g) >= 7 + NR && !(LZ && g == LASTB) ? g2_cnt(100 + 2 * g2_cover(g) + 1, 2 * g) : 63;
                static_assert(cs >= 0 && g2_wr(g - 1) >= 2 * (g2_cover(g) < 6 + NR ? g2_cover(g) - 6 : NR), "schedule");
                WAIT_VB(AWB && ca < cs ? ca : cs); WAIT_LB(0); BAR();
                if constexpr (!AWB) WAIT_V(ca);
#pragma unroll
                for (int i = 0; i < 4; ++i) if (!(AMF & 4)) TIE(fb[g & 1][i]);
                if (!(AMF & 4)) TIE(fa2[2 * g]);
            }
            if constexpr (LZ && g == lz_chk()) { WAIT_LB(0); sy_chk<0>(lzw, pall); }
            constexpr int xl = g2_wr(g - 1) - g2_wr(g - 2) + (g - 1 == EGX) + (LZ && g - 1 == lz_post());
            static_for<0, 8>([&](auto mc) {
                constexpr int m = decltype(mc)::value, i = m & 3, j = m >> 2, f = 2 * g + j;
                static_assert(g2_cnt(f, f) >= 0, "schedule");
                constexpr int jj = LSW && g == 31 ? 1 - j : j, ff = 2 * g + jj;
                if constexpr (i == 0 && (j || !bar)) { WAIT_VB(LSW && g == 31 ? 0 : g2_cnt(f, f)); if constexpr (!(AMF & 4) || g == 2 || g == 31) TIE(fa2[ff]); }
                if constexpr (W2 && g > 2 && g < 31) { if constexpr (!bar && j == 0 && (i & W2) == 0) WAIT_LB(3 - W2 + xl); } else
                if constexpr (!bar && j == 0) { WAIT_LB((gn < 32 ? 3 : 3 - i) + xl); if constexpr (!(AMF & 4) || g == 2 || g == 31) TIE(fb[g & 1][i]); }
                constexpr int is = (SNK & 2) && j ? 3 - i : i;
                if constexpr ((AMF & 4) && g > 2 && g < 31) mfma_(acc[is][jj], fa2[ff], fb[g & 1][is]); else
                acc[i][jj] = __builtin_amdgcn_mfma_f32_16x16x32_bf16(fa2[ff], fb[g & 1][i], acc[i][jj], 0, 0, 0);
                SB();
                if constexpr (LSW && g == 31 && j) *LDSP(f32x4, yw + i * 1024) = acc[i][1];
                if constexpr (g == EGX && m == 4) sy_post<5>(pmine);
                if constexpr (LZ && g == lz_post() && m == 4) sy_post<0>(pmine);
                if constexpr (LZ && g + 1 == lz_chk() && m == 4) sy_pre<0>(lzw, pall);
                if constexpr (g == 29 && m == 5) { yw = cwa + L_Y + wid * 2048; yr = yw ^ (PAIR ? 4096 : 16384); yp = pmine ^ (PAIR ? 4 : 16); yo = (cwa & 255) + ((cwa & 768) << 7); TIE(yw); TIE(yr); TIE(yp); TIE(yo); }
                if constexpr (gn < 32 && m < 4) G2_FRAG(gn, m);
                if constexpr (m >= 4 && g2_wr(g - 1) + m - 4 < g2_wr(g)) {
                    constexpr int n = g2_wr(g - 1) + m - 4, pl = g2_place(7 + (n >> 1)), pb = pl >= L_C ? L_C : 0;
                    asm volatile("ds_write_b128 %0, %1 offset:%2" :: "v"(cwa + pb), "v"(cr[n]), "n"(pl - pb + (n & 1) * 1024) : "memory");
                }
                if constexpr (i == 1 && n0 + j < n1) G2_ISSUE((n0 + j) >> 1, (n0 + j) & 1);
                if constexpr (i >= 2 && g2_ald(f, i & 1) >= 0) G2_A(g2_ald(f, i & 1));
                SB();
            });
        });
#undef G2_FRAG
    }
    TS(11);
    if (EPRIO) __builtin_amdgcn_s_setprio(EPRIO);
    {
#pragma unroll
        for (int i = 0; i < 4; ++i) if (!LSW) *LDSP(f32x4, yw + i * 1024) = acc[i][1];
        sy_post<4>(pmine);
        sy_wait<4, 0>(yp);
        char* const ob = a.out + (size_t)(tm * 128 + gm * 32 + gk * 16) * 8192 + 4096 + tn * 256;
        f32x4 w[4];
#pragma unroll
        for (int i = 0; i < 4; ++i) w[i] = *LDSP(f32x4, yr + i * 1024);
        f32x4 o[4];
#pragma unroll
        for (int r = 0; r < 4; ++r) { o[r] = f32x4{add1(w[0][r], acc[0][0][r]), add1(w[1][r], acc[1][0][r]), add1(w[2][r], acc[2][0][r]), add1(w[3][r], acc[3][0][r])}; if (EGX) TIE(o[r]); }
        if (EGX && !wn) sy_wait<5, 1>(pall);
#pragma unroll
        for (int r = 0; r < 4; ++r) GST_B128_NT(yo, o[r], ob + r * 8192, 0);
    }
    asm volatile(".Lfk_code_b:");
    TSE(12);
    TS_FLUSH();
}

}

extern "C" void kernel_launch(void* const* d_in, const int* in_sizes, int n_in, void* d_out, int out_size, void* d_ws, size_t ws_size,
                              hipStream_t stream) {
    (void)in_sizes; (void)n_in; (void)out_size; (void)ws_size;
    fk::Args a;
    a.q = (const char*)d_in[0]; a.k = (const char*)d_in[1]; a.v = (const char*)d_in[2]; a.sk = (const char*)d_in[3];
    a.ssmk = (const char*)d_in[4]; a.ssmD = (const char*)d_in[5]; a.x = (const char*)d_in[6]; a.lre = (const char*)d_in[7];
    a.lim = (const char*)d_in[8]; a.bre = (const char*)d_in[9]; a.bim = (const char*)d_in[10]; a.cre = (const char*)d_in[11];
    a.cim = (const char*)d_in[12]; a.logdt = (const char*)d_in[13]; a.dskip = (const char*)d_in[14];
    a.out = (char*)d_out; a.ws = (char*)d_ws;
    hipLaunchKernelGGL(fk::fused, dim3(256), dim3(768), 0, stream, a);
}
```

```cpp
#define X X
#include <hip/hip_runtime.h>
#include <stdint.h>
#include <type_traits>

#define ZM 0
#define RX 3
#define P19M 7
#define QSL 2
#define ZP_ 15
#define FP_ 16
#define P14 3
#define P15 3
#define P16 3
#define S16 0
#define OPRIO 3
#define OS0 0
#define OSW 0
#define ZE -1
#define EM 2
#define EM_ON "s_mov_b64 exec, vcc\n\t"
#define EM_OFF "\ts_mov_b64 exec, -1\n"
#define EMD 0
#define EMOD "sc1"
#define TCH 0
#define TCB 10
#define SQ 1
#define SN 2
#define SNK 0
namespace fk {

constexpr int S = 2048, D = 1024, P = 1024;

typedef __attribute__((ext_vector_type(8))) short bf16x8;
typedef __attribute__((ext_vector_type(4))) short s16x4;
typedef __attribute__((ext_vector_type(16))) float f32x16;
typedef __attribute__((ext_vector_type(4))) float f32x4;
typedef __attribute__((ext_vector_type(2))) float f32x2;
typedef __attribute__((ext_vector_type(4))) unsigned u32x4;
typedef __attribute__((ext_vector_type(2))) unsigned u32x2;
#define LDSP(T, a) ((__attribute__((address_space(3))) T*)(uintptr_t)(unsigned)(a))

__device__ __forceinline__ float bf2f(unsigned v) { return __uint_as_float(v << 16); }
__device__ __forceinline__ float bflo(unsigned w) { return __uint_as_float(w << 16); }
__device__ __forceinline__ float bfhi(unsigned w) { return __uint_as_float(w & 0xffff0000u); }
__device__ __forceinline__ uint16_t f2bf(float f) { __bf16 b = (__bf16)f; return __builtin_bit_cast(uint16_t, b); }
__device__ __forceinline__ float rbf(float f) { return bf2f(f2bf(f)); }
typedef __attribute__((ext_vector_type(2))) __bf16 bf16x2;
__device__ __forceinline__ unsigned pack2(float lo, float hi) { return __builtin_bit_cast(unsigned, __builtin_convertvector(f32x2{lo, hi}, bf16x2)); }
__device__ __forceinline__ float rb1(float x) { return __uint_as_float(pack2(0.f, x)); }
__device__ __forceinline__ void cmac(float& xr, float& xi, float ar, float ai, float hr, float hi) {
    asm("v_fmac_f32 %0, %2, %4\n\tv_fmac_f32 %1, %2, %5\n\tv_fma_f32 %0, -%3, %5, %0\n\tv_fmac_f32 %1, %3, %4" : "+v"(xr), "+v"(xi) : "v"(ar), "v"(ai), "v"(hr), "v"(hi));
}
__device__ __forceinline__ void cmad(float& o_r, float& o_i, float ar, float ai, float hr, float hi, float er, float ei) {
    asm("v_fma_f32 %0, %2, %4, %6\n\tv_fma_f32 %1, %2, %5, %7\n\tv_fma_f32 %0, -%3, %5, %0\n\tv_fmac_f32 %1, %3, %4" : "=&v"(o_r), "=&v"(o_i) : "v"(ar), "v"(ai), "v"(hr), "v"(hi), "v"(er), "v"(ei));
}
__device__ __forceinline__ f32x2 cmul4(f32x2 a, f32x2 h) {
    float o_r, o_i;
    asm("v_mul_f32 %0, %2, %4\n\tv_mul_f32 %1, %2, %5\n\tv_fma_f32 %0, -%3, %5, %0\n\tv_fmac_f32 %1, %3, %4" : "=&v"(o_r), "=&v"(o_i) : "v"(a[0]), "v"(a[1]), "v"(h[0]), "v"(h[1]));
    return f32x2{o_r, o_i};
}

#define PK_LO " op_sel:[0,0,0] op_sel_hi:[0,1,1]"
#define PK_HI " op_sel:[1,0,0] op_sel_hi:[1,1,1]"
#define PK_NEG " neg_lo:[1,0,0] neg_hi:[1,0,0]"
__device__ __forceinline__ void pk_cmad(f32x2& o_r, f32x2& o_i, f32x2 a, f32x2 hr, f32x2 hi, f32x2 er, f32x2 ei) {
    asm("v_pk_fma_f32 %0, %2, %3, %5" PK_LO "\n\tv_pk_fma_f32 %1, %2, %4, %6" PK_LO "\n\tv_pk_fma_f32 %0, %2, %4, %0" PK_HI PK_NEG "\n\tv_pk_fma_f32 %1, %2, %3, %1" PK_HI
        : "=&v"(o_r), "=&v"(o_i) : "v"(a), "v"(hr), "v"(hi), "v"(er), "v"(ei));
}
__device__ __forceinline__ void pk_cmac_conj(f32x2& xr, f32x2& xi, f32x2 a, f32x2 hr, f32x2 hi) {
    asm("v_pk_fma_f32 %0, %2, %3, %0" PK_LO "\n\tv_pk_fma_f32 %1, %2, %4, %1" PK_LO "\n\tv_pk_fma_f32 %0, %2, %4, %0" PK_HI "\n\tv_pk_fma_f32 %1, %2, %3, %1" PK_HI PK_NEG
        : "+v"(xr), "+v"(xi) : "v"(a), "v"(hr), "v"(hi));
}
__device__ __forceinline__ void pk_conj_mul(f32x2& o_r, f32x2& o_i, f32x2 a, f32x2 hr, f32x2 hi) {
    asm("v_pk_mul_f32 %0, %2, %3 op_sel:[0,0] op_sel_hi:[0,1]\n\tv_pk_mul_f32 %1, %2, %4 op_sel:[0,0] op_sel_hi:[0,1] neg_lo:[1,0] neg_hi:[1,0]\n\t"
        "v_pk_fma_f32 %0, %2, %4, %0" PK_HI PK_NEG "\n\tv_pk_fma_f32 %1, %2, %3, %1" PK_HI PK_NEG : "=&v"(o_r), "=&v"(o_i) : "v"(a), "v"(hr), "v"(hi));
}
__device__ __forceinline__ void pk_conj_mac(f32x2& xr, f32x2& xi, f32x2 a, f32x2 hr, f32x2 hi) {
    asm("v_pk_fma_f32 %0, %2, %3, %0" PK_LO "\n\tv_pk_fma_f32 %1, %2, %4, %1" PK_LO PK_NEG "\n\tv_pk_fma_f32 %0, %2, %4, %0" PK_HI PK_NEG "\n\tv_pk_fma_f32 %1, %2, %3, %1" PK_HI PK_NEG
        : "+v"(xr), "+v"(xi) : "v"(a), "v"(hr), "v"(hi));
}
__device__ __forceinline__ void cmac2(f32x2& x, f32x2 a, f32x2 h) {
    asm("v_pk_fma_f32 %0, %1, %2, %0" PK_LO "\n\tv_pk_fma_f32 %0, %1, %2, %0 op_sel:[1,1,0] op_sel_hi:[1,0,1] neg_lo:[1,0,0]" : "+v"(x) : "v"(a), "v"(h));
}
#define PLN 0
__device__ __forceinline__ void c2_mac(f32x2& xr, f32x2& xi, float p, float q, float r, float s, f32x2 hr, f32x2 hi) {
    float a = xr[0], b = xi[0], c = xr[1], d = xi[1];
    asm("v_fmac_f32 %0, %4, %8\n\tv_fmac_f32 %1, %6, %9\n\tv_fmac_f32 %2, %4, %10\n\tv_fmac_f32 %3, %6, %11\n\t"
        "v_fmac_f32 %0, %5, %9\n\tv_fmac_f32 %1, %7, %8\n\tv_fmac_f32 %2, %5, %11\n\tv_fmac_f32 %3, %7, %10"
        : "+v"(a), "+v"(b), "+v"(c), "+v"(d) : "v"(p), "v"(q), "v"(r), "v"(s), "v"(hr[0]), "v"(hi[0]), "v"(hr[1]), "v"(hi[1]));
    xr = f32x2{a, c}; xi = f32x2{b, d};
}
__device__ __forceinline__ void c2_mad(f32x2& xr, f32x2& xi, float p, float q, float r, float s, f32x2 hr, f32x2 hi, f32x2 er, f32x2 ei) {
    float a, b, c, d;
    asm("v_fma_f32 %0, %4, %8, %12\n\tv_fma_f32 %1, %6, %9, %13\n\tv_fma_f32 %2, %4, %10, %14\n\tv_fma_f32 %3, %6, %11, %15\n\t"
        "v_fmac_f32 %0, %5, %9\n\tv_fmac_f32 %1, %7, %8\n\tv_fmac_f32 %2, %5, %11\n\tv_fmac_f32 %3, %7, %10"
        : "=&v"(a), "=&v"(b), "=&v"(c), "=&v"(d) : "v"(p), "v"(q), "v"(r), "v"(s), "v"(hr[0]), "v"(hi[0]), "v"(hr[1]), "v"(hi[1]), "v"(er[0]), "v"(ei[0]), "v"(er[1]), "v"(ei[1]));
    xr = f32x2{a, c}; xi = f32x2{b, d};
}
__device__ __forceinline__ void c2_mul(f32x2& xr, f32x2& xi, float p, float q, float r, float s, f32x2 hr, f32x2 hi) {
    float a, b, c, d;
    asm("v_mul_f32 %0, %4, %8\n\tv_mul_f32 %1, %6, %9\n\tv_mul_f32 %2, %4, %10\n\tv_mul_f32 %3, %6, %11\n\t"
        "v_fmac_f32 %0, %5, %9\n\tv_fmac_f32 %1, %7, %8\n\tv_fmac_f32 %2, %5, %11\n\tv_fmac_f32 %3, %7, %10"
        : "=&v"(a), "=&v"(b), "=&v"(c), "=&v"(d) : "v"(p), "v"(q), "v"(r), "v"(s), "v"(hr[0]), "v"(hi[0]), "v"(hr[1]), "v"(hi[1]));
    xr = f32x2{a, c}; xi = f32x2{b, d};
}
__device__ __forceinline__ float mul1(float a, float b) { float c; asm("v_mul_f32 %0, %1, %2" : "=v"(c) : "v"(a), "v"(b)); return c; }
__device__ __forceinline__ float add1(float a, float b) { float c; asm("v_add_f32 %0, %1, %2" : "=v"(c) : "v"(a), "v"(b)); return c; }
__device__ __forceinline__ void mfma_(f32x4& c, bf16x8 a, bf16x8 b) { asm volatile("v_mfma_f32_16x16x32_bf16 %0, %1, %2, %0" : "+v"(c) : "v"(a), "v"(b)); }
__device__ __forceinline__ float sum4(float x) {
    const auto p = __builtin_amdgcn_permlane32_swap(__float_as_uint(x), __float_as_uint(x), false, false);
    x = __uint_as_float(p[0]) + __uint_as_float(p[1]);
    const auto q = __builtin_amdgcn_permlane16_swap(__float_as_uint(x), __float_as_uint(x), false, false);
    return __uint_as_float(q[0]) + __uint_as_float(q[1]);
}

template <int I, int N, class F>
__device__ __forceinline__ void static_for(F&& f) { if constexpr (I < N) { f(std::integral_constant<int, I>{}); static_for<I + 1, N>(f); } }

#define TS_DECL
#define TS0a()
#define TS0b()
#define TSE(i)
#define TS(i)
#define TS_FLUSH()

#define WAIT_V(n) asm volatile("s_waitcnt vmcnt(%0)" ::"n"((n) > 63 ? 63 : (n)) : "memory")
#define WAIT_L(n) asm volatile("s_waitcnt lgkmcnt(%0)" ::"n"((n) > 15 ? 15 : (n)) : "memory")
#define BW 1
constexpr int wenc(int v, int l) { v = v > 63 ? 63 : v; l = l > 15 ? 15 : l; return (v & 15) | 0x70 | (l << 8) | ((v >> 4) << 14); }
#define WAIT_VB(n) do { if constexpr (BW) __builtin_amdgcn_s_waitcnt(wenc(n, 15)); else WAIT_V(n); } while (0)
#define WAIT_LB(n) do { if constexpr (BW) __builtin_amdgcn_s_waitcnt(wenc(63, n)); else WAIT_L(n); } while (0)
#define BAR() asm volatile("s_barrier" ::: "memory")
#define SB() __builtin_amdgcn_sched_barrier(0)
template <class T> __device__ __forceinline__ void tie_(T& x) { asm volatile("" : "+v"(x)); }
#define TIE(x) tie_(x)
template <class T> __device__ __forceinline__ void ties_(T& x) { asm volatile("" : "+s"(x)); }
__device__ __forceinline__ unsigned take(unsigned x) { unsigned y; asm volatile("v_mov_b32 %0, %1" : "=v"(y) : "v"(x)); return y; }
#define DEF_LD(name, ins) template <int OFF, class T> __device__ __forceinline__ void name(T& dst, unsigned voff, const char* sb) { \
    asm volatile(ins : "=v"(dst) : "v"(voff), "s"(sb), "n"(OFF) : "memory"); }
DEF_LD(gld_u16_, "global_load_ushort %0, %1, %2 offset:%3")
DEF_LD(gld_b32_, "global_load_dword %0, %1, %2 offset:%3")
DEF_LD(gld_b64_, "global_load_dwordx2 %0, %1, %2 offset:%3")
DEF_LD(gld_b128_, "global_load_dwordx4 %0, %1, %2 offset:%3")
#define NTQ " nt"
#define NTK " nt"
DEF_LD(gld_b64_nt_, "global_load_dwordx2 %0, %1, %2 offset:%3" NTQ)
DEF_LD(gld_b128_nt_, "global_load_dwordx4 %0, %1, %2 offset:%3" NTQ)
DEF_LD(gld_b64_sc1_, "global_load_dwordx2 %0, %1, %2 offset:%3 sc1")
DEF_LD(gld_b128_sc1_, "global_load_dwordx4 %0, %1, %2 offset:%3 sc1")
#define DEF_ST(name, ins) template <int OFF, class T> __device__ __forceinline__ void name(unsigned voff, const T& v, const char* sb) { \
    asm volatile(ins :: "v"(voff), "v"(v), "s"(sb), "n"(OFF) : "memory"); }
DEF_ST(gst_b128_, "global_store_dwordx4 %0, %1, %2 offset:%3\n\ts_nop 1")
DEF_ST(gst_b128_nt_, "global_store_dwordx4 %0, %1, %2 offset:%3 nt\n\ts_nop 1")
DEF_ST(gst_b128_sc1_, "global_store_dwordx4 %0, %1, %2 offset:%3 sc1\n\ts_nop 1")
DEF_ST(gst_b64_sc1_, "global_store_dwordx2 %0, %1, %2 offset:%3 sc1")
#define GLD_U16(dst, voff, sb) gld_u16_<0>(dst, voff, sb)
#define GLD_B32(dst, voff, sb, off) gld_b32_<(off)>(dst, voff, sb)
#define GLD_B64(dst, voff, sb, off) gld_b64_<(off)>(dst, voff, sb)
#define GLD_B128(dst, voff, sb, off) gld_b128_<(off)>(dst, voff, sb)
#define GLD_B64_NT(dst, voff, sb, off) gld_b64_nt_<(off)>(dst, voff, sb)
#define GLD_B128_NT(dst, voff, sb, off) gld_b128_nt_<(off)>(dst, voff, sb)
#define GLD_B64_SC1(dst, voff, sb, off) gld_b64_sc1_<(off)>(dst, voff, sb)
#define GLD_B128_SC1(dst, voff, sb, off) gld_b128_sc1_<(off)>(dst, voff, sb)
#define GST_B128(voff, v, sb, off) gst_b128_<(off)>(voff, v, sb)
#define GST_B128_NT(voff, v, sb, off) gst_b128_nt_<(off)>(voff, v, sb)
#define GST_B128_SC1(voff, v, sb, off) gst_b128_sc1_<(off)>(voff, v, sb)
#define GST_B64_SC1(voff, v, sb) gst_b64_sc1_<0>(voff, v, sb)
__device__ __forceinline__ void dma_(unsigned ldsaddr, unsigned voff, const char* sb) {
    asm volatile("s_mov_b32 m0, %0\n\ts_nop 0\n\tglobal_load_lds_dwordx4 %1, %2" :: "s"(ldsaddr), "v"(voff), "s"(sb) : "memory"); }
__device__ __forceinline__ void dma_nt_(unsigned ldsaddr, unsigned voff, const char* sb) {
    asm volatile("s_mov_b32 m0, %0\n\ts_nop 0\n\tglobal_load_lds_dwordx4 %1, %2 nt" :: "s"(ldsaddr), "v"(voff), "s"(sb) : "memory"); }
#define DF 0
#define DFN 4
#define FL_0 ""
#define FL_1 " nt"
#define FL_2 " sc1"
#define FL_3 " sc1 nt"
#define FL_4 " sc0 sc1"
#define FL_5 " sc0 sc1 nt"
#define FL_6 " sc0 nt"
#define FLX_(x) FL_##x
#define FLX(x) FLX_(x)
__device__ __forceinline__ void dmaf_(unsigned ldsaddr, unsigned voff, const char* sb) {
    asm volatile("s_mov_b32 m0, %0\n\ts_nop 0\n\tglobal_load_lds_dwordx4 %1, %2" FLX(DF) :: "s"(ldsaddr), "v"(voff), "s"(sb) : "memory"); }
template <int OFF> __device__ __forceinline__ void dmaof_(unsigned voff, const char* sb) {
    asm volatile("global_load_lds_dwordx4 %0, %1 offset:%2" FLX(DF) :: "v"(voff), "s"(sb), "n"(OFF) : "memory"); }
#define DMA(ldsaddr, voff, sb) dma_(ldsaddr, voff, sb)
template <int OFF> __device__ __forceinline__ void dmao_(unsigned voff, const char* sb) {
    asm volatile("global_load_lds_dwordx4 %0, %1 offset:%2" :: "v"(voff), "s"(sb), "n"(OFF) : "memory"); }
template <int OFF, class T> __device__ __forceinline__ void dsr128_(T& dst, unsigned addr) { asm volatile("ds_read_b128 %0, %1 offset:%2" : "=v"(dst) : "v"(addr), "n"(OFF)); }
template <int OFF, class T> __device__ __forceinline__ void dsr64_(T& dst, unsigned addr) { asm volatile("ds_read_b64 %0, %1 offset:%2" : "=v"(dst) : "v"(addr), "n"(OFF)); }
template <int OFF, class T> __device__ __forceinline__ void dsr2_(T& dst, unsigned addr) { asm volatile("ds_read2_b32 %0, %1 offset0:%2 offset1:%3" : "=v"(dst) : "v"(addr), "n"(OFF), "n"(OFF + 1)); }
#define DSR128(dst, addr, off) dsr128_<(off)>(dst, addr)
#define DSR64(dst, addr, off) dsr64_<(off)>(dst, addr)

__device__ __forceinline__ void kv4(unsigned& o01, unsigned& o23, unsigned ka, unsigned kb, unsigned kc, unsigned kd, const unsigned* w, unsigned v0, unsigned v1) {
    unsigned t1, t3, p0, p1, p2, p3;
    asm(
        "v_dot2_f32_bf16 %0, %8, %19, 0\n\tv_dot2_f32_bf16 %1, %9, %18, 0\n\tv_dot2_f32_bf16 %2, %9, %19, 0\n\tv_dot2_f32_bf16 %3, %10, %18, 0\n\t"
        "v_dot2_f32_bf16 %4, %9, %16, 0\n\tv_dot2_f32_bf16 %5, %9, %17, 0\n\tv_dot2_f32_bf16 %6, %10, %16, 0\n\tv_dot2_f32_bf16 %7, %10, %17, 0\n\t"
        "v_cvt_pk_bf16_f32 %0, %0, %4\n\tv_cvt_pk_bf16_f32 %1, %1, %5\n\tv_cvt_pk_bf16_f32 %2, %2, %6\n\tv_cvt_pk_bf16_f32 %3, %3, %7\n\t"
        "v_dot2_f32_bf16 %4, %9, %15, 0\n\tv_dot2_f32_bf16 %5, %10, %14, 0\n\tv_dot2_f32_bf16 %6, %10, %15, 0\n\tv_dot2_f32_bf16 %7, %11, %14, 0\n\t"
        "v_dot2_f32_bf16 %0, %0, %22, 0\n\tv_dot2_f32_bf16 %1, %1, %22, 0\n\tv_dot2_f32_bf16 %2, %2, %22, 0\n\tv_dot2_f32_bf16 %3, %3, %22, 0\n\t"
        "v_cvt_pk_bf16_f32 %0, %0, %4\n\tv_cvt_pk_bf16_f32 %1, %1, %5\n\tv_cvt_pk_bf16_f32 %2, %2, %6\n\tv_cvt_pk_bf16_f32 %3, %3, %7\n\t"
        "v_dot2_f32_bf16 %4, %10, %12, 0\n\tv_dot2_f32_bf16 %5, %10, %13, 0\n\tv_dot2_f32_bf16 %6, %11, %12, 0\n\tv_dot2_f32_bf16 %7, %11, %13, 0\n\t"
        "v_dot2_f32_bf16 %0, %0, %22, 0\n\tv_dot2_f32_bf16 %1, %1, %22, 0\n\tv_dot2_f32_bf16 %2, %2, %22, 0\n\tv_dot2_f32_bf16 %3, %3, %22, 0\n\t"
        "v_cvt_pk_bf16_f32 %0, %0, %4\n\tv_cvt_pk_bf16_f32 %1, %1, %5\n\tv_cvt_pk_bf16_f32 %2, %2, %6\n\tv_cvt_pk_bf16_f32 %3, %3, %7\n\t"
        "v_dot2_f32_bf16 %0, %0, %22, 0\n\tv_dot2_f32_bf16 %1, %1, %22, 0\n\tv_dot2_f32_bf16 %2, %2, %22, 0\n\tv_dot2_f32_bf16 %3, %3, %22, 0\n\t"
        "v_cvt_pk_bf16_f32 %0, %0, 0\n\tv_cvt_pk_bf16_f32 %1, 0, %1\n\tv_cvt_pk_bf16_f32 %2, %2, 0\n\tv_cvt_pk_bf16_f32 %3, 0, %3\n\t"
        "v_dot2_f32_bf16 %0, %0, %20, 0\n\tv_dot2_f32_bf16 %1, %1, %20, 0\n\tv_dot2_f32_bf16 %2, %2, %21, 0\n\tv_dot2_f32_bf16 %3, %3, %21, 0\n\t"
        "s_nop 1\n\tv_cvt_pk_bf16_f32 %0, %1, %0\n\tv_cvt_pk_bf16_f32 %2, %3, %2\n\t"
        : "=&v"(o01), "=&v"(t1), "=&v"(o23), "=&v"(t3), "=&v"(p0), "=&v"(p1), "=&v"(p2), "=&v"(p3)
        : "v"(ka), "v"(kb), "v"(kc), "v"(kd), "v"(w[0]), "v"(w[1]), "v"(w[2]), "v"(w[3]), "v"(w[4]), "v"(w[5]), "v"(w[6]), "v"(w[7]), "v"(v0), "v"(v1), "s"(0x3f803f80u));
}
__device__ __forceinline__ void kv4a(unsigned* t, unsigned* e, unsigned ka, unsigned kb, unsigned kc, unsigned kd, const unsigned* w) {
    asm volatile(
        "v_and_b32 %8, %21, %15\n\tv_lshlrev_b32 %9, 16, %16\n\tv_and_b32 %10, %21, %16\n\tv_lshlrev_b32 %11, 16, %17\n\t"
        "v_and_b32 %12, %21, %17\n\tv_mul_f32 %0, %20, %8\n\tv_mul_f32 %1, %20, %9\n\tv_mul_f32 %2, %20, %10\n\t"
        "v_mul_f32 %3, %20, %11\n\tv_mul_f32 %4, %19, %9\n\tv_mul_f32 %5, %19, %10\n\tv_mul_f32 %6, %19, %11\n\t"
        "v_mul_f32 %7, %19, %12\n\tv_lshlrev_b32 %13, 16, %18\n\tv_and_b32 %14, %21, %18\n\tv_cvt_pk_bf16_f32 %0, 0, %0\n\t"
        "v_cvt_pk_bf16_f32 %1, 0, %1\n\tv_cvt_pk_bf16_f32 %2, 0, %2\n\tv_cvt_pk_bf16_f32 %3, 0, %3\n\tv_cvt_pk_bf16_f32 %4, 0, %4\n\t"
        "v_cvt_pk_bf16_f32 %5, 0, %5\n\tv_cvt_pk_bf16_f32 %6, 0, %6\n\tv_cvt_pk_bf16_f32 %7, 0, %7\n\t"
        : "=&v"(t[0]), "=&v"(t[1]), "=&v"(t[2]), "=&v"(t[3]), "=&v"(t[4]), "=&v"(t[5]), "=&v"(t[6]), "=&v"(t[7]), "=&v"(e[0]), "=&v"(e[1]), "=&v"(e[2]), "=&v"(e[3]), "=&v"(e[4]), "=&v"(e[5]), "=&v"(e[6])
        : "v"(ka), "v"(kb), "v"(kc), "v"(kd), "v"(w[2]), "v"(w[3]), "s"(0xffff0000u));
}
__device__ __forceinline__ void kv4b(unsigned* t, const unsigned* e, const unsigned* w) {
    asm volatile(
        "v_add_f32 %0, %0, %4\n\tv_add_f32 %1, %1, %5\n\tv_add_f32 %2, %2, %6\n\tv_add_f32 %3, %3, %7\n\t"
        "v_mul_f32 %4, %14, %8\n\tv_mul_f32 %5, %14, %9\n\tv_mul_f32 %6, %14, %10\n\tv_mul_f32 %7, %14, %11\n\t"
        "v_cvt_pk_bf16_f32 %0, 0, %0\n\tv_cvt_pk_bf16_f32 %1, 0, %1\n\tv_cvt_pk_bf16_f32 %2, 0, %2\n\tv_cvt_pk_bf16_f32 %3, 0, %3\n\t"
        "v_cvt_pk_bf16_f32 %4, 0, %4\n\tv_cvt_pk_bf16_f32 %5, 0, %5\n\tv_cvt_pk_bf16_f32 %6, 0, %6\n\tv_cvt_pk_bf16_f32 %7, 0, %7\n\t"
        "v_add_f32 %0, %0, %4\n\tv_add_f32 %1, %1, %5\n\tv_add_f32 %2, %2, %6\n\tv_add_f32 %3, %3, %7\n\t"
        "v_mul_f32 %4, %13, %9\n\tv_mul_f32 %5, %13, %10\n\tv_mul_f32 %6, %13, %11\n\tv_mul_f32 %7, %13, %12\n\t"
        : "+v"(t[0]), "+v"(t[1]), "+v"(t[2]), "+v"(t[3]), "+v"(t[4]), "+v"(t[5]), "+v"(t[6]), "+v"(t[7]) : "v"(e[2]), "v"(e[3]), "v"(e[4]), "v"(e[5]), "v"(e[6]), "v"(w[0]), "v"(w[1]));
}
__device__ __forceinline__ void kv4c(unsigned* t, unsigned v0, unsigned v1) {
    asm volatile(
        "v_cvt_pk_bf16_f32 %0, 0, %0\n\tv_cvt_pk_bf16_f32 %1, 0, %1\n\tv_cvt_pk_bf16_f32 %2, 0, %2\n\tv_cvt_pk_bf16_f32 %3, 0, %3\n\t"
        "v_cvt_pk_bf16_f32 %4, 0, %4\n\tv_cvt_pk_bf16_f32 %5, 0, %5\n\tv_cvt_pk_bf16_f32 %6, 0, %6\n\tv_cvt_pk_bf16_f32 %7, 0, %7\n\t"
        "v_add_f32 %0, %0, %4\n\tv_add_f32 %1, %1, %5\n\tv_add_f32 %2, %2, %6\n\tv_add_f32 %3, %3, %7\n\t"
        "v_lshlrev_b32 %4, 16, %8\n\tv_and_b32 %5, %10, %8\n\tv_lshlrev_b32 %6, 16, %9\n\tv_and_b32 %7, %10, %9\n\t"
        "v_cvt_pk_bf16_f32 %0, 0, %0\n\tv_cvt_pk_bf16_f32 %1, 0, %1\n\tv_cvt_pk_bf16_f32 %2, 0, %2\n\tv_cvt_pk_bf16_f32 %3, 0, %3\n\t"
        "v_mul_f32 %0, %0, %4\n\tv_mul_f32 %1, %1, %5\n\tv_mul_f32 %2, %2, %6\n\tv_mul_f32 %3, %3, %7\n\t"
        "v_cvt_pk_bf16_f32 %0, %1, %0\n\tv_cvt_pk_bf16_f32 %2, %3, %2\n\t"
        : "+v"(t[0]), "+v"(t[1]), "+v"(t[2]), "+v"(t[3]), "+v"(t[4]), "+v"(t[5]), "+v"(t[6]), "+v"(t[7]) : "v"(v0), "v"(v1), "s"(0xffff0000u));
}
__device__ __forceinline__ void kf4a(unsigned* t, unsigned* e, unsigned ka, unsigned kb, unsigned kc, unsigned kd, const unsigned* w) {
    asm volatile(
        "v_and_b32 %4, %16, %11\n\tv_lshlrev_b32 %5, 16, %12\n\tv_and_b32 %6, %16, %12\n\tv_lshlrev_b32 %7, 16, %13\n\t"
        "v_and_b32 %8, %16, %13\n\tv_mul_f32 %0, %15, %4\n\tv_mul_f32 %1, %15, %5\n\tv_mul_f32 %2, %15, %6\n\t"
        "v_mul_f32 %3, %15, %7\n\tv_lshlrev_b32 %9, 16, %14\n\tv_and_b32 %10, %16, %14\n\t"
        : "=&v"(t[0]), "=&v"(t[1]), "=&v"(t[2]), "=&v"(t[3]), "=&v"(e[0]), "=&v"(e[1]), "=&v"(e[2]), "=&v"(e[3]), "=&v"(e[4]), "=&v"(e[5]), "=&v"(e[6])
        : "v"(ka), "v"(kb), "v"(kc), "v"(kd), "v"(w[3]), "s"(0xffff0000u));
}
__device__ __forceinline__ void kf4b(unsigned* t, const unsigned* e, const unsigned* w) {
    asm volatile(
        "v_fmac_f32 %0, %12, %4\n\tv_fmac_f32 %1, %12, %5\n\tv_fmac_f32 %2, %12, %6\n\tv_fmac_f32 %3, %12, %7\n\t"
        "v_fmac_f32 %0, %11, %5\n\tv_fmac_f32 %1, %11, %6\n\tv_fmac_f32 %2, %11, %7\n\tv_fmac_f32 %3, %11, %8\n\t"
        "v_fmac_f32 %0, %10, %6\n\tv_fmac_f32 %1, %10, %7\n\tv_fmac_f32 %2, %10, %8\n\tv_fmac_f32 %3, %10, %9\n\t"
        : "+v"(t[0]), "+v"(t[1]), "+v"(t[2]), "+v"(t[3]) : "v"(e[1]), "v"(e[2]), "v"(e[3]), "v"(e[4]), "v"(e[5]), "v"(e[6]), "v"(w[0]), "v"(w[1]), "v"(w[2]));
}
__device__ __forceinline__ void kf4c(unsigned* t, unsigned v0, unsigned v1) {
    asm volatile(
        "v_lshlrev_b32 %4, 16, %8\n\tv_and_b32 %5, %10, %8\n\tv_lshlrev_b32 %6, 16, %9\n\tv_and_b32 %7, %10, %9\n\t"
        "v_mul_f32 %0, %0, %4\n\tv_mul_f32 %1, %1, %5\n\tv_mul_f32 %2, %2, %6\n\tv_mul_f32 %3, %3, %7\n\t"
        "v_cvt_pk_bf16_f32 %0, %1, %0\n\tv_cvt_pk_bf16_f32 %2, %3, %2\n\t"
        : "+v"(t[0]), "+v"(t[1]), "+v"(t[2]), "+v"(t[3]), "=&v"(t[4]), "=&v"(t[5]), "=&v"(t[6]), "=&v"(t[7]) : "v"(v0), "v"(v1), "s"(0xffff0000u));
}
__device__ __forceinline__ f32x4 mul4bf(u32x2 x, const unsigned* d) {
    float a, b, c, e;
    asm("v_dot2_f32_bf16 %0, %4, %6, 0\n\tv_dot2_f32_bf16 %1, %4, %7, 0\n\tv_dot2_f32_bf16 %2, %5, %8, 0\n\tv_dot2_f32_bf16 %3, %5, %9, 0\n\t"
        "v_cvt_pk_bf16_f32 %0, 0, %0\n\tv_cvt_pk_bf16_f32 %1, 0, %1\n\tv_cvt_pk_bf16_f32 %2, 0, %2\n\tv_cvt_pk_bf16_f32 %3, 0, %3"
        : "=&v"(a), "=&v"(b), "=&v"(c), "=&v"(e) : "v"(x[0]), "v"(x[1]), "v"(d[0]), "v"(d[1]), "v"(d[2]), "v"(d[3]));
    return f32x4{a, b, c, e};
}
template <int BIT> __device__ __forceinline__ void quad_swap(u32x4& a, u32x4& b) {
    unsigned n0, n1, n2, n3, b0 = b[0], b1 = b[1], b2 = b[2], b3 = b[3];
#define QS_(d, s, k) "v_cndmask_b32_dpp " d ", " s ", " k ", vcc quad_perm:[%14,%15,%16,%17] row_mask:0xf bank_mask:0xf\n\t"
    asm("s_mov_b64 vcc, %12\n\ts_nop 0\n\t" QS_("%0", "%4", "%8") QS_("%1", "%5", "%9") QS_("%2", "%6", "%10") QS_("%3", "%7", "%11")
        "s_mov_b64 vcc, %13\n\t" QS_("%4", "%8", "%4") QS_("%5", "%9", "%5") QS_("%6", "%10", "%6") QS_("%7", "%11", "%7")
        : "=&v"(n0), "=&v"(n1), "=&v"(n2), "=&v"(n3), "+v"(b0), "+v"(b1), "+v"(b2), "+v"(b3)
        : "v"(a[0]), "v"(a[1]), "v"(a[2]), "v"(a[3]), "s"(BIT ? 0x3333333333333333ull : 0x5555555555555555ull), "s"(BIT ? 0xccccccccccccccccull : 0xaaaaaaaaaaaaaaaaull),
          "n"(BIT ? 2 : 1), "n"(BIT ? 3 : 0), "n"(BIT ? 0 : 3), "n"(BIT ? 1 : 2) : "vcc");
#undef QS_
    a = u32x4{n0, n1, n2, n3}; b = u32x4{b0, b1, b2, b3};
}
__device__ __forceinline__ void kv_dma(unsigned ldsaddr, unsigned k0, unsigned k1, unsigned k2, unsigned k3, const char* sb) {
    asm volatile("s_mov_b32 m0, %5\n\ts_nop 0\n\tglobal_load_lds_dwordx4 %0, %4" NTK "\n\tglobal_load_lds_dwordx4 %1, %4 offset:1024" NTK "\n\t"
                 "global_load_lds_dwordx4 %2, %4 offset:2048" NTK "\n\tglobal_load_lds_dwordx4 %3, %4 offset:3072" NTK
                 :: "v"(k0), "v"(k1), "v"(k2), "v"(k3), "s"(sb), "s"(ldsaddr) : "memory");
}
#define IF_(c, C) "s_cmp_lg_u32 " c ", " C "\n\ts_cbranch_scc1 1f\n\t"
template <int C, int N> __device__ __forceinline__ void wait_if(int c) { asm volatile(IF_("%0", "%1") "s_waitcnt vmcnt(%2)\n1:" :: "s"(c), "n"(C), "n"(N) : "scc", "memory"); }
template <int C, int M> __device__ __forceinline__ void flag_if(int c, unsigned voff, u32x2 v, const char* sb) {
    asm volatile(IF_("%0", "%1") "s_mov_b32 exec_lo, %5\n\ts_mov_b32 exec_hi, 0\n\tglobal_store_dwordx2 %2, %3, %4 sc1\n\ts_mov_b64 exec, -1\n1:" :: "s"(c), "n"(C), "v"(voff), "v"(v), "s"(sb), "n"(M) : "scc", "memory"); }
template <int C> __device__ __forceinline__ void st4_if(int c, unsigned voff, const u32x4* v, const char* s0, const char* s1) {
    asm volatile(IF_("%0", "%1") "global_store_dwordx4 %2, %3, %7 sc1\n\tglobal_store_dwordx4 %2, %4, %7 offset:1024 sc1\n\tglobal_store_dwordx4 %2, %5, %8 sc1\n\tglobal_store_dwordx4 %2, %6, %8 offset:1024 sc1\n\ts_nop 1\n1:"
                 :: "s"(c), "n"(C), "v"(voff), "v"(v[0]), "v"(v[1]), "v"(v[2]), "v"(v[3]), "s"(s0), "s"(s1) : "scc", "memory"); }
template <int C> __device__ __forceinline__ void ld4_if(int c, unsigned voff, u32x4* v, const char* s0) {
    asm volatile(IF_("%4", "%5") "global_load_dwordx4 %0, %6, %7 nt\n\tglobal_load_dwordx4 %1, %6, %7 offset:1024 nt\n\tglobal_load_dwordx4 %2, %6, %7 offset:2048 nt\n\tglobal_load_dwordx4 %3, %6, %7 offset:3072 nt\n1:"
                 : "+v"(v[0]), "+v"(v[1]), "+v"(v[2]), "+v"(v[3]) : "s"(c), "n"(C), "v"(voff), "s"(s0) : "scc", "memory"); }
template <int C> __device__ __forceinline__ void ld8_if(int c, unsigned voff, u32x2* v, const char* s0, const char* s1) {
    asm volatile(IF_("%8", "%9") "global_load_dwordx2 %0, %10, %11 offset:-4096 nt\n\tglobal_load_dwordx2 %1, %10, %11 offset:-2048 nt\n\tglobal_load_dwordx2 %2, %10, %11 nt\n\tglobal_load_dwordx2 %3, %10, %11 offset:2048 nt\n\t"
                 "global_load_dwordx2 %4, %10, %12 offset:-4096 nt\n\tglobal_load_dwordx2 %5, %10, %12 offset:-2048 nt\n\tglobal_load_dwordx2 %6, %10, %12 nt\n\tglobal_load_dwordx2 %7, %10, %12 offset:2048 nt\n1:"
                 : "+v"(v[0]), "+v"(v[1]), "+v"(v[2]), "+v"(v[3]), "+v"(v[4]), "+v"(v[5]), "+v"(v[6]), "+v"(v[7]) : "s"(c), "n"(C), "v"(voff), "s"(s0), "s"(s1) : "scc", "memory"); }
template <int E> __device__ __forceinline__ void sy_post(unsigned myaddr) { asm volatile("ds_write_b32 %0, %0 offset:%1" :: "v"(myaddr), "n"(E * 64) : "memory"); }
template <int E, int SL = 1> __device__ __forceinline__ unsigned sy_wait(unsigned addr) {
    unsigned v;
    asm volatile("s_branch 2f\n1:\n\ts_sleep %3\n2:\n\tds_read_b32 %0, %1 offset:%2\n\ts_waitcnt lgkmcnt(0)\n\tv_cmp_eq_u32 vcc, 0, %0\n\ts_cbranch_vccnz 1b"
                 : "=&v"(v) : "v"(addr), "n"(E * 64), "n"(SL) : "vcc", "memory");
    return v;
}
template <int E> __device__ __forceinline__ void sy_pre(unsigned& v, unsigned addr) { asm volatile("ds_read_b32 %0, %1 offset:%2" : "=v"(v) : "v"(addr), "n"(E * 64) : "memory"); }
template <int E> __device__ __forceinline__ void sy_chk(unsigned& v, unsigned addr) {
    asm volatile("s_branch 2f\n1:\n\tds_read_b32 %0, %1 offset:%2\n\ts_waitcnt lgkmcnt(0)\n2:\n\tv_cmp_eq_u32 vcc, 0, %0\n\ts_cbranch_vccnz 1b"
                 : "+v"(v) : "v"(addr), "n"(E * 64) : "vcc", "memory");
}
template <int OFF, class T> __device__ __forceinline__ void dsrtr_(T& dst, unsigned addr) { asm volatile("ds_read_b64_tr_b16 %0, %1 offset:%2" : "=v"(dst) : "v"(addr), "n"(OFF)); }

constexpr size_t WS_H = 0, WS_KVT = (size_t)10 << 20, WS_ZT = (size_t)15 << 20, WS_E = (size_t)20 << 20, WS_FLAG = WS_E + (2048 << 10);
#define HP 65536
#define KP 8192
#define ZP 4096
constexpr int FL_KV = 0, FL_Z = 65536, FL_H = 131072, FL_EPOCH = 196608;
constexpr unsigned SPIN_CAP = 1u << 17;
#define ABL 0
#define CSPLIT 1
#define MERGE 3
#define CB 13
#define PAUSE 0
#define QLATE 0
#define CVPRIO 0
#define STG_SEL (idx & 3)
#define SP0 3
#define SDP 1
#define LP0 3
#define LDP 2
#define KVS 5
#define KVU 3
#define QIT 0
#define ZB 3
#define OS 2
#define MPRIO 2

#define ROT 0x4042
#define R2 0x14
#define CPF 0
#define PRE 4
#define PRO 9
#define G1NT 0
#define G1NTS 16
#define TMIX 0
#define TLBT 0
#define CDEF 0
#define CVLATE 1
#define FPRIO 3
#define YS 0
#define DP0 1
#define DP1 5
#define AWB 1
#define PK 0
#define PU 15
#define PC0 13
#define PC1 14
#define PC2 16
#define PC3 16
#define PQ 0
#define SPF 0
#define SPH 0
#define NB1 4
#define EP 0
#define ESL 0
#define CFB 0
#define P1 1
#define P0 1
#define BKE 0
#define EALL 0
#define EREP 0
#define TOUCH 0
#define FILLB 0
#define CPOST 1
#define CEARLY 0
#define NOOWN 0
#define PFIRST 0
#define ZB0 0
#define CATCH 4
#define PWE 0
#define SWAP 1
#define KVM 0x0FE
#define KFD 3
#define ZW 2
#define ZX 1
#define ZP0 3
#define BARM 0x22220
#define NR 6
#define WPS 2
#define CD0 3
#define AWX 8
#define VBM 0
#define ZOWN 1
#define ZPS 4
#define XAB 0
#define QM 0
#define OSG 0
#define KPL 0xFE
#define KPRIO 1
#define GPRIO 1
#define PST 0x7654
#define PLT 0xB964
#define LC 0
#define PRE2 PRE
#define KVD 0
#define PAIR 1
#define G2P1 0
#define EPRIO 0
#define LSW 1
#define EGX 28
#define CR1 0
#define CR2 0
#define CR3 0
#define CRL (2 * NR - CR1 - CR2 - CR3)
#define UE 0
#define UPRIO 3
#define AMF 7
#define M1 1
#define W2 1
#define W1 0
#define NBK 3
#define XB 4
#define KFB 9
#define APRE 0
#define CB0 1
#define AP0 8
#define HTSE(i)
#define KAT 2
#define SD 2
#define KV3 0x088888
#define KOWN 1
#define HKV 0
#define KVP NBK
#define LZ 1
#define AWN 8
#define CRATE 2
#define ZSB 0
#define ZFB 1
#define ZSL 4
#define OSL 0
constexpr int it_cnt(int p, int k, int last) {
    const int pos[8] = {PU, PK, PC0, PC1, PQ, PU - 1, PC2, PC3}, n[8] = {4, 4, 4, 4, 8, 1, 4, 4};
    int c = 0;
    for (int i = 0; i < 8; ++i) if (pos[i] <= last && (pos[i] > p || (pos[i] == p && i > k))) c += n[i];
    return c;
}
constexpr int nib(int t, int c) { return (t >> (4 * c)) & 15; }
constexpr bool nib_any(int t, int v) { return nib(t, 0) == v || nib(t, 1) == v || nib(t, 2) == v || nib(t, 3) == v; }
constexpr int kv_wait(int late) {
    int t = 0;
    for (int c = 0; c < 4; ++c) t |= ((nib(PLT, c) >= nib(PST, c) && nib(PLT, c) < nib(PST, c) + KFD) == late ? nib(PST, c) + KFD : 15) << (4 * c);
    return t;
}
constexpr unsigned long long sl_seq(int i, int inv) {
    int q[16] = {};
    const int near[4] = {1, 5, 6, SN}, far[13] = {2, 3, 4, 7, 8, 9, 10, 11, 12, 13, 14, 15, 0};
    for (int k = 0; k < 4; ++k) q[near[k]] = (i + k) & 3;
    for (int k = 0, n = 0; k < 13; ++k) if (far[k] != SN) q[far[k]] = 4 + (n++ + SQ * i) % 12;
    unsigned long long t = 0;
    for (int k = 0; k < 16; ++k) t |= inv ? (unsigned long long)k << (4 * q[k]) : (unsigned long long)q[k] << (4 * k);
    return t;
}
constexpr int G1STAGE = 32768;
constexpr int g1_slot(int s) { return (NB1 == 5 ? (s + 3) % 5 : s & 3) * G1STAGE; }
constexpr int L_KS = NB1 == 5 ? 65536 : 131072, L_VS = L_KS + 16384;
constexpr int L_TS = 3 * G1STAGE, L_TR = L_TS + 3072;
constexpr int CST = 4288;
constexpr int UOFF = 2 * CST + 128, UPL = 2304;
constexpr int CHSZ = UOFF + 2 * UPL + 32;
constexpr int L_AGG = 4 * CHSZ, L_ES = L_AGG + 15 * 512, L_SY = L_ES + 2048;
constexpr int L_HT = 98304;
constexpr int L_C = 98304, G2STAGE = 16384;
constexpr int L_Y = 16384;
static_assert(L_SY + 384 <= 65536 && L_AGG >= 49152 && UOFF % 16 == 0 && CHSZ % 256 == 32, "lds");

__device__ __forceinline__ int img_off(int r, int c) { return (r >> 1) * 256 + ((((r & 1) * 8 + c) ^ ((r >> 1) & 15)) << 4); }

constexpr int g1_upto(int g) {
    int n = 4 * NBK;
    for (int t = SD; t <= g; ++t) {
        int sb = (t + 1) / 2, sub = (t + 1) % 2, allowed = sb ? 4 * (sb + 3) + 2 * (sub + 1) : 12;
        if (allowed > 64) allowed = 64;
        n = n + CATCH < allowed ? n + CATCH : allowed;
    }
    return n;
}
constexpr int g1_cnt(int s) { return HKV || s >= KVP ? g1_upto(2 * s - 2) - 4 * (s + 1) : s >= KAT ? g1_upto(2 * s - 2) - 4 * KVP : g1_upto(2 * s - 2) - 4 * (s + 1) + 4; }
static_assert(KVP >= 1 && KVP <= NBK && AP0 % 4 == 0, "schedule");
static_assert(g1_upto(28) == 64, "schedule");
constexpr int AW = AWN;
constexpr int g2_place(int s) { return s < 5 ? L_C + ((s + 2) & 3) * G2STAGE : s < 7 ? (s - 1) * G2STAGE : s < 10 ? (9 - s) * G2STAGE : g2_place(s - 9); }
constexpr bool g2_bar(int g) { return (BARM >> g) & 1; }
struct G2Plan { int upto[33], wr[33], ald[64][2], seq[164], use[64]; };
constexpr G2Plan g2_plan() {
    G2Plan p = {};
    int nc = 14 + 2 * NR, nw = 0, nx = 4, lastb = 0, n = 0;
    for (int i = 0; i < 164; ++i) p.seq[i] = 1000;
    for (; nx < 4 + AW; ++nx) p.seq[nx] = ++n;
    for (int t = 0; t < 32; ++t) {
        p.upto[t] = nc; p.wr[t] = nw;
        if (t < 2) continue;
        if (g2_bar(t)) lastb = t;
        const int w = AW + nw < AWX ? AW + nw : AWX;
        for (int f = 2 * t; f < 2 * t + 2; ++f) {
            p.use[f] = n;
            if (nc < 32 && nc == p.upto[t] + (f & 1) && (f & 1) < CRATE && t >= CD0 && lastb >= (nc < 20 ? 1 : (nc | 1) - 18)) p.seq[100 + nc++] = ++n;
            for (int k = 0; k < 2; ++k) {
                p.ald[f][k] = -1;
                if (nx < 64 && nx - f - k < w) { p.ald[f][k] = nx; p.seq[nx++] = ++n; }
            }
        }
        for (int k = 0; k < WPS; ++k) if (nw < 2 * NR && lastb >= (nw < 6 ? 1 : (nw | 1) - 4)) ++nw;
    }
    p.upto[32] = nc; p.wr[32] = nw;
    return p;
}
constexpr G2Plan G2P = g2_plan();
constexpr int g2_upto(int g) { return G2P.upto[g + 1]; }
constexpr int g2_wr(int g) { return G2P.wr[g + 1]; }
constexpr int g2_ald(int f, int k) { return G2P.ald[f][k]; }
constexpr int g2_cnt(int id, int at) { return G2P.use[at] - G2P.seq[id]; }
constexpr int LASTB = 31 - __builtin_clz(BARM);
constexpr int lz_post() { int g = LASTB; while (G2P.seq[2 * g] < G2P.seq[131]) ++g; return g; }
constexpr int lz_chk() { int s = 7 + NR; while (G2P.seq[100 + 2 * s + 1] < G2P.seq[2 * LASTB]) ++s; return 2 * s - 1; }
static_assert(!LZ || (AWB && lz_post() + 1 < lz_chk() && lz_chk() < EGX), "schedule");
constexpr int g2_cover(int g) { int s = (g + 1) / 2; while (s < 15 && !g2_bar(2 * s + 1)) ++s; return s; }
static_assert(BARM >> 17 && g2_cover(3) < 7 && CRATE <= 2, "the epilogue needs stage 8 behind a barrier");

constexpr int cv_m(int o) { return (o & 1) ? 7 - (o >> 1) : o >> 1; }
constexpr int cv_first(int o) { int p = 0; for (int i = 0; i < o; ++i) p += 8 - cv_m(i); return p; }
constexpr int cv_grp(int p) { int o = 0; while (cv_first(o + 1) <= p) ++o; return o; }
constexpr int cv_read(int p) {
    int slot[40] = {};
    for (int i = 0; i < 40; ++i) slot[i] = -1;
    slot[0] = 2 * 3 + 1;
    for (int o = 0; o < 8; ++o) {
        int q = cv_first(o + 1) - 1;
        for (int h = 0; h < 2; ++h) { while (slot[q] >= 0) ++q; slot[q] = 2 * (o + 4) + h; }
    }
    const int bs[8] = {1, 3, 5, 11, 12, 13, 19, 21};
    for (int jb = 0; jb < 8; ++jb) slot[bs[jb]] = 32 + jb;
    return slot[p];
}
static_assert(cv_read(35) == 22 && cv_read(36) == 23 && cv_read(37) == -1 && cv_read(10) == 11 && cv_read(15) == 12 && cv_read(18) == 15 && cv_read(24) == 17, "conv schedule");
constexpr int cv_wait(int o) {
    int n = 0;
    for (int q = cv_first(o) - 1;; --q) {
        const int rd = cv_read(q < 0 ? q + 36 : q);
        if (rd == 2 * (o + (q < 0 ? 8 : 0)) + 1) return n;
        if (rd >= 0) ++n;
    }
}

struct Args {
    const char *q, *k, *v, *sk, *ssmk, *ssmD, *x, *lre, *lim, *bre, *bim, *cre, *cim, *logdt, *dskip;
    char *out, *ws;
};

typedef __attribute__((ext_vector_type(8))) unsigned long long u64x8;
__global__ __launch_bounds__(768, 3) void fused(Args) {
    __shared__ __attribute__((aligned(1024))) char shm[163840];
    TS0a();
    u64x8 ka0, ka1; unsigned long long ka2;
    const char* pc0;
    int code_n;
    asm volatile("s_load_dwordx16 %0, %5, 0x0\n\ts_load_dwordx16 %1, %5, 0x40\n\ts_load_dwordx2 %2, %5, 0x80\n\t"
                 "s_getpc_b64 %3\n.Lfk_code_0:\n\ts_mov_b32 %4, .Lfk_code_a-.Lfk_code_0-4\n\ts_max_i32 %4, %4, .Lfk_code_b-.Lfk_code_0-4"
                 : "=&s"(ka0), "=&s"(ka1), "=&s"(ka2), "=&s"(pc0), "=&s"(code_n) : "s"(__builtin_amdgcn_kernarg_segment_ptr()) : "scc");
    const int tid = threadIdx.x, lane = tid & 63, wraw = __builtin_amdgcn_readfirstlane(tid >> 6);
    const int wid = !SWAP ? wraw : wraw < 4 ? wraw + 8 : wraw - 4;
    const int b = blockIdx.x, xcd = b & 7, idx = b >> 3;
    asm volatile("s_cmp_lt_u32 %0, 8\n\ts_cbranch_scc1 1f\n\ts_setprio 3\n\ts_cmp_lg_u32 %0, 8\n\ts_cbranch_scc1 1f\n\t"
                 "v_lshl_add_u32 v161, %1, 7, %2\n\tv_min_i32 v161, %3, v161\n\ts_mov_b64 exec, 0xffff\n\tglobal_load_dword v161, v161, %4\n\ts_mov_b64 exec, -1\n1:"
                 :: "s"(wid), "v"(lane), "s"(idx * 2048), "s"(code_n), "s"(pc0) : "scc", "memory", "v161");
    TS_DECL
    const int tm = (xcd >> 1) * 4 + (idx >> 3), tn = (xcd & 1) * 8 + (idx & 7);
    const int wm = wid & 3, wn = (wid >> 2) & 1, l31 = lane & 31, lh = lane >> 5;
    const unsigned lb = (unsigned)(uintptr_t)shm;
    const unsigned l16 = lane * 16;
    const int l15 = lane & 15, g4 = lane >> 4, sq = 2 * wn + (wm & 1), st = sq * 16 + l15, run = (wm >> 1) * 4 + g4;
    unsigned kvv[4];
    const int trow = idx * 64 * 2048 + xcd * 256;
    if (wid < 8) {
        const int v = l15 ^ g4, hi = v >> 3;
        const int r0 = wn ? tn * 64 + (wm >> 1) * 32 : tm * 128 + (wm >> 1) * 64 + (wm & 1) * 4;
        const unsigned d0 = (r0 + ((g4 * (wn ? 2 : 9)) & (wn ? 6 : 17))) * 2048 + (v & 7) * 16, odd = wn ? 11 : 14;
        kvv[0] = d0 + (hi << odd); kvv[2] = d0 + ((hi ^ 1) << odd) + (wn ? 32768 - 2048 : 4096 - 2048);
        kvv[1] = (kvv[0] ^ 64) + (wn ? 16384 - 1024 : 65536 - 1024); kvv[3] = (kvv[2] ^ 64) + (wn ? 16384 - 1024 : 65536 - 1024);
    } else {
        kvv[0] = trow + (idx ? -4 * 2048 : 0) + g4 * 2048 + wm * 64 + l15 * 4;
        asm volatile("v_writelane_b32 %0, 0, 0\n\tv_writelane_b32 %0, 4, 1\n\tv_writelane_b32 %0, %1, 2" : "+v"(kvv[0]) : "s"((xcd * 64 + idx * 2 + (wm >> 1)) * 4));
        kvv[1] = (xcd * 128 + wm * 32 + l31) * 8;
        kvv[2] = kvv[3] = (tn * 64 + lane) * 2;
    }
    unsigned g1dst = lb + wid * 4096;
    int rot7 = ((((ROT >> 12) & 15) * (idx & 7) + ((ROT >> 8) & 15) * (idx >> 3) + ((ROT >> 4) & 15) * (xcd >> 1) + (ROT & 15) * (xcd & 1)) & 15) << 7;
    asm volatile("s_waitcnt lgkmcnt(0)" : "+s"(ka0), "+s"(ka1), "+s"(ka2), "+s"(g1dst), "+s"(rot7), "+v"(kvv[0]), "+v"(kvv[1]), "+v"(kvv[2]), "+v"(kvv[3]));
    Args a;
    a.q = (const char*)ka0[0]; a.k = (const char*)ka0[1]; a.v = (const char*)ka0[2]; a.sk = (const char*)ka0[3]; a.ssmk = (const char*)ka0[4]; a.ssmD = (const char*)ka0[5];
    a.x = (const char*)ka0[6]; a.lre = (const char*)ka0[7]; a.lim = (const char*)ka1[0]; a.bre = (const char*)ka1[1]; a.bim = (const char*)ka1[2]; a.cre = (const char*)ka1[3];
    a.cim = (const char*)ka1[4]; a.logdt = (const char*)ka1[5]; a.dskip = (const char*)ka1[6]; a.out = (char*)ka1[7]; a.ws = (char*)ka2;
    char* const ws = a.ws;
    const char* const flg = ws + WS_FLAG;
    TS(26);
    const char* g1src = wn ? ((wm & 1) ? a.bim : a.bre) : a.x;
    asm volatile("s_mov_b32 m0, %[m]\n\ts_cmp_gt_u32 %[w], 7\n\ts_cbranch_scc1 1f\n\t"
                 "global_load_lds_dwordx4 %[k0], %[g]" FLX(DF) "\n\tglobal_load_lds_dwordx4 %[k1], %[g] offset:1024" FLX(DF) "\n\t"
                 "global_load_lds_dwordx4 %[k2], %[g] offset:2048" FLX(DF) "\n\tglobal_load_lds_dwordx4 %[k3], %[g] offset:3072" FLX(DF) "\n\t"
                 "s_branch 2f\n1:\n\tglobal_load_ushort v164, %[k2], %[c1]\n\tglobal_load_ushort v165, %[k2], %[c2]\n\tglobal_load_ushort v166, %[k2], %[c3]\n\t"
                 "v_mov_b64 v[162:163], %[kv]\n\ts_mov_b64 exec, 7\n\tv_mov_b64 v[162:163], %[d]\n\ts_mov_b64 exec, 3\n\tv_mov_b64 v[162:163], %[e]\n\ts_mov_b64 exec, -1\n\t"
                 "v_mad_u64_u32 v[162:163], vcc, %[k0], 1, v[162:163]\n\tglobal_load_dword v160, v[162:163], off sc1\n\tglobal_load_dwordx2 v[162:163], %[k1], %[sk]\n2:"
                 :
                 : [w] "s"(wid), [c1] "s"(a.logdt), [c2] "s"(a.lre), [c3] "s"(a.lim), [d] "s"(a.ssmD), [e] "s"(flg + FL_EPOCH), [sk] "s"(a.sk),
                   [k0] "v"(kvv[0]), [k1] "v"(kvv[1]), [k2] "v"(kvv[2]), [k3] "v"(kvv[3]), [kv] "s"(a.k), [g] "s"(g1src + rot7), [m] "s"(g1dst)
                 : "scc", "vcc", "memory", "v160", "v161", "v162", "v163", "v164", "v165", "v166");
    TS0b();
    TS(27);
    if (P1) BAR();
#define SLICE_TABLE() unsigned slo, shi; { int ix = idx; asm volatile("" : "+s"(ix)); \
        const unsigned long long t0 = (ix & 16) ? ((ix & 8) ? sl_seq(3, 0) : sl_seq(2, 0)) : ((ix & 8) ? sl_seq(1, 0) : sl_seq(0, 0)), iv = (ix & 16) ? ((ix & 8) ? sl_seq(3, 1) : sl_seq(2, 1)) : ((ix & 8) ? sl_seq(1, 1) : sl_seq(0, 1)); \
        const unsigned x4 = (xcd >> 1) * 0x44444444u, own = tn ^ (x4 & 15), es = (unsigned)(iv >> (4 * own)) & 15; \
        const unsigned long long df = ((unsigned)t0 & 15) ^ own, tab = t0 ^ (df << (4 * es)) ^ df; \
        slo = (unsigned)tab ^ x4; shi = (unsigned)(tab >> 32) ^ x4; asm volatile("" : "+s"(slo), "+s"(shi)); }
#define SLICE(st) ((((st) < 8 ? slo : shi) >> (4 * ((st) & 7))) & 15)
    typedef __attribute__((ext_vector_type(4))) short v4i16;
    int role = wid;
    asm volatile("" : "+s"(role));

    if (role >= 8) {
        int lane;
        asm volatile("v_mbcnt_lo_u32_b32 %0, -1, 0\n\tv_mbcnt_hi_u32_b32 %0, -1, %0" : "=v"(lane));
        __builtin_assume(lane >= 0 && lane < 64);
        const int l31 = lane & 31, lh = lane >> 5, l15 = lane & 15, g4 = lane >> 4;
        const unsigned l16 = lane * 16;
        const int chn = xcd * 128 + idx * 4 + wm;
        const unsigned cb = lb + wm * CHSZ;
        const int r = l15;
        const unsigned fpub = KOWN ? (xcd * 32 + l31) * 256 + idx * 8 : xcd * 256 + idx * 8, fpoll = KOWN ? (xcd * 32 + idx) * 256 + l31 * 8 : xcd * 256 + l31 * 8;
        u32x2 fl; u32x4 sw[4], uu[4];
        unsigned tch = 0, tch2 = 0;
        if (TOUCH) {
            const int ln = wm * 4 + (lane & 3);
            const unsigned fo = (unsigned)(WS_FLAG - WS_E) + (wm == 0 ? FL_H + (tm * 16 + tn) * 128 : (wm == 1 ? FL_KV : FL_Z) + xcd * 256 + (lane & 1) * 128);
            const unsigned o = (lane & 4) && (TOUCH & 2) ? fo : ((tm * 2 + (ln >> 3)) * 1024 + tn * 64) * 16 + (ln & 7) * 128;
            asm volatile("s_mov_b64 exec, 0xff\n\tglobal_load_dword %0, %1, %2\n\ts_mov_b64 exec, -1" : "=&v"(tch) : "v"(o), "s"(ws + WS_E) : "memory");
        }
        SLICE_TABLE();
        TS(1);
        unsigned sm, cl[3]; u32x2 kw0;
        asm volatile("s_waitcnt vmcnt(0)" : "={v160}"(sm), "={v[162:163]}"(kw0), "={v164}"(cl[0]), "={v165}"(cl[1]), "={v166}"(cl[2]) :: "memory", "v161");
        TS(22);
        const unsigned hal = take(sm);
        const unsigned tagA = __builtin_amdgcn_readlane(hal, 0) + 1, tagB = __builtin_amdgcn_readlane(hal, 1) + (tagA == 0 ? 1u : 0u);
        const u32x2 tag = {tagA, tagB};
        f32x2 L1, C1;
        {
            const float dt = rbf(expf(bf2f(take(cl[0]))));
            const float lr = bf2f(take(cl[1])), li = bf2f(take(cl[2]));
            const float e = expf(dt * lr);
            float sn, cs;
            sincosf(dt * li, &sn, &cs);
            L1 = f32x2{e * cs, e * sn};
            const float nr = L1[0] - 1.f, ni = L1[1], den = lr * lr + li * li;
            C1 = f32x2{2.f * ((nr * lr + ni * li) / den), 2.f * ((ni * lr - nr * li) / den)};
            TIE(L1); TIE(C1);
        }

        f32x2 p[11];
        p[0] = L1;
        if (PWE) {
#pragma unroll
            for (int i = 1; i <= 10; ++i) { p[i] = cmul4(p[i - 1], p[i - 1]); TIE(p[i]); }
        }
        {
            if (!PWE) {
#pragma unroll
                for (int i = 1; i <= 10; ++i) p[i] = cmul4(p[i - 1], p[i - 1]);
            }
            const f32x2 one = {1.f, 0.f}, zero = {0.f, 0.f};
            const unsigned ts = lb + L_TS + l16, tr = lb + L_TR + wm * 2048 + l16;
            if (wm == 0) {
                *LDSP(f32x4, ts) = f32x4{L1[0], L1[1], C1[0], C1[1]};
                *LDSP(f32x4, ts + 1024) = f32x4{p[3][0], p[3][1], p[4][0], p[4][1]};
                *LDSP(f32x4, ts + 2048) = f32x4{p[6][0], p[6][1], __uint_as_float(tagA), __uint_as_float(tagB)};
            }
            f32x2 Q[2], V[2], W[2];
            Q[0] = cmul4(cmul4(C1, (wm & 1) ? p[5] : one), (wm & 2) ? p[6] : one); Q[1] = cmul4(Q[0], p[4]);
            V[1] = (wm & 1) ? one : p[5]; V[0] = cmul4(V[1], p[4]);
            W[1] = cmul4((wm & 1) ? one : p[9], (wm & 2) ? one : p[10]); W[0] = cmul4(W[1], p[8]);
#pragma unroll
            for (int i = 0; i < 2; ++i) {
                const f32x2 Q8 = cmul4(Q[i], p[3]), U = 4 * wm + 2 * i >= 16 - tm ? cmul4(W[i], p[7]) : zero, Wz = 4 * wm + 2 * i + 1 >= 16 - tm ? W[i] : zero;
                *LDSP(f32x4, tr + i * 1024) = f32x4{Q[i][0], Q8[0], Q[i][1], Q8[1]};
                *LDSP(f32x4, tr + i * 1024 + 8192) = f32x4{Wz[0], Wz[1], U[0], U[1]};
                *LDSP(f32x2, tr + i * 1024 + 16384) = V[i];
            }
        }
        auto b0 = [&]() {
            unsigned k0 = trow + wm * 32768 + g4 * 2048 + ((l15 ^ (2 * g4)) << 4), k1 = k0 + 7168, k2 = (k0 ^ 128) + 14336, k3 = k2 + 7168;
            if (HKV) { TIE(k0); TIE(k1); TIE(k2); TIE(k3); }
            if (HKV == 1) { kv_dma(lb + L_KS + wm * 4096, k0, k1, k2, k3, a.k); kv_dma(lb + L_VS + wm * 4096, k0, k1, k2, k3, a.v); }
            WAIT_L(0); BAR(); HTSE(1);
            if (HKV == 2) { kv_dma(lb + L_KS + wm * 4096, k0, k1, k2, k3, a.k); kv_dma(lb + L_VS + wm * 4096, k0, k1, k2, k3, a.v); }
        };
        if (CB0 == 0) b0();
        const unsigned dskl = (__builtin_amdgcn_readlane(hal, 2) >> ((wm & 1) * 16)) & 0xffffu;
        u32x4 kvo[4];
        u32x2 qv[8];
        const char* krow = a.ssmk + (size_t)chn * 4096;
        const char* qs = a.q + (size_t)(idx * 64 + wm * 16) * 2048 + xcd * 256;
        const char* kvd = ws + WS_KVT + (size_t)(xcd * 8 + wm * 2) * 65536 + idx * 2048;
        const int pl = (PLT >> ((STG_SEL) * 4)) & 15;
        const unsigned qo = lh * 8192 + l31 * 8 + 4096;
        auto hb = [&](auto nc) {
            constexpr int nb = decltype(nc)::value;
            if constexpr (nb == KAT && HKV) WAIT_V(0);
            if constexpr (nb == KFB) { WAIT_V((PK ? 0 : 4) + (PQ ? 0 : 8)); int lm = (1 << KFB) - (2 << (KAT + __builtin_popcount(KV3))); ties_(lm); wait_if<0, 0>((lm >> pl) & 1); }
            BAR();
            if constexpr (nb == KFB) flag_if<0, KOWN ? -1 : 1>(wm, fpub, tag, flg + FL_KV);
            if constexpr (TCH && nb == TCB) {
                if (TCH == 1) { const u32x4 nz = {~tagA, ~tagA, ~tagA, ~tagA}; if (lane < 32) GST_B128_SC1(((tm * 2 + (wm >> 1)) * 1024 + tn * 64 + (wm & 1) * 32) * 16 + l16, nz, ws + WS_E, 0); }
                else if (TCH == 3) {
                    const unsigned o = (tn * 64 + lane) * 2048 + ((tm & 3) * 4 + wm) * 128;
                    asm volatile("global_load_dword %0, %2, %3\n\tglobal_load_dword %1, %2, %4" : "=&v"(tch), "=&v"(tch2) : "v"(o), "s"(a.cre), "s"(a.cim) : "memory");
                } else if (TCH == 4) {
                    asm volatile("global_load_dword %0, %1, %2" : "=&v"(tch) : "v"((tn * 64 + lane) * 2048), "s"(((wm & 1) ? a.cim : a.cre) + (((wm & 2) ? SLICE(6) : SLICE(5)) << 7)) : "memory");
                }
                else asm volatile("global_load_dword %0, %1, %2 sc1" : "=&v"(tch) : "v"((lane >> 1) * 16384 + tn * 1024 + wm * 256 + (lane & 1) * 128), "s"(ws + WS_E) : "memory");
            }
            if constexpr (nib_any(PLT, nb)) { if (!PK) ld4_if<nb>(pl, l16, sw, krow); if (!PQ) ld8_if<nb>(pl, qo, qv, qs, qs + 16384); }
        };
#define HB(n) hb(std::integral_constant<int, (n) + NBK>{})
        {
            unsigned kw[2][4], ta[2], th[2];
            u32x2 hm[2];
#pragma unroll
            for (int j = 0; j < 2; ++j) {
                const unsigned x = take(kw0[j]);
                const auto y = __builtin_amdgcn_permlane16_swap(x, x, false, false);
#pragma unroll
                for (int p = 0; p < 2; ++p) { kw[p][2 * j] = y[p] << 16; kw[p][2 * j + 1] = y[p] & 0xffff0000u; }
            }
#pragma unroll
            for (int p = 0; p < 2; ++p) {
                unsigned h[3];
#pragma unroll
                for (int j = 0; j < 3; ++j) h[j] = (__shfl(hal, p * 8 + (r >> 1) + 16 * (j + 1)) >> ((r & 1) * 16)) & 0xffffu;
                hm[p] = idx ? u32x2{h[0] << 16, h[1] | (h[2] << 16)} : u32x2{0u, 0u};
                const int q = r >> 2, pp = r & 3;
                ta[p] = lb + L_KS + (8 * g4 + q) * 256 + (((wm * 4 + p * 2 + (pp >> 1)) ^ (2 * (q | ((g4 & 1) << 2)))) << 4) + (pp & 1) * 8;
                th[p] = (ta[p] ^ 128) - 1024;
                TIE(hm[p]); TIE(ta[p]); TIE(th[p]);
#pragma unroll
                for (int j = 0; j < 4; ++j) TIE(kw[p][j]);
            }
            TS(21);
            if (CB0 == 1) b0();
#pragma unroll
            for (int i = 0; i < 4; ++i) asm volatile("" : "=v"(sw[i]), "=v"(qv[2 * i]), "=v"(qv[2 * i + 1]));
            static_for<1, KAT + 1>(hb);
            TS(2);
            u32x2 kt[4][3], vt[4][2];
#pragma unroll
            for (int u = 0; u < 4; ++u) {
                const int p = u >> 1, o = (u & 1) * 8192;
                auto tr = [&](unsigned ad) -> u32x2 { return __builtin_bit_cast(u32x2, __builtin_amdgcn_ds_read_tr16_b64_v4i16(LDSP(v4i16, ad))); };
                kt[u][0] = tr(th[p] + o); kt[u][1] = tr(ta[p] + o); vt[u][0] = tr(ta[p] + o + 16384); kt[u][2] = tr(ta[p] + o + 1024); vt[u][1] = tr(ta[p] + o + 17408);
                SB();
            }
            TS(14);
            __builtin_amdgcn_s_setprio(KPRIO);
            static_for<0, 4>([&](auto uc) {
                constexpr int u = decltype(uc)::value, p = u >> 1, oh = u & 1;
                const u32x2 k0 = oh == 0 && g4 == 0 ? hm[p] : kt[u][0];
                unsigned t0[8], t1[8], e[7];
#define K3(n) do { if constexpr ((KV3 >> (6 * u + n)) & 1) hb(std::integral_constant<int, KAT + 1 + __builtin_popcount(KV3 & ((1 << (6 * u + n)) - 1))>{}); } while (0)
                if constexpr (RX & 1) {
                kf4a(t0, e, k0[0], k0[1], kt[u][1][0], kt[u][1][1], kw[p]); K3(0); kf4b(t0, e, kw[p]); K3(1); kf4c(t0, vt[u][0][0], vt[u][0][1]); K3(2);
                kf4a(t1, e, kt[u][1][0], kt[u][1][1], kt[u][2][0], kt[u][2][1], kw[p]); K3(3); kf4b(t1, e, kw[p]); K3(4); kf4c(t1, vt[u][1][0], vt[u][1][1]);
                } else {
                kv4a(t0, e, k0[0], k0[1], kt[u][1][0], kt[u][1][1], kw[p]); K3(0); kv4b(t0, e, kw[p]); K3(1); kv4c(t0, vt[u][0][0], vt[u][0][1]); K3(2);
                kv4a(t1, e, kt[u][1][0], kt[u][1][1], kt[u][2][0], kt[u][2][1], kw[p]); K3(3); kv4b(t1, e, kw[p]); K3(4); kv4c(t1, vt[u][1][0], vt[u][1][1]);
                }
                kvo[u] = u32x4{t1[2], t1[0], t0[2], t0[0]};
                GST_B128_SC1(l16, kvo[u], kvd + (u >> 1) * 65536, (u & 1) * 1024);
                K3(5);
#undef K3
            });
            hb(std::integral_constant<int, KAT + 1 + __builtin_popcount(KV3)>{});
            __builtin_amdgcn_s_setprio(3);
        }
        TS(3);
        const char* usrc = ws + WS_KVT + (size_t)(xcd * 8 + (idx >> 2)) * 65536 + wm * 16384 + (idx & 3) * 64;
        const unsigned uo = (lane >> 2) * 256 + (lane & 3) * 16;
        const char* cs = ((wm & 2) ? a.cim : a.cre) + (size_t)(tn * 64 + (wm & 1) * 2) * 2048;
        unsigned co[4];
#pragma unroll
        for (int i = 0; i < 4; ++i) {
            const int pair = i * 4 + g4, v = r ^ pair, row = pair * 2 + (v >> 3);
            co[i] = ((row & 15) * 4 + (row >> 4)) * 2048 + (v & 7) * 16 - i * 1024;
        }
        bool kv_ok = false;
#pragma unroll
        for (int it = 0; it < 4; ++it) asm volatile("" : "=v"(uu[it]));
        auto cst = [&](int place, int slice) { const char* s = cs + (slice << 7); DMA(lb + place + wm * 4096, co[0], s); dmao_<1024>(co[1], s); dmao_<2048>(co[2], s); dmao_<3072>(co[3], s); };
        auto items = [&](auto pc) {
            constexpr int p = decltype(pc)::value;
            if constexpr (PU == p) {
                WAIT_V(it_cnt(PU - 1, 5, PU - 1)); TIE(fl);
                if (TCH >= 2) TIE(tch);
                if (TCH == 3) TIE(tch2);
                kv_ok = __all(fl[0] == tagA && fl[1] == tagB);
                const char* us = kv_ok ? usrc : a.x;
                for (int it = 0; it < 4; ++it) GLD_B128_SC1(uu[it], uo, us + it * 4096, 0);
            }
            if constexpr (PK == p) ld4_if<0>(0, l16, sw, krow);
            if constexpr (PC0 == p) cst(g2_place(0), SLICE(0));
            if constexpr (PC1 == p) cst(g2_place(1), SLICE(1));
            if constexpr (PQ == p) ld8_if<0>(0, qo, qv, qs, qs + 16384);
            if constexpr (PU == p + 1) GLD_B64_SC1(fl, fpoll, flg + FL_KV, 0);
            if constexpr (PC2 == p) cst(g2_place(5), SLICE(5));
            if constexpr (PC3 == p) cst(g2_place(6), SLICE(6));
        };
#define ITEMS(p) items(std::integral_constant<int, p>{})
        static_assert(KFB > KAT + 1 + __builtin_popcount(KV3) && KV3 < (1 << 23) && KFB <= 13 && KFB <= PC0 && KFB < PU - 1 && nib(PLT, 0) >= KAT && nib(PLT, 3) < PU - 1, "schedule");
        static_for<KAT + 2 + __builtin_popcount(KV3), 15>([&](auto nc) { hb(nc); if constexpr (decltype(nc)::value < 14) items(nc); });
        const unsigned ud = lb + (lane & 3) * CHSZ + UOFF + ((lane >> 2) & 1) * UPL + (16 + wm * 32 + (lane >> 3)) * 16;
        float dz = lane == 0 ? bf2f(dskl) : 0.f;
        if (RX & 2) TIE(dz);
        auto zero = [&]() {
            const u32x4 z4 = {0, 0, 0, 0};
            if (lane < 24) *LDSP(u32x4, lb + L_SY + l16) = z4;
            if (lane < 56) *LDSP(u32x4, lb + L_AGG + wm * 896 + l16) = z4;
            *LDSP(u32x4, lane < 32 ? cb + ((lane >> 3) & 1) * CST + (lane & 7) * 16 : cb + UOFF + (g4 & 1) * UPL + r * 16) = z4;
        };
        auto fill = [&]() {
#pragma unroll
            for (int it = 0; it < 4; ++it) TIE(sw[it]);
            if (RX & 2) sw[0][0] = pack2(bflo(sw[0][0]) + dz, bfhi(sw[0][0]));
#pragma unroll
            for (int it = 0; it < 4; ++it) {
                const unsigned q = cb + 128 + it * 1024 + l16;
                const unsigned d0 = sw[it][0], d1 = sw[it][1], d2 = sw[it][2], d3 = sw[it][3];
                const unsigned o1 = it ? __builtin_amdgcn_readlane(sw[it ? it - 1 : 0][3], 63) : 0u;
                const unsigned x1 = __builtin_amdgcn_update_dpp(o1, d3, 0x138, 0xf, 0xf, false);
                *LDSP(u32x4, q) = sw[it];
                *LDSP(u32x4, q + CST) = u32x4{__builtin_amdgcn_alignbyte(d0, x1, 2), __builtin_amdgcn_alignbyte(d1, d0, 2), __builtin_amdgcn_alignbyte(d2, d1, 2), __builtin_amdgcn_alignbyte(d3, d2, 2)};
            }
        };
        auto uput = [&]() {
            WAIT_V(it_cnt(PU, 0, UE ? 15 : 16));
#pragma unroll
            for (int it = 0; it < 4; ++it) TIE(uu[it]);
            if (!kv_ok) {
                TSE(23);
                for (unsigned spin = 0; spin < SPIN_CAP; ++spin) {
                    GLD_B64_SC1(fl, fpoll, flg + FL_KV, 0);
                    WAIT_V(0); TIE(fl);
                    if (__all(fl[0] == tagA && fl[1] == tagB)) break;
                    __builtin_amdgcn_s_sleep(8);
                }
#pragma unroll
                for (int it = 0; it < 4; ++it) GLD_B128_SC1(uu[it], uo, usrc + it * 4096, 0);
                WAIT_V(0);
#pragma unroll
                for (int it = 0; it < 4; ++it) TIE(uu[it]);
            }
#pragma unroll
            for (int it = 0; it < 4; ++it) *LDSP(u32x4, ud + it * 128) = uu[it];
        };
        static_assert(PU <= (UE ? 15 : 16) && (FP_ != 14 || nib(PLT, 3) <= 12), "u");
        ITEMS(14);
        if (UE) __builtin_amdgcn_s_setprio(UPRIO);
        if (P14 != 3) __builtin_amdgcn_s_setprio(P14);
        if (UE >= 2 || ZP_ == 14) zero();
        if (UE == 2 || FP_ == 14) { WAIT_V(it_cnt(12, 8, 14)); fill(); }
        BAR();
        if (P14 != 3) __builtin_amdgcn_s_setprio(3);
        ITEMS(15);
        TS(4);
        if (P15 != 3) __builtin_amdgcn_s_setprio(P15);
        if (UE < 2 && ZP_ == 15) zero();
        if ((UE & 1) || FP_ == 15) fill();
        if (UE) uput();
        WAIT_L(0);
        TS(5);
        BAR();
        __builtin_amdgcn_s_setprio(FPRIO);
        ITEMS(16);
        if (P16 != FPRIO) __builtin_amdgcn_s_setprio(P16);
        if (S16) __builtin_amdgcn_s_sleep(S16);
        TS(11);
        const unsigned pmine = lb + L_SY + 32 + wm * 4, pall = lb + L_SY + 32 + (lane & 3) * 4;
        static_assert(!PK || (!UE && PK > KFB && PK <= 16), "ssm_k");
        static_assert(!PQ || PQ > KFB, "q");
        if (!UE) { if (PK >= PU) { uput(); sy_post<0>(pmine); WAIT_V(it_cnt(PK, 1, 16)); fill(); } else { if (FP_ == 16) fill(); TS(13); uput(); sy_post<0>(pmine); } ITEMS(17); sy_wait<0>(pall); }
        if (!CPOST) { WAIT_V(0); sy_post<1>(pmine); }
        __builtin_amdgcn_s_setprio(CVPRIO);
        TS(6);
        f32x4 ca[8];
        {
            unsigned kb[4];
#pragma unroll
            for (int i = 0; i < 4; ++i) { kb[i] = cb + (~r & 1) * CST + 2 * (64 + 16 + r - 8 * g4 - 7 + (~r & 1)) + i * 1024; TIE(kb[i]); }
            unsigned ub = cb + UOFF + (g4 & 1) * UPL + (15 + r + (g4 >> 1)) * 16 - 64;
#pragma unroll
            for (int i = 0; i < 8; ++i) { ca[i] = f32x4{0, 0, 0, 0}; TIE(ca[i]); }
            s16x4 FA[4][2]; bf16x8 FB[2][8];
#define CV_A(s, o, e) dsr2_<16 * ((s) + ((o) >> 3)) + 128 * (cv_m((o) & 7) & 1) + 2 * (e)>(FA[(o) & 3][e], kb[cv_m((o) & 7) >> 1])
#define CV_B(s, jb) DSR128(FB[(s) & 1][jb], ub, 64 + 256 * (jb) - 32 * (s))
            static_for<0, 8>([&](auto ic) { constexpr int n = decltype(ic)::value; CV_B(0, n); CV_A(0, n >> 1, n & 1); });
            WAIT_L(0);
            auto body = [&]() {
                static_for<0, 72>([&](auto ic) {
                    constexpr int s = decltype(ic)::value / 36, p = decltype(ic)::value % 36, o = cv_grp(p), m = cv_m(o), jb = p - cv_first(o);
                    if constexpr (jb == 0) { WAIT_LB(cv_wait(o)); if constexpr (!(AMF & 1)) { TIE(FA[o & 3][0]); TIE(FA[o & 3][1]); } }
                    if constexpr (p == 0 && !(AMF & 1)) { for (int i = 0; i < 8; ++i) TIE(FB[s][i]); }
                    const bf16x8 fa = __builtin_shufflevector(FA[o & 3][0], FA[o & 3][1], 0, 1, 2, 3, 4, 5, 6, 7);
                    if constexpr (AMF & 1) mfma_(ca[jb + m], fa, FB[s][jb]); else
                    ca[jb + m] = __builtin_amdgcn_mfma_f32_16x16x32_bf16(fa, FB[s][jb], ca[jb + m], 0, 0, 0);
                    SB();
                    constexpr int rd = cv_read(p);
                    if constexpr (rd >= 32) CV_B(s + 1, rd - 32);
                    else if constexpr (rd >= 0) CV_A(s, rd >> 1, rd & 1);
                    SB();
                });
                for (int i = 0; i < 4; ++i) { kb[i] += 128; TIE(kb[i]); }
                ub -= 64;
            };
#pragma unroll 1
            for (int it = 0; it < QIT; ++it) body();
            {
                TS(19);
                if (CPOST) { WAIT_V(PQ == 17 && PC3 < 17 ? 8 : 0); sy_post<1>(pmine); WAIT_L(0); }
                static_assert(PC3 != 19 || (ZFB == 1 && (BARM & 0x3ff) == 0x220), "stages 5, 6 are covered by the second barrier");
                if (PC3 == 19 || PQ == 19) { sy_wait<0>(lb + L_ES + (lane & P19M) * 256 + 8); ITEMS(19); }
                if (CVLATE) sy_wait<1>(lb + L_SY + (CVLATE == 2 ? wm + (lane & 1) * 4 : lane & 7) * 4);
                TS(20);
                if (PQ == 20 && QSL) __builtin_amdgcn_s_sleep(QSL);
                ITEMS(20);
            }
#pragma unroll 1
            for (int it = QIT; it < 4; ++it) body();
            WAIT_L(0);
            if (AMF & 1) asm volatile("s_nop 7\n\ts_nop 7" : "+v"(ca[0]), "+v"(ca[1]), "+v"(ca[2]), "+v"(ca[3]), "+v"(ca[4]), "+v"(ca[5]), "+v"(ca[6]), "+v"(ca[7]));
#pragma unroll
            for (int i = 0; i < 4; ++i) { TIE(FA[i][0]); TIE(FA[i][1]); }
#pragma unroll
            for (int i = 0; i < 8; ++i) TIE(FB[0][i]);
#undef CV_A
#undef CV_B
        }
        TS(7);
        __builtin_amdgcn_s_setprio(3);
        constexpr int NBAR = __builtin_popcount(BARM & ~VBM);
        unsigned zpub = (xcd * 32 + l31) * 256 + idx * 8, zpoll = ZOWN ? (xcd * 32 + idx) * 256 + l31 * 8 : fpoll;
        {
            const float Dd = bf2f(dskl);
            const char* zd = ws + WS_ZT + (size_t)(xcd * 128 + wm * 32) * 4096 + idx * 128;
            u32x2 kvr[8];
#pragma unroll
            for (int jt = 0; jt < 8; ++jt) if (!(RX & 2)) kvr[jt] = *LDSP(u32x2, cb + UOFF + (g4 >> 1) * UPL + (16 + 16 * jt + r) * 16 + (1 - (g4 & 1)) * 8);
#pragma unroll
            for (int jt = 0; jt < 8; ++jt) {
                const f32x4 y = ca[jt];
                const u32x2 kq = kvr[jt];
                *LDSP(u32x2, cb + (jt * 4 + (r >> 2)) * 144 + (r & 3) * 32 + g4 * 8) = (RX & 2) ? u32x2{pack2(y[0], y[1]), pack2(y[2], y[3])} : ZM ?
                    u32x2{pack2(add1(rb1(y[0]), rb1(mul1(bfhi(kq[1]), Dd))), add1(rb1(y[1]), rb1(mul1(bflo(kq[1]), Dd)))), pack2(add1(rb1(y[2]), rb1(mul1(bfhi(kq[0]), Dd))), add1(rb1(y[3]), rb1(mul1(bflo(kq[0]), Dd))))} :
                    u32x2{pack2(rb1(y[0]) + rb1(bfhi(kq[1]) * Dd), rb1(y[1]) + rb1(bflo(kq[1]) * Dd)), pack2(rb1(y[2]) + rb1(bfhi(kq[0]) * Dd), rb1(y[3]) + rb1(bflo(kq[0]) * Dd))};
            }
            unsigned za = lb + (r >> 2) * CHSZ + (wm * 8 + (g4 >> 1)) * 144 + ((g4 & 1) * 2 + ((r >> 1) & 1)) * 32 + (r & 1) * 8, zo = (lane >> 3) * 4096 + (lane & 7) * 16;
            TIE(za); TIE(zo); TIE(zpub); TIE(zpoll);
            sy_post<3>(pmine); sy_wait<3, 0>(pall);
            v4i16 t[4][2];
#pragma unroll
            for (int it = 0; it < 4; ++it)
#pragma unroll
                for (int e = 0; e < 2; ++e) t[it][e] = __builtin_amdgcn_ds_read_tr16_b64_v4i16(LDSP(v4i16, za + it * 288 + e * 16));
#pragma unroll
            for (int it = 0; it < 4; ++it) { TIE(t[it][0]); TIE(t[it][1]); }
            if (ZSB) BAR();
#pragma unroll
            for (int it = 0; it < 4; ++it) { const bf16x8 f = __builtin_shufflevector(t[it][0], t[it][1], 0, 1, 2, 3, 4, 5, 6, 7); GST_B128_SC1(zo, f, zd + it * 32768, 0); }
        }
#pragma unroll
        for (int i = ZSB; i < ZFB; ++i) BAR();
        WAIT_V(0);
        sy_post<2>(pmine);
        if (wm == 0) {
            sy_wait<2>(pall);
            if (ZOWN) { if (lane < 32) GST_B64_SC1(zpub, tag, flg + FL_Z); }
            else if (lane == 0) GST_B64_SC1(fpub, tag, flg + FL_Z);
        }
        TS(8);
        unsigned qm[8][4];
        if (QM == 1 || QM == 2) {
#pragma unroll
            for (int n = 0; n < 8; ++n) {
                TIE(qv[n]);
                if (QM == 2) { qm[n][0] = qv[n][0] << 16; qm[n][2] = qv[n][1] << 16; } else { qm[n][0] = qv[n][0] & 0xffffu; qm[n][2] = qv[n][1] & 0xffffu; }
                qm[n][1] = qv[n][0] & 0xffff0000u; qm[n][3] = qv[n][1] & 0xffff0000u;
                TIE(qm[n][0]); TIE(qm[n][1]); TIE(qm[n][2]); TIE(qm[n][3]);
            }
        }
#pragma unroll
        for (int i = ZFB; i < NBAR - 1 - (ZE >= 0); ++i) { BAR();
        }
        if (ZSL) __builtin_amdgcn_s_sleep(ZSL);
        asm volatile("s_cmp_lg_u32 %3, 0\n\ts_cbranch_scc1 1f\n\tglobal_load_dwordx2 %0, %1, %2 sc1\n1:" : "=&v"(fl) : "v"(zpoll), "s"(flg + FL_Z), "s"(wm) : "scc", "memory");
        if (NBAR > ZFB) BAR();
        if (wm == 0) {
            unsigned left = SPIN_CAP;
            asm volatile("s_branch 2f\n1:\n\ts_sleep %5\n\tglobal_load_dwordx2 %0, %2, %3 sc1\n2:\n\ts_waitcnt vmcnt(0)\n\tv_cmp_ne_u64 vcc, %4, %0\n\ts_cbranch_vccz 3f\n\ts_sub_u32 %1, %1, 1\n\ts_cbranch_scc0 1b\n3:"
                         : "+v"(fl), "+s"(left) : "v"(zpoll), "s"(flg + FL_Z), "s"(tag), "n"(ZPS) : "vcc", "scc", "memory");
            sy_post<4>(pmine);
        } else sy_wait<4, 2>(lb + L_SY + 32);
        u32x4 zv[4];
        {
            ITEMS(18);
            const char* zsrc = ws + WS_ZT + (size_t)(xcd * 128 + idx * 4 + wm) * 4096;
            static_for<0, 4>([&](auto ic) { constexpr int it = decltype(ic)::value; GLD_B128_SC1(zv[it], (l31 >> 2) * 512 + lh * 64 + (lane & 3) * 16, zsrc, it * 128); });
        }
        WAIT_V(0);
        TS(9);
#pragma unroll
        for (int it = 0; it < 4; ++it) TIE(zv[it]);
        if (!(XAB & 1)) {
        quad_swap<0>(zv[0], zv[1]); quad_swap<0>(zv[2], zv[3]);
        quad_swap<1>(zv[0], zv[2]); quad_swap<1>(zv[1], zv[3]); }
#pragma unroll
        for (int it = 0; it < 4; ++it) TIE(zv[it]);
        char* const oa = a.out + (size_t)(idx * 64 + wm * 16) * 8192 + xcd * 512;
        if (OPRIO != 3) __builtin_amdgcn_s_setprio(OPRIO);
        if (OS0) __builtin_amdgcn_s_sleep(OS0);
        if (OSW && ZE < 0) for (int i = 0; i < wm; ++i) __builtin_amdgcn_s_sleep(OSW);
#pragma unroll
        for (int n = 0; n < 8; ++n) {
            if (!QM || QM == 3) TIE(qv[n]);
            const unsigned z0 = zv[n & 3][2 * (n >> 2)], z1 = zv[n & 3][2 * (n >> 2) + 1];
            const f32x4 o = (XAB & 2) ? __builtin_bit_cast(f32x4, zv[n & 3]) : QM == 1 ? mul4bf(u32x2{z0, z1}, qm[n]) :
                QM == 3 ? f32x4{mul1(bflo(qv[n][0]), bflo(z0)), mul1(bfhi(qv[n][0]), bfhi(z0)), mul1(bflo(qv[n][1]), bflo(z1)), mul1(bfhi(qv[n][1]), bfhi(z1))} :
                QM == 2 && (RX & 4) ? f32x4{mul1(__uint_as_float(qm[n][0]), bflo(z0)), mul1(__uint_as_float(qm[n][1]), bfhi(z0)), mul1(__uint_as_float(qm[n][2]), bflo(z1)), mul1(__uint_as_float(qm[n][3]), bfhi(z1))} :
                QM == 2 ? f32x4{rb1(mul1(__uint_as_float(qm[n][0]), bflo(z0))), rb1(mul1(__uint_as_float(qm[n][1]), bfhi(z0))), rb1(mul1(__uint_as_float(qm[n][2]), bflo(z1))), rb1(mul1(__uint_as_float(qm[n][3]), bfhi(z1)))} :
                f32x4{rb1(bflo(qv[n][0]) * bflo(z0)), rb1(bfhi(qv[n][0]) * bfhi(z0)), rb1(bflo(qv[n][1]) * bflo(z1)), rb1(bfhi(qv[n][1]) * bfhi(z1))};
            if (n == ZE) { BAR(); if (OSW) for (int i = 0; i < wm; ++i) __builtin_amdgcn_s_sleep(OSW); }
            if (n == 0 && OSG) for (int i = 0; i < (idx & 3); ++i) __builtin_amdgcn_s_sleep(OSG);
            if (!(XAB & 4))
            GST_B128_NT(lh * 32768 + l31 * 16, o, oa + ((n >> 2) * 8 + (n & 3)) * 8192, 0);
            if (OSL && n < 7 && n + 1 != ZE) __builtin_amdgcn_s_sleep(OSL);
        }
        asm volatile(".Lfk_code_a:");
        TSE(10);
        TS_FLUSH();
        return;
    }

    u32x2 fl;
    const unsigned* dv = kvv;
#define G1_ISSUE(st, i) do { if constexpr ((st) < DFN) { if constexpr ((i) > 0) dmaof_<(i) * 1024>(dv[i], g1src + ((((st) << 7) + rot7) & 1920)); else dmaf_(g1dst + g1_slot(st), dv[i], g1src + ((((st) << 7) + rot7) & 1920)); } else if constexpr (M1 && (i) > 0) dmao_<(i) * 1024>(dv[i], g1src + ((((st) << 7) + rot7) & 1920)); \
        else DMA(g1dst + g1_slot(st) + (i) * 1024, dv[i], g1src + ((((st) << 7) + rot7) & 1920)); } while (0)
    unsigned k0 = trow + wm * 32768 + g4 * 2048 + ((l15 ^ (2 * g4)) << 4), k1 = k0 + 7168, k2 = (k0 ^ 128) + 14336, k3 = k2 + 7168;
    auto pre = [&](auto ic) {
        constexpr int n = decltype(ic)::value;
        if constexpr (n == KVP) { if (!HKV) kv_dma(g1dst + L_KS, k0, k1, k2, k3, wn ? a.v : a.k); }
        else static_for<0, 4>([&](auto jc) { G1_ISSUE(n - (n > KVP), decltype(jc)::value); });
    };
    static_for<1, AP0 / 4>(pre);
    TS(14);
    SLICE_TABLE();
    const int j0 = 4 * run - (32 - 2 * tm);
    const int pneg = EALL ? 0 : 16 * (wm >> 1) + 15 - (32 - 2 * tm);
    unsigned long long em0 = __ballot(j0 >= 0), em1 = __ballot(j0 + 2 >= 0);
    if (EM == 2) asm volatile("" : "+s"(em0), "+s"(em1));
    unsigned eo[2], pes, plr, est, hw[2], ta[2], po, pmine, pall, pown, pzero, g2v[2], cad[2][2], to[2][2], xo, dso, fst, fld, cwa;
    unsigned cta = lb + L_TS + st * 16, ctb = lb + L_TR + run * 1024 + st * 16;
    TIE(cta); TIE(ctb);
    f32x4 ct[5]; f32x2 V1;
    const int gm = PAIR ? wid >> 1 : wm, gk = PAIR ? wid & 1 : wn, gs = gk * 4 + (gm & 2);
    auto addrs = [&]() {
        int ln = lane;
        TIE(ln);
        const int l15 = ln & 15, g4 = ln >> 4, st = sq * 16 + l15, run = (wm >> 1) * 4 + g4, j0 = 4 * run - (32 - 2 * tm);
        pes = lb + L_ES + wid * 256 + l15 * 16;
        plr = lb + L_AGG + st * 8 + run * 512; est = ((tm * 2 + (wm >> 1)) * 1024 + tn * 64 + st) * 16 + (EREP ? g4 << 19 : 0);
#pragma unroll
        for (int i = 0; i < 2; ++i) { eo[i] = (j0 + 2 * i < 0 ? 0 : j0 + 2 * i) * 16384 + (tn * 64 + st) * 16 + (EREP == 1 ? (tm >> 2) << 19 : EREP == 2 ? (tm & 3) << 19 : 0); TIE(eo[i]); }
        TIE(plr); TIE(pes); TIE(est);
        const unsigned ht = lb + L_HT + wid * 4096;
#pragma unroll
        for (int j = 0; j < 2; ++j) {
            const int pr = (8 * (g4 & 1) + 4 * j + (l15 >> 2)) ^ ((g4 & 1) * 4), pw = l15 ^ ((l15 >> 3) * 4);
            ta[j] = ht + (g4 >> 1) * 512 + pr * 32 + ((((l15 >> 1) ^ (pr >> 2)) & 1) << 4) + 8 * (ln & 1);
            hw[j] = ht + g4 * 1024 + pw * 32 + (((j ^ (pw >> 2)) & 1) << 4);
            TIE(ta[j]); TIE(hw[j]);
        }
        po = pes + ((wm & 2) ? -512 : 512);
        pmine = lb + L_SY + wid * 4; pall = lb + L_SY + (ln & 7) * 4; pown = lb + L_SY + (ln < 4 ? 32 + ln * 4 : gs * 4 + (ln & 4)); pzero = lb + L_SY;
        xo = (tm * 128 + gm * 32 + gk * 16 + g4 * 4) * 2048 + tn * 128 + l15 * 8 + 4096; dso = xo & 2047;
        fst = (tm * 16 + l15) * 128 + tn * 8; fld = (tm * 16 + tn) * 128 + l15 * 8;
#pragma unroll
        for (int i = 0; i < 2; ++i) {
            const int pair = i * 4 + g4, v = l15 ^ pair ^ ((wm & 1) * 8);
            g2v[i] = (pair * 2 + (v >> 3)) * 8192 + (v & 7) * 16 + (tn * 64 + wm) * 2048 - i * 1024;
            TIE(g2v[i]);
#pragma unroll
            for (int j = 0; j < 2; ++j) {
                cad[i][j] = lb + L_C + (img_off((g4 >> 1) * 64 + l15, 4 * gk + (g4 & 1)) ^ (i * 32) ^ (j * 128));
                to[i][j] = ta[j] + (i ^ gk) * 1024 + (gm & 1) * 2048 + (gs - wid) * 4096;
                TIE(cad[i][j]); TIE(to[i][j]);
            }
        }
        cwa = lb + wid * 2048 + ln * 16;
        TIE(cwa); TIE(po);
        TIE(pmine); TIE(pall); TIE(pown); TIE(pzero); TIE(xo); TIE(dso); TIE(fst); TIE(fld);
    };
    if (!APRE) addrs();
    unsigned aad[2][2], bad[2];
    {
        const int aoff = img_off((wm >> 1) * 64 + l15, g4), boff = img_off(128 + wn * 64 + (wm & 1) * 16 + l15, g4);
#pragma unroll
        for (int ks = 0; ks < 2; ++ks) { aad[ks][0] = lb + (aoff ^ (ks * 64)); aad[ks][1] = lb + (aoff ^ (ks * 64) ^ 128); bad[ks] = lb + (boff ^ (ks * 64)); TIE(aad[ks][0]); TIE(aad[ks][1]); TIE(bad[ks]); }
    }
    if (!HKV && KVP >= AP0 / 4) { TIE(k0); TIE(k1); TIE(k2); TIE(k3); }
    static_for<AP0 / 4, NBK + 1>(pre);
    TS(1);
    float nL1i, nC1r, nC1i;
    unsigned tagA, tagB;
    f32x4 acc[4][2];
    {
#pragma unroll
        for (int i = 0; i < 4; ++i) { acc[i][0] = f32x4{0, 0, 0, 0}; acc[i][1] = f32x4{0, 0, 0, 0}; }
        bf16x8 fa[2][4], fb[2][2];
        if (GPRIO) __builtin_amdgcn_s_setprio(GPRIO);
        WAIT_V(g1_cnt(0)); BAR();
        TS(2);
#define G1_FRAG(g, n) do { constexpr int bo_ = g1_slot((g) >> 1), hi_ = bo_ & ~65535, x_ = (n) < 3 ? (n) >> 1 : (n) - 2; \
        if constexpr ((n) == 0 || (n) == 2) DSR128(fb[(g) & 1][x_], bad[(g) & 1] + hi_, bo_ - hi_ + x_ * 4096); \
        else DSR128(fa[(g) & 1][x_], aad[(g) & 1][x_ & 1] + hi_, bo_ - hi_ + x_ * 2048); } while (0)
        static_for<0, 6>([&](auto ic) { constexpr int n = decltype(ic)::value; G1_FRAG(0, n); });
        static_for<0, 32>([&](auto ic) {
            constexpr int g = decltype(ic)::value, gn = g + 1, n0 = g1_upto(g - 1), nd = g1_upto(g) - n0;
            constexpr bool bar = (gn & 1) == 0 && gn < 32;
            if constexpr (g == 1) {
                DSR128(ct[0], cta, 0); DSR128(ct[1], cta, 1024); DSR128(ct[2], cta, 2048); DSR128(ct[3], ctb, 0); DSR128(ct[4], ctb, 8192); DSR64(V1, ctb, 16384);
            }
            if constexpr (g == 3) {
#pragma unroll
                for (int i = 0; i < 5; ++i) TIE(ct[i]);
                TIE(V1);
                nL1i = -ct[0][1]; nC1r = -ct[0][2]; nC1i = -ct[0][3];
                TIE(nL1i); TIE(nC1r); TIE(nC1i);
                tagA = __builtin_amdgcn_readfirstlane(__float_as_uint(ct[2][2])); tagB = __builtin_amdgcn_readfirstlane(__float_as_uint(ct[2][3]));
                ties_(tagA); ties_(tagB);
            }
            if constexpr (bar) {
                static_assert(g1_cnt(gn >> 1) >= 0, "schedule");
                WAIT_VB(g1_cnt(gn >> 1)); WAIT_LB(0); BAR();
                if constexpr (((gn >> 1) & 3) == 0 || gn >= 26) TS(gn >= 26 ? 23 + (gn >> 1) - 13 : 16 + (gn >> 3));
#pragma unroll
                for (int i = 0; i < 4; ++i) if (!(AMF & 2)) TIE(fa[g & 1][i]);
                if (!(AMF & 2)) { TIE(fb[g & 1][0]); TIE(fb[g & 1][1]); }
            }
            static_for<0, 8>([&](auto mc) {
                constexpr int m = decltype(mc)::value, i = m >> 1, j = m & 1, js = (SNK & 1) && (i & 1) ? 1 - j : j;
                if constexpr (W1 && !bar && g > 0 && g < 31) {
                    if constexpr (m == 0) WAIT_LB(W1 == 1 ? 3 : 2);
                    if constexpr (m == 2 && W1 == 1) WAIT_LB(4);
                    if constexpr (m == 4) WAIT_LB(4);
                } else
                if constexpr (!bar && (i == 0 || j == 0)) {
                    WAIT_LB(5 - (i == 0 ? j + 1 : i + 2) + (gn < 32 ? (m < 6 ? m : 6) : 0));
                    if constexpr (!(AMF & 2) || g == 0) { TIE(fa[g & 1][i]); TIE(fb[g & 1][j]); }
                }
                if constexpr ((AMF & 2) && g > 0) mfma_(acc[i][js], fa[g & 1][i], fb[g & 1][js]); else
                acc[i][j] = __builtin_amdgcn_mfma_f32_16x16x32_bf16(fa[g & 1][i], fb[g & 1][j], acc[i][j], 0, 0, 0);
                SB();
                if constexpr (gn < 32 && m < 6) G1_FRAG(gn, m);
                constexpr int d = nd > 2 ? ((m & 1) ? m >> 1 : -1) : (m == DP0 ? 0 : m == DP1 ? 1 : -1);
                if constexpr (d >= 0 && d < nd) G1_ISSUE((n0 + d) >> 2, (n0 + d) & 3);
                SB();
            });
        });
#undef G1_FRAG
    }
    TS(3);
    const unsigned tagG = tagA ^ tagB ^ 0x9E3779B9u;
    __builtin_amdgcn_s_setprio(MPRIO);
    f32x2 ur[8], ui[8], hr, hi;
    const f32x2 L1 = {ct[0][0], ct[0][1]}, C1 = {ct[0][2], ct[0][3]}, L16 = {ct[1][2], ct[1][3]}, Q2r = {ct[3][0], ct[3][1]}, Q2i = {ct[3][2], ct[3][3]};
    const float L8r = ct[1][0], L8i = ct[1][1], L64r = ct[2][0], L64i = ct[2][1];
    u32x4 eg[4], cr[2 * NR + 1];
    const char* g2src = wn ? a.cim : a.cre;
#define CR_LOAD(n0, n1) static_for<n0, n1>([&](auto ic) { constexpr int n = decltype(ic)::value; GLD_B128(cr[n], g2v[n & 1], g2src + (SLICE(7 + (n >> 1)) << 7), (n & 1) * 1024); })
#define E_LOADS "global_load_dwordx4 v[104:107], %[a0], %[b0] " EMOD "\n\tglobal_load_dwordx4 v[108:111], %[a0], %[b1] " EMOD "\n\t" \
                "global_load_dwordx4 v[112:115], %[a1], %[b0] " EMOD "\n\tglobal_load_dwordx4 v[116:119], %[a1], %[b1] " EMOD "\n"
#define E_POLL() asm volatile("s_cmp_lt_i32 %[on], 0\n\ts_cbranch_scc1 1f\n\ts_mov_b64 exec, %[m1]\n\tglobal_load_dwordx4 v[112:115], %[a1], %[b0] " EMOD "\n\tglobal_load_dwordx4 v[116:119], %[a1], %[b1] " EMOD "\n\t" \
        "s_mov_b64 exec, %[m0]\n\ts_cbranch_execz 2f\n\tglobal_load_dwordx4 v[104:107], %[a0], %[b0] " EMOD "\n\tglobal_load_dwordx4 v[108:111], %[a0], %[b1] " EMOD "\n2:\n\ts_mov_b64 exec, -1\n1:" \
        : "+{v[104:107]}"(eg[0]), "+{v[108:111]}"(eg[1]), "+{v[112:115]}"(eg[2]), "+{v[116:119]}"(eg[3]) \
        : [a0] "v"(eo[0]), [a1] "v"(eo[1]), [b0] "s"(ws + WS_E), [b1] "s"(ws + WS_E + 16384), [on] "s"(pneg), [m0] "s"(em0), [m1] "s"(em1) : "scc", "memory")
    {
        asm volatile("s_nop 7\n\ts_nop 3" : "+v"(acc[0][0]), "+v"(acc[0][1]), "+v"(acc[1][0]), "+v"(acc[1][1]), "+v"(acc[2][0]), "+v"(acc[2][1]), "+v"(acc[3][0]), "+v"(acc[3][1]));
#pragma unroll
        for (int p = 0; p < 8; ++p) { ur[p] = f32x2{acc[p >> 1][0][2 * (p & 1)], acc[p >> 1][0][2 * (p & 1) + 1]}; ui[p] = f32x2{acc[p >> 1][1][2 * (p & 1)], acc[p >> 1][1][2 * (p & 1) + 1]}; }
        f32x2 gr = ur[0], gi = ui[0];
#pragma unroll
        for (int p = 1; p < 8; ++p) { f32x2 nr, ni; if (PLN & 1) c2_mad(nr, ni, L1[0], nL1i, L1[0], L1[1], gr, gi, ur[p], ui[p]); else pk_cmad(nr, ni, L1, gr, gi, ur[p], ui[p]); gr = nr; gi = ni; }
        float fr, fi;
        cmad(fr, fi, L8r, L8i, gr[0], gi[0], gr[1], gi[1]);
        {
            const f32x2 w = cmul4(V1, f32x2{fr, fi});
            const float wr = sum4(w[0]), wi = sum4(w[1]);
            const u32x4 gq = {__float_as_uint(wr), tagA, __float_as_uint(wi), tagG};
            if (EREP ? (EALL || EREP == 2 || g4 >= (tm >> 2)) : g4 == 0) GST_B128_SC1(est, gq, ws + WS_E, 0);
        }
        *LDSP(f32x2, plr + 7 * 512) = f32x2{fr, fi};
        *LDSP(unsigned, pes + 8) = 0;
        if (EM == 2) {
#pragma unroll
            for (int i = 0; i < 4; ++i) eg[i] = u32x4{0u, tagA, 0u, tagG};
            asm volatile("" : "+{v[104:107]}"(eg[0]), "+{v[108:111]}"(eg[1]), "+{v[112:115]}"(eg[2]), "+{v[116:119]}"(eg[3]));
        }
        TS(21);
        if (EP == 2) E_POLL();
        auto coef = [&]() {
#pragma unroll
            for (int p = 0; p < 8; ++p) { f32x2 nr, ni; if (PLN & 2) c2_mul(nr, ni, C1[0], nC1i, nC1r, nC1i, ur[p], ui[p]); else pk_conj_mul(nr, ni, C1, ur[p], ui[p]); ur[p] = nr; ui[p] = ni; TIE(ur[p]); TIE(ui[p]); }
        };
        if (CFB) coef();
        WAIT_L(0); BAR();
        f32x2 e[7];
#pragma unroll
        for (int j = 0; j < 7; ++j) e[j] = *LDSP(f32x2, plr + j * 512);
        if (EP == 1) E_POLL();
        CR_LOAD(0, CR1);
        if (!CFB) coef();
#pragma unroll
        for (int j = 1; j < 7; ++j) { if (PLN & 4) { float xr = e[j][0], xi = e[j][1]; cmac(xr, xi, L16[0], L16[1], e[j - 1][0], e[j - 1][1]); e[j] = f32x2{xr, xi}; } else cmac2(e[j], L16, e[j - 1]); }
        float mr, mi;
        cmad(mr, mi, L8r, L8i, e[6][0], e[6][1], gr[0], gi[0]);
        if (PLN & 4) c2_mul(hr, hi, C1[0], nC1i, nC1r, nC1i, f32x2{e[6][0], mr}, f32x2{e[6][1], mi}); else pk_conj_mul(hr, hi, C1, f32x2{e[6][0], mr}, f32x2{e[6][1], mi});
        TIE(hr); TIE(hi);
        CR_LOAD(CR1, CR1 + CR2);
        TS(4);
    }
    f32x2 G;
    {
        float pr = 0.f, pi = 0.f;
        if (!EP) E_POLL();
        if (pneg >= 0) {
            unsigned t0, t1, t2, left = SPIN_CAP;
            asm volatile("s_waitcnt vmcnt(%[c])\n\ts_branch 2f\n1:\n\ts_sleep %[sl]\n\t" EM_ON E_LOADS EM_OFF "\ts_waitcnt vmcnt(0)\n2:\n\t"
                         "v_bitop3_b32 %[t0], v105, v109, %[ta] bitop3:0x7e\n\tv_bitop3_b32 %[t1], v113, v117, %[ta] bitop3:0x7e\n\tv_bitop3_b32 %[t2], v107, v111, %[tg] bitop3:0x7e\n\t"
                         "v_or3_b32 %[t0], %[t0], %[t1], %[t2]\n\tv_bitop3_b32 %[t1], v115, v119, %[tg] bitop3:0x7e\n\tv_or_b32 %[t0], %[t0], %[t1]\n\t"
                         "v_cmp_ne_u32 vcc, 0, %[t0]\n\ts_cbranch_vccz 3f\n\ts_sub_u32 %[n], %[n], 1\n\ts_cbranch_scc0 1b\n3:"
                         : "+{v[104:107]}"(eg[0]), "+{v[108:111]}"(eg[1]), "+{v[112:115]}"(eg[2]), "+{v[116:119]}"(eg[3]), [t0] "=&v"(t0), [t1] "=&v"(t1), [t2] "=&v"(t2), [n] "+s"(left)
                         : [a0] "v"(eo[0]), [a1] "v"(eo[1]), [b0] "s"(ws + WS_E), [b1] "s"(ws + WS_E + 16384), [ta] "s"(tagA), [tg] "s"(tagG), [sl] "n"(ESL), [c] "n"(CR1 + CR2) : "vcc", "scc", "memory");
            float ar = __uint_as_float(eg[1][0]), ai = __uint_as_float(eg[1][2]), br = __uint_as_float(eg[3][0]), bi = __uint_as_float(eg[3][2]);
            cmac(ar, ai, L64r, L64i, __uint_as_float(eg[0][0]), __uint_as_float(eg[0][2]));
            cmac(br, bi, L64r, L64i, __uint_as_float(eg[2][0]), __uint_as_float(eg[2][2]));
            if (EALL) { if (j0 < 0) ar = ai = 0.f; if (j0 + 2 < 0) br = bi = 0.f; }
            const f32x2 pu = cmul4(f32x2{ct[4][2], ct[4][3]}, f32x2{ar, ai});
            pr = pu[0]; pi = pu[1];
            cmac(pr, pi, ct[4][0], ct[4][1], br, bi);
            pr = sum4(pr); pi = sum4(pi);
        }
        TS(5);
        *LDSP(u32x4, pes) = u32x4{__float_as_uint(pr), __float_as_uint(pi), 1u, 0u};
        CR_LOAD(CR1 + CR2, CR1 + CR2 + CR3);
        u32x4 o;
        asm volatile("1:\n\tds_read_b128 %0, %1\n\ts_waitcnt lgkmcnt(0)\n\tv_cmp_eq_u32 vcc, 0, v122\n\ts_cbranch_vccnz 1b" : "=&{v[120:123]}"(o) : "v"(po) : "vcc", "memory");
        G = f32x2{pr + __uint_as_float(o[0]), pi + __uint_as_float(o[1])};
    }
    TS(6);
    {
        if (PLN & 8) c2_mac(hr, hi, G[0], -G[1], -G[0], -G[1], Q2r, Q2i); else pk_conj_mac(hr, hi, G, Q2r, Q2i);
#pragma unroll
        for (int p = 0; p < 8; ++p) { if (PLN & 8) c2_mac(ur[p], ui[p], L1[0], L1[1], L1[0], nL1i, hr, hi); else pk_cmac_conj(ur[p], ui[p], L1, hr, hi); hr = ur[p]; hi = ui[p]; }
        const char* hd = ws + WS_H + (size_t)((tm * 4 + (wm & 2)) * 2 + wn) * HP + (tn * 4 + (wm & 1) * 2) * 1024;
#pragma unroll
        for (int e2 = 0; e2 < 2; ++e2)
#pragma unroll
            for (int c = 0; c < 2; ++c) {
                const f32x2* o = c ? ui : ur;
                *LDSP(u32x4, hw[e2] + c * 512) = u32x4{pack2(o[0][e2], o[1][e2]), pack2(o[2][e2], o[3][e2]), pack2(o[4][e2], o[5][e2]), pack2(o[6][e2], o[7][e2])};
            }
        v4i16 t[4][2];
#pragma unroll
        for (int n = 0; n < 4; ++n)
#pragma unroll
            for (int j = 0; j < 2; ++j) t[n][j] = __builtin_amdgcn_ds_read_tr16_b64_v4i16(LDSP(v4i16, ta[j] + n * 1024));
        sy_post<1>(pmine);
        static_for<0, 4>([&](auto ic) {
            constexpr int n = decltype(ic)::value;
            const bf16x8 f = __builtin_shufflevector(t[n][0], t[n][1], 0, 1, 2, 3, 4, 5, 6, 7);
            GST_B128_SC1(l16, f, hd + (n >> 1) * 2 * HP + ((n & 1) ^ wn) * 1024, 0);
        });
    }
    TS(7);
    bf16x8 fa2[64];
    u32x2 xv[4], dsv;
    const char* Aw = ws + WS_H + (size_t)((tm * 4 + gm) * 2 + gk) * HP;
    const unsigned g2dst = lb + wid * 2048;
#define G2_ISSUE(st, i) do { if constexpr (i) dmao_<1024>(g2v[1], g2src + (SLICE(st) << 7)); else DMA(g2dst + g2_place(st), g2v[0], g2src + (SLICE(st) << 7)); } while (0)
#define G2_A(g) GLD_B128_SC1(fa2[g], l16, Aw + (SLICE((g) >> 2) << 12), ((g) & 3) * 1024)
    {
        {
            sy_wait<1>(pown);
            s16x4 fo[4][2]; bf16x8 c[8];
            static_for<0, 2>([&](auto kc) {
                constexpr int ks = decltype(kc)::value;
                static_for<0, 4>([&](auto ic) { constexpr int i = decltype(ic)::value; dsrtr_<ks * 4096>(fo[2 * ks + (i >> 1)][i & 1], to[i >> 1][i & 1]); });
                static_for<0, 4>([&](auto ic) { constexpr int i = decltype(ic)::value; DSR128(c[4 * ks + i], cad[ks][i & 1], 2 * G2STAGE + i * 2048); });
            });
            static_for<0, 8>([&](auto ic) {
                constexpr int n = decltype(ic)::value, ks = n >> 2, i = n & 3;
                WAIT_L(ks ? 3 - i : 11 - i);
                TIE(c[n]);
                if constexpr (i == 0) { TIE(fo[2 * ks][0]); TIE(fo[2 * ks][1]); TIE(fo[2 * ks + 1][0]); TIE(fo[2 * ks + 1][1]); }
#pragma unroll
                for (int j = 0; j < 2; ++j)
                    acc[i][j] = __builtin_amdgcn_mfma_f32_16x16x32_bf16(__builtin_shufflevector(fo[2 * ks + j][0], fo[2 * ks + j][1], 0, 1, 2, 3, 4, 5, 6, 7), c[n], ks ? acc[i][j] : f32x4{0, 0, 0, 0}, 0, 0, 0);
                SB();
            });
#pragma unroll
            for (int i = 0; i < 4; ++i) { TIE(acc[i][0]); TIE(acc[i][1]); }
        }
        if (CEARLY) sy_post<0>(pmine);
        static_for<0, 4>([&](auto ic) { constexpr int it = decltype(ic)::value; GLD_B64(xv[it], xo, a.x, it * 2048 - 4096); });
        GLD_B64(dsv, dso, a.dskip, 0);
        if (CEARLY) {
            sy_wait<0>(pall);
            static_for<4, 4 + CEARLY>([&](auto ic) { constexpr int n = decltype(ic)::value; G2_ISSUE(n >> 1, n & 1); });
        }
        TS(16);
        WAIT_V(5 + CEARLY);
        sy_post<2>(pmine);
        if (!CEARLY || wid == (NOOWN ? 1 : 0) || (NOOWN == 1 && wid == 0)) sy_wait<2, 0>(pall);
        const u32x2 tagv = {__float_as_uint(ct[2][2]), __float_as_uint(ct[2][3])};
        if (wid == (NOOWN ? 1 : 0)) asm volatile("s_mov_b64 exec, 0xffff\n\tglobal_store_dwordx2 %0, %1, %2 sc1\n\ts_mov_b64 exec, -1" :: "v"(fst), "v"(tagv), "s"(flg + FL_H) : "memory");
        if (PFIRST) asm volatile("s_cmp_lg_u32 %3, 0\n\ts_cbranch_scc1 1f\n\tglobal_load_dwordx2 %0, %1, %2 sc1\n1:" : "=&v"(fl) : "v"(fld), "s"(flg + FL_H), "s"(wid) : "scc", "memory");
        TS(8);
        static_for<4 + CEARLY, 10>([&](auto ic) { constexpr int n = decltype(ic)::value; G2_ISSUE(n >> 1, n & 1); });
        CR_LOAD(2 * NR - CRL, 2 * NR);
        TS(13);
        if (wid == 0) {
            unsigned left = SPIN_CAP;
            if (PFIRST)
            asm volatile("s_waitcnt vmcnt(6)\n\ts_branch 3f\n1:\n\tglobal_load_dwordx2 %0, %2, %3 sc1\n\ts_waitcnt vmcnt(0)\n3:\n\tv_cmp_ne_u64 vcc, %0, %4\n\ts_cbranch_vccz 2f\n\ts_sub_u32 %1, %1, 1\n\ts_cbranch_scc0 1b\n2:"
                         : "+v"(fl), "+s"(left) : "v"(fld), "s"(flg + FL_H), "v"(tagv) : "vcc", "scc", "memory");
            else
            asm volatile("1:\n\tglobal_load_dwordx2 %0, %2, %3 sc1\n\ts_waitcnt vmcnt(0)\n\tv_cmp_ne_u64 vcc, %0, %4\n\ts_cbranch_vccz 2f\n\ts_sub_u32 %1, %1, 1\n\ts_cbranch_scc0 1b\n2:"
                         : "=&v"(fl), "+s"(left) : "v"(fld), "s"(flg + FL_H), "v"(tagv) : "vcc", "scc", "memory");
            sy_post<3>(pmine);
            if (b == 255 && lane == 0) GST_B64_SC1(0u * lane, tagv, flg + FL_EPOCH);
        } else if (g2_bar(3)) sy_wait<3>(pzero);
        else { WAIT_V(CRL); sy_post<3>(pmine); }
        if (!g2_bar(3)) sy_wait<3>(pall);
        TS(20);
        static_for<4, 4 + AW>([&](auto ic) { constexpr int g = decltype(ic)::value; G2_A(g); });
    }
    TS(9);
    __builtin_amdgcn_s_setprio(0);
    unsigned yw, yr, yp, yo, lzw;
    static_assert(PAIR, "yr");
    unsigned dm[4];
    auto dmask = [&]() { TIE(dsv); dm[0] = dsv[0] & 0xffffu; dm[1] = dsv[0] & 0xffff0000u; dm[2] = dsv[1] & 0xffffu; dm[3] = dsv[1] & 0xffff0000u; TIE(dm[0]); TIE(dm[1]); TIE(dm[2]); TIE(dm[3]); };
    auto skip = [&](int it) { TIE(xv[it]); const f32x4 y = mul4bf(xv[it], dm); for (int i = 0; i < 4; ++i) acc[i][0][it] = add1(acc[i][0][it], y[i]); if (it & 1) { for (int i = 0; i < 4; ++i) TIE(acc[i][0]); } };
    {
        bf16x8 fb[2][4];
#define G2_FRAG(g, i) do { constexpr int pl_ = g2_place((g) >> 1), b_ = pl_ >= L_C ? L_C : pl_ >= 32768 ? 32768 : 0; DSR128(fb[(g) & 1][i], cad[(g) & 1][(i) & 1] + (b_ - L_C), pl_ - b_ + (i) * 2048); } while (0)
        static_for<0, 4>([&](auto ic) { constexpr int i = decltype(ic)::value; G2_FRAG(2, i); });
        if (YS == 0) { WAIT_V(AW + 6 + CRL); dmask(); skip(0); skip(1); skip(2); skip(3); }
        static_for<2, 32>([&](auto ic) {
            constexpr int g = decltype(ic)::value, gn = g + 1;
            constexpr bool bar = g2_bar(g) && !((VBM >> g) & 1);
            constexpr int n0 = g2_upto(g - 1), n1 = g2_upto(g);
            if constexpr (bar) {
                constexpr int ca = g2_cnt(2 * g, 2 * g), cs = g2_cover(g) >= 7 + NR && !(LZ && g == LASTB) ? g2_cnt(100 + 2 * g2_cover(g) + 1, 2 * g) : 63;
                static_assert(cs >= 0 && g2_wr(g - 1) >= 2 * (g2_cover(g) < 6 + NR ? g2_cover(g) - 6 : NR), "schedule");
                WAIT_VB(AWB && ca < cs ? ca : cs); WAIT_LB(0); BAR();
                if constexpr (!AWB) WAIT_V(ca);
#pragma unroll
                for (int i = 0; i < 4; ++i) if (!(AMF & 4)) TIE(fb[g & 1][i]);
                if (!(AMF & 4)) TIE(fa2[2 * g]);
            }
            if constexpr (LZ && g == lz_chk()) { WAIT_LB(0); sy_chk<0>(lzw, pall); }
            constexpr int xl = g2_wr(g - 1) - g2_wr(g - 2) + (g - 1 == EGX) + (LZ && g - 1 == lz_post());
            static_for<0, 8>([&](auto mc) {
                constexpr int m = decltype(mc)::value, i = m & 3, j = m >> 2, f = 2 * g + j;
                static_assert(g2_cnt(f, f) >= 0, "schedule");
                constexpr int jj = LSW && g == 31 ? 1 - j : j, ff = 2 * g + jj;
                if constexpr (i == 0 && (j || !bar)) { WAIT_VB(LSW && g == 31 ? 0 : g2_cnt(f, f)); if constexpr (!(AMF & 4) || g == 2 || g == 31) TIE(fa2[ff]); }
                if constexpr (W2 && g > 2 && g < 31) { if constexpr (!bar && j == 0 && (i & W2) == 0) WAIT_LB(3 - W2 + xl); } else
                if constexpr (!bar && j == 0) { WAIT_LB((gn < 32 ? 3 : 3 - i) + xl); if constexpr (!(AMF & 4) || g == 2 || g == 31) TIE(fb[g & 1][i]); }
                constexpr int is = (SNK & 2) && j ? 3 - i : i;
                if constexpr ((AMF & 4) && g > 2 && g < 31) mfma_(acc[is][jj], fa2[ff], fb[g & 1][is]); else
                acc[i][jj] = __builtin_amdgcn_mfma_f32_16x16x32_bf16(fa2[ff], fb[g & 1][i], acc[i][jj], 0, 0, 0);
                SB();
                if constexpr (LSW && g == 31 && j) *LDSP(f32x4, yw + i * 1024) = acc[i][1];
                if constexpr (g == EGX && m == 4) sy_post<5>(pmine);
                if constexpr (LZ && g == lz_post() && m == 4) sy_post<0>(pmine);
                if constexpr (LZ && g + 1 == lz_chk() && m == 4) sy_pre<0>(lzw, pall);
                if constexpr (g == 29 && m == 5) { yw = cwa + L_Y + wid * 2048; yr = yw ^ (PAIR ? 4096 : 16384); yp = pmine ^ (PAIR ? 4 : 16); yo = (cwa & 255) + ((cwa & 768) << 7); TIE(yw); TIE(yr); TIE(yp); TIE(yo); }
                if constexpr (gn < 32 && m < 4) G2_FRAG(gn, m);
                if constexpr (m >= 4 && g2_wr(g - 1) + m - 4 < g2_wr(g)) {
                    constexpr int n = g2_wr(g - 1) + m - 4, pl = g2_place(7 + (n >> 1)), pb = pl >= L_C ? L_C : 0;
                    asm volatile("ds_write_b128 %0, %1 offset:%2" :: "v"(cwa + pb), "v"(cr[n]), "n"(pl - pb + (n & 1) * 1024) : "memory");
                }
                if constexpr (i == 1 && n0 + j < n1) G2_ISSUE((n0 + j) >> 1, (n0 + j) & 1);
                if constexpr (i >= 2 && g2_ald(f, i & 1) >= 0) G2_A(g2_ald(f, i & 1));
                SB();
            });
        });
#undef G2_FRAG
    }
    TS(11);
    if (EPRIO) __builtin_amdgcn_s_setprio(EPRIO);
    {
#pragma unroll
        for (int i = 0; i < 4; ++i) if (!LSW) *LDSP(f32x4, yw + i * 1024) = acc[i][1];
        sy_post<4>(pmine);
        sy_wait<4, 0>(yp);
        char* const ob = a.out + (size_t)(tm * 128 + gm * 32 + gk * 16) * 8192 + 4096 + tn * 256;
        f32x4 w[4];
#pragma unroll
        for (int i = 0; i < 4; ++i) w[i] = *LDSP(f32x4, yr + i * 1024);
        f32x4 o[4];
#pragma unroll
        for (int r = 0; r < 4; ++r) { o[r] = f32x4{add1(w[0][r], acc[0][0][r]), add1(w[1][r], acc[1][0][r]), add1(w[2][r], acc[2][0][r]), add1(w[3][r], acc[3][0][r])}; if (EGX) TIE(o[r]); }
        if (EGX && !wn) sy_wait<5, 1>(pall);
#pragma unroll
        for (int r = 0; r < 4; ++r) GST_B128_NT(yo, o[r], ob + r * 8192, 0);
    }
    asm volatile(".Lfk_code_b:");
    TSE(12);
    TS_FLUSH();
}

}

extern "C" void kernel_launch(void* const* d_in, const int* in_sizes, int n_in, void* d_out, int out_size, void* d_ws, size_t ws_size,
                              hipStream_t stream) {
    (void)in_sizes; (void)n_in; (void)out_size; (void)ws_size;
    fk::Args a;
    a.q = (const char*)d_in[0]; a.k = (const char*)d_in[1]; a.v = (const char*)d_in[2]; a.sk = (const char*)d_in[3];
    a.ssmk = (const char*)d_in[4]; a.ssmD = (const char*)d_in[5]; a.x = (const char*)d_in[6]; a.lre = (const char*)d_in[7];
    a.lim = (const char*)d_in[8]; a.bre = (const char*)d_in[9]; a.bim = (const char*)d_in[10]; a.cre = (const char*)d_in[11];
    a.cim = (const char*)d_in[12]; a.logdt = (const char*)d_in[13]; a.dskip = (const char*)d_in[14];
    a.out = (char*)d_out; a.ws = (char*)d_ws;
    hipLaunchKernelGGL(fk::fused, dim3(256), dim3(768), 0, stream, a);
}
```

```cpp
#include <hip/hip_runtime.h>
#include <stdint.h>
#include <type_traits>

#define ZM 0
#define RX 10
#define P19M 7
#define QSL 2
#define ZP_ 15
#define FP_ 16
#define P14 3
#define P15 3
#define P16 3
#define S16 0
#define OPRIO 3
#define OS0 0
#define OSW 0
#define ZE -1
#define EM 2
#define EM_ON "s_mov_b64 exec, vcc\n\t"
#define EM_OFF "\ts_mov_b64 exec, -1\n"
#define EMD 0
#define EMOD "sc1"
#define TCH 0
#define TCB 10
#define SQ 1
#define SN 2
#define FMD 0
#define FMOD "sc1"
#define SMD 0
#define SMOD "sc1"
#define FX 0
#define FX_ON
#define FX_OFF
#define CVE 0
#define CVS 1
#define CVN 4
#define G2PRIO 0
#define CVZ 0
#define QP 0
#define FSL 0
#define SNK 0
namespace fk {

constexpr int S = 2048, D = 1024, P = 1024;

typedef __attribute__((ext_vector_type(8))) short bf16x8;
typedef __attribute__((ext_vector_type(4))) short s16x4;
typedef __attribute__((ext_vector_type(16))) float f32x16;
typedef __attribute__((ext_vector_type(4))) float f32x4;
typedef __attribute__((ext_vector_type(2))) float f32x2;
typedef __attribute__((ext_vector_type(4))) unsigned u32x4;
typedef __attribute__((ext_vector_type(2))) unsigned u32x2;
#define LDSP(T, a) ((__attribute__((address_space(3))) T*)(uintptr_t)(unsigned)(a))

__device__ __forceinline__ float bf2f(unsigned v) { return __uint_as_float(v << 16); }
__device__ __forceinline__ float bflo(unsigned w) { return __uint_as_float(w << 16); }
__device__ __forceinline__ float bfhi(unsigned w) { return __uint_as_float(w & 0xffff0000u); }
__device__ __forceinline__ uint16_t f2bf(float f) { __bf16 b = (__bf16)f; return __builtin_bit_cast(uint16_t, b); }
__device__ __forceinline__ float rbf(float f) { return bf2f(f2bf(f)); }
typedef __attribute__((ext_vector_type(2))) __bf16 bf16x2;
__device__ __forceinline__ unsigned pack2(float lo, float hi) { return __builtin_bit_cast(unsigned, __builtin_convertvector(f32x2{lo, hi}, bf16x2)); }
__device__ __forceinline__ float rb1(float x) { return __uint_as_float(pack2(0.f, x)); }
__device__ __forceinline__ void cmac(float& xr, float& xi, float ar, float ai, float hr, float hi) {
    asm("v_fmac_f32 %0, %2, %4\n\tv_fmac_f32 %1, %2, %5\n\tv_fma_f32 %0, -%3, %5, %0\n\tv_fmac_f32 %1, %3, %4" : "+v"(xr), "+v"(xi) : "v"(ar), "v"(ai), "v"(hr), "v"(hi));
}
__device__ __forceinline__ void cmad(float& o_r, float& o_i, float ar, float ai, float hr, float hi, float er, float ei) {
    asm("v_fma_f32 %0, %2, %4, %6\n\tv_fma_f32 %1, %2, %5, %7\n\tv_fma_f32 %0, -%3, %5, %0\n\tv_fmac_f32 %1, %3, %4" : "=&v"(o_r), "=&v"(o_i) : "v"(ar), "v"(ai), "v"(hr), "v"(hi), "v"(er), "v"(ei));
}
__device__ __forceinline__ f32x2 cmul4(f32x2 a, f32x2 h) {
    float o_r, o_i;
    asm("v_mul_f32 %0, %2, %4\n\tv_mul_f32 %1, %2, %5\n\tv_fma_f32 %0, -%3, %5, %0\n\tv_fmac_f32 %1, %3, %4" : "=&v"(o_r), "=&v"(o_i) : "v"(a[0]), "v"(a[1]), "v"(h[0]), "v"(h[1]));
    return f32x2{o_r, o_i};
}

#define PK_LO " op_sel:[0,0,0] op_sel_hi:[0,1,1]"
#define PK_HI " op_sel:[1,0,0] op_sel_hi:[1,1,1]"
#define PK_NEG " neg_lo:[1,0,0] neg_hi:[1,0,0]"
__device__ __forceinline__ void pk_cmad(f32x2& o_r, f32x2& o_i, f32x2 a, f32x2 hr, f32x2 hi, f32x2 er, f32x2 ei) {
    asm("v_pk_fma_f32 %0, %2, %3, %5" PK_LO "\n\tv_pk_fma_f32 %1, %2, %4, %6" PK_LO "\n\tv_pk_fma_f32 %0, %2, %4, %0" PK_HI PK_NEG "\n\tv_pk_fma_f32 %1, %2, %3, %1" PK_HI
        : "=&v"(o_r), "=&v"(o_i) : "v"(a), "v"(hr), "v"(hi), "v"(er), "v"(ei));
}
__device__ __forceinline__ void pk_cmac_conj(f32x2& xr, f32x2& xi, f32x2 a, f32x2 hr, f32x2 hi) {
    asm("v_pk_fma_f32 %0, %2, %3, %0" PK_LO "\n\tv_pk_fma_f32 %1, %2, %4, %1" PK_LO "\n\tv_pk_fma_f32 %0, %2, %4, %0" PK_HI "\n\tv_pk_fma_f32 %1, %2, %3, %1" PK_HI PK_NEG
        : "+v"(xr), "+v"(xi) : "v"(a), "v"(hr), "v"(hi));
}
__device__ __forceinline__ void pk_conj_mul(f32x2& o_r, f32x2& o_i, f32x2 a, f32x2 hr, f32x2 hi) {
    asm("v_pk_mul_f32 %0, %2, %3 op_sel:[0,0] op_sel_hi:[0,1]\n\tv_pk_mul_f32 %1, %2, %4 op_sel:[0,0] op_sel_hi:[0,1] neg_lo:[1,0] neg_hi:[1,0]\n\t"
        "v_pk_fma_f32 %0, %2, %4, %0" PK_HI PK_NEG "\n\tv_pk_fma_f32 %1, %2, %3, %1" PK_HI PK_NEG : "=&v"(o_r), "=&v"(o_i) : "v"(a), "v"(hr), "v"(hi));
}
__device__ __forceinline__ void pk_conj_mac(f32x2& xr, f32x2& xi, f32x2 a, f32x2 hr, f32x2 hi) {
    asm("v_pk_fma_f32 %0, %2, %3, %0" PK_LO "\n\tv_pk_fma_f32 %1, %2, %4, %1" PK_LO PK_NEG "\n\tv_pk_fma_f32 %0, %2, %4, %0" PK_HI PK_NEG "\n\tv_pk_fma_f32 %1, %2, %3, %1" PK_HI PK_NEG
        : "+v"(xr), "+v"(xi) : "v"(a), "v"(hr), "v"(hi));
}
__device__ __forceinline__ void cmac2(f32x2& x, f32x2 a, f32x2 h) {
    asm("v_pk_fma_f32 %0, %1, %2, %0" PK_LO "\n\tv_pk_fma_f32 %0, %1, %2, %0 op_sel:[1,1,0] op_sel_hi:[1,0,1] neg_lo:[1,0,0]" : "+v"(x) : "v"(a), "v"(h));
}
#define PLN 0
__device__ __forceinline__ void c2_mac(f32x2& xr, f32x2& xi, float p, float q, float r, float s, f32x2 hr, f32x2 hi) {
    float a = xr[0], b = xi[0], c = xr[1], d = xi[1];
    asm("v_fmac_f32 %0, %4, %8\n\tv_fmac_f32 %1, %6, %9\n\tv_fmac_f32 %2, %4, %10\n\tv_fmac_f32 %3, %6, %11\n\t"
        "v_fmac_f32 %0, %5, %9\n\tv_fmac_f32 %1, %7, %8\n\tv_fmac_f32 %2, %5, %11\n\tv_fmac_f32 %3, %7, %10"
        : "+v"(a), "+v"(b), "+v"(c), "+v"(d) : "v"(p), "v"(q), "v"(r), "v"(s), "v"(hr[0]), "v"(hi[0]), "v"(hr[1]), "v"(hi[1]));
    xr = f32x2{a, c}; xi = f32x2{b, d};
}
__device__ __forceinline__ void c2_mad(f32x2& xr, f32x2& xi, float p, float q, float r, float s, f32x2 hr, f32x2 hi, f32x2 er, f32x2 ei) {
    float a, b, c, d;
    asm("v_fma_f32 %0, %4, %8, %12\n\tv_fma_f32 %1, %6, %9, %13\n\tv_fma_f32 %2, %4, %10, %14\n\tv_fma_f32 %3, %6, %11, %15\n\t"
        "v_fmac_f32 %0, %5, %9\n\tv_fmac_f32 %1, %7, %8\n\tv_fmac_f32 %2, %5, %11\n\tv_fmac_f32 %3, %7, %10"
        : "=&v"(a), "=&v"(b), "=&v"(c), "=&v"(d) : "v"(p), "v"(q), "v"(r), "v"(s), "v"(hr[0]), "v"(hi[0]), "v"(hr[1]), "v"(hi[1]), "v"(er[0]), "v"(ei[0]), "v"(er[1]), "v"(ei[1]));
    xr = f32x2{a, c}; xi = f32x2{b, d};
}
__device__ __forceinline__ void c2_mul(f32x2& xr, f32x2& xi, float p, float q, float r, float s, f32x2 hr, f32x2 hi) {
    float a, b, c, d;
    asm("v_mul_f32 %0, %4, %8\n\tv_mul_f32 %1, %6, %9\n\tv_mul_f32 %2, %4, %10\n\tv_mul_f32 %3, %6, %11\n\t"
        "v_fmac_f32 %0, %5, %9\n\tv_fmac_f32 %1, %7, %8\n\tv_fmac_f32 %2, %5, %11\n\tv_fmac_f32 %3, %7, %10"
        : "=&v"(a), "=&v"(b), "=&v"(c), "=&v"(d) : "v"(p), "v"(q), "v"(r), "v"(s), "v"(hr[0]), "v"(hi[0]), "v"(hr[1]), "v"(hi[1]));
    xr = f32x2{a, c}; xi = f32x2{b, d};
}
__device__ __forceinline__ float mul1(float a, float b) { float c; asm("v_mul_f32 %0, %1, %2" : "=v"(c) : "v"(a), "v"(b)); return c; }
__device__ __forceinline__ float add1(float a, float b) { float c; asm("v_add_f32 %0, %1, %2" : "=v"(c) : "v"(a), "v"(b)); return c; }
__device__ __forceinline__ void mfma_(f32x4& c, bf16x8 a, bf16x8 b) { asm volatile("v_mfma_f32_16x16x32_bf16 %0, %1, %2, %0" : "+v"(c) : "v"(a), "v"(b)); }
__device__ __forceinline__ float sum4(float x) {
    const auto p = __builtin_amdgcn_permlane32_swap(__float_as_uint(x), __float_as_uint(x), false, false);
    x = __uint_as_float(p[0]) + __uint_as_float(p[1]);
    const auto q = __builtin_amdgcn_permlane16_swap(__float_as_uint(x), __float_as_uint(x), false, false);
    return __uint_as_float(q[0]) + __uint_as_float(q[1]);
}

template <int I, int N, class F>
__device__ __forceinline__ void static_for(F&& f) { if constexpr (I < N) { f(std::integral_constant<int, I>{}); static_for<I + 1, N>(f); } }

#define TS_DECL
#define TS0a()
#define TS0b()
#define TSE(i)
#define TS(i)
#define TS_FLUSH()

#define WAIT_V(n) asm volatile("s_waitcnt vmcnt(%0)" ::"n"((n) > 63 ? 63 : (n)) : "memory")
#define WAIT_L(n) asm volatile("s_waitcnt lgkmcnt(%0)" ::"n"((n) > 15 ? 15 : (n)) : "memory")
#define BW 1
constexpr int wenc(int v, int l) { v = v > 63 ? 63 : v; l = l > 15 ? 15 : l; return (v & 15) | 0x70 | (l << 8) | ((v >> 4) << 14); }
#define WAIT_VB(n) do { if constexpr (BW) __builtin_amdgcn_s_waitcnt(wenc(n, 15)); else WAIT_V(n); } while (0)
#define WAIT_LB(n) do { if constexpr (BW) __builtin_amdgcn_s_waitcnt(wenc(63, n)); else WAIT_L(n); } while (0)
#define BAR() asm volatile("s_barrier" ::: "memory")
#define SB() __builtin_amdgcn_sched_barrier(0)
template <class T> __device__ __forceinline__ void tie_(T& x) { asm volatile("" : "+v"(x)); }
#define TIE(x) tie_(x)
template <class T> __device__ __forceinline__ void ties_(T& x) { asm volatile("" : "+s"(x)); }
__device__ __forceinline__ unsigned take(unsigned x) { unsigned y; asm volatile("v_mov_b32 %0, %1" : "=v"(y) : "v"(x)); return y; }
#define DEF_LD(name, ins) template <int OFF, class T> __device__ __forceinline__ void name(T& dst, unsigned voff, const char* sb) { \
    asm volatile(ins : "=v"(dst) : "v"(voff), "s"(sb), "n"(OFF) : "memory"); }
DEF_LD(gld_u16_, "global_load_ushort %0, %1, %2 offset:%3")
DEF_LD(gld_b32_, "global_load_dword %0, %1, %2 offset:%3")
DEF_LD(gld_b64_, "global_load_dwordx2 %0, %1, %2 offset:%3")
DEF_LD(gld_b128_, "global_load_dwordx4 %0, %1, %2 offset:%3")
#define NTQ " nt"
#define NTK " nt"
DEF_LD(gld_b64_nt_, "global_load_dwordx2 %0, %1, %2 offset:%3" NTQ)
DEF_LD(gld_b128_nt_, "global_load_dwordx4 %0, %1, %2 offset:%3" NTQ)
DEF_LD(gld_b64_sc1_, "global_load_dwordx2 %0, %1, %2 offset:%3 sc1")
DEF_LD(gld_b128_sc1_, "global_load_dwordx4 %0, %1, %2 offset:%3 sc1")
#define DEF_ST(name, ins) template <int OFF, class T> __device__ __forceinline__ void name(unsigned voff, const T& v, const char* sb) { \
    asm volatile(ins :: "v"(voff), "v"(v), "s"(sb), "n"(OFF) : "memory"); }
DEF_ST(gst_b128_, "global_store_dwordx4 %0, %1, %2 offset:%3\n\ts_nop 1")
DEF_ST(gst_b128_nt_, "global_store_dwordx4 %0, %1, %2 offset:%3 nt\n\ts_nop 1")
DEF_ST(gst_b128_sc1_, "global_store_dwordx4 %0, %1, %2 offset:%3 sc1\n\ts_nop 1")
DEF_ST(gst_b64_sc1_, "global_store_dwordx2 %0, %1, %2 offset:%3 sc1")
#define GLD_U16(dst, voff, sb) gld_u16_<0>(dst, voff, sb)
#define GLD_B32(dst, voff, sb, off) gld_b32_<(off)>(dst, voff, sb)
#define GLD_B64(dst, voff, sb, off) gld_b64_<(off)>(dst, voff, sb)
#define GLD_B128(dst, voff, sb, off) gld_b128_<(off)>(dst, voff, sb)
#define GLD_B64_NT(dst, voff, sb, off) gld_b64_nt_<(off)>(dst, voff, sb)
#define GLD_B128_NT(dst, voff, sb, off) gld_b128_nt_<(off)>(dst, voff, sb)
#define GLD_B64_SC1(dst, voff, sb, off) gld_b64_sc1_<(off)>(dst, voff, sb)
#define GLD_B128_SC1(dst, voff, sb, off) gld_b128_sc1_<(off)>(dst, voff, sb)
#define GST_B128(voff, v, sb, off) gst_b128_<(off)>(voff, v, sb)
#define GST_B128_NT(voff, v, sb, off) gst_b128_nt_<(off)>(voff, v, sb)
#define GST_B128_SC1(voff, v, sb, off) gst_b128_sc1_<(off)>(voff, v, sb)
#define GST_B64_SC1(voff, v, sb) gst_b64_sc1_<0>(voff, v, sb)
__device__ __forceinline__ void dma_(unsigned ldsaddr, unsigned voff, const char* sb) {
    asm volatile("s_mov_b32 m0, %0\n\ts_nop 0\n\tglobal_load_lds_dwordx4 %1, %2" :: "s"(ldsaddr), "v"(voff), "s"(sb) : "memory"); }
__device__ __forceinline__ void dma_nt_(unsigned ldsaddr, unsigned voff, const char* sb) {
    asm volatile("s_mov_b32 m0, %0\n\ts_nop 0\n\tglobal_load_lds_dwordx4 %1, %2 nt" :: "s"(ldsaddr), "v"(voff), "s"(sb) : "memory"); }
#define DF 0
#define DFN 4
#define FL_0 ""
#define FL_1 " nt"
#define FL_2 " sc1"
#define FL_3 " sc1 nt"
#define FL_4 " sc0 sc1"
#define FL_5 " sc0 sc1 nt"
#define FL_6 " sc0 nt"
#define FLX_(x) FL_##x
#define FLX(x) FLX_(x)
__device__ __forceinline__ void dmaf_(unsigned ldsaddr, unsigned voff, const char* sb) {
    asm volatile("s_mov_b32 m0, %0\n\ts_nop 0\n\tglobal_load_lds_dwordx4 %1, %2" FLX(DF) :: "s"(ldsaddr), "v"(voff), "s"(sb) : "memory"); }
template <int OFF> __device__ __forceinline__ void dmaof_(unsigned voff, const char* sb) {
    asm volatile("global_load_lds_dwordx4 %0, %1 offset:%2" FLX(DF) :: "v"(voff), "s"(sb), "n"(OFF) : "memory"); }
#define DMA(ldsaddr, voff, sb) dma_(ldsaddr, voff, sb)
template <int OFF> __device__ __forceinline__ void dmao_(unsigned voff, const char* sb) {
    asm volatile("global_load_lds_dwordx4 %0, %1 offset:%2" :: "v"(voff), "s"(sb), "n"(OFF) : "memory"); }
template <int OFF, class T> __device__ __forceinline__ void dsr128_(T& dst, unsigned addr) { asm volatile("ds_read_b128 %0, %1 offset:%2" : "=v"(dst) : "v"(addr), "n"(OFF)); }
template <int OFF, class T> __device__ __forceinline__ void dsr64_(T& dst, unsigned addr) { asm volatile("ds_read_b64 %0, %1 offset:%2" : "=v"(dst) : "v"(addr), "n"(OFF)); }
template <int OFF, class T> __device__ __forceinline__ void dsr2_(T& dst, unsigned addr) { asm volatile("ds_read2_b32 %0, %1 offset0:%2 offset1:%3" : "=v"(dst) : "v"(addr), "n"(OFF), "n"(OFF + 1)); }
#define DSR128(dst, addr, off) dsr128_<(off)>(dst, addr)
#define DSR64(dst, addr, off) dsr64_<(off)>(dst, addr)

__device__ __forceinline__ void kv4(unsigned& o01, unsigned& o23, unsigned ka, unsigned kb, unsigned kc, unsigned kd, const unsigned* w, unsigned v0, unsigned v1) {
    unsigned t1, t3, p0, p1, p2, p3;
    asm(
        "v_dot2_f32_bf16 %0, %8, %19, 0\n\tv_dot2_f32_bf16 %1, %9, %18, 0\n\tv_dot2_f32_bf16 %2, %9, %19, 0\n\tv_dot2_f32_bf16 %3, %10, %18, 0\n\t"
        "v_dot2_f32_bf16 %4, %9, %16, 0\n\tv_dot2_f32_bf16 %5, %9, %17, 0\n\tv_dot2_f32_bf16 %6, %10, %16, 0\n\tv_dot2_f32_bf16 %7, %10, %17, 0\n\t"
        "v_cvt_pk_bf16_f32 %0, %0, %4\n\tv_cvt_pk_bf16_f32 %1, %1, %5\n\tv_cvt_pk_bf16_f32 %2, %2, %6\n\tv_cvt_pk_bf16_f32 %3, %3, %7\n\t"
        "v_dot2_f32_bf16 %4, %9, %15, 0\n\tv_dot2_f32_bf16 %5, %10, %14, 0\n\tv_dot2_f32_bf16 %6, %10, %15, 0\n\tv_dot2_f32_bf16 %7, %11, %14, 0\n\t"
        "v_dot2_f32_bf16 %0, %0, %22, 0\n\tv_dot2_f32_bf16 %1, %1, %22, 0\n\tv_dot2_f32_bf16 %2, %2, %22, 0\n\tv_dot2_f32_bf16 %3, %3, %22, 0\n\t"
        "v_cvt_pk_bf16_f32 %0, %0, %4\n\tv_cvt_pk_bf16_f32 %1, %1, %5\n\tv_cvt_pk_bf16_f32 %2, %2, %6\n\tv_cvt_pk_bf16_f32 %3, %3, %7\n\t"
        "v_dot2_f32_bf16 %4, %10, %12, 0\n\tv_dot2_f32_bf16 %5, %10, %13, 0\n\tv_dot2_f32_bf16 %6, %11, %12, 0\n\tv_dot2_f32_bf16 %7, %11, %13, 0\n\t"
        "v_dot2_f32_bf16 %0, %0, %22, 0\n\tv_dot2_f32_bf16 %1, %1, %22, 0\n\tv_dot2_f32_bf16 %2, %2, %22, 0\n\tv_dot2_f32_bf16 %3, %3, %22, 0\n\t"
        "v_cvt_pk_bf16_f32 %0, %0, %4\n\tv_cvt_pk_bf16_f32 %1, %1, %5\n\tv_cvt_pk_bf16_f32 %2, %2, %6\n\tv_cvt_pk_bf16_f32 %3, %3, %7\n\t"
        "v_dot2_f32_bf16 %0, %0, %22, 0\n\tv_dot2_f32_bf16 %1, %1, %22, 0\n\tv_dot2_f32_bf16 %2, %2, %22, 0\n\tv_dot2_f32_bf16 %3, %3, %22, 0\n\t"
        "v_cvt_pk_bf16_f32 %0, %0, 0\n\tv_cvt_pk_bf16_f32 %1, 0, %1\n\tv_cvt_pk_bf16_f32 %2, %2, 0\n\tv_cvt_pk_bf16_f32 %3, 0, %3\n\t"
        "v_dot2_f32_bf16 %0, %0, %20, 0\n\tv_dot2_f32_bf16 %1, %1, %20, 0\n\tv_dot2_f32_bf16 %2, %2, %21, 0\n\tv_dot2_f32_bf16 %3, %3, %21, 0\n\t"
        "s_nop 1\n\tv_cvt_pk_bf16_f32 %0, %1, %0\n\tv_cvt_pk_bf16_f32 %2, %3, %2\n\t"
        : "=&v"(o01), "=&v"(t1), "=&v"(o23), "=&v"(t3), "=&v"(p0), "=&v"(p1), "=&v"(p2), "=&v"(p3)
        : "v"(ka), "v"(kb), "v"(kc), "v"(kd), "v"(w[0]), "v"(w[1]), "v"(w[2]), "v"(w[3]), "v"(w[4]), "v"(w[5]), "v"(w[6]), "v"(w[7]), "v"(v0), "v"(v1), "s"(0x3f803f80u));
}
__device__ __forceinline__ void kv4a(unsigned* t, unsigned* e, unsigned ka, unsigned kb, unsigned kc, unsigned kd, const unsigned* w) {
    asm volatile(
        "v_and_b32 %8, %21, %15\n\tv_lshlrev_b32 %9, 16, %16\n\tv_and_b32 %10, %21, %16\n\tv_lshlrev_b32 %11, 16, %17\n\t"
        "v_and_b32 %12, %21, %17\n\tv_mul_f32 %0, %20, %8\n\tv_mul_f32 %1, %20, %9\n\tv_mul_f32 %2, %20, %10\n\t"
        "v_mul_f32 %3, %20, %11\n\tv_mul_f32 %4, %19, %9\n\tv_mul_f32 %5, %19, %10\n\tv_mul_f32 %6, %19, %11\n\t"
        "v_mul_f32 %7, %19, %12\n\tv_lshlrev_b32 %13, 16, %18\n\tv_and_b32 %14, %21, %18\n\tv_cvt_pk_bf16_f32 %0, 0, %0\n\t"
        "v_cvt_pk_bf16_f32 %1, 0, %1\n\tv_cvt_pk_bf16_f32 %2, 0, %2\n\tv_cvt_pk_bf16_f32 %3, 0, %3\n\tv_cvt_pk_bf16_f32 %4, 0, %4\n\t"
        "v_cvt_pk_bf16_f32 %5, 0, %5\n\tv_cvt_pk_bf16_f32 %6, 0, %6\n\tv_cvt_pk_bf16_f32 %7, 0, %7\n\t"
        : "=&v"(t[0]), "=&v"(t[1]), "=&v"(t[2]), "=&v"(t[3]), "=&v"(t[4]), "=&v"(t[5]), "=&v"(t[6]), "=&v"(t[7]), "=&v"(e[0]), "=&v"(e[1]), "=&v"(e[2]), "=&v"(e[3]), "=&v"(e[4]), "=&v"(e[5]), "=&v"(e[6])
        : "v"(ka), "v"(kb), "v"(kc), "v"(kd), "v"(w[2]), "v"(w[3]), "s"(0xffff0000u));
}
__device__ __forceinline__ void kv4b(unsigned* t, const unsigned* e, const unsigned* w) {
    asm volatile(
        "v_add_f32 %0, %0, %4\n\tv_add_f32 %1, %1, %5\n\tv_add_f32 %2, %2, %6\n\tv_add_f32 %3, %3, %7\n\t"
        "v_mul_f32 %4, %14, %8\n\tv_mul_f32 %5, %14, %9\n\tv_mul_f32 %6, %14, %10\n\tv_mul_f32 %7, %14, %11\n\t"
        "v_cvt_pk_bf16_f32 %0, 0, %0\n\tv_cvt_pk_bf16_f32 %1, 0, %1\n\tv_cvt_pk_bf16_f32 %2, 0, %2\n\tv_cvt_pk_bf16_f32 %3, 0, %3\n\t"
        "v_cvt_pk_bf16_f32 %4, 0, %4\n\tv_cvt_pk_bf16_f32 %5, 0, %5\n\tv_cvt_pk_bf16_f32 %6, 0, %6\n\tv_cvt_pk_bf16_f32 %7, 0, %7\n\t"
        "v_add_f32 %0, %0, %4\n\tv_add_f32 %1, %1, %5\n\tv_add_f32 %2, %2, %6\n\tv_add_f32 %3, %3, %7\n\t"
        "v_mul_f32 %4, %13, %9\n\tv_mul_f32 %5, %13, %10\n\tv_mul_f32 %6, %13, %11\n\tv_mul_f32 %7, %13, %12\n\t"
        : "+v"(t[0]), "+v"(t[1]), "+v"(t[2]), "+v"(t[3]), "+v"(t[4]), "+v"(t[5]), "+v"(t[6]), "+v"(t[7]) : "v"(e[2]), "v"(e[3]), "v"(e[4]), "v"(e[5]), "v"(e[6]), "v"(w[0]), "v"(w[1]));
}
__device__ __forceinline__ void kv4c(unsigned* t, unsigned v0, unsigned v1) {
    asm volatile(
        "v_cvt_pk_bf16_f32 %0, 0, %0\n\tv_cvt_pk_bf16_f32 %1, 0, %1\n\tv_cvt_pk_bf16_f32 %2, 0, %2\n\tv_cvt_pk_bf16_f32 %3, 0, %3\n\t"
        "v_cvt_pk_bf16_f32 %4, 0, %4\n\tv_cvt_pk_bf16_f32 %5, 0, %5\n\tv_cvt_pk_bf16_f32 %6, 0, %6\n\tv_cvt_pk_bf16_f32 %7, 0, %7\n\t"
        "v_add_f32 %0, %0, %4\n\tv_add_f32 %1, %1, %5\n\tv_add_f32 %2, %2, %6\n\tv_add_f32 %3, %3, %7\n\t"
        "v_lshlrev_b32 %4, 16, %8\n\tv_and_b32 %5, %10, %8\n\tv_lshlrev_b32 %6, 16, %9\n\tv_and_b32 %7, %10, %9\n\t"
        "v_cvt_pk_bf16_f32 %0, 0, %0\n\tv_cvt_pk_bf16_f32 %1, 0, %1\n\tv_cvt_pk_bf16_f32 %2, 0, %2\n\tv_cvt_pk_bf16_f32 %3, 0, %3\n\t"
        "v_mul_f32 %0, %0, %4\n\tv_mul_f32 %1, %1, %5\n\tv_mul_f32 %2, %2, %6\n\tv_mul_f32 %3, %3, %7\n\t"
        "v_cvt_pk_bf16_f32 %0, %1, %0\n\tv_cvt_pk_bf16_f32 %2, %3, %2\n\t"
        : "+v"(t[0]), "+v"(t[1]), "+v"(t[2]), "+v"(t[3]), "+v"(t[4]), "+v"(t[5]), "+v"(t[6]), "+v"(t[7]) : "v"(v0), "v"(v1), "s"(0xffff0000u));
}
__device__ __forceinline__ void kf4a(unsigned* t, unsigned* e, unsigned ka, unsigned kb, unsigned kc, unsigned kd, const unsigned* w) {
    asm volatile(
        "v_and_b32 %4, %16, %11\n\tv_lshlrev_b32 %5, 16, %12\n\tv_and_b32 %6, %16, %12\n\tv_lshlrev_b32 %7, 16, %13\n\t"
        "v_and_b32 %8, %16, %13\n\tv_mul_f32 %0, %15, %4\n\tv_mul_f32 %1, %15, %5\n\tv_mul_f32 %2, %15, %6\n\t"
        "v_mul_f32 %3, %15, %7\n\tv_lshlrev_b32 %9, 16, %14\n\tv_and_b32 %10, %16, %14\n\t"
        : "=&v"(t[0]), "=&v"(t[1]), "=&v"(t[2]), "=&v"(t[3]), "=&v"(e[0]), "=&v"(e[1]), "=&v"(e[2]), "=&v"(e[3]), "=&v"(e[4]), "=&v"(e[5]), "=&v"(e[6])
        : "v"(ka), "v"(kb), "v"(kc), "v"(kd), "v"(w[3]), "s"(0xffff0000u));
}
__device__ __forceinline__ void kf4b(unsigned* t, const unsigned* e, const unsigned* w) {
    asm volatile(
        "v_fmac_f32 %0, %12, %4\n\tv_fmac_f32 %1, %12, %5\n\tv_fmac_f32 %2, %12, %6\n\tv_fmac_f32 %3, %12, %7\n\t"
        "v_fmac_f32 %0, %11, %5\n\tv_fmac_f32 %1, %11, %6\n\tv_fmac_f32 %2, %11, %7\n\tv_fmac_f32 %3, %11, %8\n\t"
        "v_fmac_f32 %0, %10, %6\n\tv_fmac_f32 %1, %10, %7\n\tv_fmac_f32 %2, %10, %8\n\tv_fmac_f32 %3, %10, %9\n\t"
        : "+v"(t[0]), "+v"(t[1]), "+v"(t[2]), "+v"(t[3]) : "v"(e[1]), "v"(e[2]), "v"(e[3]), "v"(e[4]), "v"(e[5]), "v"(e[6]), "v"(w[0]), "v"(w[1]), "v"(w[2]));
}
__device__ __forceinline__ void kf4c(unsigned* t, unsigned v0, unsigned v1) {
    asm volatile(
        "v_lshlrev_b32 %4, 16, %8\n\tv_and_b32 %5, %10, %8\n\tv_lshlrev_b32 %6, 16, %9\n\tv_and_b32 %7, %10, %9\n\t"
        "v_mul_f32 %0, %0, %4\n\tv_mul_f32 %1, %1, %5\n\tv_mul_f32 %2, %2, %6\n\tv_mul_f32 %3, %3, %7\n\t"
        "v_cvt_pk_bf16_f32 %0, %1, %0\n\tv_cvt_pk_bf16_f32 %2, %3, %2\n\t"
        : "+v"(t[0]), "+v"(t[1]), "+v"(t[2]), "+v"(t[3]), "=&v"(t[4]), "=&v"(t[5]), "=&v"(t[6]), "=&v"(t[7]) : "v"(v0), "v"(v1), "s"(0xffff0000u));
}
__device__ __forceinline__ f32x4 mul4bf(u32x2 x, const unsigned* d) {
    float a, b, c, e;
    asm("v_dot2_f32_bf16 %0, %4, %6, 0\n\tv_dot2_f32_bf16 %1, %4, %7, 0\n\tv_dot2_f32_bf16 %2, %5, %8, 0\n\tv_dot2_f32_bf16 %3, %5, %9, 0\n\t"
        "v_cvt_pk_bf16_f32 %0, 0, %0\n\tv_cvt_pk_bf16_f32 %1, 0, %1\n\tv_cvt_pk_bf16_f32 %2, 0, %2\n\tv_cvt_pk_bf16_f32 %3, 0, %3"
        : "=&v"(a), "=&v"(b), "=&v"(c), "=&v"(e) : "v"(x[0]), "v"(x[1]), "v"(d[0]), "v"(d[1]), "v"(d[2]), "v"(d[3]));
    return f32x4{a, b, c, e};
}
template <int BIT> __device__ __forceinline__ void quad_swap(u32x4& a, u32x4& b) {
    unsigned n0, n1, n2, n3, b0 = b[0], b1 = b[1], b2 = b[2], b3 = b[3];
#define QS_(d, s, k) "v_cndmask_b32_dpp " d ", " s ", " k ", vcc quad_perm:[%14,%15,%16,%17] row_mask:0xf bank_mask:0xf\n\t"
    asm("s_mov_b64 vcc, %12\n\ts_nop 0\n\t" QS_("%0", "%4", "%8") QS_("%1", "%5", "%9") QS_("%2", "%6", "%10") QS_("%3", "%7", "%11")
        "s_mov_b64 vcc, %13\n\t" QS_("%4", "%8", "%4") QS_("%5", "%9", "%5") QS_("%6", "%10", "%6") QS_("%7", "%11", "%7")
        : "=&v"(n0), "=&v"(n1), "=&v"(n2), "=&v"(n3), "+v"(b0), "+v"(b1), "+v"(b2), "+v"(b3)
        : "v"(a[0]), "v"(a[1]), "v"(a[2]), "v"(a[3]), "s"(BIT ? 0x3333333333333333ull : 0x5555555555555555ull), "s"(BIT ? 0xccccccccccccccccull : 0xaaaaaaaaaaaaaaaaull),
          "n"(BIT ? 2 : 1), "n"(BIT ? 3 : 0), "n"(BIT ? 0 : 3), "n"(BIT ? 1 : 2) : "vcc");
#undef QS_
    a = u32x4{n0, n1, n2, n3}; b = u32x4{b0, b1, b2, b3};
}
__device__ __forceinline__ void kv_dma(unsigned ldsaddr, unsigned k0, unsigned k1, unsigned k2, unsigned k3, const char* sb) {
    asm volatile("s_mov_b32 m0, %5\n\ts_nop 0\n\tglobal_load_lds_dwordx4 %0, %4" NTK "\n\tglobal_load_lds_dwordx4 %1, %4 offset:1024" NTK "\n\t"
                 "global_load_lds_dwordx4 %2, %4 offset:2048" NTK "\n\tglobal_load_lds_dwordx4 %3, %4 offset:3072" NTK
                 :: "v"(k0), "v"(k1), "v"(k2), "v"(k3), "s"(sb), "s"(ldsaddr) : "memory");
}
#define IF_(c, C) "s_cmp_lg_u32 " c ", " C "\n\ts_cbranch_scc1 1f\n\t"
template <int C, int N> __device__ __forceinline__ void wait_if(int c) { asm volatile(IF_("%0", "%1") "s_waitcnt vmcnt(%2)\n1:" :: "s"(c), "n"(C), "n"(N) : "scc", "memory"); }
template <int C, int M> __device__ __forceinline__ void flag_if(int c, unsigned voff, u32x2 v, const char* sb) {
    asm volatile(IF_("%0", "%1") "s_mov_b32 exec_lo, %5\n\ts_mov_b32 exec_hi, 0\n\tglobal_store_dwordx2 %2, %3, %4 sc1\n\ts_mov_b64 exec, -1\n1:" :: "s"(c), "n"(C), "v"(voff), "v"(v), "s"(sb), "n"(M) : "scc", "memory"); }
template <int C> __device__ __forceinline__ void st4_if(int c, unsigned voff, const u32x4* v, const char* s0, const char* s1) {
    asm volatile(IF_("%0", "%1") "global_store_dwordx4 %2, %3, %7 sc1\n\tglobal_store_dwordx4 %2, %4, %7 offset:1024 sc1\n\tglobal_store_dwordx4 %2, %5, %8 sc1\n\tglobal_store_dwordx4 %2, %6, %8 offset:1024 sc1\n\ts_nop 1\n1:"
                 :: "s"(c), "n"(C), "v"(voff), "v"(v[0]), "v"(v[1]), "v"(v[2]), "v"(v[3]), "s"(s0), "s"(s1) : "scc", "memory"); }
template <int C> __device__ __forceinline__ void ld4_if(int c, unsigned voff, u32x4* v, const char* s0) {
    asm volatile(IF_("%4", "%5") "global_load_dwordx4 %0, %6, %7 nt\n\tglobal_load_dwordx4 %1, %6, %7 offset:1024 nt\n\tglobal_load_dwordx4 %2, %6, %7 offset:2048 nt\n\tglobal_load_dwordx4 %3, %6, %7 offset:3072 nt\n1:"
                 : "+v"(v[0]), "+v"(v[1]), "+v"(v[2]), "+v"(v[3]) : "s"(c), "n"(C), "v"(voff), "s"(s0) : "scc", "memory"); }
template <int C> __device__ __forceinline__ void ld8_if(int c, unsigned voff, u32x2* v, const char* s0, const char* s1) {
    asm volatile(IF_("%8", "%9") "global_load_dwordx2 %0, %10, %11 offset:-4096 nt\n\tglobal_load_dwordx2 %1, %10, %11 offset:-2048 nt\n\tglobal_load_dwordx2 %2, %10, %11 nt\n\tglobal_load_dwordx2 %3, %10, %11 offset:2048 nt\n\t"
                 "global_load_dwordx2 %4, %10, %12 offset:-4096 nt\n\tglobal_load_dwordx2 %5, %10, %12 offset:-2048 nt\n\tglobal_load_dwordx2 %6, %10, %12 nt\n\tglobal_load_dwordx2 %7, %10, %12 offset:2048 nt\n1:"
                 : "+v"(v[0]), "+v"(v[1]), "+v"(v[2]), "+v"(v[3]), "+v"(v[4]), "+v"(v[5]), "+v"(v[6]), "+v"(v[7]) : "s"(c), "n"(C), "v"(voff), "s"(s0), "s"(s1) : "scc", "memory"); }
template <int E> __device__ __forceinline__ void sy_post(unsigned myaddr) { asm volatile("ds_write_b32 %0, %0 offset:%1" :: "v"(myaddr), "n"(E * 64) : "memory"); }
template <int E, int SL = 1> __device__ __forceinline__ unsigned sy_wait(unsigned addr) {
    unsigned v;
    asm volatile("s_branch 2f\n1:\n\ts_sleep %3\n2:\n\tds_read_b32 %0, %1 offset:%2\n\ts_waitcnt lgkmcnt(0)\n\tv_cmp_eq_u32 vcc, 0, %0\n\ts_cbranch_vccnz 1b"
                 : "=&v"(v) : "v"(addr), "n"(E * 64), "n"(SL) : "vcc", "memory");
    return v;
}
template <int E> __device__ __forceinline__ void sy_pre(unsigned& v, unsigned addr) { asm volatile("ds_read_b32 %0, %1 offset:%2" : "=v"(v) : "v"(addr), "n"(E * 64) : "memory"); }
template <int E> __device__ __forceinline__ void sy_chk(unsigned& v, unsigned addr) {
    asm volatile("s_branch 2f\n1:\n\tds_read_b32 %0, %1 offset:%2\n\ts_waitcnt lgkmcnt(0)\n2:\n\tv_cmp_eq_u32 vcc, 0, %0\n\ts_cbranch_vccnz 1b"
                 : "+v"(v) : "v"(addr), "n"(E * 64) : "vcc", "memory");
}
template <int OFF, class T> __device__ __forceinline__ void dsrtr_(T& dst, unsigned addr) { asm volatile("ds_read_b64_tr_b16 %0, %1 offset:%2" : "=v"(dst) : "v"(addr), "n"(OFF)); }

constexpr size_t WS_H = 0, WS_KVT = (size_t)10 << 20, WS_ZT = (size_t)15 << 20, WS_E = (size_t)20 << 20, WS_FLAG = WS_E + (2048 << 10);
#define HP 65536
#define KP 8192
#define ZP 4096
constexpr int FL_KV = 0, FL_Z = 65536, FL_H = 131072, FL_EPOCH = 196608;
constexpr unsigned SPIN_CAP = 1u << 17;
#define ABL 0
#define CSPLIT 1
#define MERGE 3
#define CB 13
#define PAUSE 0
#define QLATE 0
#define CVPRIO 0
#define STG_SEL (idx & 3)
#define SP0 3
#define SDP 1
#define LP0 3
#define LDP 2
#define KVS 5
#define KVU 3
#define QIT 0
#define ZB 3
#define OS 2
#define MPRIO 2

#define ROT 0x4042
#define R2 0x14
#define CPF 0
#define PRE 4
#define PRO 9
#define G1NT 0
#define G1NTS 16
#define TMIX 0
#define TLBT 0
#define CDEF 0
#define CVLATE 1
#define FPRIO 3
#define YS 0
#define DP0 1
#define DP1 5
#define AWB 1
#define PK 0
#define PU 15
#define PC0 13
#define PC1 14
#define PC2 16
#define PC3 16
#define PQ 0
#define SPF 0
#define SPH 0
#define NB1 4
#define EP 0
#define ESL 0
#define CFB 0
#define P1 1
#define P0 1
#define BKE 0
#define EALL 0
#define EREP 0
#define TOUCH 0
#define FILLB 0
#define CPOST 1
#define CEARLY 0
#define NOOWN 0
#define PFIRST 0
#define ZB0 0
#define CATCH 4
#define PWE 0
#define SWAP 1
#define KVM 0x0FE
#define KFD 3
#define ZW 2
#define ZX 1
#define ZP0 3
#define BARM 0x22220
#define NR 6
#define WPS 2
#define CD0 3
#define AWX 8
#define VBM 0
#define ZOWN 1
#define ZPS 4
#define XAB 0
#define QM 0
#define OSG 0
#define KPL 0xFE
#define KPRIO 1
#define GPRIO 1
#define PST 0x7654
#define PLT 0xB964
#define LC 0
#define PRE2 PRE
#define KVD 0
#define PAIR 1
#define G2P1 0
#define EPRIO 0
#define LSW 1
#define EGX 28
#define CR1 0
#define CR2 0
#define CR3 0
#define CR4 0
#define CR5 0
#define CRL (2 * NR - CR1 - CR2 - CR3 - CR4 - CR5)
#define UE 0
#define UPRIO 3
#define AMF 7
#define M1 1
#define W2 1
#define W1 0
#define NBK 3
#define XB 4
#define KFB 9
#define APRE 0
#define CB0 1
#define AP0 8
#define HTSE(i)
#define KAT 2
#define SD 2
#define KV3 0x088888
#define KOWN 1
#define HKV 0
#define KVP NBK
#define LZ 1
#define AWN 8
#define CRATE 2
#define ZSB 0
#define ZFB 1
#define ZSL 4
#define OSL 0
constexpr int it_cnt(int p, int k, int last) {
    const int pos[8] = {PU, PK, PC0, PC1, PQ, PU - 1, PC2, PC3}, n[8] = {4, 4, 4, 4, 8, 1, 4, 4};
    int c = 0;
    for (int i = 0; i < 8; ++i) if (pos[i] <= last && (pos[i] > p || (pos[i] == p && i > k))) c += n[i];
    return c;
}
constexpr int nib(int t, int c) { return (t >> (4 * c)) & 15; }
constexpr bool nib_any(int t, int v) { return nib(t, 0) == v || nib(t, 1) == v || nib(t, 2) == v || nib(t, 3) == v; }
constexpr int kv_wait(int late) {
    int t = 0;
    for (int c = 0; c < 4; ++c) t |= ((nib(PLT, c) >= nib(PST, c) && nib(PLT, c) < nib(PST, c) + KFD) == late ? nib(PST, c) + KFD : 15) << (4 * c);
    return t;
}
constexpr unsigned long long sl_seq(int i, int inv) {
    int q[16] = {};
    const int near[4] = {1, 5, 6, SN}, far[13] = {2, 3, 4, 7, 8, 9, 10, 11, 12, 13, 14, 15, 0};
    for (int k = 0; k < 4; ++k) q[near[k]] = (i + k) & 3;
    for (int k = 0, n = 0; k < 13; ++k) if (far[k] != SN) q[far[k]] = 4 + (n++ + SQ * i) % 12;
    unsigned long long t = 0;
    for (int k = 0; k < 16; ++k) t |= inv ? (unsigned long long)k << (4 * q[k]) : (unsigned long long)q[k] << (4 * k);
    return t;
}
constexpr int G1STAGE = 32768;
constexpr int g1_slot(int s) { return (NB1 == 5 ? (s + 3) % 5 : s & 3) * G1STAGE; }
constexpr int L_KS = NB1 == 5 ? 65536 : 131072, L_VS = L_KS + 16384;
constexpr int L_TS = 3 * G1STAGE, L_TR = L_TS + 3072;
constexpr int CST = 4288;
constexpr int UOFF = 2 * CST + 128, UPL = 2304;
constexpr int CHSZ = UOFF + 2 * UPL + 32;
constexpr int L_AGG = 4 * CHSZ, L_ES = L_AGG + 15 * 512, L_SY = L_ES + 2048;
constexpr int L_HT = 98304;
constexpr int L_C = 98304, G2STAGE = 16384;
constexpr int L_Y = 16384;
static_assert(L_SY + 448 <= 65536 && L_AGG >= 49152 && UOFF % 16 == 0 && CHSZ % 256 == 32, "lds");

__device__ __forceinline__ int img_off(int r, int c) { return (r >> 1) * 256 + ((((r & 1) * 8 + c) ^ ((r >> 1) & 15)) << 4); }

constexpr int g1_upto(int g) {
    int n = 4 * NBK;
    for (int t = SD; t <= g; ++t) {
        int sb = (t + 1) / 2, sub = (t + 1) % 2, allowed = sb ? 4 * (sb + 3) + 2 * (sub + 1) : 12;
        if (allowed > 64) allowed = 64;
        n = n + CATCH < allowed ? n + CATCH : allowed;
    }
    return n;
}
constexpr int g1_cnt(int s) { return HKV || s >= KVP ? g1_upto(2 * s - 2) - 4 * (s + 1) : s >= KAT ? g1_upto(2 * s - 2) - 4 * KVP : g1_upto(2 * s - 2) - 4 * (s + 1) + 4; }
static_assert(KVP >= 1 && KVP <= NBK && AP0 % 4 == 0, "schedule");
static_assert(g1_upto(28) == 64, "schedule");
constexpr int AW = AWN;
constexpr int g2_place(int s) { return s < 5 ? L_C + ((s + 2) & 3) * G2STAGE : s < 7 ? (s - 1) * G2STAGE : s < 10 ? (9 - s) * G2STAGE : g2_place(s - 9); }
constexpr bool g2_bar(int g) { return (BARM >> g) & 1; }
struct G2Plan { int upto[33], wr[33], ald[64][2], seq[164], use[64]; };
constexpr G2Plan g2_plan() {
    G2Plan p = {};
    int nc = 14 + 2 * NR, nw = 0, nx = 4, lastb = 0, n = 0;
    for (int i = 0; i < 164; ++i) p.seq[i] = 1000;
    for (; nx < 4 + AW; ++nx) p.seq[nx] = ++n;
    for (int t = 0; t < 32; ++t) {
        p.upto[t] = nc; p.wr[t] = nw;
        if (t < 2) continue;
        if (g2_bar(t)) lastb = t;
        const int w = AW + nw < AWX ? AW + nw : AWX;
        for (int f = 2 * t; f < 2 * t + 2; ++f) {
            p.use[f] = n;
            if (nc < 32 && nc == p.upto[t] + (f & 1) && (f & 1) < CRATE && t >= CD0 && lastb >= (nc < 20 ? 1 : (nc | 1) - 18)) p.seq[100 + nc++] = ++n;
            for (int k = 0; k < 2; ++k) {
                p.ald[f][k] = -1;
                if (nx < 64 && nx - f - k < w) { p.ald[f][k] = nx; p.seq[nx++] = ++n; }
            }
        }
        for (int k = 0; k < WPS; ++k) if (nw < 2 * NR && lastb >= (nw < 6 ? 1 : (nw | 1) - 4)) ++nw;
    }
    p.upto[32] = nc; p.wr[32] = nw;
    return p;
}
constexpr G2Plan G2P = g2_plan();
constexpr int g2_upto(int g) { return G2P.upto[g + 1]; }
constexpr int g2_wr(int g) { return G2P.wr[g + 1]; }
constexpr int g2_ald(int f, int k) { return G2P.ald[f][k]; }
constexpr int g2_cnt(int id, int at) { return G2P.use[at] - G2P.seq[id]; }
constexpr int LASTB = 31 - __builtin_clz(BARM);
constexpr int lz_post() { int g = LASTB; while (G2P.seq[2 * g] < G2P.seq[131]) ++g; return g; }
constexpr int lz_chk() { int s = 7 + NR; while (G2P.seq[100 + 2 * s + 1] < G2P.seq[2 * LASTB]) ++s; return 2 * s - 1; }
static_assert(!LZ || (AWB && lz_post() + 1 < lz_chk() && lz_chk() < EGX), "schedule");
constexpr int g2_cover(int g) { int s = (g + 1) / 2; while (s < 15 && !g2_bar(2 * s + 1)) ++s; return s; }
static_assert(BARM >> 17 && g2_cover(3) < 7 && CRATE <= 2, "the epilogue needs stage 8 behind a barrier");

constexpr int cv_m(int o) { return (o & 1) ? 7 - (o >> 1) : o >> 1; }
constexpr int cv_first(int o) { int p = 0; for (int i = 0; i < o; ++i) p += 8 - cv_m(i); return p; }
constexpr int cv_grp(int p) { int o = 0; while (cv_first(o + 1) <= p) ++o; return o; }
constexpr int cv_read(int p) {
    int slot[40] = {};
    for (int i = 0; i < 40; ++i) slot[i] = -1;
    slot[0] = 2 * 3 + 1;
    for (int o = 0; o < 8; ++o) {
        int q = cv_first(o + 1) - 1;
        for (int h = 0; h < 2; ++h) { while (slot[q] >= 0) ++q; slot[q] = 2 * (o + 4) + h; }
    }
    const int bs[8] = {1, 3, 5, 11, 12, 13, 19, 21};
    for (int jb = 0; jb < 8; ++jb) slot[bs[jb]] = 32 + jb;
    return slot[p];
}
static_assert(cv_read(35) == 22 && cv_read(36) == 23 && cv_read(37) == -1 && cv_read(10) == 11 && cv_read(15) == 12 && cv_read(18) == 15 && cv_read(24) == 17, "conv schedule");
constexpr int cv_wait(int o) {
    int n = 0;
    for (int q = cv_first(o) - 1;; --q) {
        const int rd = cv_read(q < 0 ? q + 36 : q);
        if (rd == 2 * (o + (q < 0 ? 8 : 0)) + 1) return n;
        if (rd >= 0) ++n;
    }
}

struct Args {
    const char *q, *k, *v, *sk, *ssmk, *ssmD, *x, *lre, *lim, *bre, *bim, *cre, *cim, *logdt, *dskip;
    char *out, *ws;
};

typedef __attribute__((ext_vector_type(8))) unsigned long long u64x8;
__global__ __launch_bounds__(768, 3) void fused(Args) {
    __shared__ __attribute__((aligned(1024))) char shm[163840];
    TS0a();
    u64x8 ka0, ka1; unsigned long long ka2;
    const char* pc0;
    int code_n;
    asm volatile("s_load_dwordx16 %0, %5, 0x0\n\ts_load_dwordx16 %1, %5, 0x40\n\ts_load_dwordx2 %2, %5, 0x80\n\t"
                 "s_getpc_b64 %3\n.Lfk_code_0:\n\ts_mov_b32 %4, .Lfk_code_a-.Lfk_code_0-4\n\ts_max_i32 %4, %4, .Lfk_code_b-.Lfk_code_0-4"
                 : "=&s"(ka0), "=&s"(ka1), "=&s"(ka2), "=&s"(pc0), "=&s"(code_n) : "s"(__builtin_amdgcn_kernarg_segment_ptr()) : "scc");
    const int tid = threadIdx.x, lane = tid & 63, wraw = __builtin_amdgcn_readfirstlane(tid >> 6);
    const int wid = !SWAP ? wraw : wraw < 4 ? wraw + 8 : wraw - 4;
    const int b = blockIdx.x, xcd = b & 7, idx = b >> 3;
    asm volatile("s_cmp_lt_u32 %0, 8\n\ts_cbranch_scc1 1f\n\ts_setprio 3\n\ts_cmp_lg_u32 %0, 8\n\ts_cbranch_scc1 1f\n\t"
                 "v_lshl_add_u32 v161, %1, 7, %2\n\tv_min_i32 v161, %3, v161\n\ts_mov_b64 exec, 0xffff\n\tglobal_load_dword v161, v161, %4\n\ts_mov_b64 exec, -1\n1:"
                 :: "s"(wid), "v"(lane), "s"(idx * 2048), "s"(code_n), "s"(pc0) : "scc", "memory", "v161");
    TS_DECL
    const int tm = (xcd >> 1) * 4 + (idx >> 3), tn = (xcd & 1) * 8 + (idx & 7);
    const int wm = wid & 3, wn = (wid >> 2) & 1, l31 = lane & 31, lh = lane >> 5;
    const unsigned lb = (unsigned)(uintptr_t)shm;
    const unsigned l16 = lane * 16;
    const int l15 = lane & 15, g4 = lane >> 4, sq = 2 * wn + (wm & 1), st = sq * 16 + l15, run = (wm >> 1) * 4 + g4;
    unsigned kvv[4];
    const int trow = idx * 64 * 2048 + xcd * 256;
    if (wid < 8) {
        const int v = l15 ^ g4, hi = v >> 3;
        const int r0 = wn ? tn * 64 + (wm >> 1) * 32 : tm * 128 + (wm >> 1) * 64 + (wm & 1) * 4;
        const unsigned d0 = (r0 + ((g4 * (wn ? 2 : 9)) & (wn ? 6 : 17))) * 2048 + (v & 7) * 16, odd = wn ? 11 : 14;
        kvv[0] = d0 + (hi << odd); kvv[2] = d0 + ((hi ^ 1) << odd) + (wn ? 32768 - 2048 : 4096 - 2048);
        kvv[1] = (kvv[0] ^ 64) + (wn ? 16384 - 1024 : 65536 - 1024); kvv[3] = (kvv[2] ^ 64) + (wn ? 16384 - 1024 : 65536 - 1024);
    } else {
        kvv[0] = trow + (idx ? -4 * 2048 : 0) + g4 * 2048 + wm * 64 + l15 * 4;
        asm volatile("v_writelane_b32 %0, 0, 0\n\tv_writelane_b32 %0, 4, 1\n\tv_writelane_b32 %0, %1, 2" : "+v"(kvv[0]) : "s"((xcd * 64 + idx * 2 + (wm >> 1)) * 4));
        kvv[1] = (xcd * 128 + wm * 32 + l31) * 8;
        kvv[2] = kvv[3] = (tn * 64 + lane) * 2;
    }
    unsigned g1dst = lb + wid * 4096;
    int rot7 = ((((ROT >> 12) & 15) * (idx & 7) + ((ROT >> 8) & 15) * (idx >> 3) + ((ROT >> 4) & 15) * (xcd >> 1) + (ROT & 15) * (xcd & 1)) & 15) << 7;
    asm volatile("s_waitcnt lgkmcnt(0)" : "+s"(ka0), "+s"(ka1), "+s"(ka2), "+s"(g1dst), "+s"(rot7), "+v"(kvv[0]), "+v"(kvv[1]), "+v"(kvv[2]), "+v"(kvv[3]));
    Args a;
    a.q = (const char*)ka0[0]; a.k = (const char*)ka0[1]; a.v = (const char*)ka0[2]; a.sk = (const char*)ka0[3]; a.ssmk = (const char*)ka0[4]; a.ssmD = (const char*)ka0[5];
    a.x = (const char*)ka0[6]; a.lre = (const char*)ka0[7]; a.lim = (const char*)ka1[0]; a.bre = (const char*)ka1[1]; a.bim = (const char*)ka1[2]; a.cre = (const char*)ka1[3];
    a.cim = (const char*)ka1[4]; a.logdt = (const char*)ka1[5]; a.dskip = (const char*)ka1[6]; a.out = (char*)ka1[7]; a.ws = (char*)ka2;
    char* const ws = a.ws;
    const char* const flg = ws + WS_FLAG;
    TS(26);
    const char* g1src = wn ? ((wm & 1) ? a.bim : a.bre) : a.x;
    asm volatile("s_mov_b32 m0, %[m]\n\ts_cmp_gt_u32 %[w], 7\n\ts_cbranch_scc1 1f\n\t"
                 "global_load_lds_dwordx4 %[k0], %[g]" FLX(DF) "\n\tglobal_load_lds_dwordx4 %[k1], %[g] offset:1024" FLX(DF) "\n\t"
                 "global_load_lds_dwordx4 %[k2], %[g] offset:2048" FLX(DF) "\n\tglobal_load_lds_dwordx4 %[k3], %[g] offset:3072" FLX(DF) "\n\t"
                 "s_branch 2f\n1:\n\tglobal_load_ushort v164, %[k2], %[c1]\n\tglobal_load_ushort v165, %[k2], %[c2]\n\tglobal_load_ushort v166, %[k2], %[c3]\n\t"
                 "v_mov_b64 v[162:163], %[kv]\n\ts_mov_b64 exec, 7\n\tv_mov_b64 v[162:163], %[d]\n\ts_mov_b64 exec, 3\n\tv_mov_b64 v[162:163], %[e]\n\ts_mov_b64 exec, -1\n\t"
                 "v_mad_u64_u32 v[162:163], vcc, %[k0], 1, v[162:163]\n\tglobal_load_dword v160, v[162:163], off sc1\n\tglobal_load_dwordx2 v[162:163], %[k1], %[sk]\n2:"
                 :
                 : [w] "s"(wid), [c1] "s"(a.logdt), [c2] "s"(a.lre), [c3] "s"(a.lim), [d] "s"(a.ssmD), [e] "s"(flg + FL_EPOCH), [sk] "s"(a.sk),
                   [k0] "v"(kvv[0]), [k1] "v"(kvv[1]), [k2] "v"(kvv[2]), [k3] "v"(kvv[3]), [kv] "s"(a.k), [g] "s"(g1src + rot7), [m] "s"(g1dst)
                 : "scc", "vcc", "memory", "v160", "v161", "v162", "v163", "v164", "v165", "v166");
    TS0b();
    TS(27);
    if (P1) BAR();
#define SLICE_TABLE() unsigned slo, shi; { int ix = idx; asm volatile("" : "+s"(ix)); \
        const unsigned long long t0 = (ix & 16) ? ((ix & 8) ? sl_seq(3, 0) : sl_seq(2, 0)) : ((ix & 8) ? sl_seq(1, 0) : sl_seq(0, 0)), iv = (ix & 16) ? ((ix & 8) ? sl_seq(3, 1) : sl_seq(2, 1)) : ((ix & 8) ? sl_seq(1, 1) : sl_seq(0, 1)); \
        const unsigned x4 = (xcd >> 1) * 0x44444444u, own = tn ^ (x4 & 15), es = (unsigned)(iv >> (4 * own)) & 15; \
        const unsigned long long df = ((unsigned)t0 & 15) ^ own, tab = t0 ^ (df << (4 * es)) ^ df; \
        slo = (unsigned)tab ^ x4; shi = (unsigned)(tab >> 32) ^ x4; asm volatile("" : "+s"(slo), "+s"(shi)); }
#define SLICE(st) ((((st) < 8 ? slo : shi) >> (4 * ((st) & 7))) & 15)
    typedef __attribute__((ext_vector_type(4))) short v4i16;
    int role = wid;
    asm volatile("" : "+s"(role));

    if (role >= 8) {
        int lane;
        asm volatile("v_mbcnt_lo_u32_b32 %0, -1, 0\n\tv_mbcnt_hi_u32_b32 %0, -1, %0" : "=v"(lane));
        __builtin_assume(lane >= 0 && lane < 64);
        const int l31 = lane & 31, lh = lane >> 5, l15 = lane & 15, g4 = lane >> 4;
        const unsigned l16 = lane * 16;
        const int chn = xcd * 128 + idx * 4 + wm;
        const unsigned cb = lb + wm * CHSZ;
        const int r = l15;
        const unsigned fpub = KOWN ? (xcd * 32 + l31) * 256 + idx * 8 : xcd * 256 + idx * 8, fpoll = KOWN ? (xcd * 32 + idx) * 256 + l31 * 8 : xcd * 256 + l31 * 8;
        u32x2 fl; u32x4 sw[4], uu[4];
        unsigned tch = 0, tch2 = 0;
        if (TOUCH) {
            const int ln = wm * 4 + (lane & 3);
            const unsigned fo = (unsigned)(WS_FLAG - WS_E) + (wm == 0 ? FL_H + (tm * 16 + tn) * 128 : (wm == 1 ? FL_KV : FL_Z) + xcd * 256 + (lane & 1) * 128);
            const unsigned o = (lane & 4) && (TOUCH & 2) ? fo : ((tm * 2 + (ln >> 3)) * 1024 + tn * 64) * 16 + (ln & 7) * 128;
            asm volatile("s_mov_b64 exec, 0xff\n\tglobal_load_dword %0, %1, %2\n\ts_mov_b64 exec, -1" : "=&v"(tch) : "v"(o), "s"(ws + WS_E) : "memory");
        }
        SLICE_TABLE();
        TS(1);
        unsigned sm, cl[3]; u32x2 kw0;
        asm volatile("s_waitcnt vmcnt(0)" : "={v160}"(sm), "={v[162:163]}"(kw0), "={v164}"(cl[0]), "={v165}"(cl[1]), "={v166}"(cl[2]) :: "memory", "v161");
        TS(22);
        const unsigned hal = take(sm);
        const unsigned tagA = __builtin_amdgcn_readlane(hal, 0) + 1, tagB = __builtin_amdgcn_readlane(hal, 1) + (tagA == 0 ? 1u : 0u);
        const u32x2 tag = {tagA, tagB};
        f32x2 L1, C1;
        {
            const float dt = rbf(expf(bf2f(take(cl[0]))));
            const float lr = bf2f(take(cl[1])), li = bf2f(take(cl[2]));
            const float e = expf(dt * lr);
            float sn, cs;
            sincosf(dt * li, &sn, &cs);
            L1 = f32x2{e * cs, e * sn};
            const float nr = L1[0] - 1.f, ni = L1[1], den = lr * lr + li * li;
            C1 = f32x2{2.f * ((nr * lr + ni * li) / den), 2.f * ((ni * lr - nr * li) / den)};
            TIE(L1); TIE(C1);
        }

        f32x2 p[11];
        p[0] = L1;
        if (PWE) {
#pragma unroll
            for (int i = 1; i <= 10; ++i) { p[i] = cmul4(p[i - 1], p[i - 1]); TIE(p[i]); }
        }
        {
            if (!PWE) {
#pragma unroll
                for (int i = 1; i <= 10; ++i) p[i] = cmul4(p[i - 1], p[i - 1]);
            }
            const f32x2 one = {1.f, 0.f}, zero = {0.f, 0.f};
            const unsigned ts = lb + L_TS + l16, tr = lb + L_TR + wm * 2048 + l16;
            if (wm == 0) {
                *LDSP(f32x4, ts) = f32x4{L1[0], L1[1], C1[0], C1[1]};
                *LDSP(f32x4, ts + 1024) = f32x4{p[3][0], p[3][1], p[4][0], p[4][1]};
                *LDSP(f32x4, ts + 2048) = f32x4{p[6][0], p[6][1], __uint_as_float(tagA), __uint_as_float(tagB)};
            }
            f32x2 Q[2], V[2], W[2];
            Q[0] = cmul4(cmul4(C1, (wm & 1) ? p[5] : one), (wm & 2) ? p[6] : one); Q[1] = cmul4(Q[0], p[4]);
            V[1] = (wm & 1) ? one : p[5]; V[0] = cmul4(V[1], p[4]);
            W[1] = cmul4((wm & 1) ? one : p[9], (wm & 2) ? one : p[10]); W[0] = cmul4(W[1], p[8]);
#pragma unroll
            for (int i = 0; i < 2; ++i) {
                const f32x2 Q8 = cmul4(Q[i], p[3]), U = 4 * wm + 2 * i >= 16 - tm ? cmul4(W[i], p[7]) : zero, Wz = 4 * wm + 2 * i + 1 >= 16 - tm ? W[i] : zero;
                *LDSP(f32x4, tr + i * 1024) = f32x4{Q[i][0], Q8[0], Q[i][1], Q8[1]};
                *LDSP(f32x4, tr + i * 1024 + 8192) = f32x4{Wz[0], Wz[1], U[0], U[1]};
                *LDSP(f32x2, tr + i * 1024 + 16384) = V[i];
            }
        }
        auto b0 = [&]() {
            unsigned k0 = trow + wm * 32768 + g4 * 2048 + ((l15 ^ (2 * g4)) << 4), k1 = k0 + 7168, k2 = (k0 ^ 128) + 14336, k3 = k2 + 7168;
            if (HKV) { TIE(k0); TIE(k1); TIE(k2); TIE(k3); }
            if (HKV == 1) { kv_dma(lb + L_KS + wm * 4096, k0, k1, k2, k3, a.k); kv_dma(lb + L_VS + wm * 4096, k0, k1, k2, k3, a.v); }
            WAIT_L(0); BAR(); HTSE(1);
            if (HKV == 2) { kv_dma(lb + L_KS + wm * 4096, k0, k1, k2, k3, a.k); kv_dma(lb + L_VS + wm * 4096, k0, k1, k2, k3, a.v); }
        };
        if (CB0 == 0) b0();
        const unsigned dskl = (__builtin_amdgcn_readlane(hal, 2) >> ((wm & 1) * 16)) & 0xffffu;
        u32x4 kvo[4];
        u32x2 qv[8];
        const char* krow = a.ssmk + (size_t)chn * 4096;
        const char* qs = a.q + (size_t)(idx * 64 + wm * 16) * 2048 + xcd * 256;
        const char* kvd = ws + WS_KVT + (size_t)(xcd * 8 + wm * 2) * 65536 + idx * 2048;
        const int pl = (PLT >> ((STG_SEL) * 4)) & 15;
        const unsigned qo = lh * 8192 + l31 * 8 + 4096;
        auto hb = [&](auto nc) {
            constexpr int nb = decltype(nc)::value;
            if constexpr (nb == KAT && HKV) WAIT_V(0);
            if constexpr (nb == KFB) { WAIT_V((PK ? 0 : 4) + (PQ ? 0 : 8)); int lm = (1 << KFB) - (2 << (KAT + __builtin_popcount(KV3))); ties_(lm); wait_if<0, 0>((lm >> pl) & 1); }
            BAR();
            if constexpr (nb == KFB) flag_if<0, KOWN ? -1 : 1>(wm, fpub, tag, flg + FL_KV);
            if constexpr (TCH && nb == TCB) {
                if (TCH == 1) { const u32x4 nz = {~tagA, ~tagA, ~tagA, ~tagA}; if (lane < 32) GST_B128_SC1(((tm * 2 + (wm >> 1)) * 1024 + tn * 64 + (wm & 1) * 32) * 16 + l16, nz, ws + WS_E, 0); }
                else if (TCH == 3) {
                    const unsigned o = (tn * 64 + lane) * 2048 + ((tm & 3) * 4 + wm) * 128;
                    asm volatile("global_load_dword %0, %2, %3\n\tglobal_load_dword %1, %2, %4" : "=&v"(tch), "=&v"(tch2) : "v"(o), "s"(a.cre), "s"(a.cim) : "memory");
                } else if (TCH == 4) {
                    asm volatile("global_load_dword %0, %1, %2" : "=&v"(tch) : "v"((tn * 64 + lane) * 2048), "s"(((wm & 1) ? a.cim : a.cre) + (((wm & 2) ? SLICE(6) : SLICE(5)) << 7)) : "memory");
                }
                else asm volatile("global_load_dword %0, %1, %2 sc1" : "=&v"(tch) : "v"((lane >> 1) * 16384 + tn * 1024 + wm * 256 + (lane & 1) * 128), "s"(ws + WS_E) : "memory");
            }
            if constexpr (nib_any(PLT, nb)) { if (!PK) ld4_if<nb>(pl, l16, sw, krow); if (!PQ) ld8_if<nb>(pl, qo, qv, qs, qs + 16384); }
        };
#define HB(n) hb(std::integral_constant<int, (n) + NBK>{})
        {
            unsigned kw[2][4], ta[2], th[2];
            u32x2 hm[2];
#pragma unroll
            for (int j = 0; j < 2; ++j) {
                const unsigned x = take(kw0[j]);
                const auto y = __builtin_amdgcn_permlane16_swap(x, x, false, false);
#pragma unroll
                for (int p = 0; p < 2; ++p) { kw[p][2 * j] = y[p] << 16; kw[p][2 * j + 1] = y[p] & 0xffff0000u; }
            }
#pragma unroll
            for (int p = 0; p < 2; ++p) {
                unsigned h[3];
#pragma unroll
                for (int j = 0; j < 3; ++j) h[j] = (__shfl(hal, p * 8 + (r >> 1) + 16 * (j + 1)) >> ((r & 1) * 16)) & 0xffffu;
                hm[p] = idx ? u32x2{h[0] << 16, h[1] | (h[2] << 16)} : u32x2{0u, 0u};
                const int q = r >> 2, pp = r & 3;
                ta[p] = lb + L_KS + (8 * g4 + q) * 256 + (((wm * 4 + p * 2 + (pp >> 1)) ^ (2 * (q | ((g4 & 1) << 2)))) << 4) + (pp & 1) * 8;
                th[p] = (ta[p] ^ 128) - 1024;
                TIE(hm[p]); TIE(ta[p]); TIE(th[p]);
#pragma unroll
                for (int j = 0; j < 4; ++j) TIE(kw[p][j]);
            }
            TS(21);
            if (CB0 == 1) b0();
#pragma unroll
            for (int i = 0; i < 4; ++i) asm volatile("" : "=v"(sw[i]), "=v"(qv[2 * i]), "=v"(qv[2 * i + 1]));
            static_for<1, KAT + 1>(hb);
            TS(2);
            u32x2 kt[4][3], vt[4][2];
#pragma unroll
            for (int u = 0; u < 4; ++u) {
                const int p = u >> 1, o = (u & 1) * 8192;
                auto tr = [&](unsigned ad) -> u32x2 { return __builtin_bit_cast(u32x2, __builtin_amdgcn_ds_read_tr16_b64_v4i16(LDSP(v4i16, ad))); };
                kt[u][0] = tr(th[p] + o); kt[u][1] = tr(ta[p] + o); vt[u][0] = tr(ta[p] + o + 16384); kt[u][2] = tr(ta[p] + o + 1024); vt[u][1] = tr(ta[p] + o + 17408);
                SB();
            }
            TS(14);
            __builtin_amdgcn_s_setprio(KPRIO);
            static_for<0, 4>([&](auto uc) {
                constexpr int u = decltype(uc)::value, p = u >> 1, oh = u & 1;
                const u32x2 k0 = oh == 0 && g4 == 0 ? hm[p] : kt[u][0];
                unsigned t0[8], t1[8], e[7];
#define K3(n) do { if constexpr ((KV3 >> (6 * u + n)) & 1) hb(std::integral_constant<int, KAT + 1 + __builtin_popcount(KV3 & ((1 << (6 * u + n)) - 1))>{}); } while (0)
                if constexpr (RX & 1) {
                kf4a(t0, e, k0[0], k0[1], kt[u][1][0], kt[u][1][1], kw[p]); K3(0); kf4b(t0, e, kw[p]); K3(1); kf4c(t0, vt[u][0][0], vt[u][0][1]); K3(2);
                kf4a(t1, e, kt[u][1][0], kt[u][1][1], kt[u][2][0], kt[u][2][1], kw[p]); K3(3); kf4b(t1, e, kw[p]); K3(4); kf4c(t1, vt[u][1][0], vt[u][1][1]);
                } else {
                kv4a(t0, e, k0[0], k0[1], kt[u][1][0], kt[u][1][1], kw[p]); K3(0); kv4b(t0, e, kw[p]); K3(1); kv4c(t0, vt[u][0][0], vt[u][0][1]); K3(2);
                kv4a(t1, e, kt[u][1][0], kt[u][1][1], kt[u][2][0], kt[u][2][1], kw[p]); K3(3); kv4b(t1, e, kw[p]); K3(4); kv4c(t1, vt[u][1][0], vt[u][1][1]);
                }
                kvo[u] = u32x4{t1[2], t1[0], t0[2], t0[0]};
                GST_B128_SC1(l16, kvo[u], kvd + (u >> 1) * 65536, (u & 1) * 1024);
                K3(5);
#undef K3
            });
            hb(std::integral_constant<int, KAT + 1 + __builtin_popcount(KV3)>{});
            __builtin_amdgcn_s_setprio(3);
        }
        TS(3);
        const char* usrc = ws + WS_KVT + (size_t)(xcd * 8 + (idx >> 2)) * 65536 + wm * 16384 + (idx & 3) * 64;
        const unsigned uo = (lane >> 2) * 256 + (lane & 3) * 16;
        const char* cs = ((wm & 2) ? a.cim : a.cre) + (size_t)(tn * 64 + (wm & 1) * 2) * 2048;
        unsigned co[4];
#pragma unroll
        for (int i = 0; i < 4; ++i) {
            const int pair = i * 4 + g4, v = r ^ pair, row = pair * 2 + (v >> 3);
            co[i] = ((row & 15) * 4 + (row >> 4)) * 2048 + (v & 7) * 16 - i * 1024;
        }
        bool kv_ok = false;
#pragma unroll
        for (int it = 0; it < 4; ++it) asm volatile("" : "=v"(uu[it]));
        auto cst = [&](int place, int slice) { const char* s = cs + (slice << 7); DMA(lb + place + wm * 4096, co[0], s); dmao_<1024>(co[1], s); dmao_<2048>(co[2], s); dmao_<3072>(co[3], s); };
        auto items = [&](auto pc) {
            constexpr int p = decltype(pc)::value;
            if constexpr (PU == p) {
                WAIT_V(it_cnt(PU - 1, 5, PU - 1)); TIE(fl);
                if (TCH >= 2) TIE(tch);
                if (TCH == 3) TIE(tch2);
                kv_ok = __all(fl[0] == tagA && fl[1] == tagB);
                const char* us = kv_ok ? usrc : a.x;
                for (int it = 0; it < 4; ++it) GLD_B128_SC1(uu[it], uo, us + it * 4096, 0);
            }
            if constexpr (PK == p) ld4_if<0>(0, l16, sw, krow);
            if constexpr (PC0 == p) cst(g2_place(0), SLICE(0));
            if constexpr (PC1 == p) cst(g2_place(1), SLICE(1));
            if constexpr (PQ == p) ld8_if<0>(0, qo, qv, qs, qs + 16384);
            if constexpr (PU == p + 1) GLD_B64_SC1(fl, fpoll, flg + FL_KV, 0);
            if constexpr (PC2 == p) cst(g2_place(5), SLICE(5));
            if constexpr (PC3 == p) cst(g2_place(6), SLICE(6));
        };
#define ITEMS(p) items(std::integral_constant<int, p>{})
        static_assert(KFB > KAT + 1 + __builtin_popcount(KV3) && KV3 < (1 << 23) && KFB <= 13 && KFB <= PC0 && KFB < PU - 1 && nib(PLT, 0) >= KAT && nib(PLT, 3) < PU - 1, "schedule");
        static_for<KAT + 2 + __builtin_popcount(KV3), 15>([&](auto nc) { hb(nc); if constexpr (decltype(nc)::value < 14) items(nc); });
        const unsigned ud = lb + (lane & 3) * CHSZ + UOFF + ((lane >> 2) & 1) * UPL + (16 + wm * 32 + (lane >> 3)) * 16;
        float dz = lane == 0 ? bf2f(dskl) : 0.f;
        if (RX & 2) TIE(dz);
        auto zero = [&]() {
            const u32x4 z4 = {0, 0, 0, 0};
            if (lane < 28) *LDSP(u32x4, lb + L_SY + l16) = z4;
            if (lane < 56) *LDSP(u32x4, lb + L_AGG + wm * 896 + l16) = z4;
            *LDSP(u32x4, lane < 32 ? cb + ((lane >> 3) & 1) * CST + (lane & 7) * 16 : cb + UOFF + (g4 & 1) * UPL + r * 16) = z4;
        };
        auto fill = [&]() {
#pragma unroll
            for (int it = 0; it < 4; ++it) TIE(sw[it]);
            if (RX & 2) sw[0][0] = pack2(bflo(sw[0][0]) + dz, bfhi(sw[0][0]));
#pragma unroll
            for (int it = 0; it < 4; ++it) {
                const unsigned q = cb + 128 + it * 1024 + l16;
                const unsigned d0 = sw[it][0], d1 = sw[it][1], d2 = sw[it][2], d3 = sw[it][3];
                const unsigned o1 = it ? __builtin_amdgcn_readlane(sw[it ? it - 1 : 0][3], 63) : 0u;
                const unsigned x1 = __builtin_amdgcn_update_dpp(o1, d3, 0x138, 0xf, 0xf, false);
                *LDSP(u32x4, q) = sw[it];
                *LDSP(u32x4, q + CST) = u32x4{__builtin_amdgcn_alignbyte(d0, x1, 2), __builtin_amdgcn_alignbyte(d1, d0, 2), __builtin_amdgcn_alignbyte(d2, d1, 2), __builtin_amdgcn_alignbyte(d3, d2, 2)};
            }
        };
        auto uput = [&]() {
            WAIT_V(it_cnt(PU, 0, UE ? 15 : 16));
#pragma unroll
            for (int it = 0; it < 4; ++it) TIE(uu[it]);
            if (!kv_ok) {
                TSE(23);
                for (unsigned spin = 0; spin < SPIN_CAP; ++spin) {
                    GLD_B64_SC1(fl, fpoll, flg + FL_KV, 0);
                    WAIT_V(0); TIE(fl);
                    if (__all(fl[0] == tagA && fl[1] == tagB)) break;
                    __builtin_amdgcn_s_sleep(8);
                }
#pragma unroll
                for (int it = 0; it < 4; ++it) GLD_B128_SC1(uu[it], uo, usrc + it * 4096, 0);
                WAIT_V(0);
#pragma unroll
                for (int it = 0; it < 4; ++it) TIE(uu[it]);
            }
#pragma unroll
            for (int it = 0; it < 4; ++it) *LDSP(u32x4, ud + it * 128) = uu[it];
        };
        static_assert(PU <= (UE ? 15 : 16) && (FP_ != 14 || nib(PLT, 3) <= 12), "u");
        ITEMS(14);
        if (UE) __builtin_amdgcn_s_setprio(UPRIO);
        if (P14 != 3) __builtin_amdgcn_s_setprio(P14);
        if (UE >= 2 || ZP_ == 14) zero();
        if (UE == 2 || FP_ == 14) { WAIT_V(it_cnt(12, 8, 14)); fill(); }
        BAR();
        if (P14 != 3) __builtin_amdgcn_s_setprio(3);
        ITEMS(15);
        TS(4);
        if (P15 != 3) __builtin_amdgcn_s_setprio(P15);
        if (UE < 2 && ZP_ == 15) zero();
        if (FSL) __builtin_amdgcn_s_sleep(FSL);
        if ((UE & 1) || FP_ == 15) fill();
        if (UE) uput();
        WAIT_L(0);
        TS(5);
        BAR();
        __builtin_amdgcn_s_setprio(FPRIO);
        ITEMS(16);
        if (P16 != FPRIO) __builtin_amdgcn_s_setprio(P16);
        if (S16) __builtin_amdgcn_s_sleep(S16);
        TS(11);
        const unsigned pmine = lb + L_SY + 32 + wm * 4, pall = lb + L_SY + 32 + (lane & 3) * 4;
        static_assert(!PK || (!UE && PK > KFB && PK <= 16), "ssm_k");
        static_assert(!PQ || PQ > KFB, "q");
        if (!UE) { if (PK >= PU) { uput(); sy_post<0>(pmine); WAIT_V(it_cnt(PK, 1, 16)); fill(); } else { if (FP_ == 16) fill(); TS(13); uput(); sy_post<0>(pmine); } ITEMS(17); sy_wait<0>(pall); }
        if (!CPOST) { WAIT_V(0); sy_post<1>(pmine); }
        __builtin_amdgcn_s_setprio(CVPRIO);
        TS(6);
        f32x4 ca[8];
        {
            unsigned kb[4];
#pragma unroll
            for (int i = 0; i < 4; ++i) { kb[i] = cb + (~r & 1) * CST + 2 * (64 + 16 + r - 8 * g4 - 7 + (~r & 1)) + i * 1024; TIE(kb[i]); }
            unsigned ub = cb + UOFF + (g4 & 1) * UPL + (15 + r + (g4 >> 1)) * 16 - 64;
#pragma unroll
            for (int i = 0; i < 8; ++i) { ca[i] = f32x4{0, 0, 0, 0}; TIE(ca[i]); }
            s16x4 FA[4][2]; bf16x8 FB[2][8];
#define CV_A(s, o, e) dsr2_<16 * ((s) + ((o) >> 3)) + 128 * (cv_m((o) & 7) & 1) + 2 * (e)>(FA[(o) & 3][e], kb[cv_m((o) & 7) >> 1])
#define CV_B(s, jb) DSR128(FB[(s) & 1][jb], ub, 64 + 256 * (jb) - 32 * (s))
            static_for<0, 8>([&](auto ic) { constexpr int n = decltype(ic)::value; CV_B(0, n); CV_A(0, n >> 1, n & 1); });
            WAIT_L(0);
            unsigned pw = lb + L_SY + (CVLATE == 2 ? wm + (lane & 1) * 4 : lane & 7) * 4;
            if (QP) TIE(pw);
            auto body = [&](int it) {
                static_for<0, 72>([&](auto ic) {
                    constexpr int s = decltype(ic)::value / 36, p = decltype(ic)::value % 36, o = cv_grp(p), m = cv_m(o), jb = p - cv_first(o);
                    if constexpr (QP && decltype(ic)::value == QP) {
                        unsigned t;
                        asm volatile("s_cmp_lg_u32 %2, 0\n\ts_cbranch_scc1 3f\n\ts_waitcnt vmcnt(0)\n\tds_write_b32 %3, %3 offset:64\n\ts_branch 2f\n1:\n\ts_sleep 1\n2:\n\tds_read_b32 %0, %1 offset:64\n\ts_waitcnt lgkmcnt(0)\n\t"
                                     "v_cmp_eq_u32 vcc, 0, %0\n\ts_cbranch_vccnz 1b\n3:" : "=&v"(t) : "v"(pw), "s"(it), "v"(pmine) : "vcc", "scc", "memory");
                    }
                    if constexpr (jb == 0) { WAIT_LB(cv_wait(o)); if constexpr (!(AMF & 1)) { TIE(FA[o & 3][0]); TIE(FA[o & 3][1]); } }
                    if constexpr (p == 0 && !(AMF & 1)) { for (int i = 0; i < 8; ++i) TIE(FB[s][i]); }
                    const bf16x8 fa = __builtin_shufflevector(FA[o & 3][0], FA[o & 3][1], 0, 1, 2, 3, 4, 5, 6, 7);
                    if constexpr (AMF & 1) mfma_(ca[jb + m], fa, FB[s][jb]); else
                    ca[jb + m] = __builtin_amdgcn_mfma_f32_16x16x32_bf16(fa, FB[s][jb], ca[jb + m], 0, 0, 0);
                    SB();
                    constexpr int rd = cv_read(p);
                    if constexpr (rd >= 32) CV_B(s + 1, rd - 32);
                    else if constexpr (rd >= 0) CV_A(s, rd >> 1, rd & 1);
                    SB();
                });
                for (int i = 0; i < 4; ++i) { kb[i] += 128; TIE(kb[i]); }
                ub -= 64;
            };
#pragma unroll 1
            for (int it = 0; it < QIT; ++it) body(it);
            {
                TS(19);
                static_assert(!QP || (CPOST && !QIT && !EP && !CVE && CVLATE && PC3 < 19), "pause");
                if (QP) sy_wait<6, 0>(pw); else
                if (CPOST) { WAIT_V(PQ == 17 && PC3 < 17 ? 8 : 0); sy_post<1>(pmine); WAIT_L(0); }
                static_assert(PC3 != 19 || (ZFB == 1 && (BARM & 0x3ff) == 0x220), "stages 5, 6 are covered by the second barrier");
                if (PC3 == 19 || PQ == 19) { sy_wait<0>(lb + L_ES + (lane & P19M) * 256 + 8); ITEMS(19); }
                if (CVLATE && !QP) sy_wait<CVE ? 6 : 1, CVS>(lb + L_SY + (CVLATE == 2 ? wm + (lane & 1) * 4 : lane & 7) * 4);
                TS(20);
                if (PQ == 20 && QSL) __builtin_amdgcn_s_sleep(QSL);
                ITEMS(20);
            }
#pragma unroll 1
            for (int it = QIT; it < CVN; ++it) body(it);
            if (CVZ) __builtin_amdgcn_s_sleep(CVZ);
            WAIT_L(0);
            if (AMF & 1) asm volatile("s_nop 7\n\ts_nop 7" : "+v"(ca[0]), "+v"(ca[1]), "+v"(ca[2]), "+v"(ca[3]), "+v"(ca[4]), "+v"(ca[5]), "+v"(ca[6]), "+v"(ca[7]));
#pragma unroll
            for (int i = 0; i < 4; ++i) { TIE(FA[i][0]); TIE(FA[i][1]); }
#pragma unroll
            for (int i = 0; i < 8; ++i) TIE(FB[0][i]);
#undef CV_A
#undef CV_B
        }
        TS(7);
        __builtin_amdgcn_s_setprio(3);
        constexpr int NBAR = __builtin_popcount(BARM & ~VBM);
        unsigned zpub = (xcd * 32 + l31) * 256 + idx * 8, zpoll = ZOWN ? (xcd * 32 + idx) * 256 + l31 * 8 : fpoll;
        {
            const float Dd = bf2f(dskl);
            const char* zd = ws + WS_ZT + (size_t)(xcd * 128 + wm * 32) * 4096 + idx * 128;
            u32x2 kvr[8];
#pragma unroll
            for (int jt = 0; jt < 8; ++jt) if (!(RX & 2)) kvr[jt] = *LDSP(u32x2, cb + UOFF + (g4 >> 1) * UPL + (16 + 16 * jt + r) * 16 + (1 - (g4 & 1)) * 8);
#pragma unroll
            for (int jt = 0; jt < 8; ++jt) {
                const f32x4 y = ca[jt];
                const u32x2 kq = kvr[jt];
                *LDSP(u32x2, cb + (jt * 4 + (r >> 2)) * 144 + (r & 3) * 32 + g4 * 8) = (RX & 2) ? u32x2{pack2(y[0], y[1]), pack2(y[2], y[3])} : ZM ?
                    u32x2{pack2(add1(rb1(y[0]), rb1(mul1(bfhi(kq[1]), Dd))), add1(rb1(y[1]), rb1(mul1(bflo(kq[1]), Dd)))), pack2(add1(rb1(y[2]), rb1(mul1(bfhi(kq[0]), Dd))), add1(rb1(y[3]), rb1(mul1(bflo(kq[0]), Dd))))} :
                    u32x2{pack2(rb1(y[0]) + rb1(bfhi(kq[1]) * Dd), rb1(y[1]) + rb1(bflo(kq[1]) * Dd)), pack2(rb1(y[2]) + rb1(bfhi(kq[0]) * Dd), rb1(y[3]) + rb1(bflo(kq[0]) * Dd))};
            }
            unsigned za = lb + (r >> 2) * CHSZ + (wm * 8 + (g4 >> 1)) * 144 + ((g4 & 1) * 2 + ((r >> 1) & 1)) * 32 + (r & 1) * 8, zo = (lane >> 3) * 4096 + (lane & 7) * 16;
            TIE(za); TIE(zo); TIE(zpub); TIE(zpoll);
            sy_post<3>(pmine); sy_wait<3, 0>(pall);
            v4i16 t[4][2];
#pragma unroll
            for (int it = 0; it < 4; ++it)
#pragma unroll
                for (int e = 0; e < 2; ++e) t[it][e] = __builtin_amdgcn_ds_read_tr16_b64_v4i16(LDSP(v4i16, za + it * 288 + e * 16));
#pragma unroll
            for (int it = 0; it < 4; ++it) { TIE(t[it][0]); TIE(t[it][1]); }
            if (ZSB) BAR();
#pragma unroll
            for (int it = 0; it < 4; ++it) { const bf16x8 f = __builtin_shufflevector(t[it][0], t[it][1], 0, 1, 2, 3, 4, 5, 6, 7); GST_B128_SC1(zo, f, zd + it * 32768, 0); }
        }
#pragma unroll
        for (int i = ZSB; i < ZFB; ++i) BAR();
        WAIT_V(0);
        sy_post<2>(pmine);
        if (wm == 0) {
            sy_wait<2>(pall);
            if (ZOWN) { if (lane < 32) GST_B64_SC1(zpub, tag, flg + FL_Z); }
            else if (lane == 0) GST_B64_SC1(fpub, tag, flg + FL_Z);
        }
        TS(8);
        unsigned qm[8][4];
        if (QM == 1 || QM == 2) {
#pragma unroll
            for (int n = 0; n < 8; ++n) {
                TIE(qv[n]);
                if (QM == 2) { qm[n][0] = qv[n][0] << 16; qm[n][2] = qv[n][1] << 16; } else { qm[n][0] = qv[n][0] & 0xffffu; qm[n][2] = qv[n][1] & 0xffffu; }
                qm[n][1] = qv[n][0] & 0xffff0000u; qm[n][3] = qv[n][1] & 0xffff0000u;
                TIE(qm[n][0]); TIE(qm[n][1]); TIE(qm[n][2]); TIE(qm[n][3]);
            }
        }
#pragma unroll
        for (int i = ZFB; i < NBAR - 1 - (ZE >= 0); ++i) { BAR();
        }
        if (ZSL) __builtin_amdgcn_s_sleep(ZSL);
        asm volatile("s_cmp_lg_u32 %3, 0\n\ts_cbranch_scc1 1f\n\tglobal_load_dwordx2 %0, %1, %2 sc1\n1:" : "=&v"(fl) : "v"(zpoll), "s"(flg + FL_Z), "s"(wm) : "scc", "memory");
        if (NBAR > ZFB) BAR();
        if (wm == 0) {
            unsigned left = SPIN_CAP;
            asm volatile("s_branch 2f\n1:\n\ts_sleep %5\n\tglobal_load_dwordx2 %0, %2, %3 sc1\n2:\n\ts_waitcnt vmcnt(0)\n\tv_cmp_ne_u64 vcc, %4, %0\n\ts_cbranch_vccz 3f\n\ts_sub_u32 %1, %1, 1\n\ts_cbranch_scc0 1b\n3:"
                         : "+v"(fl), "+s"(left) : "v"(zpoll), "s"(flg + FL_Z), "s"(tag), "n"(ZPS) : "vcc", "scc", "memory");
            sy_post<4>(pmine);
        } else sy_wait<4, 2>(lb + L_SY + 32);
        u32x4 zv[4];
        {
            ITEMS(18);
            const char* zsrc = ws + WS_ZT + (size_t)(xcd * 128 + idx * 4 + wm) * 4096;
            static_for<0, 4>([&](auto ic) { constexpr int it = decltype(ic)::value; GLD_B128_SC1(zv[it], (l31 >> 2) * 512 + lh * 64 + (lane & 3) * 16, zsrc, it * 128); });
        }
        WAIT_V(0);
        TS(9);
#pragma unroll
        for (int it = 0; it < 4; ++it) TIE(zv[it]);
        if (!(XAB & 1)) {
        quad_swap<0>(zv[0], zv[1]); quad_swap<0>(zv[2], zv[3]);
        quad_swap<1>(zv[0], zv[2]); quad_swap<1>(zv[1], zv[3]); }
#pragma unroll
        for (int it = 0; it < 4; ++it) TIE(zv[it]);
        char* const oa = a.out + (size_t)(idx * 64 + wm * 16) * 8192 + xcd * 512;
        if (OPRIO != 3) __builtin_amdgcn_s_setprio(OPRIO);
        if (OS0) __builtin_amdgcn_s_sleep(OS0);
        if (OSW && ZE < 0) for (int i = 0; i < wm; ++i) __builtin_amdgcn_s_sleep(OSW);
#pragma unroll
        for (int n = 0; n < 8; ++n) {
            if (!QM || QM == 3) TIE(qv[n]);
            const unsigned z0 = zv[n & 3][2 * (n >> 2)], z1 = zv[n & 3][2 * (n >> 2) + 1];
            const f32x4 o = (XAB & 2) ? __builtin_bit_cast(f32x4, zv[n & 3]) : QM == 1 ? mul4bf(u32x2{z0, z1}, qm[n]) :
                QM == 3 ? f32x4{mul1(bflo(qv[n][0]), bflo(z0)), mul1(bfhi(qv[n][0]), bfhi(z0)), mul1(bflo(qv[n][1]), bflo(z1)), mul1(bfhi(qv[n][1]), bfhi(z1))} :
                QM == 2 && (RX & 4) ? f32x4{mul1(__uint_as_float(qm[n][0]), bflo(z0)), mul1(__uint_as_float(qm[n][1]), bfhi(z0)), mul1(__uint_as_float(qm[n][2]), bflo(z1)), mul1(__uint_as_float(qm[n][3]), bfhi(z1))} :
                QM == 2 ? f32x4{rb1(mul1(__uint_as_float(qm[n][0]), bflo(z0))), rb1(mul1(__uint_as_float(qm[n][1]), bfhi(z0))), rb1(mul1(__uint_as_float(qm[n][2]), bflo(z1))), rb1(mul1(__uint_as_float(qm[n][3]), bfhi(z1)))} :
                f32x4{rb1(bflo(qv[n][0]) * bflo(z0)), rb1(bfhi(qv[n][0]) * bfhi(z0)), rb1(bflo(qv[n][1]) * bflo(z1)), rb1(bfhi(qv[n][1]) * bfhi(z1))};
            if (n == ZE) { BAR(); if (OSW) for (int i = 0; i < wm; ++i) __builtin_amdgcn_s_sleep(OSW); }
            if (n == 0 && OSG) for (int i = 0; i < (idx & 3); ++i) __builtin_amdgcn_s_sleep(OSG);
            if (!(XAB & 4))
            GST_B128_NT(lh * 32768 + l31 * 16, o, oa + ((n >> 2) * 8 + (n & 3)) * 8192, 0);
            if (OSL && n < 7 && n + 1 != ZE) __builtin_amdgcn_s_sleep(OSL);
        }
        asm volatile(".Lfk_code_a:");
        TSE(10);
        TS_FLUSH();
        return;
    }

    u32x2 fl;
    const unsigned* dv = kvv;
#define G1_ISSUE(st, i) do { if constexpr ((st) < DFN) { if constexpr ((i) > 0) dmaof_<(i) * 1024>(dv[i], g1src + ((((st) << 7) + rot7) & 1920)); else dmaf_(g1dst + g1_slot(st), dv[i], g1src + ((((st) << 7) + rot7) & 1920)); } else if constexpr (M1 && (i) > 0) dmao_<(i) * 1024>(dv[i], g1src + ((((st) << 7) + rot7) & 1920)); \
        else DMA(g1dst + g1_slot(st) + (i) * 1024, dv[i], g1src + ((((st) << 7) + rot7) & 1920)); } while (0)
    unsigned k0 = trow + wm * 32768 + g4 * 2048 + ((l15 ^ (2 * g4)) << 4), k1 = k0 + 7168, k2 = (k0 ^ 128) + 14336, k3 = k2 + 7168;
    auto pre = [&](auto ic) {
        constexpr int n = decltype(ic)::value;
        if constexpr (n == KVP) { if (!HKV) kv_dma(g1dst + L_KS, k0, k1, k2, k3, wn ? a.v : a.k); }
        else static_for<0, 4>([&](auto jc) { G1_ISSUE(n - (n > KVP), decltype(jc)::value); });
    };
    static_for<1, AP0 / 4>(pre);
    TS(14);
    SLICE_TABLE();
    const int j0 = 4 * run - (32 - 2 * tm);
    const int pneg = EALL ? 0 : 16 * (wm >> 1) + 15 - (32 - 2 * tm);
    unsigned long long em0 = __ballot(j0 >= 0), em1 = __ballot(j0 + 2 >= 0);
    if (EM == 2) asm volatile("" : "+s"(em0), "+s"(em1));
    unsigned eo[2], pes, plr, est, hw[2], ta[2], po, pmine, pall, pown, pzero, g2v[2], cad[2][2], to[2][2], xo, dso, fst, fld, cwa;
    unsigned cta = lb + L_TS + st * 16, ctb = lb + L_TR + run * 1024 + st * 16;
    TIE(cta); TIE(ctb);
    f32x4 ct[5]; f32x2 V1;
    const int gm = PAIR ? wid >> 1 : wm, gk = PAIR ? wid & 1 : wn, gs = gk * 4 + (gm & 2);
    auto addrs = [&]() {
        int ln = lane;
        TIE(ln);
        const int l15 = ln & 15, g4 = ln >> 4, st = sq * 16 + l15, run = (wm >> 1) * 4 + g4, j0 = 4 * run - (32 - 2 * tm);
        pes = lb + L_ES + wid * 256 + l15 * 16;
        plr = lb + L_AGG + st * 8 + run * 512; est = ((tm * 2 + (wm >> 1)) * 1024 + tn * 64 + st) * 16 + (EREP ? g4 << 19 : 0);
#pragma unroll
        for (int i = 0; i < 2; ++i) { eo[i] = (j0 + 2 * i < 0 ? 0 : j0 + 2 * i) * 16384 + (tn * 64 + st) * 16 + (EREP == 1 ? (tm >> 2) << 19 : EREP == 2 ? (tm & 3) << 19 : 0); TIE(eo[i]); }
        TIE(plr); TIE(pes); TIE(est);
        const unsigned ht = lb + L_HT + wid * 4096;
#pragma unroll
        for (int j = 0; j < 2; ++j) {
            const int pr = (8 * (g4 & 1) + 4 * j + (l15 >> 2)) ^ ((g4 & 1) * 4), pw = l15 ^ ((l15 >> 3) * 4);
            ta[j] = ht + (g4 >> 1) * 512 + pr * 32 + ((((l15 >> 1) ^ (pr >> 2)) & 1) << 4) + 8 * (ln & 1);
            hw[j] = ht + g4 * 1024 + pw * 32 + (((j ^ (pw >> 2)) & 1) << 4);
            TIE(ta[j]); TIE(hw[j]);
        }
        po = pes + ((wm & 2) ? -512 : 512);
        pmine = lb + L_SY + wid * 4; pall = lb + L_SY + (ln & 7) * 4; pown = lb + L_SY + (ln < 4 ? 32 + ln * 4 : gs * 4 + (ln & 4)); pzero = lb + L_SY;
        xo = (tm * 128 + gm * 32 + gk * 16 + g4 * 4) * 2048 + tn * 128 + l15 * 8 + 4096; dso = xo & 2047;
        fst = (tm * 16 + l15) * 128 + tn * 8; fld = (tm * 16 + tn) * 128 + l15 * 8;
#pragma unroll
        for (int i = 0; i < 2; ++i) {
            const int pair = i * 4 + g4, v = l15 ^ pair ^ ((wm & 1) * 8);
            g2v[i] = (pair * 2 + (v >> 3)) * 8192 + (v & 7) * 16 + (tn * 64 + wm) * 2048 - i * 1024;
            TIE(g2v[i]);
#pragma unroll
            for (int j = 0; j < 2; ++j) {
                cad[i][j] = lb + L_C + (img_off((g4 >> 1) * 64 + l15, 4 * gk + (g4 & 1)) ^ (i * 32) ^ (j * 128));
                to[i][j] = ta[j] + (i ^ gk) * 1024 + (gm & 1) * 2048 + (gs - wid) * 4096;
                TIE(cad[i][j]); TIE(to[i][j]);
            }
        }
        cwa = lb + wid * 2048 + ln * 16;
        TIE(cwa); TIE(po);
        TIE(pmine); TIE(pall); TIE(pown); TIE(pzero); TIE(xo); TIE(dso); TIE(fst); TIE(fld);
    };
    if (!APRE) addrs();
    unsigned aad[2][2], bad[2];
    {
        const int aoff = img_off((wm >> 1) * 64 + l15, g4), boff = img_off(128 + wn * 64 + (wm & 1) * 16 + l15, g4);
#pragma unroll
        for (int ks = 0; ks < 2; ++ks) { aad[ks][0] = lb + (aoff ^ (ks * 64)); aad[ks][1] = lb + (aoff ^ (ks * 64) ^ 128); bad[ks] = lb + (boff ^ (ks * 64)); TIE(aad[ks][0]); TIE(aad[ks][1]); TIE(bad[ks]); }
    }
    if (!HKV && KVP >= AP0 / 4) { TIE(k0); TIE(k1); TIE(k2); TIE(k3); }
    static_for<AP0 / 4, NBK + 1>(pre);
    TS(1);
    float nL1i, nC1r, nC1i;
    unsigned tagA, tagB;
    f32x4 acc[4][2];
    {
#pragma unroll
        for (int i = 0; i < 4; ++i) { acc[i][0] = f32x4{0, 0, 0, 0}; acc[i][1] = f32x4{0, 0, 0, 0}; }
        bf16x8 fa[2][4], fb[2][2];
        if (GPRIO) __builtin_amdgcn_s_setprio(GPRIO);
        WAIT_V(g1_cnt(0)); BAR();
        TS(2);
#define G1_FRAG(g, n) do { constexpr int bo_ = g1_slot((g) >> 1), hi_ = bo_ & ~65535, x_ = (n) < 3 ? (n) >> 1 : (n) - 2; \
        if constexpr ((n) == 0 || (n) == 2) DSR128(fb[(g) & 1][x_], bad[(g) & 1] + hi_, bo_ - hi_ + x_ * 4096); \
        else DSR128(fa[(g) & 1][x_], aad[(g) & 1][x_ & 1] + hi_, bo_ - hi_ + x_ * 2048); } while (0)
        static_for<0, 6>([&](auto ic) { constexpr int n = decltype(ic)::value; G1_FRAG(0, n); });
        static_for<0, 32>([&](auto ic) {
            constexpr int g = decltype(ic)::value, gn = g + 1, n0 = g1_upto(g - 1), nd = g1_upto(g) - n0;
            constexpr bool bar = (gn & 1) == 0 && gn < 32;
            if constexpr (g == 1) {
                DSR128(ct[0], cta, 0); DSR128(ct[1], cta, 1024); DSR128(ct[2], cta, 2048); DSR128(ct[3], ctb, 0); DSR128(ct[4], ctb, 8192); DSR64(V1, ctb, 16384);
            }
            if constexpr (g == 3) {
#pragma unroll
                for (int i = 0; i < 5; ++i) TIE(ct[i]);
                TIE(V1);
                nL1i = -ct[0][1]; nC1r = -ct[0][2]; nC1i = -ct[0][3];
                TIE(nL1i); TIE(nC1r); TIE(nC1i);
                tagA = __builtin_amdgcn_readfirstlane(__float_as_uint(ct[2][2])); tagB = __builtin_amdgcn_readfirstlane(__float_as_uint(ct[2][3]));
                ties_(tagA); ties_(tagB);
            }
            if constexpr (bar) {
                static_assert(g1_cnt(gn >> 1) >= 0, "schedule");
                WAIT_VB(g1_cnt(gn >> 1)); WAIT_LB(0); BAR();
                if constexpr (((gn >> 1) & 3) == 0 || gn >= 26) TS(gn >= 26 ? 23 + (gn >> 1) - 13 : 16 + (gn >> 3));
#pragma unroll
                for (int i = 0; i < 4; ++i) if (!(AMF & 2)) TIE(fa[g & 1][i]);
                if (!(AMF & 2)) { TIE(fb[g & 1][0]); TIE(fb[g & 1][1]); }
            }
            static_for<0, 8>([&](auto mc) {
                constexpr int m = decltype(mc)::value, i = m >> 1, j = m & 1, js = (SNK & 1) && (i & 1) ? 1 - j : j;
                if constexpr (W1 && !bar && g > 0 && g < 31) {
                    if constexpr (m == 0) WAIT_LB(W1 == 1 ? 3 : 2);
                    if constexpr (m == 2 && W1 == 1) WAIT_LB(4);
                    if constexpr (m == 4) WAIT_LB(4);
                } else
                if constexpr (!bar && (i == 0 || j == 0)) {
                    WAIT_LB(5 - (i == 0 ? j + 1 : i + 2) + (gn < 32 ? (m < 6 ? m : 6) : 0));
                    if constexpr (!(AMF & 2) || g == 0) { TIE(fa[g & 1][i]); TIE(fb[g & 1][j]); }
                }
                if constexpr ((AMF & 2) && g > 0) mfma_(acc[i][js], fa[g & 1][i], fb[g & 1][js]); else
                acc[i][j] = __builtin_amdgcn_mfma_f32_16x16x32_bf16(fa[g & 1][i], fb[g & 1][j], acc[i][j], 0, 0, 0);
                SB();
                if constexpr (gn < 32 && m < 6) G1_FRAG(gn, m);
                constexpr int d = nd > 2 ? ((m & 1) ? m >> 1 : -1) : (m == DP0 ? 0 : m == DP1 ? 1 : -1);
                if constexpr (d >= 0 && d < nd) G1_ISSUE((n0 + d) >> 2, (n0 + d) & 3);
                SB();
            });
        });
#undef G1_FRAG
    }
    TS(3);
    const unsigned tagG = tagA ^ tagB ^ 0x9E3779B9u;
    __builtin_amdgcn_s_setprio(MPRIO);
    f32x2 ur[8], ui[8], hr, hi;
    const f32x2 L1 = {ct[0][0], ct[0][1]}, C1 = {ct[0][2], ct[0][3]}, L16 = {ct[1][2], ct[1][3]}, Q2r = {ct[3][0], ct[3][1]}, Q2i = {ct[3][2], ct[3][3]};
    const float L8r = ct[1][0], L8i = ct[1][1], L64r = ct[2][0], L64i = ct[2][1];
    u32x4 eg[4], cr[2 * NR + 1];
    const char* g2src = wn ? a.cim : a.cre;
#define CR_LOAD(n0, n1) static_for<n0, n1>([&](auto ic) { constexpr int n = decltype(ic)::value; GLD_B128(cr[n], g2v[n & 1], g2src + (SLICE(7 + (n >> 1)) << 7), (n & 1) * 1024); })
#define E_LOADS "global_load_dwordx4 v[104:107], %[a0], %[b0] " EMOD "\n\tglobal_load_dwordx4 v[108:111], %[a0], %[b1] " EMOD "\n\t" \
                "global_load_dwordx4 v[112:115], %[a1], %[b0] " EMOD "\n\tglobal_load_dwordx4 v[116:119], %[a1], %[b1] " EMOD "\n"
#define E_POLL() asm volatile("s_cmp_lt_i32 %[on], 0\n\ts_cbranch_scc1 1f\n\ts_mov_b64 exec, %[m1]\n\tglobal_load_dwordx4 v[112:115], %[a1], %[b0] " EMOD "\n\tglobal_load_dwordx4 v[116:119], %[a1], %[b1] " EMOD "\n\t" \
        "s_mov_b64 exec, %[m0]\n\ts_cbranch_execz 2f\n\tglobal_load_dwordx4 v[104:107], %[a0], %[b0] " EMOD "\n\tglobal_load_dwordx4 v[108:111], %[a0], %[b1] " EMOD "\n2:\n\ts_mov_b64 exec, -1\n1:" \
        : "+{v[104:107]}"(eg[0]), "+{v[108:111]}"(eg[1]), "+{v[112:115]}"(eg[2]), "+{v[116:119]}"(eg[3]) \
        : [a0] "v"(eo[0]), [a1] "v"(eo[1]), [b0] "s"(ws + WS_E), [b1] "s"(ws + WS_E + 16384), [on] "s"(pneg), [m0] "s"(em0), [m1] "s"(em1) : "scc", "memory")
    {
        asm volatile("s_nop 7\n\ts_nop 3" : "+v"(acc[0][0]), "+v"(acc[0][1]), "+v"(acc[1][0]), "+v"(acc[1][1]), "+v"(acc[2][0]), "+v"(acc[2][1]), "+v"(acc[3][0]), "+v"(acc[3][1]));
#pragma unroll
        for (int p = 0; p < 8; ++p) { ur[p] = f32x2{acc[p >> 1][0][2 * (p & 1)], acc[p >> 1][0][2 * (p & 1) + 1]}; ui[p] = f32x2{acc[p >> 1][1][2 * (p & 1)], acc[p >> 1][1][2 * (p & 1) + 1]}; }
        f32x2 gr = ur[0], gi = ui[0];
#pragma unroll
        for (int p = 1; p < 8; ++p) { f32x2 nr, ni; if (PLN & 1) c2_mad(nr, ni, L1[0], nL1i, L1[0], L1[1], gr, gi, ur[p], ui[p]); else pk_cmad(nr, ni, L1, gr, gi, ur[p], ui[p]); gr = nr; gi = ni; }
        float fr, fi;
        cmad(fr, fi, L8r, L8i, gr[0], gi[0], gr[1], gi[1]);
        {
            const f32x2 w = cmul4(V1, f32x2{fr, fi});
            const float wr = sum4(w[0]), wi = sum4(w[1]);
            const u32x4 gq = {__float_as_uint(wr), tagA, __float_as_uint(wi), tagG};
            if (EREP ? (EALL || EREP == 2 || g4 >= (tm >> 2)) : g4 == 0) GST_B128_SC1(est, gq, ws + WS_E, 0);
        }
        *LDSP(f32x2, plr + 7 * 512) = f32x2{fr, fi};
        *LDSP(unsigned, pes + 8) = 0;
        if (EM == 2) {
#pragma unroll
            for (int i = 0; i < 4; ++i) eg[i] = u32x4{0u, tagA, 0u, tagG};
            asm volatile("" : "+{v[104:107]}"(eg[0]), "+{v[108:111]}"(eg[1]), "+{v[112:115]}"(eg[2]), "+{v[116:119]}"(eg[3]));
        }
        TS(21);
        if (EP == 2) E_POLL();
        auto coef = [&]() {
#pragma unroll
            for (int p = 0; p < 8; ++p) { f32x2 nr, ni; if (PLN & 2) c2_mul(nr, ni, C1[0], nC1i, nC1r, nC1i, ur[p], ui[p]); else pk_conj_mul(nr, ni, C1, ur[p], ui[p]); ur[p] = nr; ui[p] = ni; TIE(ur[p]); TIE(ui[p]); }
        };
        if (CFB) coef();
        WAIT_L(0); BAR();
        f32x2 e[7];
#pragma unroll
        for (int j = 0; j < 7; ++j) e[j] = *LDSP(f32x2, plr + j * 512);
        if (EP == 1) E_POLL();
        CR_LOAD(0, CR1);
        if (!CFB) coef();
#pragma unroll
        for (int j = 1; j < 7; ++j) { if (PLN & 4) { float xr = e[j][0], xi = e[j][1]; cmac(xr, xi, L16[0], L16[1], e[j - 1][0], e[j - 1][1]); e[j] = f32x2{xr, xi}; } else cmac2(e[j], L16, e[j - 1]); }
        float mr, mi;
        cmad(mr, mi, L8r, L8i, e[6][0], e[6][1], gr[0], gi[0]);
        if (PLN & 4) c2_mul(hr, hi, C1[0], nC1i, nC1r, nC1i, f32x2{e[6][0], mr}, f32x2{e[6][1], mi}); else pk_conj_mul(hr, hi, C1, f32x2{e[6][0], mr}, f32x2{e[6][1], mi});
        TIE(hr); TIE(hi);
        CR_LOAD(CR1, CR1 + CR2);
        TS(4);
    }
    f32x2 G;
    {
        float pr = 0.f, pi = 0.f;
        if (QP) sy_post<6>(pmine);
        if (!EP) E_POLL();
        if (pneg >= 0) {
            unsigned t0, t1, t2, left = SPIN_CAP;
            asm volatile("s_waitcnt vmcnt(%[c])\n\ts_branch 2f\n1:\n\ts_sleep %[sl]\n\t" EM_ON E_LOADS EM_OFF "\ts_waitcnt vmcnt(0)\n2:\n\t"
                         "v_bitop3_b32 %[t0], v105, v109, %[ta] bitop3:0x7e\n\tv_bitop3_b32 %[t1], v113, v117, %[ta] bitop3:0x7e\n\tv_bitop3_b32 %[t2], v107, v111, %[tg] bitop3:0x7e\n\t"
                         "v_or3_b32 %[t0], %[t0], %[t1], %[t2]\n\tv_bitop3_b32 %[t1], v115, v119, %[tg] bitop3:0x7e\n\tv_or_b32 %[t0], %[t0], %[t1]\n\t"
                         "v_cmp_ne_u32 vcc, 0, %[t0]\n\ts_cbranch_vccz 3f\n\ts_sub_u32 %[n], %[n], 1\n\ts_cbranch_scc0 1b\n3:"
                         : "+{v[104:107]}"(eg[0]), "+{v[108:111]}"(eg[1]), "+{v[112:115]}"(eg[2]), "+{v[116:119]}"(eg[3]), [t0] "=&v"(t0), [t1] "=&v"(t1), [t2] "=&v"(t2), [n] "+s"(left)
                         : [a0] "v"(eo[0]), [a1] "v"(eo[1]), [b0] "s"(ws + WS_E), [b1] "s"(ws + WS_E + 16384), [ta] "s"(tagA), [tg] "s"(tagG), [sl] "n"(ESL), [c] "n"(CR1 + CR2) : "vcc", "scc", "memory");
            float ar = __uint_as_float(eg[1][0]), ai = __uint_as_float(eg[1][2]), br = __uint_as_float(eg[3][0]), bi = __uint_as_float(eg[3][2]);
            cmac(ar, ai, L64r, L64i, __uint_as_float(eg[0][0]), __uint_as_float(eg[0][2]));
            cmac(br, bi, L64r, L64i, __uint_as_float(eg[2][0]), __uint_as_float(eg[2][2]));
            if (EALL) { if (j0 < 0) ar = ai = 0.f; if (j0 + 2 < 0) br = bi = 0.f; }
            const f32x2 pu = cmul4(f32x2{ct[4][2], ct[4][3]}, f32x2{ar, ai});
            pr = pu[0]; pi = pu[1];
            cmac(pr, pi, ct[4][0], ct[4][1], br, bi);
            pr = sum4(pr); pi = sum4(pi);
        }
        TS(5);
        *LDSP(u32x4, pes) = u32x4{__float_as_uint(pr), __float_as_uint(pi), 1u, 0u};
        CR_LOAD(CR1 + CR2, CR1 + CR2 + CR3);
        u32x4 o;
        asm volatile("1:\n\tds_read_b128 %0, %1\n\ts_waitcnt lgkmcnt(0)\n\tv_cmp_eq_u32 vcc, 0, v122\n\ts_cbranch_vccnz 1b" : "=&{v[120:123]}"(o) : "v"(po) : "vcc", "memory");
        G = f32x2{pr + __uint_as_float(o[0]), pi + __uint_as_float(o[1])};
    }
    TS(6);
    {
        if (PLN & 8) c2_mac(hr, hi, G[0], -G[1], -G[0], -G[1], Q2r, Q2i); else pk_conj_mac(hr, hi, G, Q2r, Q2i);
#pragma unroll
        for (int p = 0; p < 8; ++p) { if (PLN & 8) c2_mac(ur[p], ui[p], L1[0], L1[1], L1[0], nL1i, hr, hi); else pk_cmac_conj(ur[p], ui[p], L1, hr, hi); hr = ur[p]; hi = ui[p];
            if (CVE && p == 8 - CVE) { TIE(hr); TIE(hi); sy_post<6>(pmine); } }
        const char* hd = ws + WS_H + (size_t)((tm * 4 + (wm & 2)) * 2 + wn) * HP + (tn * 4 + (wm & 1) * 2) * 1024;
#pragma unroll
        for (int e2 = 0; e2 < 2; ++e2)
#pragma unroll
            for (int c = 0; c < 2; ++c) {
                const f32x2* o = c ? ui : ur;
                *LDSP(u32x4, hw[e2] + c * 512) = u32x4{pack2(o[0][e2], o[1][e2]), pack2(o[2][e2], o[3][e2]), pack2(o[4][e2], o[5][e2]), pack2(o[6][e2], o[7][e2])};
            }
        v4i16 t[4][2];
#pragma unroll
        for (int n = 0; n < 4; ++n)
#pragma unroll
            for (int j = 0; j < 2; ++j) t[n][j] = __builtin_amdgcn_ds_read_tr16_b64_v4i16(LDSP(v4i16, ta[j] + n * 1024));
        sy_post<1>(pmine);
        static_for<0, 4>([&](auto ic) {
            constexpr int n = decltype(ic)::value;
            const bf16x8 f = __builtin_shufflevector(t[n][0], t[n][1], 0, 1, 2, 3, 4, 5, 6, 7);
            GST_B128_SC1(l16, f, hd + (n >> 1) * 2 * HP + ((n & 1) ^ wn) * 1024, 0);
        });
    }
    CR_LOAD(CR1 + CR2 + CR3, CR1 + CR2 + CR3 + CR5);
    TS(7);
    bf16x8 fa2[64];
    u32x2 xv[4], dsv;
    const char* Aw = ws + WS_H + (size_t)((tm * 4 + gm) * 2 + gk) * HP;
    const unsigned g2dst = lb + wid * 2048;
#define G2_ISSUE(st, i) do { if constexpr (i) dmao_<1024>(g2v[1], g2src + (SLICE(st) << 7)); else DMA(g2dst + g2_place(st), g2v[0], g2src + (SLICE(st) << 7)); } while (0)
#define G2_A(g) GLD_B128_SC1(fa2[g], l16, Aw + (SLICE((g) >> 2) << 12), ((g) & 3) * 1024)
    {
        {
            sy_wait<1>(pown);
            s16x4 fo[4][2]; bf16x8 c[8];
            static_for<0, 2>([&](auto kc) {
                constexpr int ks = decltype(kc)::value;
                static_for<0, 4>([&](auto ic) { constexpr int i = decltype(ic)::value; dsrtr_<ks * 4096>(fo[2 * ks + (i >> 1)][i & 1], to[i >> 1][i & 1]); });
                static_for<0, 4>([&](auto ic) { constexpr int i = decltype(ic)::value; DSR128(c[4 * ks + i], cad[ks][i & 1], 2 * G2STAGE + i * 2048); });
            });
            static_for<0, 8>([&](auto ic) {
                constexpr int n = decltype(ic)::value, ks = n >> 2, i = n & 3;
                WAIT_L(ks ? 3 - i : 11 - i);
                TIE(c[n]);
                if constexpr (i == 0) { TIE(fo[2 * ks][0]); TIE(fo[2 * ks][1]); TIE(fo[2 * ks + 1][0]); TIE(fo[2 * ks + 1][1]); }
#pragma unroll
                for (int j = 0; j < 2; ++j)
                    acc[i][j] = __builtin_amdgcn_mfma_f32_16x16x32_bf16(__builtin_shufflevector(fo[2 * ks + j][0], fo[2 * ks + j][1], 0, 1, 2, 3, 4, 5, 6, 7), c[n], ks ? acc[i][j] : f32x4{0, 0, 0, 0}, 0, 0, 0);
                SB();
            });
#pragma unroll
            for (int i = 0; i < 4; ++i) { TIE(acc[i][0]); TIE(acc[i][1]); }
        }
        if (CEARLY) sy_post<0>(pmine);
        static_for<0, 4>([&](auto ic) { constexpr int it = decltype(ic)::value; GLD_B64(xv[it], xo, a.x, it * 2048 - 4096); });
        GLD_B64(dsv, dso, a.dskip, 0);
        if (CEARLY) {
            sy_wait<0>(pall);
            static_for<4, 4 + CEARLY>([&](auto ic) { constexpr int n = decltype(ic)::value; G2_ISSUE(n >> 1, n & 1); });
        }
        CR_LOAD(CR1 + CR2 + CR3 + CR5, CR1 + CR2 + CR3 + CR5 + CR4);
        TS(16);
        WAIT_V(5 + CEARLY + CR4 + CR5);
        sy_post<2>(pmine);
        if (!CEARLY || wid == (NOOWN ? 1 : 0) || (NOOWN == 1 && wid == 0)) sy_wait<2, 0>(pall);
        const u32x2 tagv = {__float_as_uint(ct[2][2]), __float_as_uint(ct[2][3])};
        if (wid == (NOOWN ? 1 : 0)) asm volatile("s_mov_b64 exec, 0xffff\n\tglobal_store_dwordx2 %0, %1, %2 " SMOD "\n\ts_mov_b64 exec, -1" :: "v"(fst), "v"(tagv), "s"(flg + FL_H) : "memory");
        if (PFIRST) asm volatile("s_cmp_lg_u32 %3, 0\n\ts_cbranch_scc1 1f\n\tglobal_load_dwordx2 %0, %1, %2 sc1\n1:" : "=&v"(fl) : "v"(fld), "s"(flg + FL_H), "s"(wid) : "scc", "memory");
        TS(8);
        static_for<4 + CEARLY, 10>([&](auto ic) { constexpr int n = decltype(ic)::value; G2_ISSUE(n >> 1, n & 1); });
        CR_LOAD(2 * NR - CRL, 2 * NR);
        TS(13);
        if (wid == 0) {
            unsigned left = SPIN_CAP;
            if (PFIRST)
            asm volatile("s_waitcnt vmcnt(6)\n\ts_branch 3f\n1:\n\tglobal_load_dwordx2 %0, %2, %3 sc1\n\ts_waitcnt vmcnt(0)\n3:\n\tv_cmp_ne_u64 vcc, %0, %4\n\ts_cbranch_vccz 2f\n\ts_sub_u32 %1, %1, 1\n\ts_cbranch_scc0 1b\n2:"
                         : "+v"(fl), "+s"(left) : "v"(fld), "s"(flg + FL_H), "v"(tagv) : "vcc", "scc", "memory");
            else
            asm volatile(FX_ON "1:\n\tglobal_load_dwordx2 %0, %2, %3 " FMOD "\n\ts_waitcnt vmcnt(0)\n\tv_cmp_ne_u64 vcc, %0, %4\n\ts_cbranch_vccz 2f\n\ts_sub_u32 %1, %1, 1\n\ts_cbranch_scc0 1b\n2:" FX_OFF
                         : "=&v"(fl), "+s"(left) : "v"(fld), "s"(flg + FL_H), "v"(tagv) : "vcc", "scc", "memory");
            sy_post<3>(pmine);
            if (b == 255 && lane == 0) GST_B64_SC1(0u * lane, tagv, flg + FL_EPOCH);
        } else if (g2_bar(3)) sy_wait<3>(pzero);
        else { WAIT_V(CRL); sy_post<3>(pmine); }
        if (!g2_bar(3)) sy_wait<3>(pall);
        TS(20);
        static_for<4, 4 + AW>([&](auto ic) { constexpr int g = decltype(ic)::value; G2_A(g); });
    }
    TS(9);
    static_assert(EP || CR1 + CR2 == 0, "count");
    __builtin_amdgcn_s_setprio(G2PRIO);
    unsigned yw, yr, yp, yo, lzw;
    static_assert(PAIR, "yr");
    unsigned dm[4];
    auto dmask = [&]() { TIE(dsv); dm[0] = (RX & 8) ? dsv[0] << 16 : dsv[0] & 0xffffu; dm[1] = dsv[0] & 0xffff0000u; dm[2] = (RX & 8) ? dsv[1] << 16 : dsv[1] & 0xffffu; dm[3] = dsv[1] & 0xffff0000u; TIE(dm[0]); TIE(dm[1]); TIE(dm[2]); TIE(dm[3]); };
    auto skip = [&](int it) { TIE(xv[it]);
        if (RX & 8) {
            const float x4[4] = {bflo(xv[it][0]), bfhi(xv[it][0]), bflo(xv[it][1]), bfhi(xv[it][1])};
            for (int i = 0; i < 4; ++i) { float t = acc[i][0][it]; asm("v_fmac_f32 %0, %1, %2" : "+v"(t) : "v"(x4[i]), "v"(dm[i])); acc[i][0][it] = t; }
            if (it & 1) { for (int i = 0; i < 4; ++i) TIE(acc[i][0]); }
            return;
        }
        const f32x4 y = mul4bf(xv[it], dm); for (int i = 0; i < 4; ++i) acc[i][0][it] = add1(acc[i][0][it], y[i]); if (it & 1) { for (int i = 0; i < 4; ++i) TIE(acc[i][0]); } };
    {
        bf16x8 fb[2][4];
#define G2_FRAG(g, i) do { constexpr int pl_ = g2_place((g) >> 1), b_ = pl_ >= L_C ? L_C : pl_ >= 32768 ? 32768 : 0; DSR128(fb[(g) & 1][i], cad[(g) & 1][(i) & 1] + (b_ - L_C), pl_ - b_ + (i) * 2048); } while (0)
        static_for<0, 4>([&](auto ic) { constexpr int i = decltype(ic)::value; G2_FRAG(2, i); });
        if (YS == 0) { WAIT_V(AW + 6 + CRL + CR4); dmask(); skip(0); skip(1); skip(2); skip(3); }
        static_for<2, 32>([&](auto ic) {
            constexpr int g = decltype(ic)::value, gn = g + 1;
            constexpr bool bar = g2_bar(g) && !((VBM >> g) & 1);
            constexpr int n0 = g2_upto(g - 1), n1 = g2_upto(g);
            if constexpr (bar) {
                constexpr int ca = g2_cnt(2 * g, 2 * g), cs = g2_cover(g) >= 7 + NR && !(LZ && g == LASTB) ? g2_cnt(100 + 2 * g2_cover(g) + 1, 2 * g) : 63;
                static_assert(cs >= 0 && g2_wr(g - 1) >= 2 * (g2_cover(g) < 6 + NR ? g2_cover(g) - 6 : NR), "schedule");
                WAIT_VB(AWB && ca < cs ? ca : cs); WAIT_LB(0); BAR();
                if constexpr (!AWB) WAIT_V(ca);
#pragma unroll
                for (int i = 0; i < 4; ++i) if (!(AMF & 4)) TIE(fb[g & 1][i]);
                if (!(AMF & 4)) TIE(fa2[2 * g]);
            }
            if constexpr (LZ && g == lz_chk()) { WAIT_LB(0); sy_chk<0>(lzw, pall); }
            constexpr int xl = g2_wr(g - 1) - g2_wr(g - 2) + (g - 1 == EGX) + (LZ && g - 1 == lz_post());
            static_for<0, 8>([&](auto mc) {
                constexpr int m = decltype(mc)::value, i = m & 3, j = m >> 2, f = 2 * g + j;
                static_assert(g2_cnt(f, f) >= 0, "schedule");
                constexpr int jj = LSW && g == 31 ? 1 - j : j, ff = 2 * g + jj;
                if constexpr (i == 0 && (j || !bar)) { WAIT_VB(LSW && g == 31 ? 0 : g2_cnt(f, f)); if constexpr (!(AMF & 4) || g == 2 || g == 31) TIE(fa2[ff]); }
                if constexpr (W2 && g > 2 && g < 31) { if constexpr (!bar && j == 0 && (i & W2) == 0) WAIT_LB(3 - W2 + xl); } else
                if constexpr (!bar && j == 0) { WAIT_LB((gn < 32 ? 3 : 3 - i) + xl); if constexpr (!(AMF & 4) || g == 2 || g == 31) TIE(fb[g & 1][i]); }
                constexpr int is = (SNK & 2) && j ? 3 - i : i;
                if constexpr ((AMF & 4) && g > 2 && g < 31) mfma_(acc[is][jj], fa2[ff], fb[g & 1][is]); else
                acc[i][jj] = __builtin_amdgcn_mfma_f32_16x16x32_bf16(fa2[ff], fb[g & 1][i], acc[i][jj], 0, 0, 0);
                SB();
                if constexpr (LSW && g == 31 && j) *LDSP(f32x4, yw + i * 1024) = acc[i][1];
                if constexpr (g == EGX && m == 4) sy_post<5>(pmine);
                if constexpr (LZ && g == lz_post() && m == 4) sy_post<0>(pmine);
                if constexpr (LZ && g + 1 == lz_chk() && m == 4) sy_pre<0>(lzw, pall);
                if constexpr (g == 29 && m == 5) { yw = cwa + L_Y + wid * 2048; yr = yw ^ (PAIR ? 4096 : 16384); yp = pmine ^ (PAIR ? 4 : 16); yo = (cwa & 255) + ((cwa & 768) << 7); TIE(yw); TIE(yr); TIE(yp); TIE(yo); }
                if constexpr (gn < 32 && m < 4) G2_FRAG(gn, m);
                if constexpr (m >= 4 && g2_wr(g - 1) + m - 4 < g2_wr(g)) {
                    constexpr int n = g2_wr(g - 1) + m - 4, pl = g2_place(7 + (n >> 1)), pb = pl >= L_C ? L_C : 0;
                    asm volatile("ds_write_b128 %0, %1 offset:%2" :: "v"(cwa + pb), "v"(cr[n]), "n"(pl - pb + (n & 1) * 1024) : "memory");
                }
                if constexpr (i == 1 && n0 + j < n1) G2_ISSUE((n0 + j) >> 1, (n0 + j) & 1);
                if constexpr (i >= 2 && g2_ald(f, i & 1) >= 0) G2_A(g2_ald(f, i & 1));
                SB();
            });
        });
#undef G2_FRAG
    }
    TS(11);
    if (EPRIO) __builtin_amdgcn_s_setprio(EPRIO);
    {
#pragma unroll
        for (int i = 0; i < 4; ++i) if (!LSW) *LDSP(f32x4, yw + i * 1024) = acc[i][1];
        sy_post<4>(pmine);
        sy_wait<4, 0>(yp);
        char* const ob = a.out + (size_t)(tm * 128 + gm * 32 + gk * 16) * 8192 + 4096 + tn * 256;
        f32x4 w[4];
#pragma unroll
        for (int i = 0; i < 4; ++i) w[i] = *LDSP(f32x4, yr + i * 1024);
        f32x4 o[4];
#pragma unroll
        for (int r = 0; r < 4; ++r) { o[r] = f32x4{add1(w[0][r], acc[0][0][r]), add1(w[1][r], acc[1][0][r]), add1(w[2][r], acc[2][0][r]), add1(w[3][r], acc[3][0][r])}; if (EGX) TIE(o[r]); }
        if (EGX && !wn) sy_wait<5, 1>(pall);
#pragma unroll
        for (int r = 0; r < 4; ++r) GST_B128_NT(yo, o[r], ob + r * 8192, 0);
    }
    asm volatile(".Lfk_code_b:");
    TSE(12);
    TS_FLUSH();
}

}

extern "C" void kernel_launch(void* const* d_in, const int* in_sizes, int n_in, void* d_out, int out_size, void* d_ws, size_t ws_size,
                              hipStream_t stream) {
    (void)in_sizes; (void)n_in; (void)out_size; (void)ws_size;
    fk::Args a;
    a.q = (const char*)d_in[0]; a.k = (const char*)d_in[1]; a.v = (const char*)d_in[2]; a.sk = (const char*)d_in[3];
    a.ssmk = (const char*)d_in[4]; a.ssmD = (const char*)d_in[5]; a.x = (const char*)d_in[6]; a.lre = (const char*)d_in[7];
    a.lim = (const char*)d_in[8]; a.bre = (const char*)d_in[9]; a.bim = (const char*)d_in[10]; a.cre = (const char*)d_in[11];
    a.cim = (const char*)d_in[12]; a.logdt = (const char*)d_in[13]; a.dskip = (const char*)d_in[14];
    a.out = (char*)d_out; a.ws = (char*)d_ws;
    hipLaunchKernelGGL(fk::fused, dim3(256), dim3(768), 0, stream, a);
}
```
